```python
import torch

def _softmax(x, axis=-1):
    return torch.softmax(x, dim=axis)
def _gelu(x):
    return torch.nn.functional.gelu(x, approximate="tanh")
def _silu(x):
    return torch.nn.functional.silu(x)
def _sigmoid(x):
    return torch.sigmoid(x)
def _layernorm(x, axis=-1, eps=1e-5):
    m = x.mean(dim=axis, keepdim=True)
    v = x.var(dim=axis, keepdim=True, unbiased=False)
    return (x - m) / torch.sqrt(v + eps)
def _scatter_add(tgt, idx, src, axis=0):
    return tgt.clone().index_add_(axis, idx, src)
def _segment_sum(src, seg, n):
    out = torch.zeros((n,) + src.shape[1:], dtype=src.dtype)
    return out.index_add_(0, seg, src)
def _pad(x, pw):
    flat = tuple(v for pair in reversed(pw) for v in pair)
    return torch.nn.functional.pad(x, flat)

kernel_name = 'interleave_layers__ssm_conv_h3__swiglu_ffn__prompt'

def setup_inputs():

    _w = (lambda a: (lambda t: t.to(torch.bfloat16) if t.is_floating_point() else t)(torch.as_tensor(a)))
    def _A_setup():
        B, S, D = 1, 2048, 640
        ks = 4
        return {
            "q": _w((lambda *s: torch.randn(*s, dtype=torch.bfloat16) * 0.05)(B, S, D)),
            "k": _w((lambda *s: torch.randn(*s, dtype=torch.bfloat16) * 0.8)(B, S, D)),
            "v": _w((lambda *s: torch.randn(*s, dtype=torch.bfloat16) * 0.8)(B, S, D)),
            "shift_k": _w((lambda *s: torch.randn(*s, dtype=torch.bfloat16) * 0.8)(D, ks) * 0.5),
            "ssm_k": _w((lambda *s: torch.randn(*s, dtype=torch.bfloat16) * 0.8)(D, S) * (1.0 / S ** 0.5)),
            "ssm_D": _w((lambda *s: torch.randn(*s, dtype=torch.bfloat16) * 0.8)(D)),
        }
    _A_in = _A_setup()

    def _B_setup():
        B, S, D, F = 1, 2048, 640, 1536
        return {
            "x": _w((lambda *s: torch.randn(*s, dtype=torch.bfloat16) * 0.05)(B, S, D)),
            "w_gate": _w((lambda *s: torch.randn(*s, dtype=torch.bfloat16) * 0.1)(D, F)),
            "w_up": _w((lambda *s: torch.randn(*s, dtype=torch.bfloat16) * 0.1)(D, F)),
            "w_down": _w((lambda *s: torch.randn(*s, dtype=torch.bfloat16) * 0.1)(F, D)),
        }
    _B_in = _B_setup()

    return {
        "A_q": _A_in["q"],
        "A_k": _A_in["k"],
        "A_v": _A_in["v"],
        "A_shift_k": _A_in["shift_k"],
        "A_ssm_k": _A_in["ssm_k"],
        "A_ssm_D": _A_in["ssm_D"],
        "B_x": _B_in["x"],
        "B_w_gate": _B_in["w_gate"],
        "B_w_up": _B_in["w_up"],
        "B_w_down": _B_in["w_down"],
    }

def reference(A_q, A_k, A_v, A_shift_k, A_ssm_k, A_ssm_D, B_x, B_w_gate, B_w_up, B_w_down):
    def _A_ref(q, k, v, shift_k, ssm_k, ssm_D):
        B, S, D = q.shape
        ks = shift_k.shape[-1]
        k_pad = _pad(k, ((0, 0), (ks - 1, 0), (0, 0)))
        k_shift = torch.zeros_like(k)
        for t in range(ks):
            k_shift = k_shift + k_pad[:, t:t + S, :] * shift_k[:, ks - 1 - t][None, None, :]
        kv = k_shift * v
        L = 2 * S
        kv_pad = _pad(kv, ((0, 0), (0, S), (0, 0)))
        kf_pad = _pad(ssm_k, ((0, 0), (0, S)))
        kv_f = (lambda x, axes=(-2,-1): torch.fft.rfft2(x.float(), dim=axes))(kv_pad[:, None, :, :], axes=(1, 2))
        kf_f = (lambda x, axes=(-2,-1): torch.fft.rfft2(x.float(), dim=axes))(kf_pad[:, None, :], axes=(1, 2))
        kf_f = (lambda x, *d: x.permute(*d))(kf_f, 1, 2, 0)
        y_f = kv_f * kf_f[None, :, :, :]
        y = (lambda x, s, axes=(-2,-1): torch.fft.irfft2(x, s=s, dim=axes).to(torch.bfloat16))(y_f, (1, L), axes=(1, 2))[:, 0, :S, :]
        y = y + kv * ssm_D[None, None, :]
        return q * y

    def _B_ref(x, w_gate, w_up, w_down):
        g = _silu(x @ w_gate)
        u = x @ w_up
        return (g * u) @ w_down

    x = A_q
    x = x + _A_ref(_layernorm(x, axis=-1), A_k, A_v, A_shift_k, A_ssm_k, A_ssm_D)
    x = x + _B_ref(_layernorm(x, axis=-1), B_w_gate, B_w_up, B_w_down)
    x = x + _A_ref(_layernorm(x, axis=-1), A_k, A_v, A_shift_k, A_ssm_k, A_ssm_D)
    x = x + _B_ref(_layernorm(x, axis=-1), B_w_gate, B_w_up, B_w_down)
    return x
```

```cpp
#include <hip/hip_runtime.h>
#include <hip/hip_bf16.h>
typedef unsigned short u16;
constexpr int S = 2048, D = 640, F = 1536;
__device__ __forceinline__ float b2f(u16 b) { return __builtin_bit_cast(float, (unsigned)b << 16); }
__device__ __forceinline__ u16 f2b(float f) { __hip_bfloat16 h = __float2bfloat16(f); return __builtin_bit_cast(u16, h); }
__device__ __forceinline__ float rb(float f) { return b2f(f2b(f)); }

__global__ void k_kv(const u16* k, const u16* v, const u16* sk, u16* kvT) {
  int i = blockIdx.x * 256 + threadIdx.x; int s = i / D, d = i % D;
  float acc = 0.f;
  for (int t = 0; t < 4; ++t) { int ss = s + t - 3; float kk = ss >= 0 ? b2f(k[ss * D + d]) : 0.f; acc += kk * b2f(sk[d * 4 + 3 - t]); }
  kvT[d * S + s] = f2b(acc * b2f(v[i]));
}
__global__ void k_conv(const u16* kvT, const u16* h, const u16* Dv, float* Y) {
  __shared__ float su[S], sh[S];
  int d = blockIdx.x;
  for (int i = threadIdx.x; i < S; i += 256) { su[i] = b2f(kvT[d * S + i]); sh[i] = b2f(h[d * S + i]); }
  __syncthreads();
  for (int t = threadIdx.x; t < S; t += 256) {
    float acc = 0.f;
    for (int j = 0; j <= t; ++j) acc += su[j] * sh[t - j];
    Y[t * D + d] = acc + su[t] * b2f(Dv[d]);
  }
}
__global__ void k_row(const u16* xin_b, const float* xin_f, const float* Y, float* xo, u16* xn) {
  int s = blockIdx.x; int t = threadIdx.x;
  float x[10]; float sum = 0.f;
  for (int i = 0; i < 10; ++i) { int d = t + 64 * i; x[i] = xin_b ? b2f(xin_b[s * D + d]) : xin_f[s * D + d]; sum += x[i]; }
  for (int o = 32; o; o >>= 1) sum += __shfl_xor(sum, o);
  float m = sum / D, vs = 0.f;
  for (int i = 0; i < 10; ++i) vs += (x[i] - m) * (x[i] - m);
  for (int o = 32; o; o >>= 1) vs += __shfl_xor(vs, o);
  float r = rsqrtf(vs / D + 1e-5f);
  sum = 0.f;
  for (int i = 0; i < 10; ++i) { int d = t + 64 * i; x[i] = x[i] + (x[i] - m) * r * Y[s * D + d]; sum += x[i]; xo[s * D + d] = x[i]; }
  for (int o = 32; o; o >>= 1) sum += __shfl_xor(sum, o);
  m = sum / D; vs = 0.f;
  for (int i = 0; i < 10; ++i) vs += (x[i] - m) * (x[i] - m);
  for (int o = 32; o; o >>= 1) vs += __shfl_xor(vs, o);
  r = rsqrtf(vs / D + 1e-5f);
  for (int i = 0; i < 10; ++i) { int d = t + 64 * i; xn[s * D + d] = f2b((x[i] - m) * r); }
}
__global__ void k_g1(const u16* xn, const u16* wg, const u16* wu, u16* H) {
  __shared__ float sx[8][D];
  int s0 = blockIdx.y * 8; int f = blockIdx.x * 256 + threadIdx.x;
  for (int i = threadIdx.x; i < 8 * D; i += 256) sx[i / D][i % D] = b2f(xn[s0 * D + i]);
  __syncthreads();
  float g[8] = {}, u[8] = {};
  for (int d = 0; d < D; ++d) { float a = b2f(wg[d * F + f]), b = b2f(wu[d * F + f]); for (int i = 0; i < 8; ++i) { g[i] += sx[i][d] * a; u[i] += sx[i][d] * b; } }
  for (int i = 0; i < 8; ++i) H[(s0 + i) * F + f] = f2b(g[i] / (1.f + __expf(-g[i])) * u[i]);
}
__global__ void k_g2(const u16* H, const u16* wd, const float* xres, float* xo, u16* out) {
  __shared__ float sx[8][F];
  int s0 = blockIdx.y * 8; int d = blockIdx.x * 128 + threadIdx.x;
  for (int i = threadIdx.x; i < 8 * F; i += 128) sx[i / F][i % F] = b2f(H[s0 * F + i]);
  __syncthreads();
  float o[8] = {};
  for (int f = 0; f < F; ++f) { float a = b2f(wd[f * D + d]); for (int i = 0; i < 8; ++i) o[i] += sx[i][f] * a; }
  for (int i = 0; i < 8; ++i) { float r = xres[(s0 + i) * D + d] + o[i]; if (xo) xo[(s0 + i) * D + d] = r; if (out) out[(s0 + i) * D + d] = f2b(r); }
}
extern "C" void kernel_launch(void* const* d_in, const int* in_sizes, int n_in, void* d_out, int out_size, void* d_ws, size_t ws_size, hipStream_t stream) {
  const u16* q = (const u16*)d_in[0]; const u16* k = (const u16*)d_in[1]; const u16* v = (const u16*)d_in[2];
  const u16* sk = (const u16*)d_in[3]; const u16* h = (const u16*)d_in[4]; const u16* Dv = (const u16*)d_in[5];
  const u16* wg = (const u16*)d_in[7]; const u16* wu = (const u16*)d_in[8]; const u16* wd = (const u16*)d_in[9];
  char* ws = (char*)d_ws;
  u16* kvT = (u16*)ws; float* Y = (float*)(ws + (8 << 20)); float* xa = (float*)(ws + (16 << 20)); float* xb = (float*)(ws + (24 << 20));
  u16* xn = (u16*)(ws + (32 << 20)); u16* H = (u16*)(ws + (40 << 20));
  k_kv<<<S * D / 256, 256, 0, stream>>>(k, v, sk, kvT);
  k_conv<<<D, 256, 0, stream>>>(kvT, h, Dv, Y);
  k_row<<<S, 64, 0, stream>>>(q, nullptr, Y, xa, xn);
  k_g1<<<dim3(F / 256, S / 8), 256, 0, stream>>>(xn, wg, wu, H);
  k_g2<<<dim3(D / 128, S / 8), 128, 0, stream>>>(H, wd, xa, xb, nullptr);
  k_row<<<S, 64, 0, stream>>>(nullptr, xb, Y, xa, xn);
  k_g1<<<dim3(F / 256, S / 8), 256, 0, stream>>>(xn, wg, wu, H);
  k_g2<<<dim3(D / 128, S / 8), 128, 0, stream>>>(H, wd, xa, nullptr, (u16*)d_out);
}
```

```cpp
#include <hip/hip_runtime.h>
typedef unsigned short u16;
typedef unsigned u32;
typedef unsigned u32x4 __attribute__((ext_vector_type(4)));
typedef unsigned u32x2 __attribute__((ext_vector_type(2)));
typedef float f32x4 __attribute__((ext_vector_type(4)));
typedef float f32x2 __attribute__((ext_vector_type(2)));
typedef __bf16 bf16x8 __attribute__((ext_vector_type(8)));
typedef __bf16 bf16x2 __attribute__((ext_vector_type(2)));
typedef short s16x4 __attribute__((ext_vector_type(4)));
#define GAS __attribute__((address_space(1)))
#define LAS __attribute__((address_space(3)))
#define DI __device__ __forceinline__
constexpr int S = 2048, D = 640, F = 1536;
constexpr size_t OFF_UR = 0;
constexpr size_t OFF_QN = OFF_UR + 640 * 4096;
constexpr size_t OFF_WG = OFF_QN + 160 * 32768;
constexpr size_t OFF_WD = OFF_WG + 16 * 240 * 1024;
constexpr size_t OFF_WS = OFF_WD + 8 * 240 * 1024;
constexpr size_t OFF_X1 = OFF_WS + 20 * 3072 * 4;
constexpr size_t OFF_Y  = OFF_X1 + 80 * 32768;
constexpr size_t OFF_X2 = OFF_Y + 80 * 32768;
constexpr size_t OFF_X3 = OFF_X2 + 80 * 32768;
constexpr size_t OFF_H  = OFF_X3 + 80 * 32768;
constexpr size_t WS_END = OFF_H + 192 * 32768;

DI f32x4 mfma(u32x4 a, u32x4 b, f32x4 c) { return __builtin_amdgcn_mfma_f32_16x16x32_bf16(__builtin_bit_cast(bf16x8, a), __builtin_bit_cast(bf16x8, b), c, 0, 0, 0); }
DI u32 cvtpk(float lo, float hi) { f32x2 v = {lo, hi}; bf16x2 b = __builtin_convertvector(v, bf16x2); return __builtin_bit_cast(u32, b); }
DI float blo(u32 x) { return __builtin_bit_cast(float, x << 16); }
DI float bhi(u32 x) { return __builtin_bit_cast(float, x & 0xffff0000u); }
DI float rbf(float x) { return blo(cvtpk(x, x)); }
DI u32x2 trread(const LAS char* p) { return __builtin_bit_cast(u32x2, __builtin_amdgcn_ds_read_tr16_b64_v4i16((LAS s16x4*)p)); }
DI float dot2(u32 a, u32 b, float c) { return __builtin_amdgcn_fdot2_f32_bf16(__builtin_bit_cast(bf16x2, a), __builtin_bit_cast(bf16x2, b), c, false); }
DI u32 perm(u32 hi, u32 lo, u32 sel) { return __builtin_amdgcn_perm(hi, lo, sel); }
template <typename T> DI T ntload(const void* p) { return __builtin_nontemporal_load((const GAS T*)p); }
DI void ln_scalars(float s1, float s2, float& m, float& sd) {
  float mean = s1 * (1.f / D);
  float var = fmaxf(s2 * (1.f / D) - mean * mean, 0.f);
  m = rbf(mean);
  sd = rbf(__builtin_amdgcn_sqrtf(rbf(rbf(var) + 1e-5f)));
}

__global__ __launch_bounds__(768) void k_prep(const u16* __restrict__ q, const u16* __restrict__ k, const u16* __restrict__ v, const u16* __restrict__ sk,
                                              const u16* __restrict__ wg, const u16* __restrict__ wu, const u16* __restrict__ wd, char* __restrict__ ws) {
  __shared__ __attribute__((aligned(16))) char smem[32 * 800 + 256];
  const int tid = threadIdx.x, lane = tid & 63, w = __builtin_amdgcn_readfirstlane(tid >> 6);
  const int bb = (blockIdx.x & 7) * 32 + (blockIdx.x >> 3);
  LAS char* L = (LAS char*)smem;
  LAS float* red = (LAS float*)(L + 32 * 800);
  if (w < 3) {
    const int rg = bb >> 3, cg = bb & 7;
    const int qd = tid >> 3, oc = tid & 7;
    if (qd < 20) {
      const int ch = cg * 80 + qd * 4, t0 = rg * 64 + oc * 8;
      u32x2 kk[11], vv[8];
#pragma unroll
      for (int i = 0; i < 11; ++i) { int t = t0 - 3 + i; kk[i] = t >= 0 ? *(const u32x2*)(k + t * D + ch) : u32x2{0, 0}; }
#pragma unroll
      for (int i = 0; i < 8; ++i) vv[i] = ntload<u32x2>(v + (t0 + i) * D + ch);
      u32x4 s01 = *(const u32x4*)(sk + ch * 4), s23 = *(const u32x4*)(sk + ch * 4 + 8);
#pragma unroll
      for (int c = 0; c < 4; ++c) {
        u32 sa = c == 0 ? s01[0] : c == 1 ? s01[2] : c == 2 ? s23[0] : s23[2];
        u32 sb = c == 0 ? s01[1] : c == 1 ? s01[3] : c == 2 ? s23[1] : s23[3];
        float f0 = blo(sa), f1 = bhi(sa), f2 = blo(sb), f3 = bhi(sb);
        float kf[11], o[8];
#pragma unroll
        for (int i = 0; i < 11; ++i) kf[i] = (c & 1) ? bhi(kk[i][c >> 1]) : blo(kk[i][c >> 1]);
#pragma unroll
        for (int i = 0; i < 8; ++i) {
          float a = kf[i] * f3; a = fmaf(kf[i + 1], f2, a); a = fmaf(kf[i + 2], f1, a); a = fmaf(kf[i + 3], f0, a);
          float vf = (c & 1) ? bhi(vv[i][c >> 1]) : blo(vv[i][c >> 1]);
          o[i] = a * vf;
        }
        u32x4 r = {cvtpk(o[7], o[6]), cvtpk(o[5], o[4]), cvtpk(o[3], o[2]), cvtpk(o[1], o[0])};
        *(u32x4*)(ws + OFF_UR + (size_t)(ch + c) * 4096 + (2040 - t0) * 2) = r;
      }
    }
    __syncthreads();
  } else if (w == 3) {
    __syncthreads();
  } else if (w < 8) {
    const int ww = w - 4, qs = lane >> 3, tk = lane & 7, t = bb * 8 + tk;
    u32x2 x[5]; float s1 = 0.f, s2 = 0.f;
#pragma unroll
    for (int i = 0; i < 5; ++i) x[i] = ntload<u32x2>(q + t * D + ((ww * 5 + i) * 8 + qs) * 4);
#pragma unroll
    for (int i = 0; i < 5; ++i) { s1 = dot2(x[i][0], 0x3f803f80u, s1); s1 = dot2(x[i][1], 0x3f803f80u, s1); s2 = dot2(x[i][0], x[i][0], s2); s2 = dot2(x[i][1], x[i][1], s2); }
    for (int o = 8; o < 64; o <<= 1) { s1 += __shfl_xor(s1, o); s2 += __shfl_xor(s2, o); }
    if (lane < 8) { red[(ww * 8 + tk) * 2] = s1; red[(ww * 8 + tk) * 2 + 1] = s2; }
    __syncthreads();
    s1 = 0.f; s2 = 0.f;
#pragma unroll
    for (int i = 0; i < 4; ++i) { s1 += red[(i * 8 + tk) * 2]; s2 += red[(i * 8 + tk) * 2 + 1]; }
    float m, sd; ln_scalars(s1, s2, m, sd);
    float rs = 1.f / sd;
#pragma unroll
    for (int i = 0; i < 5; ++i) {
      u32x4 o;
#pragma unroll
      for (int j = 0; j < 2; ++j) {
        u32 d = cvtpk(blo(x[i][j]) - m, bhi(x[i][j]) - m);
        u32 n = cvtpk(blo(d) * rs, bhi(d) * rs);
        o[2 * j] = perm(n, x[i][j], 0x05040100);
        o[2 * j + 1] = perm(n, x[i][j], 0x07060302);
      }
      *(u32x4*)(ws + OFF_QN + (size_t)((ww * 5 + i) * 8 + qs) * 32768 + t * 16) = o;
    }
  } else {
    const int ww = w - 8, t4 = tid - 512;
    const int g = lane >> 4, qq = (lane & 15) >> 2, p = lane & 3;
    if (bb < 160) {
      const int ks = bb >> 3, sp = bb & 7;
      u32x4 a[3], b[3];
#pragma unroll
      for (int i = 0; i < 3; ++i) { int it = t4 + 256 * i, row = it / 24, c = it % 24; size_t o = (size_t)(ks * 32 + row) * F + sp * 192 + c * 8; a[i] = ntload<u32x4>(wg + o); b[i] = ntload<u32x4>(wu + o); }
#pragma unroll
      for (int i = 0; i < 3; ++i) { int it = t4 + 256 * i, row = it / 24, c = it % 24;
        u32x4 lo = {perm(b[i][0], a[i][0], 0x05040100), perm(b[i][0], a[i][0], 0x07060302), perm(b[i][1], a[i][1], 0x05040100), perm(b[i][1], a[i][1], 0x07060302)};
        u32x4 hi = {perm(b[i][2], a[i][2], 0x05040100), perm(b[i][2], a[i][2], 0x07060302), perm(b[i][3], a[i][3], 0x05040100), perm(b[i][3], a[i][3], 0x07060302)};
        *(LAS u32x4*)(L + row * 800 + c * 32) = lo; *(LAS u32x4*)(L + row * 800 + c * 32 + 16) = hi; }
      __syncthreads();
#pragma unroll
      for (int i = 0; i < 6; ++i) {
        const int c = ww * 6 + i;
        u32x2 r0 = trread(L + (8 * g + qq) * 800 + c * 32 + p * 8), r1 = trread(L + (8 * g + 4 + qq) * 800 + c * 32 + p * 8);
        u32x4 fr = {r0[0], r0[1], r1[0], r1[1]};
        const int sl = 2 * sp + c / 12, wv = (c % 12) / 3, ft = c % 3;
        *(u32x4*)(ws + OFF_WG + ((size_t)((sl * 4 + wv) * 20 + ks) * 3 + ft) * 1024 + lane * 16) = fr;
        f32x4 cs = mfma(fr, u32x4{0x3f803f80u, 0x3f803f80u, 0x3f803f80u, 0x3f803f80u}, f32x4{0, 0, 0, 0});
        if ((lane & 15) == 0) *(f32x4*)(ws + OFF_WS + ((size_t)ks * 3072 + (sp * 24 + c) * 16 + g * 4) * 4) = cs;
      }
    } else {
      const int ks = (bb - 160) >> 1, hf = (bb - 160) & 1;
      u32x4 a[5];
#pragma unroll
      for (int i = 0; i < 5; ++i) { int it = t4 + 256 * i, row = it / 40, c = it % 40; a[i] = ntload<u32x4>(wd + (size_t)(ks * 32 + row) * D + hf * 320 + c * 8); }
#pragma unroll
      for (int i = 0; i < 5; ++i) { int it = t4 + 256 * i, row = it / 40, c = it % 40; *(LAS u32x4*)(L + row * 800 + c * 16) = a[i]; }
      __syncthreads();
#pragma unroll
      for (int dt = 0; dt < 5; ++dt) {
        const int col = dt < 4 ? (dt >> 1) * 32 + 8 * p + 4 * (dt & 1) : 64 + 4 * p;
        u32x2 r0 = trread(L + (8 * g + qq) * 800 + ww * 160 + col * 2), r1 = trread(L + (8 * g + 4 + qq) * 800 + ww * 160 + col * 2);
        u32x4 fr = {r0[0], r0[1], r1[0], r1[1]};
        *(u32x4*)(ws + OFF_WD + ((size_t)((4 * hf + ww) * 48 + ks) * 5 + dt) * 1024 + lane * 16) = fr;
      }
    }
  }
}

constexpr int HCS = 4160, CH_LDS = 4 * HCS + 4608;
__global__ __launch_bounds__(256, 1) void k_conv(const u16* __restrict__ h, const u16* __restrict__ Dv, char* __restrict__ ws) {
  extern __shared__ __attribute__((aligned(16))) char smem[];
  const int tid = threadIdx.x, lane = tid & 63, w = __builtin_amdgcn_readfirstlane(tid >> 6);
  const int quad = blockIdx.x, ch = quad * 4 + w;
  LAS char* L = (LAS char*)smem + w * CH_LDS;
  LAS char* LU = L + 4 * HCS;
  {
    u32x4 a[4], u[4]; u32x2 b[4];
#pragma unroll
    for (int i = 0; i < 4; ++i) { int m = i * 64 + lane; a[i] = ntload<u32x4>(h + (size_t)ch * S + m * 8); b[i] = m < 255 ? *(const u32x2*)(h + (size_t)ch * S + m * 8 + 8) : u32x2{0, 0};
      u[i] = *(const u32x4*)(ws + OFF_UR + (size_t)ch * 4096 + m * 16); }
    if (lane < 30) *(LAS u32x4*)(LU + 4096 + lane * 16) = u32x4{0, 0, 0, 0};
#pragma unroll
    for (int i = 0; i < 4; ++i) { int m = i * 64 + lane;
      u32 w0 = a[i][0], w1 = a[i][1], w2 = a[i][2], w3 = a[i][3], w4 = b[i][0], w5 = b[i][1];
      u32 o0 = __builtin_amdgcn_alignbit(w1, w0, 16), o1 = __builtin_amdgcn_alignbit(w2, w1, 16), o2 = __builtin_amdgcn_alignbit(w3, w2, 16), o3 = __builtin_amdgcn_alignbit(w4, w3, 16), o4 = __builtin_amdgcn_alignbit(w5, w4, 16);
      *(LAS u32x4*)(L + 0 * HCS + 32 + m * 16) = u32x4{w0, w1, w2, w3};
      *(LAS u32x4*)(L + 1 * HCS + 32 + m * 16) = u32x4{o0, o1, o2, o3};
      *(LAS u32x4*)(L + 2 * HCS + 32 + m * 16) = u32x4{w1, w2, w3, w4};
      *(LAS u32x4*)(L + 3 * HCS + 32 + m * 16) = u32x4{o1, o2, o3, o4};
      *(LAS u32x4*)(LU + m * 16) = u[i];
      if (i == 0 && lane == 0) {
#pragma unroll
        for (int s = 0; s < 4; ++s) *(LAS u32x4*)(L + s * HCS) = u32x4{0, 0, 0, 0};
        *(LAS u32x4*)(L + 0 * HCS + 16) = u32x4{0, 0, 0, 0};
        *(LAS u32x4*)(L + 1 * HCS + 16) = u32x4{0, 0, 0, w0 << 16};
        *(LAS u32x4*)(L + 2 * HCS + 16) = u32x4{0, 0, 0, w0};
        *(LAS u32x4*)(L + 3 * HCS + 16) = u32x4{0, 0, w0 << 16, o0};
      }
    }
  }
  __syncthreads();
  const int r = lane & 15, g = lane >> 4;
  const int s = (r + 1) & 3;
  const LAS char* pa = L + s * HCS + (8 * g + r + 1 - s) * 2;
  const LAS char* pb = LU + (2032 - 16 * r + 8 * g) * 2;
  f32x4 acc[8];
  {
    u32 dv = Dv[ch]; u32x4 ad;
    const int kk = 15 - r - 8 * g;
#pragma unroll
    for (int j = 0; j < 4; ++j) ad[j] = (kk == 2 * j ? dv : 0u) | (kk == 2 * j + 1 ? dv << 16 : 0u);
#pragma unroll
    for (int c = 0; c < 8; ++c) acc[c] = mfma(ad, *(const LAS u32x4*)(pb - 64 * 8 * c), f32x4{0, 0, 0, 0});
  }
#pragma unroll 1
  for (int rho = 0; rho < 8; ++rho) {
    u32x4 A[8], B[8];
#pragma unroll
    for (int a = 0; a < 8; ++a) { u32x2 x0 = *(const LAS u32x2*)(pa + 64 * (8 * a + rho)), x1 = *(const LAS u32x2*)(pa + 64 * (8 * a + rho) + 8); A[a] = u32x4{x0[0], x0[1], x1[0], x1[1]};
      B[a] = *(const LAS u32x4*)(pb - 64 * (8 * a - rho)); }
#pragma unroll
    for (int a = 0; a < 8; ++a)
#pragma unroll
      for (int b = 0; a + b < 8; ++b) acc[a + b] = mfma(A[a], B[b], acc[a + b]);
  }
  __syncthreads();
  LAS char* LY = (LAS char*)smem;
#pragma unroll
  for (int c = 0; c < 8; ++c) *(LAS u32x2*)(LY + w * 4128 + (256 * c + 16 * r + 4 * g) * 2) = u32x2{cvtpk(acc[c][0], acc[c][1]), cvtpk(acc[c][2], acc[c][3])};
  __syncthreads();
  const int qq = (lane & 15) >> 2, p = lane & 3;
#pragma unroll
  for (int i = 0; i < 8; ++i) {
    const int t0 = (i * 4 + w) * 64, t = t0 + lane;
    u32x2 y = trread(LY + qq * 4128 + (t0 + 16 * g + 4 * p) * 2);
    u32x4 qn = *(const u32x4*)(ws + OFF_QN + (size_t)quad * 32768 + t * 16);
    float x0 = dot2(qn[0], (y[0] << 16) | 0x3f80u, 0.f), x1 = dot2(qn[1], (y[0] & 0xffff0000u) | 0x3f80u, 0.f);
    float x2 = dot2(qn[2], (y[1] << 16) | 0x3f80u, 0.f), x3 = dot2(qn[3], (y[1] & 0xffff0000u) | 0x3f80u, 0.f);
    const size_t o = (size_t)(quad >> 1) * 32768 + t * 16 + (quad & 1) * 8;
    *(u32x2*)(ws + OFF_X1 + o) = u32x2{cvtpk(x0, x1), cvtpk(x2, x3)};
    *(u32x2*)(ws + OFF_Y + o) = y;
  }
}

__global__ __launch_bounds__(256, 1) void k_g1(char* __restrict__ ws, size_t xoff) {
  extern __shared__ __attribute__((aligned(16))) char smem[];
  const int tid = threadIdx.x, lane = tid & 63, w = __builtin_amdgcn_readfirstlane(tid >> 6);
  const int x = blockIdx.x & 7, r = blockIdx.x >> 3;
  const int sl = (x & 3) * 4 + (r & 3), pn = (x >> 2) * 8 + (r >> 2);
  LAS char* L = (LAS char*)smem;
  const char* X = ws + xoff + (size_t)pn * 128 * 16;
  const int g = lane >> 4, tt = lane & 15;
  auto dma = [&](int t, int slot) {
#pragma unroll
    for (int i = 0; i < 5; ++i) { int ks = w * 5 + i; __builtin_amdgcn_global_load_lds((const GAS void*)(X + (size_t)(4 * ks + g) * 32768 + (t * 16 + tt) * 16), (LAS void*)(L + slot * 20480 + ks * 1024), 16, 0, 0); }
  };
  for (int t = 0; t < 7; ++t) dma(t, t);
  u32x4 wgt[60];
#pragma unroll
  for (int i = 0; i < 60; ++i) { wgt[i] = *(const u32x4*)(ws + OFF_WG + ((size_t)(sl * 4 + w) * 60 + i) * 1024 + lane * 16); asm("" : "+a"(wgt[i])); }
  LAS float* wsum = (LAS float*)(L + 7 * 20480);
  LAS float* st = (LAS float*)(L + 7 * 20480 + 768);
  {
    float sacc = 0.f;
    if (lane < 48) {
#pragma unroll
      for (int ks = 0; ks < 20; ++ks) sacc += *(const float*)(ws + OFF_WS + ((size_t)ks * 3072 + (sl * 4 + w) * 48 + lane) * 4);
      wsum[w * 48 + lane] = sacc;
    }
  }
  __syncthreads();
  f32x4 wsv[3];
#pragma unroll
  for (int f = 0; f < 3; ++f) wsv[f] = *(LAS f32x4*)(wsum + w * 48 + f * 16 + g * 4);
  const u32x4 ones = {0x3f803f80u, 0x3f803f80u, 0x3f803f80u, 0x3f803f80u};
#pragma unroll 1
  for (int hp = 0; hp < 2; ++hp) {
    u32 P[3][4];
#pragma unroll
    for (int t4 = 0; t4 < 4; ++t4) {
      const int t = hp * 4 + t4;
      const int slot = t == 7 ? 0 : t;
      f32x4 acc[3] = {}, a1 = {}, a2 = {};
#pragma unroll
      for (int ks = 0; ks < 20; ++ks) {
        u32x4 b = *(const LAS u32x4*)(L + slot * 20480 + ks * 1024 + lane * 16);
#pragma unroll
        for (int f = 0; f < 3; ++f) acc[f] = mfma(wgt[ks * 3 + f], b, acc[f]);
      }
#pragma unroll
      for (int i = 0; i < 5; ++i) { u32x4 b = *(const LAS u32x4*)(L + slot * 20480 + (w * 5 + i) * 1024 + lane * 16); a1 = mfma(ones, b, a1); a2 = mfma(b, b, a2); }
      float dg = (tt & 3) == 0 ? a2[0] : (tt & 3) == 1 ? a2[1] : (tt & 3) == 2 ? a2[2] : a2[3];
      if ((tt >> 2) == g) *(LAS f32x2*)(st + (((t & 1) * 4 + w) * 16 + tt) * 2) = f32x2{a1[0], dg};
      __syncthreads();
      if (t == 0) dma(7, 0);
      float s1 = 0.f, s2 = 0.f;
#pragma unroll
      for (int i = 0; i < 4; ++i) { f32x2 z = *(LAS f32x2*)(st + (((t & 1) * 4 + i) * 16 + tt) * 2); s1 += z[0]; s2 += z[1]; }
      float m, sd; ln_scalars(s1, s2, m, sd);
      const float c1 = -1.44269504f / sd, vv = sd * sd;
#pragma unroll
      for (int f = 0; f < 3; ++f) {
        float hh[2];
#pragma unroll
        for (int j = 0; j < 2; ++j) {
          float G = fmaf(-m, wsv[f][2 * j], acc[f][2 * j]), U = fmaf(-m, wsv[f][2 * j + 1], acc[f][2 * j + 1]);
          float e = __builtin_amdgcn_exp2f(G * c1);
          hh[j] = G * U * __builtin_amdgcn_rcpf(fmaf(e, vv, vv));
        }
        P[f][t4] = cvtpk(hh[0], hh[1]);
      }
    }
#pragma unroll
    for (int f = 0; f < 3; ++f) {
      auto a = __builtin_amdgcn_permlane32_swap(P[f][0], P[f][2], false, false);
      auto b = __builtin_amdgcn_permlane32_swap(P[f][1], P[f][3], false, false);
      auto c = __builtin_amdgcn_permlane16_swap(a[0], b[0], false, false);
      auto d = __builtin_amdgcn_permlane16_swap(a[1], b[1], false, false);
      *(u32x4*)(ws + OFF_H + (size_t)(sl * 12 + w * 3 + f) * 32768 + (pn * 128 + hp * 64 + lane) * 16) = u32x4{c[0], c[1], d[0], d[1]};
    }
  }
}

__global__ __launch_bounds__(256, 1) void k_g2(char* __restrict__ ws, size_t roff, size_t ooff, u16* __restrict__ out) {
  extern __shared__ __attribute__((aligned(16))) char smem[];
  const int tid = threadIdx.x, lane = tid & 63, w = __builtin_amdgcn_readfirstlane(tid >> 6);
  const int x = blockIdx.x & 7, r = blockIdx.x >> 3;
  const int ds = r & 7, tg = x * 4 + (r >> 3);
  const int g = lane >> 4, tt = lane & 15;
  LAS char* L = (LAS char*)smem;
  u32x4 hf[4][12];
#pragma unroll
  for (int T = 0; T < 4; ++T)
#pragma unroll
    for (int i = 0; i < 12; ++i) hf[T][i] = *(const u32x4*)(ws + OFF_H + (size_t)(4 * (12 * w + i) + g) * 32768 + (tg * 64 + T * 16 + tt) * 16);
  u32x4 wgt[60];
#pragma unroll
  for (int i = 0; i < 57; ++i) { wgt[i] = *(const u32x4*)(ws + OFF_WD + ((size_t)(ds * 4 + w) * 60 + i) * 1024 + lane * 16); asm("" : "+a"(wgt[i])); }
#pragma unroll
  for (int i = 57; i < 60; ++i) { wgt[i] = *(const u32x4*)(ws + OFF_WD + ((size_t)(ds * 4 + w) * 60 + i) * 1024 + lane * 16); }
  const int tok = tg * 64 + w * 16 + tt;
  const char* R = ws + roff + (size_t)(ds * 10) * 32768 + tok * 16;
  u32x4 r0 = *(const u32x4*)(R + (size_t)g * 32768), r1 = *(const u32x4*)(R + (size_t)(4 + g) * 32768);
  u32x2 r2 = *(const u32x2*)(R + (size_t)(8 + (g >> 1)) * 32768 + (g & 1) * 8);
#pragma unroll
  for (int T = 0; T < 4; ++T) {
    f32x4 acc[5] = {};
#pragma unroll
    for (int i = 0; i < 12; ++i)
#pragma unroll
      for (int dt = 0; dt < 5; ++dt) acc[dt] = mfma(wgt[i * 5 + dt], hf[T][i], acc[dt]);
#pragma unroll
    for (int dt = 0; dt < 5; ++dt) *(LAS f32x4*)(L + ((T * 4 + w) * 5 + dt) * 1024 + lane * 16) = acc[dt];
  }
  __syncthreads();
  f32x4 o[5];
#pragma unroll
  for (int dt = 0; dt < 5; ++dt) { o[dt] = f32x4{0, 0, 0, 0};
#pragma unroll
    for (int i = 0; i < 4; ++i) o[dt] += *(LAS f32x4*)(L + ((w * 4 + i) * 5 + dt) * 1024 + lane * 16); }
  u32 res[10] = {r0[0], r0[1], r0[2], r0[3], r1[0], r1[1], r1[2], r1[3], r2[0], r2[1]};
  u32 pk[10];
#pragma unroll
  for (int i = 0; i < 10; ++i) { u32 d = cvtpk(o[i >> 1][(i & 1) * 2], o[i >> 1][(i & 1) * 2 + 1]); pk[i] = cvtpk(blo(res[i]) + blo(d), bhi(res[i]) + bhi(d)); }
  if (out) {
    u16* O = out + (size_t)tok * D + ds * 80;
    *(u32x4*)(O + 8 * g) = u32x4{pk[0], pk[1], pk[2], pk[3]};
    *(u32x4*)(O + 32 + 8 * g) = u32x4{pk[4], pk[5], pk[6], pk[7]};
    *(u32x2*)(O + 64 + 4 * g) = u32x2{pk[8], pk[9]};
  } else {
    char* O = ws + ooff + (size_t)(ds * 10) * 32768 + tok * 16;
    *(u32x4*)(O + (size_t)g * 32768) = u32x4{pk[0], pk[1], pk[2], pk[3]};
    *(u32x4*)(O + (size_t)(4 + g) * 32768) = u32x4{pk[4], pk[5], pk[6], pk[7]};
    *(u32x2*)(O + (size_t)(8 + (g >> 1)) * 32768 + (g & 1) * 8) = u32x2{pk[8], pk[9]};
  }
}

__global__ __launch_bounds__(320) void k_x3(char* __restrict__ ws) {
  __shared__ float red[5][8][2];
  const int tid = threadIdx.x, lane = tid & 63, w = tid >> 6;
  const int tk = tid & 7, os = tid >> 3, t = blockIdx.x * 8 + tk;
  u32x4 x[2], y[2]; float s1 = 0.f, s2 = 0.f;
#pragma unroll
  for (int i = 0; i < 2; ++i) { size_t o = (size_t)(os + 40 * i) * 32768 + t * 16; x[i] = *(const u32x4*)(ws + OFF_X2 + o); y[i] = *(const u32x4*)(ws + OFF_Y + o); }
#pragma unroll
  for (int i = 0; i < 2; ++i)
#pragma unroll
    for (int j = 0; j < 4; ++j) { s1 = dot2(x[i][j], 0x3f803f80u, s1); s2 = dot2(x[i][j], x[i][j], s2); }
  for (int o = 8; o < 64; o <<= 1) { s1 += __shfl_xor(s1, o); s2 += __shfl_xor(s2, o); }
  if (lane < 8) { red[w][tk][0] = s1; red[w][tk][1] = s2; }
  __syncthreads();
  s1 = 0.f; s2 = 0.f;
#pragma unroll
  for (int i = 0; i < 5; ++i) { s1 += red[i][tk][0]; s2 += red[i][tk][1]; }
  float m, sd; ln_scalars(s1, s2, m, sd);
  float rs = 1.f / sd;
#pragma unroll
  for (int i = 0; i < 2; ++i) { u32x4 o;
#pragma unroll
    for (int j = 0; j < 4; ++j) {
      float a = blo(x[i][j]), b = bhi(x[i][j]);
      o[j] = cvtpk(fmaf((a - m) * rs, blo(y[i][j]), a), fmaf((b - m) * rs, bhi(y[i][j]), b));
    }
    *(u32x4*)(ws + OFF_X3 + (size_t)(os + 40 * i) * 32768 + t * 16) = o; }
}

extern "C" void kernel_launch(void* const* d_in, const int* in_sizes, int n_in, void* d_out, int out_size, void* d_ws, size_t ws_size, hipStream_t stream) {
  const u16* q = (const u16*)d_in[0]; const u16* k = (const u16*)d_in[1]; const u16* v = (const u16*)d_in[2];
  const u16* sk = (const u16*)d_in[3]; const u16* h = (const u16*)d_in[4]; const u16* Dv = (const u16*)d_in[5];
  const u16* wg = (const u16*)d_in[7]; const u16* wu = (const u16*)d_in[8]; const u16* wd = (const u16*)d_in[9];
  char* ws = (char*)d_ws;
  (void)hipFuncSetAttribute((const void*)k_conv, hipFuncAttributeMaxDynamicSharedMemorySize, 4 * CH_LDS);
  (void)hipFuncSetAttribute((const void*)k_g1, hipFuncAttributeMaxDynamicSharedMemorySize, 7 * 20480 + 2048);
  (void)hipFuncSetAttribute((const void*)k_g2, hipFuncAttributeMaxDynamicSharedMemorySize, 81920);
  k_prep<<<256, 768, 0, stream>>>(q, k, v, sk, wg, wu, wd, ws);
  k_conv<<<160, 256, 4 * CH_LDS, stream>>>(h, Dv, ws);
  k_g1<<<256, 256, 7 * 20480 + 2048, stream>>>(ws, OFF_X1);
  k_g2<<<256, 256, 81920, stream>>>(ws, OFF_X1, OFF_X2, nullptr);
  k_x3<<<256, 320, 0, stream>>>(ws);
  k_g1<<<256, 256, 7 * 20480 + 2048, stream>>>(ws, OFF_X3);
  k_g2<<<256, 256, 81920, stream>>>(ws, OFF_X3, 0, (u16*)d_out);
}
```

```cpp
#include <hip/hip_runtime.h>
typedef unsigned short u16;
typedef unsigned u32;
typedef unsigned u32x4 __attribute__((ext_vector_type(4)));
typedef unsigned u32x2 __attribute__((ext_vector_type(2)));
typedef float f32x4 __attribute__((ext_vector_type(4)));
typedef float f32x2 __attribute__((ext_vector_type(2)));
typedef __bf16 bf16x8 __attribute__((ext_vector_type(8)));
typedef __bf16 bf16x2 __attribute__((ext_vector_type(2)));
typedef short s16x4 __attribute__((ext_vector_type(4)));
#define GAS __attribute__((address_space(1)))
#define LAS __attribute__((address_space(3)))
#define DI __device__ __forceinline__
constexpr int S = 2048, D = 640, F = 1536;
constexpr unsigned O_EPOCH = 0u;
constexpr unsigned O_F1 = 4096u;
constexpr unsigned O_FQ = 24576u;
constexpr unsigned O_FWD = 57344u;
constexpr unsigned O_F3 = 73728u;
constexpr unsigned O_ST = 139264u;
constexpr unsigned O_CS = 294912u;
constexpr unsigned O_DBG2 = 344064u;
constexpr unsigned O_DBG = 393216u;
constexpr unsigned O_GR = 655360u;
constexpr unsigned O_F6 = 163840u;
constexpr unsigned O_F4 = 1179648u;
constexpr unsigned O_UC = 1703936u;
constexpr unsigned O_UR = 2359296u;
constexpr unsigned O_QT = 4980736u;
constexpr unsigned O_WD = 7602176u;
constexpr unsigned O_H1 = 9568256u;
constexpr unsigned O_Y = 2359296u;
constexpr unsigned O_X1 = 4980736u;
constexpr unsigned O_X3 = 15859712u;
constexpr unsigned O_H2 = 15335424u;
constexpr unsigned O_END = 21626880u;
constexpr int XB = 21504, L_ST = 64512, L_WD = 65536, HCS = 4160, CH_LDS = 21248, L_Y = 150528, YRS = 2080;

constexpr int GS = 1056;
constexpr u32 OOB = 0x7ffffff0u;

DI f32x4 mfma(u32x4 a, u32x4 b, f32x4 c) { return __builtin_amdgcn_mfma_f32_16x16x32_bf16(__builtin_bit_cast(bf16x8, a), __builtin_bit_cast(bf16x8, b), c, 0, 0, 0); }
DI u32 cvtpk(float lo, float hi) { f32x2 v = {lo, hi}; bf16x2 b = __builtin_convertvector(v, bf16x2); return __builtin_bit_cast(u32, b); }
DI float blo(u32 x) { return __builtin_bit_cast(float, x << 16); }
DI float bhi(u32 x) { return __builtin_bit_cast(float, x & 0xffff0000u); }
DI u32x2 trread(const LAS char* p) { return __builtin_bit_cast(u32x2, __builtin_amdgcn_ds_read_tr16_b64_v4i16((LAS s16x4*)p)); }
DI float dot2(u32 a, u32 b, float c) { return __builtin_amdgcn_fdot2_f32_bf16(__builtin_bit_cast(bf16x2, a), __builtin_bit_cast(bf16x2, b), c, false); }
DI u32 perm(u32 hi, u32 lo, u32 sel) { return __builtin_amdgcn_perm(hi, lo, sel); }
template <typename T> DI T ntload(const void* p) { return __builtin_nontemporal_load((const GAS T*)p); }
template <typename T> DI T gload(const void* p) { return *(const GAS T*)p; }
DI u32 uni(u32 x) { return __builtin_amdgcn_readfirstlane(x); }
template <int CTRL> DI float dppadd(float x) { return x + __builtin_bit_cast(float, __builtin_amdgcn_update_dpp(0, __builtin_bit_cast(int, x), CTRL, 0xf, 0xf, false)); }
#define CAS __attribute__((address_space(4)))
#define BAR() asm volatile("s_waitcnt lgkmcnt(0)\n s_barrier" ::: "memory")
#define PST(k) do { __builtin_amdgcn_sched_barrier(0); if (k == 99) { t0 = (u32)__builtin_amdgcn_s_memrealtime(); __builtin_amdgcn_sched_barrier(0); } } while (0)
#define WARM() asm volatile("" :: "s"(w) : "s98", "s99", "scc")
#define DRAIN() asm volatile("s_waitcnt vmcnt(0)" ::: "memory")

typedef unsigned long long u64;
DI u32x4 srd(u64 p, u32 n) { return u32x4{(u32)p, (u32)(p >> 32), n, 0x00020000u}; }

__global__ __launch_bounds__(256, 1) __attribute__((aligned(4096))) void k_main(const u16*, const u16*, const u16*, const u16*, char*, const u16*, const u16*, const u16*, const u16*, const u16*, u16*) {
  extern __shared__ __attribute__((aligned(16))) char smem[];
  LAS char* L = (LAS char*)smem;
  typedef u64 u64x4 __attribute__((ext_vector_type(4)));
  typedef u64 u64x2 __attribute__((ext_vector_type(2)));
  u64x4 ka, kb; u64x2 kc; u64 kd;
  asm volatile("s_load_dwordx8 %0, %4, 0\n s_load_dwordx8 %1, %4, 32\n s_load_dwordx4 %2, %4, 64\n s_load_dwordx2 %3, %4, 80" : "=&s"(ka), "=&s"(kb), "=&s"(kc), "=&s"(kd) : "s"(__builtin_amdgcn_kernarg_segment_ptr()));
  const int tid = threadIdx.x, lane = tid & 63, w = uni(tid >> 6);
  const int b = blockIdx.x, x = b & 7, r = b >> 3;
  u32 t0 = 0, rt0 = 0; u64 pst[8];
  u64 pc; u32 csz, pf;
  asm volatile("s_getpc_b64 %0\nkx_start:\n s_mov_b32 %1, kx_end-kx_start" : "=s"(pc), "=s"(csz));
  const u32 ppv = lane < 5 ? (u32)lane * 128 : OOB, ppo = (r * 4 + w) * 640;
  asm volatile("buffer_load_dword %0, %1, %2, %3 offen" : "={v254}"(pf) : "v"(ppv), "s"(srd(pc, csz)), "s"(ppo));
  const bool isconv = r < 20;
  const int ci = x * 20 + r, ni = x * 12 + (r - 20);
  const int g = lane >> 4, tt = lane & 15, qq = tt >> 2, p = lane & 3;
  const int rg = ci / 10, cg = ci % 10, cc = ci * 4 + w;
  const int chq = lane >> 3, ch = cg * 64 + (w & 1) * 32 + chq * 4, kg_ = (w >> 1) * 8 + 7 - (lane & 7), ts = rg * 128 + kg_ * 8;
  u32 uo = ch * 4096 + (2040 - ts) * 2;
  u32x4 s01, s23, qx[5]; u32 tg0, dw0, tag, dw, st, su, d0, d1, x2;
  {
    u32 vo = (lane >> 3) * 1280 + (lane & 7) * 16, v2 = lane < 40 || rg + w == 0 ? OOB : vo, v3 = ch * 8;
    u32 so = (rg * 128 + w * 32) * 1280 + cg * 128, lb = L_WD + HCS + w * CH_LDS + (w & 1) * 128, dvo = isconv ? (cc & ~1) * 2 : 0;
    asm volatile("s_waitcnt lgkmcnt(0)" : "+s"(ka), "+s"(kb), "+s"(kc), "+s"(kd) : "v"(vo), "v"(v2), "v"(v3), "s"(so), "s"(lb), "s"(dvo));
    PST(6);
    asm volatile("s_load_dword %0, %2, 0\n s_load_dword %1, %3, %4" : "=&s"(tg0), "=&s"(dw0) : "s"(kb[0]), "s"(ka[3]), "s"(dvo));
    asm volatile("s_cmp_lt_u32 %[r], 20\n s_cbranch_scc0 1f\n"
      " s_mov_b32 m0, %[lb]\n s_sub_u32 %[t], %[so], 10240\n buffer_load_dwordx4 %[v2], %[ra], %[t] offen lds\n global_load_dwordx4 %[a0], %[v3], %[sk]\n global_load_dwordx4 %[c0], %[v3], %[sk] offset:16\n"
      " s_add_u32 m0, %[lb], 1056\n s_add_u32 %[t], %[so], 0\n buffer_load_dwordx4 %[vo], %[ra], %[t] offen lds\n s_add_u32 m0, %[lb], 5280\n s_nop 0\n buffer_load_dwordx4 %[vo], %[rb], %[t] offen lds\n"
      " s_add_u32 m0, %[lb], 2112\n s_add_u32 %[t], %[so], 10240\n buffer_load_dwordx4 %[vo], %[ra], %[t] offen lds\n s_add_u32 m0, %[lb], 6336\n s_nop 0\n buffer_load_dwordx4 %[vo], %[rb], %[t] offen lds\n"
      " s_add_u32 m0, %[lb], 3168\n s_add_u32 %[t], %[so], 20480\n buffer_load_dwordx4 %[vo], %[ra], %[t] offen lds\n s_add_u32 m0, %[lb], 7392\n s_nop 0\n buffer_load_dwordx4 %[vo], %[rb], %[t] offen lds\n"
      " s_add_u32 m0, %[lb], 4224\n s_add_u32 %[t], %[so], 30720\n buffer_load_dwordx4 %[vo], %[ra], %[t] offen lds\n s_add_u32 m0, %[lb], 8448\n s_nop 0\n buffer_load_dwordx4 %[vo], %[rb], %[t] offen lds\n"
      "1:"
      : [a0] "=&v"(s01), [c0] "=&v"(s23), [t] "=&s"(st)
      : [vo] "v"(vo), [v2] "v"(v2), [v3] "v"(v3), [ra] "s"(srd(ka[0], S * D * 2)), [rb] "s"(srd(ka[1], S * D * 2)),
        [so] "s"(so), [lb] "s"(lb), [sk] "s"(ka[2]), [r] "s"(r)
      : "memory", "scc");
    asm volatile("s_cmp_lt_u32 %[r], 20\n s_cbranch_scc1 3f\n v_lshlrev_b32 %[x2], 7, %[ln]\n s_lshl2_add_u32 %[t], %[r], %[w]\n s_cmp_ge_u32 %[r], 28\n s_cbranch_scc1 1f\n"
      " s_and_b32 %[u], %[t], 7\n s_sub_u32 %[t], %[t], %[u]\n s_lshl2_add_u32 %[t], %[t], %[u]\n s_lshl_b32 %[u], %[x], 8\n s_add_u32 %[t], %[t], %[u]\n s_sub_u32 %[t], %[t], 296\n s_mul_i32 %[t], %[t], 1280\n"
      " v_add_u32 %[x2], %[t], %[x2]\n s_mov_b64 exec, 0xffc00\n v_add_u32 %[x2], 162560, %[x2]\n s_mov_b64 exec, 0xfffff\n"
      " global_load_dword %[d0], %[x2], %[pk]\n global_load_dword %[d1], %[x2], %[pv]\n s_mov_b64 exec, -1\n s_sleep 14\n v_lshrrev_b32 %[x2], 4, %[tid]\n v_mul_u32_u24 %[x2], 1024, %[x2]\n v_lshl_add_u32 %[x2], %[tid], 4, %[x2]\n s_lshl3_add_u32 %[u], %[x], %[r]\n s_sub_u32 %[u], %[u], 20\n s_mul_i32 %[u], %[u], 20480\n v_add_u32 %[x2], %[u], %[x2]\n global_load_dwordx4 %[q0], %[x2], %[pq] offset:0 nt\n global_load_dwordx4 %[q1], %[x2], %[pq] offset:256 nt\n global_load_dwordx4 %[q2], %[x2], %[pq] offset:512 nt\n global_load_dwordx4 %[q3], %[x2], %[pq] offset:768 nt\n global_load_dwordx4 %[q4], %[x2], %[pq] offset:1024 nt\n s_branch 2f\n"
      "1: s_sleep 20\n v_mul_u32_u24 %[d0], 43, %[ln]\n v_lshrrev_b32 %[d0], 7, %[d0]\n v_mul_u32_u24 %[d0], 3072 - 2 * 192 + 0, %[d0]\n s_sub_u32 %[t], %[t], 112\n s_mul_i32 %[t], %[t], 8 * 3072\n s_and_b32 %[u], %[x], 16 / 2 - 1\n s_mul_i32 %[u], %[u], 2 * 192\n s_add_u32 %[t], %[t], %[u]\n"
      " v_add3_u32 %[x2], %[x2], %[d0], %[t]\n s_bfm_b64 exec, 8 * 3, 0\n global_load_dword %[d0], %[x2], %[pg]\n global_load_dword %[d1], %[x2], %[pu]\n"
      "2: s_mov_b64 exec, -1\n3:"
      : [d0] "=&{v252}"(d0), [d1] "=&{v253}"(d1), [x2] "=&v"(x2), [t] "=&s"(st), [u] "=&s"(su),
        [q0] "=&{v[128:131]}"(qx[0]), [q1] "=&{v[132:135]}"(qx[1]), [q2] "=&{v[136:139]}"(qx[2]), [q3] "=&{v[140:143]}"(qx[3]), [q4] "=&{v[144:147]}"(qx[4])
      : [r] "s"(r), [x] "s"(x), [w] "s"(w), [ln] "v"(lane), [tid] "v"(tid), [pq] "s"(kb[2]), [pk] "s"(ka[0]), [pv] "s"(ka[1]), [pg] "s"(kb[3]), [pu] "s"(kc[0]) : "memory", "scc");
  }
  const __amdgpu_buffer_rsrc_t rw = __builtin_amdgcn_make_buffer_rsrc((char*)kb[0], 0, O_END, 0x00020000);
  auto st4 = [&](u32 off, u32 so, u32x4 d) { __builtin_amdgcn_raw_buffer_store_b128(d, rw, off, so, 16); };
  PST(7);
  const int xs = x % (16 / 2), sl = xs * 2 + r % 2, pn = x / (16 / 2) * (32 / 2) + r / 2;
  const int ds = 1 ? (x & 1) * 4 + (r & 3) : r & 7, tg = 1 ? (x >> 1) * 8 + (r >> 2) : x * 4 + (r >> 3), T0 = 16 * (tg & 1) + (tg >> 1);
  u32 V[36], pa;
  {
    const int hh = lane >> 5, kg = (lane >> 3) & 3, jj = lane & 7;
    V[0] = lane * 16; V[1] = lane * 128; V[2] = 0; V[3] = (2 * kg + hh) * 128 + jj * 16;
    V[4] = g * 128 + (tt & 7) * 16 + (tt >> 3) * 8; V[5] = V[4] + w * 5120;
    const int c0 = 3 * w + (p >> 1) + 2 * (g & 1), c1 = 3 * w + 2 + 2 * (g & 1);
    V[6] = (8 * g + qq) * 192 + (c0 >= 12 ? c0 - 12 : c0) * 16 + (p & 1) * 8;
    V[7] = (8 * g + qq) * 192 + (c1 >= 12 ? c1 - 12 : c1) * 16 + (p >> 1) * 8 + (p & 1) * 6144;
    V[8] = qq == g ? L_ST + tt * 32 + w * 8 : 0x30000; V[9] = L_ST + tt * 32; V[10] = lane < 48 ? lane * 16 : OOB; V[11] = L_WD + w * 24576 + lane * 16;
    const int j = 2 * w + (g >> 1), hf = g & 1, n = j + 8 * hf;
    V[12] = w * 5376 + tt * 336 + g * 16; V[13] = n * 336 + tt * 16; V[14] = n * 336 + 256 + tt * 4; V[15] = tt < 8 ? w * 1024 + tt * 128 + g * 16 : OOB;
    const int qm = 20 * ds + tt, qe = 20 * ds + 16 + qq, qx = hf ? qe : qm;
    V[16] = tt * 128 + (j ^ (tt & 7)) * 16 + hf * 8; V[17] = (16 + qq) * 128 + (j ^ qq) * 16 + hf * 8 + p * 2;
    V[18] = ((qx >> 2) * 512 + (j >> 1) * 128 + (qx & 3) * 32 + (j & 1) * 16) | (hf * p == 0 ? 0 : OOB); V[19] = tt == 0 ? w * 1024 + ds * 128 + g * 16 : OOB;
#pragma unroll
    for (int i = 0; i < 3; ++i) { const int e = i * 64 + lane, rw_ = e / 12, c = e - rw_ * 12 - 2 * (rw_ >> 3);
      V[20 + i] = (rw_ + (w & 1) * 16) * 3072 + sl * 192 + (c < 0 ? c + 12 : c) * 16 - i * 1024 + (rw_ & 7) * 0 + 0 * ((sl & 1) - (rw_ & 1)) * (3072 - 192); }
    const int l3 = lane / 3, u = r * 2 + (w & 1), co = xs * 2 * 192 + (lane - 3 * l3) * 128 + l3 * 3072;
    const int pr = r >= 28 ? 16 : isconv ? 5 : 0, p0 = 16 * 8 + (r >= 28 ? -56 * 16 : 8 * 16 + (isconv ? 0 : 40 * 5 - 40 * 0));
    pa = ((p0 + u * pr) * 3072 + co + ((p0 + u * pr + l3) & 7) * 0) | (l3 < pr ? 0 : OOB);
    V[33] = ((u - 40) * 12 * 3072 + co + (((u - 40) * 12 + l3) & 7) * 0) | (l3 < 12 ? 0 : OOB);
    V[26] = L_Y + qq * YRS + (w * 128 + 32 * g + 16 * (p >> 1) + 4 * (p & 1)) * 2;
  }
  u32x4 srdw = srd(kb[0], O_END), srdo = srd(kd, S * D * 2), srdm = srd(w < 2 ? kb[3] : kc[0], D * F * 2);
  u32x4 cq;
  u32 Sx[24] = {
    (u32)(0),
    (u32)(0),
    (u32)(0),
    (u32)(0),
    (u32)(0),
    (u32)(O_CS + (sl * 4 + w) * 512),
    (u32)(w * 1024),
    (u32)(O_WD + (ds * 4 + w) * 61440),
    (u32)(O_FWD + ((ds >> 2) * 48 + w * 12) * 128),
    (u32)((pn + 16 * 0 * (r & 1)) * 20480 + w * 5120),
    (u32)(w * 5120),
    (u32)((pn + 16 * 0 * (r & 1)) * 49152 + (sl * 4 + w) * 768),
    (u32)((((pn + 16 * 0 * (r & 1)) * 4 + (sl >> 2)) * 16 + (sl & 3)) * 128),
    (u32)((pn * 4 + w) * 2048),
    (u32)(T0 * 49152 + w * 12288),
    (u32)((T0 * 4 + w) * 2048),
    (u32)(T0 * 20480),
    (u32)(T0 * 4096),
    (u32)((((tg >> 1) * 4 + (ds >> 1)) * 16 + (tg & 1) * 2 + (ds & 1)) * 128),
    (u32)((T0 + 32 * w) * 20480 + ds * 2560),
    (u32)((T0 + 32 * w) * 128 + ds * 20 * 16384),
    (u32)(L_WD + w * 24576),
    (u32)(w * 2560 + ds * 160),
    (u32)((b * 4 + w) * 256)
  };
#define PINV(a, b) _Pragma("unroll") for (int i = a; i < b; ++i) asm volatile("" : "+v"(V[i]))
#define PINS(a, b) _Pragma("unroll") for (int i = a; i < b; ++i) { Sx[i] = uni(Sx[i]); asm volatile("" : "+s"(Sx[i])); }
  PINV(0, 23); PINV(26, 27); PINV(33, 34); PINS(5, 24);
  srdm[0] = uni(srdm[0]); srdm[1] = uni(srdm[1]);
  asm volatile("" : "+s"(srdm));
  auto pfw = [&]() { asm volatile("buffer_load_dword %2, %0, %1, 0 offen" :: "v"(pa), "s"(srdm), "{v253}"(d1)); };
  PST(0);
  if (isconv) {
    asm volatile("s_mov_b32 m0, %0\n s_nop 0\n buffer_load_dwordx4 %1, %2, %3 offen nt lds\n buffer_load_dwordx4 %1, %2, %3 offen offset:1024 nt lds\n buffer_load_dwordx4 %1, %2, %3 offen offset:2048 nt lds\n buffer_load_dwordx4 %1, %2, %3 offen offset:3072 nt lds"
      :: "s"(L_WD + w * CH_LDS + 32), "v"(V[0]), "s"(srd(kb[1], S * D * 2)), "s"(cc * 4096) : "memory");
    auto fill = [&](int n0, int n1) {
#pragma unroll
      for (int n = n0; n < n1; ++n) asm volatile("s_add_u32 m0, %1, %2\n s_mov_b32 %0, %3\n buffer_load_dwordx4 %4, %5, %0 offen offset:%6 lds"
        : "=&s"(st) : "s"(w * 3072), "n"(n / 3 * 12288), "n"(n / 3 * 32 * 3072), "v"(V[20 + n % 3]), "s"(srdm), "n"(n % 3 * 1024) : "memory", "scc"); };
    if (1 == 0) pfw();
    fill(0, 0);
    if (1 == 1) pfw();
    Sx[0] = O_F1 + (cg * 16 + rg) * 128; PINS(0, 1);
    auto ctab = [&]() {
    { const int s = (tt + 1) & 3, cw = L_WD + w * CH_LDS;
      V[23] = cw + s * HCS + (8 * g + tt + 1 - s) * 2; V[24] = cw + 4 * HCS + (2032 - 16 * tt + 8 * g) * 2 - 64 * 56; V[25] = L_Y + w * YRS + (16 * tt + 4 * g) * 2;
      V[27] = (lane >> 3) * 20480 + (lane & 7) * 16; V[28] = lane * 16; V[29] = V[30] = V[31] = V[32] = V[34] = V[28];
      V[35] = cw + (lane < 8 ? (lane >> 1) * HCS + (lane & 1) * 16 : 4 * HCS + 4096 - 128 + min(lane, 37) * 16);
      cq = u32x4{O_F1 + (ci >> 4) * 2048, O_UC + ci * 4096 + w * 1024, (u32)cw, O_FQ + w * 2048};
      Sx[1] = ci * 128 + w * 8 * 20480; Sx[2] = ci * 16; Sx[3] = ci * 16384 + w * 1024; Sx[4] = dw >> ((cc & 1) * 16);
      PINV(23, 26); PINV(27, 29); PINV(35, 36); PINS(1, 5); asm volatile("" : "+s"(cq)); }
    };
    asm volatile("s_waitcnt lgkmcnt(0)\n s_add_u32 %0, %2, 1\n s_mov_b32 %1, %3" : "=&s"(tag), "=&s"(dw) : "s"(tg0), "s"(dw0) : "scc");
    if (1 == 0) ctab();
    WARM();
    PST(1);
    u32 kvo = L_WD + HCS + (kg_ >> 2) * CH_LDS + (kg_ & 4) * 32 + (kg_ & 3) * GS + (w & 1) * 64 + chq * 8, fa = tid ? OOB : 0u, uc = uo - ch * 3072, vt = tag;
    asm volatile("" : "+v"(kvo), "+v"(fa), "+v"(uo), "+v"(uc), "+v"(vt));
    auto hint = [&]() { __builtin_amdgcn_sched_barrier(0); asm volatile("buffer_store_dword %0, %1, %2, %3 offen sc1" :: "v"(vt), "v"(fa), "s"(srdw), "s"(Sx[0]) : "memory"); PST(5); };
    asm volatile("s_waitcnt vmcnt(%3)\n s_barrier" : "+v"(s01), "+v"(s23) : "v"(pf), "n"(5 + 0) : "memory");
    PST(2);
    u32x2 kk[11], vv[8];
    { const LAS char* kb = L + kvo;
#pragma unroll
      for (int i = 0; i < 3; ++i) kk[i] = *(const LAS u32x2*)(kb + (5 + i) * 128);
#pragma unroll
      for (int i = 0; i < 8; ++i) { kk[i + 3] = *(const LAS u32x2*)(kb + GS + i * 128); vv[i] = *(const LAS u32x2*)(kb + 5 * GS + i * 128); } }
    if (9 == 0) hint();
    u32x4 cs;
    __builtin_amdgcn_sched_barrier(0);
#pragma unroll
    for (int i = 0; i < 3; ++i) kk[i] = ts ? kk[i] : u32x2{0, 0};
#pragma unroll
    for (int c2 = 0; c2 < 2; ++c2) {
      const u32x4 sx = c2 ? s23 : s01;
      const f32x2 f0 = {blo(sx[0]), blo(sx[2])}, f1 = {bhi(sx[0]), bhi(sx[2])}, f2 = {blo(sx[1]), blo(sx[3])}, f3 = {bhi(sx[1]), bhi(sx[3])};
      f32x2 kf[11], o[8];
#pragma unroll
      for (int i = 0; i < 11; ++i) kf[i] = f32x2{blo(kk[i][c2]), bhi(kk[i][c2])};
#pragma unroll
      for (int i = 0; i < 8; ++i) {
        if ((c2 * 8 + i) % 2 == 0 && 0 + (c2 * 8 + i) / 2 < 0) { __builtin_amdgcn_sched_barrier(0); fill(0 + (c2 * 8 + i) / 2, 0 + (c2 * 8 + i) / 2 + 1); __builtin_amdgcn_sched_barrier(0); }
        f32x2 a = kf[i] * f3; a = __builtin_elementwise_fma(kf[i + 1], f2, a); a = __builtin_elementwise_fma(kf[i + 2], f1, a); a = __builtin_elementwise_fma(kf[i + 3], f0, a);
        o[i] = a * f32x2{blo(vv[i][c2]), bhi(vv[i][c2])};
      }
      if (c2 == 0) PST(3);
      if (9 == 1 + c2) hint();
#pragma unroll
      for (int c = 0; c < 2; ++c) {
        const u32x4 d = {cvtpk(o[7][c], o[6][c]), cvtpk(o[5][c], o[4][c]), cvtpk(o[3][c], o[2][c]), cvtpk(o[1][c], o[0][c])};
        st4(uo, O_UR + (2 * c2 + c) * 4096, d); cs[2 * c2 + c] = tag - (d[0] + d[1] + d[2] + d[3]); }
    }
    st4(uc, O_UC, cs);
    if (9 > 2) PST(5);
    PST(4);
    if (1 == 1) ctab();
  } else {
    const bool dbl = r >= 28;
    pfw();
    const int qrow = tid >> 4, qt = dbl ? x * 8 + 2 * r + 8 : x * 8 + r - 20, ks = 6 * x + (0 ? (r - 20) >> 1 : r >= 26 ? 5 : (r - 20) >> 1), hd = r & 1;
    asm volatile("s_waitcnt lgkmcnt(0)\n s_add_u32 %0, %2, 1\n s_mov_b32 %1, %3 ; nc" : "=&s"(tag), "=&s"(dw) : "s"(tg0), "s"(dw0) : "scc");
    const int kap = 32 * ks + (tid >> 3), pw = kap / 24, e = kap - 24 * pw, f = 24 * pw + 2 * (int)((0xba6475319820ull >> (2 * (e & ~1))) & 15) + (e & 1);
    V[23] = f * 1280 + hd * 640 + (tid & 7) * 16; V[24] = L_WD + 46080 + (tid >> 3) * 800 + (tid & 7) * 16; V[25] = L_WD + 46080 + (8 * g + qq) * 800 + w * 160 + p * 8;
    V[27] = V[28] = OOB; V[30] = lane * 8 | (r < 20 + 2 && x == xs ? 0 : OOB);
    V[29] = qt * 20480 + qrow * 1280 + tt * 16; V[31] = L_WD + qrow * 1296 + tt * 16; V[32] = L_WD + (tid & 7) * 1296 + (tid >> 3) * 8;
    V[34] = tt == 0 ? O_ST + qt * 128 + (qrow & 7) * 16 + (qrow >> 3) * 8 : OOB;
    const int k12 = ks / 12, i = ks - 12 * k12;
    cq = srd(kc[1], D * F * 2); V[35] = qt * 20480 + tid * 16;
    Sx[0] = O_FQ + ((qt & 64) + ((qt >> 3) & 3) * 16 + ((qt >> 5) & 1) * 8 + (qt & 7)) * 128 + (w ? 0 : dbl ? 3 : 1);
    Sx[1] = 43260160u + i * 50868355u; Sx[2] = (160 + ni) * 16;
    Sx[3] = O_WD + ((4 * hd + w) * 4 + k12) * 61440; Sx[4] = O_FWD + (hd * 48 + ks) * 128;
    PINS(0, 5);
    PST(1); PST(2); PST(3); PST(4); PST(5);
  }
  asm volatile(
    "v_mov_b32 v20, 0x3f803f80\n"
    "v_mov_b32 v21, 0x3f803f80\n"
    "v_mov_b32 v22, 0x3f803f80\n"
    "v_mov_b32 v23, 0x3f803f80\n"
    "s_mov_b32 s48, 0x22222222\n"
    "s_mov_b32 s49, 0x22222222\n"
    "s_mov_b32 s50, 0x44444444\n"
    "s_mov_b32 s51, 0x44444444\n"
    "s_mov_b32 s52, 0x88888888\n"
    "s_mov_b32 s53, 0x88888888\n"
    "s_mov_b32 s54, 0x40000\n"
    "s_cmp_eq_u32 %[w1k], 0\n"
    "s_cselect_b32 s94, 1, 0\n"
    "s_mov_b32 s95, 0\n"
    "s_mul_i32 s93, %[w1k], 3\n"
    "v_writelane_b32 v2, %[tag], 0\n"
    "s_cmp_ge_u32 %[dbg], 163840\n"
    "s_cbranch_scc1 LNC_%=\n"
    "s_nop 0\n"
    "s_add_u32 s57, %[w1k], 82176\n"
    "v_add_u32 v40, s57, v0\n"
    "s_barrier\n"
    "v_mov_b32 v44, 0\n"
    "v_mov_b32 v45, 0\n"
    "v_mov_b32 v46, 0\n"
    "v_mov_b32 v47, 0\n"
    "ds_write_b128 v255, v[44:47]\n"
    "s_and_b32 s58, %[fwp], 0xffff\n"
    "v_mov_b32 v50, s58\n"
    "s_mov_b64 exec, 0x555555\n"
    "v_lshlrev_b32 v50, 16, v50\n"
    "s_mov_b64 exec, -1\n"
    "s_mov_b64 vcc, 0xc0c000\n"
    "v_cndmask_b32 v248, 0, v50, vcc\n"
    "s_mov_b64 vcc, 0x303000\n"
    "v_cndmask_b32 v249, 0, v50, vcc\n"
    "s_mov_b64 vcc, 0xc0c00\n"
    "v_cndmask_b32 v250, 0, v50, vcc\n"
    "s_mov_b64 vcc, 0x30300\n"
    "v_cndmask_b32 v251, 0, v50, vcc\n"
    "v_add_u32 v41, s46, v0\n"
    "s_waitcnt vmcnt(6)\n"
    "ds_read_b128 v[176:179], v41 offset:32\n"
    "ds_read_b64 v[180:181], v41 offset:48\n"
    "ds_read_b128 v[184:187], v41 offset:1056\n"
    "ds_read_b64 v[188:189], v41 offset:1072\n"
    "ds_read_b128 v[192:195], v41 offset:2080\n"
    "ds_read_b64 v[196:197], v41 offset:2096\n"
    "ds_read_b128 v[200:203], v41 offset:3104\n"
    "ds_read_b64 v[204:205], v41 offset:3120\n"
    "v_lshrrev_b32 v48, 1, v0\n"
    "v_sub_u32 v48, 72, v48\n"
    "s_add_u32 m0, s93, 0\n"
    "s_mov_b32 s68, 0\n"
    "buffer_load_dwordx4 v76, %[srdm], s68 offen lds\n"
    "buffer_load_dwordx4 v77, %[srdm], s68 offen offset:1024 lds\n"
    "buffer_load_dwordx4 v247, %[srdm], s68 offen offset:2048 lds\n"
    "s_add_u32 m0, s93, 12288\n"
    "s_mov_b32 s68, 98304\n"
    "buffer_load_dwordx4 v76, %[srdm], s68 offen lds\n"
    "buffer_load_dwordx4 v77, %[srdm], s68 offen offset:1024 lds\n"
    "s_waitcnt lgkmcnt(7)\n"
    "v_alignbit_b32 v112, v177, v176, 16\n"
    "v_alignbit_b32 v113, v178, v177, 16\n"
    "v_alignbit_b32 v114, v179, v178, 16\n"
    "s_waitcnt lgkmcnt(6)\n"
    "v_alignbit_b32 v115, v180, v179, 16\n"
    "v_alignbit_b32 v116, v181, v180, 16\n"
    "v_pk_mov_b32 v[120:121], v[176:177], v[178:179] op_sel:[1,0]\n"
    "v_pk_mov_b32 v[122:123], v[178:179], v[180:181] op_sel:[1,0]\n"
    "v_pk_mov_b32 v[124:125], v[112:113], v[114:115] op_sel:[1,0]\n"
    "v_pk_mov_b32 v[126:127], v[114:115], v[116:117] op_sel:[1,0]\n"
    "ds_write_b128 v41, v[112:115] offset:4192\n"
    "ds_write_b128 v41, v[120:123] offset:8352\n"
    "ds_write_b128 v41, v[124:127] offset:12512\n"
    "s_waitcnt lgkmcnt(8)\n"
    "v_alignbit_b32 v128, v185, v184, 16\n"
    "v_alignbit_b32 v129, v186, v185, 16\n"
    "v_alignbit_b32 v130, v187, v186, 16\n"
    "s_waitcnt lgkmcnt(7)\n"
    "v_alignbit_b32 v131, v188, v187, 16\n"
    "v_alignbit_b32 v132, v189, v188, 16\n"
    "v_pk_mov_b32 v[136:137], v[184:185], v[186:187] op_sel:[1,0]\n"
    "v_pk_mov_b32 v[138:139], v[186:187], v[188:189] op_sel:[1,0]\n"
    "v_pk_mov_b32 v[140:141], v[128:129], v[130:131] op_sel:[1,0]\n"
    "v_pk_mov_b32 v[142:143], v[130:131], v[132:133] op_sel:[1,0]\n"
    "ds_write_b128 v41, v[128:131] offset:5216\n"
    "ds_write_b128 v41, v[136:139] offset:9376\n"
    "ds_write_b128 v41, v[140:143] offset:13536\n"
    "s_waitcnt lgkmcnt(9)\n"
    "v_alignbit_b32 v112, v193, v192, 16\n"
    "v_alignbit_b32 v113, v194, v193, 16\n"
    "v_alignbit_b32 v114, v195, v194, 16\n"
    "s_waitcnt lgkmcnt(8)\n"
    "v_alignbit_b32 v115, v196, v195, 16\n"
    "v_alignbit_b32 v116, v197, v196, 16\n"
    "v_pk_mov_b32 v[120:121], v[192:193], v[194:195] op_sel:[1,0]\n"
    "v_pk_mov_b32 v[122:123], v[194:195], v[196:197] op_sel:[1,0]\n"
    "v_pk_mov_b32 v[124:125], v[112:113], v[114:115] op_sel:[1,0]\n"
    "v_pk_mov_b32 v[126:127], v[114:115], v[116:117] op_sel:[1,0]\n"
    "ds_write_b128 v41, v[112:115] offset:6240\n"
    "ds_write_b128 v41, v[120:123] offset:10400\n"
    "ds_write_b128 v41, v[124:127] offset:14560\n"
    "s_waitcnt lgkmcnt(10)\n"
    "v_alignbit_b32 v128, v201, v200, 16\n"
    "v_alignbit_b32 v129, v202, v201, 16\n"
    "v_alignbit_b32 v130, v203, v202, 16\n"
    "s_waitcnt lgkmcnt(9)\n"
    "v_alignbit_b32 v131, v204, v203, 16\n"
    "v_alignbit_b32 v132, v205, v204, 16\n"
    "v_pk_mov_b32 v[136:137], v[200:201], v[202:203] op_sel:[1,0]\n"
    "v_pk_mov_b32 v[138:139], v[202:203], v[204:205] op_sel:[1,0]\n"
    "v_pk_mov_b32 v[140:141], v[128:129], v[130:131] op_sel:[1,0]\n"
    "v_pk_mov_b32 v[142:143], v[130:131], v[132:133] op_sel:[1,0]\n"
    "ds_write_b128 v41, v[128:131] offset:7264\n"
    "ds_write_b128 v41, v[136:139] offset:11424\n"
    "ds_write_b128 v41, v[140:143] offset:15584\n"
    "v_readfirstlane_b32 s42, v176\n"
    "v_readfirstlane_b32 s43, v177\n"
    "buffer_load_dwordx4 v247, %[srdm], s68 offen offset:2048 lds\n"
    "s_nop 0\n"
    "v_lshlrev_b64 v[48:49], v48, s[42:43]\n"
    "s_mov_b64 exec, 0xa8\n"
    "ds_write_b64 v255, v[48:49] offset:8\n"
    "s_mov_b64 exec, -1\n"
    "ds_read_b64 v[92:93], v240 offset:1536\n"
    "ds_read_b64 v[94:95], v240 offset:1544\n"
    "ds_read_b64 v[88:89], v240 offset:1024\n"
    "ds_read_b64 v[90:91], v240 offset:1032\n"
    "ds_read_b64 v[84:85], v240 offset:512\n"
    "ds_read_b64 v[86:87], v240 offset:520\n"
    "ds_read_b64 v[80:81], v240 offset:0\n"
    "ds_read_b64 v[82:83], v240 offset:8\n"
    "ds_read_b64 v[124:125], v240 offset:1600\n"
    "ds_read_b64 v[126:127], v240 offset:1608\n"
    "ds_read_b64 v[120:121], v240 offset:1088\n"
    "ds_read_b64 v[122:123], v240 offset:1096\n"
    "ds_read_b64 v[116:117], v240 offset:576\n"
    "ds_read_b64 v[118:119], v240 offset:584\n"
    "ds_read_b64 v[112:113], v240 offset:64\n"
    "ds_read_b64 v[114:115], v240 offset:72\n"
    "ds_read_b64 v[108:109], v240 offset:3584\n"
    "ds_read_b64 v[110:111], v240 offset:3592\n"
    "ds_read_b64 v[104:105], v240 offset:3072\n"
    "ds_read_b64 v[106:107], v240 offset:3080\n"
    "ds_read_b64 v[100:101], v240 offset:2560\n"
    "ds_read_b64 v[102:103], v240 offset:2568\n"
    "ds_read_b64 v[96:97], v240 offset:2048\n"
    "ds_read_b64 v[98:99], v240 offset:2056\n"
    "s_waitcnt vmcnt(6)\n"
    "s_add_u32 m0, %[w1k], 82176\n"
    "s_add_u32 s68, %[yst], 2359296\n"
    "buffer_load_dwordx4 v0, %[srdw], s68 offen sc1 lds\n"
    "s_add_u32 m0, %[w1k], 103424\n"
    "s_add_u32 s68, %[yst], 2363392\n"
    "buffer_load_dwordx4 v0, %[srdw], s68 offen sc1 lds\n"
    "s_add_u32 m0, %[w1k], 124672\n"
    "s_add_u32 s68, %[yst], 2367488\n"
    "buffer_load_dwordx4 v0, %[srdw], s68 offen sc1 lds\n"
    "s_add_u32 m0, %[w1k], 145920\n"
    "s_add_u32 s68, %[yst], 2371584\n"
    "buffer_load_dwordx4 v0, %[srdw], s68 offen sc1 lds\n"
    "buffer_load_dwordx4 v[52:55], v0, %[srdw], s45 offen sc1\n"
    "s_mov_b64 exec, 0\n"
    "s_getpc_b64 s[98:99]\n"
    "kx_c0:\n"
    "s_add_u32 s98, s98, kx_r0-kx_c0\n"
    "s_addc_u32 s99, s99, 0\n"
    "s_cmp_eq_u32 %[w1k], 0\n"
    "s_cbranch_scc1 kx_a0\n"
    "s_cmp_eq_u32 %[w1k], 1024\n"
    "s_cbranch_scc1 kx_a1\n"
    "s_cmp_eq_u32 %[w1k], 2048\n"
    "s_cbranch_scc1 kx_a2\n"
    "s_branch kx_a3\n"
    "kx_ret:\n"
    "s_setpc_b64 s[98:99]\n"
    "kx_r0:\n"
    "s_mov_b64 exec, -1\n"
    "s_add_u32 m0, s93, 24576\n"
    "s_mov_b32 s68, 196608\n"
    "buffer_load_dwordx4 v76, %[srdm], s68 offen lds\n"
    "buffer_load_dwordx4 v77, %[srdm], s68 offen offset:1024 lds\n"
    "buffer_load_dwordx4 v247, %[srdm], s68 offen offset:2048 lds\n"
    "s_add_u32 m0, s93, 36864\n"
    "s_mov_b32 s68, 294912\n"
    "buffer_load_dwordx4 v76, %[srdm], s68 offen lds\n"
    "s_waitcnt vmcnt(12)\n"
    "s_barrier\n"
    "ds_read_b64_tr_b16 a[0:1], v6 offset:0\n"
    "ds_read_b64_tr_b16 a[2:3], v6 offset:768\n"
    "ds_read_b64_tr_b16 a[4:5], v6 offset:6144\n"
    "ds_read_b64_tr_b16 a[6:7], v6 offset:6912\n"
    "ds_read_b64_tr_b16 a[8:9], v7 offset:0\n"
    "ds_read_b64_tr_b16 a[10:11], v7 offset:768\n"
    "buffer_load_dwordx4 v77, %[srdm], s68 offen offset:1024 lds\n"
    "s_waitcnt vmcnt(9)\n"
    "ds_read_b128 v[24:27], v40 offset:0\n"
    "buffer_load_dwordx4 v247, %[srdm], s68 offen offset:2048 lds\n"
    "s_waitcnt vmcnt(9)\n"
    "ds_read_b128 v[28:31], v40 offset:21248\n"
    "s_waitcnt lgkmcnt(2)\n"
    "s_barrier\n"
    "ds_read_b64_tr_b16 a[12:13], v6 offset:12288\n"
    "ds_read_b64_tr_b16 a[14:15], v6 offset:13056\n"
    "ds_read_b64_tr_b16 a[16:17], v6 offset:18432\n"
    "ds_read_b64_tr_b16 a[18:19], v6 offset:19200\n"
    "s_waitcnt lgkmcnt(5)\n"
    "v_add3_u32 v24, v24, v25, v26\n"
    "s_waitcnt vmcnt(8)\n"
    "ds_read_b128 v[32:35], v40 offset:42496\n"
    "s_add_u32 m0, s93, 49152\n"
    "s_mov_b32 s68, 393216\n"
    "buffer_load_dwordx4 v76, %[srdm], s68 offen lds\n"
    "ds_read_b64_tr_b16 a[20:21], v7 offset:12288\n"
    "ds_read_b64_tr_b16 a[22:23], v7 offset:13056\n"
    "s_waitcnt lgkmcnt(7)\n"
    "v_add3_u32 v28, v28, v29, v30\n"
    "s_waitcnt vmcnt(8)\n"
    "ds_read_b128 v[36:39], v40 offset:63744\n"
    "buffer_load_dwordx4 v77, %[srdm], s68 offen offset:1024 lds\n"
    "s_waitcnt lgkmcnt(3)\n"
    "v_add3_u32 v32, v32, v33, v34\n"
    "s_waitcnt vmcnt(8)\n"
    "v_add3_u32 v24, v24, v27, v52\n"
    "v_add3_u32 v28, v28, v31, v53\n"
    "v_add3_u32 v32, v32, v35, v54\n"
    "v_bitop3_b32 v24, v24, v28, %[tag] bitop3:0x7e\n"
    "buffer_load_dwordx4 v247, %[srdm], s68 offen offset:2048 lds\n"
    "s_waitcnt lgkmcnt(0)\n"
    "v_add3_u32 v36, v36, v37, v38\n"
    "v_add3_u32 v36, v36, v39, v55\n"
    "v_bitop3_b32 v32, v32, v36, %[tag] bitop3:0x7e\n"
    "v_or_b32 v24, v24, v32\n"
    "v_cmp_ne_u32 vcc, 0, v24\n"
    "s_nop 0\n"
    "s_cmp_lg_u64 vcc, 0\n"
    "s_cbranch_scc1 L1_%=\n"
    "L2_%=:\n"
    "s_barrier\n"
    "ds_read_b128 v[144:147], v241 offset:3584\n"
    "ds_read_b128 v[148:151], v241 offset:3072\n"
    "ds_read_b128 v[152:155], v241 offset:2560\n"
    "ds_read_b128 v[156:159], v241 offset:2048\n"
    "s_add_u32 m0, s93, 0\n"
    "s_mov_b32 s68, 491520\n"
    "buffer_load_dwordx4 v76, %[srdm], s68 offen lds\n"
    "ds_read_b128 v[176:179], v241 offset:3648\n"
    "ds_read_b128 v[180:183], v241 offset:3136\n"
    "ds_read_b128 v[184:187], v241 offset:2624\n"
    "ds_read_b128 v[188:191], v241 offset:2112\n"
    "buffer_load_dwordx4 v77, %[srdm], s68 offen offset:1024 lds\n"
    "ds_read_b128 v[160:163], v241 offset:1536\n"
    "ds_read_b128 v[164:167], v241 offset:1024\n"
    "ds_read_b128 v[168:171], v241 offset:512\n"
    "ds_read_b128 v[172:175], v241 offset:0\n"
    "s_waitcnt lgkmcnt(11)\n"
    "v_mfma_f32_16x16x32_bf16 v[24:27], v[248:251], v[144:147], 0\n"
    "s_waitcnt lgkmcnt(10)\n"
    "s_add_u32 s59, s47, 0\n"
    "s_add_u32 s60, %[q], 4980736\n"
    "v_mfma_f32_16x16x32_bf16 v[28:31], v[248:251], v[148:151], 0\n"
    "s_waitcnt lgkmcnt(9)\n"
    "s_mov_b64 exec, 0xffff\n"
    "buffer_load_dword v79, v1, %[srdw], s59 offen sc1\n"
    "s_mov_b64 exec, -1\n"
    "v_mfma_f32_16x16x32_bf16 v[32:35], v[248:251], v[152:155], 0\n"
    "s_waitcnt lgkmcnt(8)\n"
    "s_add_u32 s61, %[w1k], 139264\n"
    "s_add_u32 s62, %[q], 5636096\n"
    "v_mfma_f32_16x16x32_bf16 v[36:39], v[248:251], v[156:159], 0\n"
    "s_add_u32 s63, %[w1k], 143360\n"
    "s_waitcnt vmcnt(9)\n"
    "s_barrier\n"
    "v_mfma_f32_16x16x32_bf16 v[36:39], v[92:95], v[144:147], v[36:39]\n"
    "ds_read_b64_tr_b16 a[24:25], v6 offset:24576\n"
    "ds_read_b64_tr_b16 a[26:27], v6 offset:25344\n"
    "v_mfma_f32_16x16x32_bf16 v[32:35], v[88:91], v[144:147], v[32:35]\n"
    "ds_read_b64_tr_b16 a[28:29], v6 offset:30720\n"
    "ds_read_b64_tr_b16 a[30:31], v6 offset:31488\n"
    "v_mfma_f32_16x16x32_bf16 v[36:39], v[88:91], v[148:151], v[36:39]\n"
    "ds_read_b64_tr_b16 a[32:33], v7 offset:24576\n"
    "ds_read_b64_tr_b16 a[34:35], v7 offset:25344\n"
    "v_mfma_f32_16x16x32_bf16 v[28:31], v[84:87], v[144:147], v[28:31]\n"
    "v_mfma_f32_16x16x32_bf16 v[32:35], v[84:87], v[148:151], v[32:35]\n"
    "v_mfma_f32_16x16x32_bf16 v[36:39], v[84:87], v[152:155], v[36:39]\n"
    "v_mfma_f32_16x16x32_bf16 v[24:27], v[80:83], v[144:147], v[24:27]\n"
    "v_mfma_f32_16x16x32_bf16 v[28:31], v[80:83], v[148:151], v[28:31]\n"
    "v_mfma_f32_16x16x32_bf16 v[32:35], v[80:83], v[152:155], v[32:35]\n"
    "v_mfma_f32_16x16x32_bf16 v[36:39], v[80:83], v[156:159], v[36:39]\n"
    "s_waitcnt lgkmcnt(13)\n"
    "ds_read_b64 v[92:93], v240 offset:1664\n"
    "ds_read_b64 v[94:95], v240 offset:1672\n"
    "v_mfma_f32_16x16x32_bf16 v[36:39], v[124:127], v[176:179], v[36:39]\n"
    "ds_read_b128 v[144:147], v241 offset:3712\n"
    "v_mfma_f32_16x16x32_bf16 v[32:35], v[120:123], v[176:179], v[32:35]\n"
    "s_waitcnt lgkmcnt(15)\n"
    "ds_read_b64 v[88:89], v240 offset:1152\n"
    "ds_read_b64 v[90:91], v240 offset:1160\n"
    "v_mfma_f32_16x16x32_bf16 v[36:39], v[120:123], v[180:183], v[36:39]\n"
    "s_waitcnt lgkmcnt(5) vmcnt(6)\n"
    "s_barrier\n"
    "v_mfma_f32_16x16x32_bf16 v[28:31], v[116:119], v[176:179], v[28:31]\n"
    "ds_read_b64_tr_b16 a[36:37], v6 offset:36864\n"
    "ds_read_b64_tr_b16 a[38:39], v6 offset:37632\n"
    "v_mfma_f32_16x16x32_bf16 v[32:35], v[116:119], v[180:183], v[32:35]\n"
    "buffer_load_dwordx4 v247, %[srdm], s68 offen offset:2048 lds\n"
    "ds_read_b64_tr_b16 a[40:41], v6 offset:43008\n"
    "v_mfma_f32_16x16x32_bf16 v[36:39], v[116:119], v[184:187], v[36:39]\n"
    "ds_read_b64_tr_b16 a[42:43], v6 offset:43776\n"
    "ds_read_b64_tr_b16 a[44:45], v7 offset:36864\n"
    "v_mfma_f32_16x16x32_bf16 v[24:27], v[112:115], v[176:179], v[24:27]\n"
    "ds_read_b64_tr_b16 a[46:47], v7 offset:37632\n"
    "ds_read_b128 v[148:151], v241 offset:3200\n"
    "v_mfma_f32_16x16x32_bf16 v[28:31], v[112:115], v[180:183], v[28:31]\n"
    "ds_read_b64 v[84:85], v240 offset:640\n"
    "v_mfma_f32_16x16x32_bf16 v[32:35], v[112:115], v[184:187], v[32:35]\n"
    "ds_read_b64 v[86:87], v240 offset:648\n"
    "ds_read_b128 v[152:155], v241 offset:2688\n"
    "v_mfma_f32_16x16x32_bf16 v[36:39], v[112:115], v[188:191], v[36:39]\n"
    "s_waitcnt lgkmcnt(12)\n"
    "s_add_u32 m0, s93, 12288\n"
    "s_mov_b32 s68, 589824\n"
    "buffer_load_dwordx4 v76, %[srdm], s68 offen lds\n"
    "v_mfma_f32_16x16x32_bf16 v[36:39], v[92:95], v[144:147], v[36:39]\n"
    "s_waitcnt lgkmcnt(10)\n"
    "ds_read_b64 v[80:81], v240 offset:128\n"
    "ds_read_b64 v[82:83], v240 offset:136\n"
    "v_mfma_f32_16x16x32_bf16 v[32:35], v[88:91], v[144:147], v[32:35]\n"
    "s_waitcnt lgkmcnt(5)\n"
    "ds_read_b128 v[156:159], v241 offset:2176\n"
    "buffer_load_dwordx4 v77, %[srdm], s68 offen offset:1024 lds\n"
    "buffer_load_dwordx4 v247, %[srdm], s68 offen offset:2048 lds\n"
    "s_add_u32 m0, s93, 24576\n"
    "s_mov_b32 s68, 688128\n"
    "buffer_load_dwordx4 v76, %[srdm], s68 offen lds\n"
    "v_mfma_f32_16x16x32_bf16 v[36:39], v[88:91], v[148:151], v[36:39]\n"
    "s_waitcnt lgkmcnt(4)\n"
    "ds_read_b64 v[124:125], v240 offset:1728\n"
    "ds_read_b64 v[126:127], v240 offset:1736\n"
    "v_mfma_f32_16x16x32_bf16 v[28:31], v[84:87], v[144:147], v[28:31]\n"
    "ds_read_b128 v[176:179], v241 offset:3776\n"
    "v_mfma_f32_16x16x32_bf16 v[32:35], v[84:87], v[148:151], v[32:35]\n"
    "s_waitcnt lgkmcnt(6)\n"
    "s_waitcnt vmcnt(8)\n"
    "s_barrier\n"
    "v_mfma_f32_16x16x32_bf16 v[36:39], v[84:87], v[152:155], v[36:39]\n"
    "s_waitcnt lgkmcnt(4)\n"
    "ds_read_b64_tr_b16 a[48:49], v6 offset:49152\n"
    "ds_read_b64_tr_b16 a[50:51], v6 offset:49920\n"
    "v_mfma_f32_16x16x32_bf16 v[24:27], v[80:83], v[144:147], v[24:27]\n"
    "ds_read_b64_tr_b16 a[52:53], v6 offset:55296\n"
    "ds_read_b64_tr_b16 a[54:55], v6 offset:56064\n"
    "v_mfma_f32_16x16x32_bf16 v[28:31], v[80:83], v[148:151], v[28:31]\n"
    "ds_read_b64_tr_b16 a[56:57], v7 offset:49152\n"
    "ds_read_b64_tr_b16 a[58:59], v7 offset:49920\n"
    "v_mfma_f32_16x16x32_bf16 v[32:35], v[80:83], v[152:155], v[32:35]\n"
    "s_waitcnt lgkmcnt(9)\n"
    "ds_read_b64 v[120:121], v240 offset:1216\n"
    "ds_read_b64 v[122:123], v240 offset:1224\n"
    "v_mfma_f32_16x16x32_bf16 v[36:39], v[80:83], v[156:159], v[36:39]\n"
    "s_waitcnt lgkmcnt(8)\n"
    "ds_read_b128 v[180:183], v241 offset:3264\n"
    "v_mfma_f32_16x16x32_bf16 v[36:39], v[124:127], v[176:179], v[36:39]\n"
    "s_waitcnt lgkmcnt(1)\n"
    "ds_read_b64 v[116:117], v240 offset:704\n"
    "ds_read_b64 v[118:119], v240 offset:712\n"
    "buffer_load_dwordx4 v77, %[srdm], s68 offen offset:1024 lds\n"
    "buffer_load_dwordx4 v247, %[srdm], s68 offen offset:2048 lds\n"
    "ds_read_b128 v[184:187], v241 offset:2752\n"
    "ds_read_b64 v[112:113], v240 offset:192\n"
    "ds_read_b64 v[114:115], v240 offset:200\n"
    "ds_read_b128 v[188:191], v241 offset:2240\n"
    "ds_read_b64 v[92:93], v240 offset:1792\n"
    "s_add_u32 m0, s93, 36864\n"
    "s_mov_b32 s68, 786432\n"
    "buffer_load_dwordx4 v76, %[srdm], s68 offen lds\n"
    "s_waitcnt vmcnt(8)\n"
    "v_cmp_ne_u32 vcc, %[tag], v79\n"
    "s_nop 0\n"
    "s_and_b32 vcc_lo, vcc_lo, 0xffff\n"
    "s_cbranch_scc1 L3_%=\n"
    "L4_%=:\n"
    "v_mfma_f32_16x16x32_bf16 v[32:35], v[120:123], v[176:179], v[32:35]\n"
    "s_waitcnt lgkmcnt(7)\n"
    "buffer_load_dwordx4 v[208:211], v244, %[srdw], s60 offen sc1\n"
    "v_mfma_f32_16x16x32_bf16 v[36:39], v[120:123], v[180:183], v[36:39]\n"
    "s_waitcnt lgkmcnt(5)\n"
    "buffer_load_dwordx4 v[212:215], v0, %[srdw], s61 offen sc1\n"
    "v_mfma_f32_16x16x32_bf16 v[28:31], v[116:119], v[176:179], v[28:31]\n"
    "buffer_load_dwordx4 v[216:219], v244, %[srdw], s62 offen sc1\n"
    "buffer_load_dwordx4 v[220:223], v0, %[srdw], s63 offen sc1\n"
    "v_mfma_f32_16x16x32_bf16 v[32:35], v[116:119], v[180:183], v[32:35]\n"
    "s_waitcnt lgkmcnt(4)\n"
    "ds_read_b64 v[94:95], v240 offset:1800\n"
    "ds_read_b128 v[144:147], v241 offset:3840\n"
    "v_mfma_f32_16x16x32_bf16 v[36:39], v[116:119], v[184:187], v[36:39]\n"
    "s_waitcnt lgkmcnt(4)\n"
    "ds_read_b64 v[88:89], v240 offset:1280\n"
    "v_mfma_f32_16x16x32_bf16 v[24:27], v[112:115], v[176:179], v[24:27]\n"
    "ds_read_b64 v[90:91], v240 offset:1288\n"
    "ds_read_b128 v[148:151], v241 offset:3328\n"
    "v_mfma_f32_16x16x32_bf16 v[28:31], v[112:115], v[180:183], v[28:31]\n"
    "ds_read_b64 v[84:85], v240 offset:768\n"
    "v_mfma_f32_16x16x32_bf16 v[32:35], v[112:115], v[184:187], v[32:35]\n"
    "s_waitcnt lgkmcnt(7)\n"
    "ds_read_b64 v[86:87], v240 offset:776\n"
    "ds_read_b128 v[152:155], v241 offset:2816\n"
    "v_mfma_f32_16x16x32_bf16 v[36:39], v[112:115], v[188:191], v[36:39]\n"
    "s_waitcnt lgkmcnt(6)\n"
    "ds_read_b64 v[80:81], v240 offset:256\n"
    "s_waitcnt vmcnt(11)\n"
    "s_barrier\n"
    "ds_read_b64_tr_b16 a[60:61], v6 offset:0\n"
    "ds_read_b64_tr_b16 a[62:63], v6 offset:768\n"
    "ds_read_b64_tr_b16 a[64:65], v6 offset:6144\n"
    "v_mfma_f32_16x16x32_bf16 v[36:39], v[92:95], v[144:147], v[36:39]\n"
    "s_waitcnt lgkmcnt(8)\n"
    "ds_read_b64_tr_b16 a[66:67], v6 offset:6912\n"
    "ds_read_b64_tr_b16 a[68:69], v7 offset:0\n"
    "v_mfma_f32_16x16x32_bf16 v[32:35], v[88:91], v[144:147], v[32:35]\n"
    "s_waitcnt lgkmcnt(9)\n"
    "ds_read_b64_tr_b16 a[70:71], v7 offset:768\n"
    "ds_read_b64 v[82:83], v240 offset:264\n"
    "v_mfma_f32_16x16x32_bf16 v[36:39], v[88:91], v[148:151], v[36:39]\n"
    "s_waitcnt lgkmcnt(9)\n"
    "ds_read_b128 v[156:159], v241 offset:2304\n"
    "v_mfma_f32_16x16x32_bf16 v[28:31], v[84:87], v[144:147], v[28:31]\n"
    "ds_read_b64 v[124:125], v240 offset:1856\n"
    "ds_read_b64 v[126:127], v240 offset:1864\n"
    "v_mfma_f32_16x16x32_bf16 v[32:35], v[84:87], v[148:151], v[32:35]\n"
    "kx_a0:\n"
    "s_cbranch_execz kx_a4\n"
    "s_waitcnt lgkmcnt(11)\n"
    "ds_read_b128 v[176:179], v241 offset:3904\n"
    "v_mfma_f32_16x16x32_bf16 v[36:39], v[84:87], v[152:155], v[36:39]\n"
    "s_waitcnt lgkmcnt(4)\n"
    "ds_read_b64 v[120:121], v240 offset:1344\n"
    "ds_read_b64 v[122:123], v240 offset:1352\n"
    "ds_read_b128 v[180:183], v241 offset:3392\n"
    "ds_read_b64 v[116:117], v240 offset:832\n"
    "ds_read_b64 v[118:119], v240 offset:840\n"
    "ds_read_b128 v[184:187], v241 offset:2880\n"
    "ds_read_b64 v[112:113], v240 offset:320\n"
    "ds_read_b64 v[114:115], v240 offset:328\n"
    "ds_read_b128 v[188:191], v241 offset:2368\n"
    "v_mfma_f32_16x16x32_bf16 v[24:27], v[80:83], v[144:147], v[24:27]\n"
    "v_mfma_f32_16x16x32_bf16 v[28:31], v[80:83], v[148:151], v[28:31]\n"
    "v_mfma_f32_16x16x32_bf16 v[32:35], v[80:83], v[152:155], v[32:35]\n"
    "s_waitcnt lgkmcnt(12)\n"
    "v_mfma_f32_16x16x32_bf16 v[36:39], v[80:83], v[156:159], v[36:39]\n"
    "s_waitcnt lgkmcnt(9)\n"
    "s_waitcnt vmcnt(8)\n"
    "s_barrier\n"
    "v_mfma_f32_16x16x32_bf16 v[36:39], v[124:127], v[176:179], v[36:39]\n"
    "s_waitcnt lgkmcnt(7)\n"
    "ds_read_b64_tr_b16 a[72:73], v6 offset:12288\n"
    "ds_read_b64_tr_b16 a[74:75], v6 offset:13056\n"
    "v_mfma_f32_16x16x32_bf16 v[32:35], v[120:123], v[176:179], v[32:35]\n"
    "s_waitcnt lgkmcnt(8)\n"
    "ds_read_b64_tr_b16 a[76:77], v6 offset:18432\n"
    "ds_read_b64_tr_b16 a[78:79], v6 offset:19200\n"
    "v_mfma_f32_16x16x32_bf16 v[36:39], v[120:123], v[180:183], v[36:39]\n"
    "s_waitcnt lgkmcnt(8)\n"
    "ds_read_b64_tr_b16 a[80:81], v7 offset:12288\n"
    "ds_read_b64_tr_b16 a[82:83], v7 offset:13056\n"
    "v_mfma_f32_16x16x32_bf16 v[28:31], v[116:119], v[176:179], v[28:31]\n"
    "ds_read_b64 v[92:93], v240 offset:1920\n"
    "ds_read_b64 v[94:95], v240 offset:1928\n"
    "v_mfma_f32_16x16x32_bf16 v[32:35], v[116:119], v[180:183], v[32:35]\n"
    "s_waitcnt lgkmcnt(11)\n"
    "ds_read_b128 v[144:147], v241 offset:3968\n"
    "v_mfma_f32_16x16x32_bf16 v[36:39], v[116:119], v[184:187], v[36:39]\n"
    "s_waitcnt lgkmcnt(10)\n"
    "ds_read_b64 v[88:89], v240 offset:1408\n"
    "ds_read_b64 v[90:91], v240 offset:1416\n"
    "v_mfma_f32_16x16x32_bf16 v[24:27], v[112:115], v[176:179], v[24:27]\n"
    "ds_read_b128 v[148:151], v241 offset:3456\n"
    "v_mfma_f32_16x16x32_bf16 v[28:31], v[112:115], v[180:183], v[28:31]\n"
    "ds_read_b64 v[84:85], v240 offset:896\n"
    "ds_read_b64 v[86:87], v240 offset:904\n"
    "v_mfma_f32_16x16x32_bf16 v[32:35], v[112:115], v[184:187], v[32:35]\n"
    "s_waitcnt lgkmcnt(14)\n"
    "ds_read_b128 v[152:155], v241 offset:2944\n"
    "v_mfma_f32_16x16x32_bf16 v[36:39], v[112:115], v[188:191], v[36:39]\n"
    "s_waitcnt lgkmcnt(6)\n"
    "buffer_load_dwordx4 v77, %[srdm], s68 offen offset:1024 lds\n"
    "buffer_load_dwordx4 v247, %[srdm], s68 offen offset:2048 lds\n"
    "s_add_u32 m0, s93, 49152\n"
    "s_mov_b32 s68, 884736\n"
    "buffer_load_dwordx4 v76, %[srdm], s68 offen lds\n"
    "ds_read_b64 v[80:81], v240 offset:384\n"
    "v_mfma_f32_16x16x32_bf16 v[36:39], v[92:95], v[144:147], v[36:39]\n"
    "s_waitcnt lgkmcnt(5)\n"
    "ds_read_b64 v[82:83], v240 offset:392\n"
    "ds_read_b128 v[156:159], v241 offset:2432\n"
    "v_mfma_f32_16x16x32_bf16 v[32:35], v[88:91], v[144:147], v[32:35]\n"
    "s_waitcnt lgkmcnt(6)\n"
    "ds_read_b64 v[124:125], v240 offset:1984\n"
    "v_mfma_f32_16x16x32_bf16 v[36:39], v[88:91], v[148:151], v[36:39]\n"
    "s_waitcnt lgkmcnt(5)\n"
    "s_waitcnt vmcnt(8)\n"
    "s_barrier\n"
    "v_mfma_f32_16x16x32_bf16 v[28:31], v[84:87], v[144:147], v[28:31]\n"
    "ds_read_b64_tr_b16 a[84:85], v6 offset:24576\n"
    "ds_read_b64_tr_b16 a[86:87], v6 offset:25344\n"
    "v_mfma_f32_16x16x32_bf16 v[32:35], v[84:87], v[148:151], v[32:35]\n"
    "s_waitcnt lgkmcnt(6)\n"
    "ds_read_b64_tr_b16 a[88:89], v6 offset:30720\n"
    "ds_read_b64_tr_b16 a[90:91], v6 offset:31488\n"
    "v_mfma_f32_16x16x32_bf16 v[36:39], v[84:87], v[152:155], v[36:39]\n"
    "s_waitcnt lgkmcnt(6)\n"
    "ds_read_b64_tr_b16 a[92:93], v7 offset:24576\n"
    "ds_read_b64_tr_b16 a[94:95], v7 offset:25344\n"
    "buffer_load_dwordx4 v77, %[srdm], s68 offen offset:1024 lds\n"
    "buffer_load_dwordx4 v247, %[srdm], s68 offen offset:2048 lds\n"
    "ds_read_b64 v[126:127], v240 offset:1992\n"
    "ds_read_b128 v[176:179], v241 offset:4032\n"
    "ds_read_b64 v[120:121], v240 offset:1472\n"
    "v_mfma_f32_16x16x32_bf16 v[24:27], v[80:83], v[144:147], v[24:27]\n"
    "ds_read_b64 v[122:123], v240 offset:1480\n"
    "v_mfma_f32_16x16x32_bf16 v[28:31], v[80:83], v[148:151], v[28:31]\n"
    "ds_read_b128 v[180:183], v241 offset:3520\n"
    "v_mfma_f32_16x16x32_bf16 v[32:35], v[80:83], v[152:155], v[32:35]\n"
    "s_waitcnt lgkmcnt(12)\n"
    "ds_read_b64 v[116:117], v240 offset:960\n"
    "ds_read_b64 v[118:119], v240 offset:968\n"
    "v_mfma_f32_16x16x32_bf16 v[36:39], v[80:83], v[156:159], v[36:39]\n"
    "s_waitcnt lgkmcnt(5)\n"
    "ds_read_b64 v[92:93], v240 offset:1536\n"
    "ds_read_b64 v[94:95], v240 offset:1544\n"
    "ds_read_b128 v[144:147], v241 offset:3584\n"
    "ds_read_b64 v[88:89], v240 offset:1024\n"
    "ds_read_b64 v[90:91], v240 offset:1032\n"
    "ds_read_b128 v[148:151], v241 offset:3072\n"
    "ds_read_b64 v[84:85], v240 offset:512\n"
    "ds_read_b64 v[86:87], v240 offset:520\n"
    "ds_read_b128 v[152:155], v241 offset:2560\n"
    "ds_read_b64 v[80:81], v240 offset:0\n"
    "v_mfma_f32_16x16x32_bf16 v[36:39], v[124:127], v[176:179], v[36:39]\n"
    "s_waitcnt lgkmcnt(13)\n"
    "ds_read_b64 v[82:83], v240 offset:8\n"
    "ds_read_b128 v[156:159], v241 offset:2048\n"
    "v_mfma_f32_16x16x32_bf16 v[32:35], v[120:123], v[176:179], v[32:35]\n"
    "s_waitcnt lgkmcnt(14)\n"
    "s_add_u32 s64, %[q], 4980736\n"
    "s_add_u32 s65, %[q], 5636096\n"
    "v_mfma_f32_16x16x32_bf16 v[36:39], v[120:123], v[180:183], v[36:39]\n"
    "s_waitcnt lgkmcnt(12)\n"
    "v_mfma_f32_16x16x32_bf16 v[28:31], v[116:119], v[176:179], v[28:31]\n"
    "s_waitcnt vmcnt(8)\n"
    "v_lshlrev_b32 v128, 16, v208\n"
    "v_and_b32 v129, 0xffff0000, v208\n"
    "v_lshlrev_b32 v130, 16, v209\n"
    "v_and_b32 v131, 0xffff0000, v209\n"
    "v_lshlrev_b32 v132, 16, v210\n"
    "v_and_b32 v133, 0xffff0000, v210\n"
    "v_lshlrev_b32 v134, 16, v211\n"
    "v_and_b32 v135, 0xffff0000, v211\n"
    "v_mfma_f32_16x16x32_bf16 v[32:35], v[116:119], v[180:183], v[32:35]\n"
    "ds_read_b128 v[184:187], v241 offset:3008\n"
    "s_add_u32 m0, s93, 0\n"
    "s_mov_b32 s68, 983040\n"
    "buffer_load_dwordx4 v76, %[srdm], s68 offen lds\n"
    "buffer_load_dwordx4 v77, %[srdm], s68 offen offset:1024 lds\n"
    "buffer_load_dwordx4 v247, %[srdm], s68 offen offset:2048 lds\n"
    "ds_read_b64 v[112:113], v240 offset:448\n"
    "s_waitcnt vmcnt(10)\n"
    "v_fma_f32 v224, v128, v212, v213\n"
    "v_fma_f32 v225, v129, v212, v213\n"
    "v_fma_f32 v226, v130, v212, v213\n"
    "v_fma_f32 v227, v131, v212, v213\n"
    "v_fma_f32 v228, v132, v214, v215\n"
    "v_fma_f32 v229, v133, v214, v215\n"
    "v_fma_f32 v230, v134, v214, v215\n"
    "v_fma_f32 v231, v135, v214, v215\n"
    "ds_read_b64 v[114:115], v240 offset:456\n"
    "ds_read_b128 v[188:191], v241 offset:2496\n"
    "s_waitcnt lgkmcnt(3)\n"
    "v_mfma_f32_16x16x32_bf16 v[36:39], v[116:119], v[184:187], v[36:39]\n"
    "s_waitcnt lgkmcnt(1) vmcnt(9)\n"
    "v_lshlrev_b32 v136, 16, v216\n"
    "v_and_b32 v137, 0xffff0000, v216\n"
    "v_lshlrev_b32 v138, 16, v217\n"
    "v_and_b32 v139, 0xffff0000, v217\n"
    "v_lshlrev_b32 v140, 16, v218\n"
    "v_and_b32 v141, 0xffff0000, v218\n"
    "v_lshlrev_b32 v142, 16, v219\n"
    "v_and_b32 v143, 0xffff0000, v219\n"
    "v_mfma_f32_16x16x32_bf16 v[24:27], v[112:115], v[176:179], v[24:27]\n"
    "s_waitcnt vmcnt(8)\n"
    "v_fma_f32 v232, v136, v220, v221\n"
    "v_fma_f32 v233, v137, v220, v221\n"
    "v_fma_f32 v234, v138, v220, v221\n"
    "v_fma_f32 v235, v139, v220, v221\n"
    "v_fma_f32 v236, v140, v222, v223\n"
    "v_fma_f32 v237, v141, v222, v223\n"
    "v_fma_f32 v238, v142, v222, v223\n"
    "v_fma_f32 v239, v143, v222, v223\n"
    "v_mfma_f32_16x16x32_bf16 v[28:31], v[112:115], v[180:183], v[28:31]\n"
    "v_mfma_f32_16x16x32_bf16 v[32:35], v[112:115], v[184:187], v[32:35]\n"
    "s_waitcnt lgkmcnt(0)\n"
    "v_mfma_f32_16x16x32_bf16 v[36:39], v[112:115], v[188:191], v[36:39]\n"
    "s_lshl_b32 s66, %[f3p], 3\n"
    "ds_read_b128 v[176:179], v241 offset:3648\n"
    "v_cvt_pk_bf16_f32 v24, v24, v25\n"
    "v_cvt_pk_bf16_f32 v25, v26, v27\n"
    "ds_write_b64 v242, v[24:25] offset:0\n"
    "v_cvt_pk_bf16_f32 v28, v28, v29\n"
    "v_cvt_pk_bf16_f32 v29, v30, v31\n"
    "v_cvt_pk_bf16_f32 v32, v32, v33\n"
    "v_cvt_pk_bf16_f32 v33, v34, v35\n"
    "v_mfma_f32_16x16x32_bf16 v[40:43], v[248:251], v[160:163], 0\n"
    "s_add_u32 s67, s66, 73728\n"
    "ds_write_b64 v242, v[28:29] offset:512\n"
    "ds_write_b64 v242, v[32:33] offset:1024\n"
    "v_cvt_pk_bf16_f32 v36, v36, v37\n"
    "v_cvt_pk_bf16_f32 v37, v38, v39\n"
    "v_mfma_f32_16x16x32_bf16 v[44:47], v[248:251], v[164:167], 0\n"
    "s_add_u32 s78, %[ldsx], 73728\n"
    "ds_write_b64 v242, v[36:37] offset:1536\n"
    "v_mfma_f32_16x16x32_bf16 v[48:51], v[248:251], v[168:171], 0\n"
    "ds_read_b128 v[180:183], v241 offset:3136\n"
    "v_mfma_f32_16x16x32_bf16 v[52:55], v[248:251], v[172:175], 0\n"
    "ds_read_b128 v[184:187], v241 offset:2624\n"
    "v_mfma_f32_16x16x32_bf16 v[52:55], v[108:111], v[144:147], v[52:55]\n"
    "ds_read_b128 v[188:191], v241 offset:2112\n"
    "v_mfma_f32_16x16x32_bf16 v[48:51], v[104:107], v[144:147], v[48:51]\n"
    "s_waitcnt lgkmcnt(3)\n"
    "s_barrier\n"
    "ds_read_b64_tr_b16 v[56:57], v243 offset:0\n"
    "ds_read_b64_tr_b16 v[58:59], v243 offset:16\n"
    "ds_read_b64_tr_b16 v[64:65], v243 offset:1024\n"
    "ds_read_b64_tr_b16 v[66:67], v243 offset:1040\n"
    "v_mfma_f32_16x16x32_bf16 v[52:55], v[104:107], v[148:151], v[52:55]\n"
    "s_add_u32 m0, s93, 12288\n"
    "s_mov_b32 s68, 1081344\n"
    "buffer_load_dwordx4 v76, %[srdm], s68 offen lds\n"
    "v_mfma_f32_16x16x32_bf16 v[44:47], v[100:103], v[144:147], v[44:47]\n"
    "s_waitcnt vmcnt(7)\n"
    "s_barrier\n"
    "v_mfma_f32_16x16x32_bf16 v[48:51], v[100:103], v[148:151], v[48:51]\n"
    "buffer_load_dwordx4 v77, %[srdm], s68 offen offset:1024 lds\n"
    "ds_read_b64_tr_b16 a[96:97], v6 offset:36864\n"
    "v_mfma_f32_16x16x32_bf16 v[52:55], v[100:103], v[152:155], v[52:55]\n"
    "ds_read_b64_tr_b16 a[98:99], v6 offset:37632\n"
    "ds_read_b64_tr_b16 a[100:101], v6 offset:43008\n"
    "v_mfma_f32_16x16x32_bf16 v[40:43], v[96:99], v[144:147], v[40:43]\n"
    "ds_read_b64_tr_b16 a[102:103], v6 offset:43776\n"
    "ds_read_b64_tr_b16 a[104:105], v7 offset:36864\n"
    "v_mfma_f32_16x16x32_bf16 v[44:47], v[96:99], v[148:151], v[44:47]\n"
    "s_waitcnt lgkmcnt(8)\n"
    "v_lshlrev_b32 v208, 16, v56\n"
    "v_and_b32 v212, 0xffff0000, v56\n"
    "v_fmac_f32 v128, v224, v208\n"
    "v_fmac_f32 v129, v225, v212\n"
    "v_cvt_pk_bf16_f32 v60, v128, v129\n"
    "v_lshlrev_b32 v209, 16, v57\n"
    "v_and_b32 v213, 0xffff0000, v57\n"
    "v_fmac_f32 v130, v226, v209\n"
    "v_fmac_f32 v131, v227, v213\n"
    "v_cvt_pk_bf16_f32 v61, v130, v131\n"
    "v_mfma_f32_16x16x32_bf16 v[48:51], v[96:99], v[152:155], v[48:51]\n"
    "s_waitcnt lgkmcnt(7)\n"
    "v_lshlrev_b32 v210, 16, v58\n"
    "v_and_b32 v214, 0xffff0000, v58\n"
    "v_fmac_f32 v132, v228, v210\n"
    "v_fmac_f32 v133, v229, v214\n"
    "v_cvt_pk_bf16_f32 v62, v132, v133\n"
    "v_lshlrev_b32 v211, 16, v59\n"
    "v_and_b32 v215, 0xffff0000, v59\n"
    "v_fmac_f32 v134, v230, v211\n"
    "v_fmac_f32 v135, v231, v215\n"
    "v_cvt_pk_bf16_f32 v63, v134, v135\n"
    "v_mfma_f32_16x16x32_bf16 v[52:55], v[96:99], v[156:159], v[52:55]\n"
    "s_waitcnt lgkmcnt(6)\n"
    "buffer_store_dwordx4 v[60:63], v244, %[srdw], s64 offen sc1\n"
    "v_lshlrev_b32 v216, 16, v64\n"
    "v_and_b32 v220, 0xffff0000, v64\n"
    "v_fmac_f32 v136, v232, v216\n"
    "v_fmac_f32 v137, v233, v220\n"
    "v_cvt_pk_bf16_f32 v68, v136, v137\n"
    "v_mfma_f32_16x16x32_bf16 v[40:43], v[92:95], v[148:151], v[40:43]\n"
    "v_lshlrev_b32 v217, 16, v65\n"
    "v_and_b32 v221, 0xffff0000, v65\n"
    "v_fmac_f32 v138, v234, v217\n"
    "v_fmac_f32 v139, v235, v221\n"
    "v_cvt_pk_bf16_f32 v69, v138, v139\n"
    "s_waitcnt lgkmcnt(5)\n"
    "v_lshlrev_b32 v218, 16, v66\n"
    "v_and_b32 v222, 0xffff0000, v66\n"
    "v_fmac_f32 v140, v236, v218\n"
    "v_fmac_f32 v141, v237, v222\n"
    "v_mfma_f32_16x16x32_bf16 v[44:47], v[92:95], v[152:155], v[44:47]\n"
    "v_cvt_pk_bf16_f32 v70, v140, v141\n"
    "v_lshlrev_b32 v219, 16, v67\n"
    "v_and_b32 v223, 0xffff0000, v67\n"
    "v_fmac_f32 v142, v238, v219\n"
    "v_fmac_f32 v143, v239, v223\n"
    "v_cvt_pk_bf16_f32 v71, v142, v143\n"
    "v_mfma_f32_16x16x32_bf16 v[48:51], v[92:95], v[156:159], v[48:51]\n"
    "buffer_store_dwordx4 v[68:71], v244, %[srdw], s65 offen sc1\n"
    "v_mfma_f32_16x16x32_bf16 v[52:55], v[92:95], v[160:163], v[52:55]\n"
    "buffer_load_dwordx4 v247, %[srdm], s68 offen offset:2048 lds\n"
    "ds_read_b64_tr_b16 a[106:107], v7 offset:37632\n"
    "v_mfma_f32_16x16x32_bf16 v[40:43], v[88:91], v[152:155], v[40:43]\n"
    "ds_read_b64_tr_b16 a[240:241], v243 offset:0\n"
    "ds_read_b64_tr_b16 a[242:243], v243 offset:16\n"
    "v_mfma_f32_16x16x32_bf16 v[44:47], v[88:91], v[156:159], v[44:47]\n"
    "ds_read_b64_tr_b16 a[244:245], v243 offset:1024\n"
    "ds_read_b64_tr_b16 a[246:247], v243 offset:1040\n"
    "v_mfma_f32_16x16x32_bf16 v[48:51], v[88:91], v[160:163], v[48:51]\n"
    "ds_read_b64 v[140:141], v240 offset:3648\n"
    "ds_read_b64 v[142:143], v240 offset:3656\n"
    "v_mfma_f32_16x16x32_bf16 v[52:55], v[88:91], v[164:167], v[52:55]\n"
    "ds_read_b64 v[136:137], v240 offset:3136\n"
    "ds_read_b64 v[138:139], v240 offset:3144\n"
    "v_mfma_f32_16x16x32_bf16 v[40:43], v[84:87], v[156:159], v[40:43]\n"
    "s_add_u32 m0, s93, 24576\n"
    "s_mov_b32 s68, 1179648\n"
    "buffer_load_dwordx4 v76, %[srdm], s68 offen lds\n"
    "v_mfma_f32_16x16x32_bf16 v[44:47], v[84:87], v[160:163], v[44:47]\n"
    "ds_read_b64 v[132:133], v240 offset:2624\n"
    "ds_read_b64 v[134:135], v240 offset:2632\n"
    "v_mfma_f32_16x16x32_bf16 v[48:51], v[84:87], v[164:167], v[48:51]\n"
    "ds_read_b64 v[128:129], v240 offset:2112\n"
    "ds_read_b64 v[130:131], v240 offset:2120\n"
    "v_mfma_f32_16x16x32_bf16 v[52:55], v[84:87], v[168:171], v[52:55]\n"
    "s_waitcnt lgkmcnt(12) vmcnt(9)\n"
    "s_barrier\n"
    "v_mfma_f32_16x16x32_bf16 v[40:43], v[80:83], v[160:163], v[40:43]\n"
    "ds_read_b64_tr_b16 a[108:109], v6 offset:49152\n"
    "ds_read_b64_tr_b16 a[110:111], v6 offset:49920\n"
    "v_mfma_f32_16x16x32_bf16 v[44:47], v[80:83], v[164:167], v[44:47]\n"
    "buffer_load_dwordx4 v77, %[srdm], s68 offen offset:1024 lds\n"
    "ds_read_b64_tr_b16 a[112:113], v6 offset:55296\n"
    "v_mfma_f32_16x16x32_bf16 v[48:51], v[80:83], v[168:171], v[48:51]\n"
    "ds_read_b64_tr_b16 a[114:115], v6 offset:56064\n"
    "ds_read_b64_tr_b16 a[116:117], v7 offset:49152\n"
    "v_mfma_f32_16x16x32_bf16 v[52:55], v[80:83], v[172:175], v[52:55]\n"
    "s_waitcnt lgkmcnt(11)\n"
    "ds_read_b64_tr_b16 a[118:119], v7 offset:49920\n"
    "ds_read_b64 v[124:125], v240 offset:1600\n"
    "v_mfma_f32_16x16x32_bf16 v[52:55], v[140:143], v[176:179], v[52:55]\n"
    "s_waitcnt lgkmcnt(11)\n"
    "ds_read_b64 v[126:127], v240 offset:1608\n"
    "ds_read_b128 v[192:195], v241 offset:1600\n"
    "v_mfma_f32_16x16x32_bf16 v[48:51], v[136:139], v[176:179], v[48:51]\n"
    "ds_read_b64 v[120:121], v240 offset:1088\n"
    "v_mfma_f32_16x16x32_bf16 v[52:55], v[136:139], v[180:183], v[52:55]\n"
    "s_waitcnt lgkmcnt(12)\n"
    "buffer_load_dwordx4 v247, %[srdm], s68 offen offset:2048 lds\n"
    "v_mfma_f32_16x16x32_bf16 v[44:47], v[132:135], v[176:179], v[44:47]\n"
    "ds_read_b64 v[122:123], v240 offset:1096\n"
    "ds_read_b128 v[196:199], v241 offset:1088\n"
    "v_mfma_f32_16x16x32_bf16 v[48:51], v[132:135], v[180:183], v[48:51]\n"
    "ds_read_b64 v[116:117], v240 offset:576\n"
    "v_mfma_f32_16x16x32_bf16 v[52:55], v[132:135], v[184:187], v[52:55]\n"
    "s_waitcnt lgkmcnt(13)\n"
    "ds_read_b64 v[118:119], v240 offset:584\n"
    "ds_read_b128 v[200:203], v241 offset:576\n"
    "v_mfma_f32_16x16x32_bf16 v[40:43], v[128:131], v[176:179], v[40:43]\n"
    "ds_read_b64 v[112:113], v240 offset:64\n"
    "v_mfma_f32_16x16x32_bf16 v[44:47], v[128:131], v[180:183], v[44:47]\n"
    "s_add_u32 m0, s93, 36864\n"
    "s_mov_b32 s68, 1277952\n"
    "buffer_load_dwordx4 v76, %[srdm], s68 offen lds\n"
    "v_mfma_f32_16x16x32_bf16 v[48:51], v[128:131], v[184:187], v[48:51]\n"
    "s_waitcnt vmcnt(9) lgkmcnt(10)\n"
    "s_barrier\n"
    "v_mfma_f32_16x16x32_bf16 v[52:55], v[128:131], v[188:191], v[52:55]\n"
    "s_waitcnt lgkmcnt(8)\n"
    "ds_read_b64_tr_b16 a[120:121], v6 offset:0\n"
    "ds_read_b64_tr_b16 a[122:123], v6 offset:768\n"
    "v_mfma_f32_16x16x32_bf16 v[40:43], v[124:127], v[180:183], v[40:43]\n"
    "ds_read_b64_tr_b16 a[124:125], v6 offset:6144\n"
    "ds_read_b64_tr_b16 a[126:127], v6 offset:6912\n"
    "v_mfma_f32_16x16x32_bf16 v[44:47], v[124:127], v[184:187], v[44:47]\n"
    "ds_read_b64_tr_b16 a[128:129], v7 offset:0\n"
    "ds_read_b64_tr_b16 a[130:131], v7 offset:768\n"
    "v_mfma_f32_16x16x32_bf16 v[48:51], v[124:127], v[188:191], v[48:51]\n"
    "s_waitcnt lgkmcnt(13)\n"
    "buffer_load_dwordx4 v77, %[srdm], s68 offen offset:1024 lds\n"
    "v_mfma_f32_16x16x32_bf16 v[52:55], v[124:127], v[192:195], v[52:55]\n"
    "s_waitcnt lgkmcnt(11)\n"
    "ds_read_b64 v[114:115], v240 offset:72\n"
    "ds_read_b128 v[204:207], v241 offset:64\n"
    "v_mfma_f32_16x16x32_bf16 v[40:43], v[120:123], v[184:187], v[40:43]\n"
    "ds_read_b64 v[108:109], v240 offset:3712\n"
    "v_mfma_f32_16x16x32_bf16 v[44:47], v[120:123], v[188:191], v[44:47]\n"
    "ds_read_b64 v[110:111], v240 offset:3720\n"
    "ds_read_b128 v[144:147], v241 offset:3712\n"
    "v_mfma_f32_16x16x32_bf16 v[48:51], v[120:123], v[192:195], v[48:51]\n"
    "s_waitcnt lgkmcnt(15)\n"
    "ds_read_b64 v[104:105], v240 offset:3200\n"
    "v_mfma_f32_16x16x32_bf16 v[52:55], v[120:123], v[196:199], v[52:55]\n"
    "s_waitcnt lgkmcnt(14)\n"
    "buffer_load_dwordx4 v247, %[srdm], s68 offen offset:2048 lds\n"
    "v_mfma_f32_16x16x32_bf16 v[40:43], v[116:119], v[188:191], v[40:43]\n"
    "ds_read_b64 v[106:107], v240 offset:3208\n"
    "ds_read_b128 v[148:151], v241 offset:3200\n"
    "v_mfma_f32_16x16x32_bf16 v[44:47], v[116:119], v[192:195], v[44:47]\n"
    "ds_read_b64 v[100:101], v240 offset:2688\n"
    "v_mfma_f32_16x16x32_bf16 v[48:51], v[116:119], v[196:199], v[48:51]\n"
    "s_waitcnt lgkmcnt(15)\n"
    "ds_read_b64 v[102:103], v240 offset:2696\n"
    "ds_read_b128 v[152:155], v241 offset:2688\n"
    "v_mfma_f32_16x16x32_bf16 v[52:55], v[116:119], v[200:203], v[52:55]\n"
    "s_waitcnt lgkmcnt(10)\n"
    "ds_read_b64 v[96:97], v240 offset:2176\n"
    "v_mfma_f32_16x16x32_bf16 v[40:43], v[112:115], v[192:195], v[40:43]\n"
    "s_add_u32 m0, s93, 49152\n"
    "s_mov_b32 s68, 1376256\n"
    "buffer_load_dwordx4 v76, %[srdm], s68 offen lds\n"
    "v_mfma_f32_16x16x32_bf16 v[44:47], v[112:115], v[196:199], v[44:47]\n"
    "ds_read_b64 v[98:99], v240 offset:2184\n"
    "ds_read_b128 v[156:159], v241 offset:2176\n"
    "v_mfma_f32_16x16x32_bf16 v[48:51], v[112:115], v[200:203], v[48:51]\n"
    "s_waitcnt lgkmcnt(12)\n"
    "ds_read_b64 v[92:93], v240 offset:1664\n"
    "v_mfma_f32_16x16x32_bf16 v[52:55], v[112:115], v[204:207], v[52:55]\n"
    "s_waitcnt lgkmcnt(10)\n"
    "ds_read_b64 v[94:95], v240 offset:1672\n"
    "ds_read_b128 v[160:163], v241 offset:1664\n"
    "v_mfma_f32_16x16x32_bf16 v[52:55], v[108:111], v[144:147], v[52:55]\n"
    "s_waitcnt lgkmcnt(10)\n"
    "ds_read_b64 v[88:89], v240 offset:1152\n"
    "v_mfma_f32_16x16x32_bf16 v[48:51], v[104:107], v[144:147], v[48:51]\n"
    "s_waitcnt lgkmcnt(10)\n"
    "buffer_load_dwordx4 v77, %[srdm], s68 offen offset:1024 lds\n"
    "v_mfma_f32_16x16x32_bf16 v[52:55], v[104:107], v[148:151], v[52:55]\n"
    "s_waitcnt lgkmcnt(8)\n"
    "ds_read_b64 v[90:91], v240 offset:1160\n"
    "ds_read_b128 v[164:167], v241 offset:1152\n"
    "v_mfma_f32_16x16x32_bf16 v[44:47], v[100:103], v[144:147], v[44:47]\n"
    "ds_read_b64 v[84:85], v240 offset:640\n"
    "v_mfma_f32_16x16x32_bf16 v[48:51], v[100:103], v[148:151], v[48:51]\n"
    "s_waitcnt lgkmcnt(10)\n"
    "ds_read_b64 v[86:87], v240 offset:648\n"
    "ds_read_b128 v[168:171], v241 offset:640\n"
    "v_mfma_f32_16x16x32_bf16 v[52:55], v[100:103], v[152:155], v[52:55]\n"
    "s_waitcnt lgkmcnt(10)\n"
    "ds_read_b64 v[80:81], v240 offset:128\n"
    "v_mfma_f32_16x16x32_bf16 v[40:43], v[96:99], v[144:147], v[40:43]\n"
    "buffer_load_dwordx4 v247, %[srdm], s68 offen offset:2048 lds\n"
    "ds_read_b64 v[82:83], v240 offset:136\n"
    "v_mfma_f32_16x16x32_bf16 v[44:47], v[96:99], v[148:151], v[44:47]\n"
    "ds_read_b128 v[172:175], v241 offset:128\n"
    "v_mfma_f32_16x16x32_bf16 v[48:51], v[96:99], v[152:155], v[48:51]\n"
    "s_waitcnt lgkmcnt(12)\n"
    "ds_read_b64 v[140:141], v240 offset:3776\n"
    "ds_read_b64 v[142:143], v240 offset:3784\n"
    "v_mfma_f32_16x16x32_bf16 v[52:55], v[96:99], v[156:159], v[52:55]\n"
    "s_waitcnt lgkmcnt(12)\n"
    "ds_read_b128 v[176:179], v241 offset:3776\n"
    "v_mfma_f32_16x16x32_bf16 v[40:43], v[92:95], v[148:151], v[40:43]\n"
    "ds_read_b64 v[136:137], v240 offset:3264\n"
    "ds_read_b64 v[138:139], v240 offset:3272\n"
    "v_mfma_f32_16x16x32_bf16 v[44:47], v[92:95], v[152:155], v[44:47]\n"
    "ds_read_b128 v[180:183], v241 offset:3264\n"
    "v_mfma_f32_16x16x32_bf16 v[48:51], v[92:95], v[156:159], v[48:51]\n"
    "s_waitcnt lgkmcnt(15)\n"
    "ds_read_b64 v[132:133], v240 offset:2752\n"
    "ds_read_b64 v[134:135], v240 offset:2760\n"
    "v_mfma_f32_16x16x32_bf16 v[52:55], v[92:95], v[160:163], v[52:55]\n"
    "s_waitcnt lgkmcnt(15)\n"
    "ds_read_b128 v[184:187], v241 offset:2752\n"
    "v_mfma_f32_16x16x32_bf16 v[40:43], v[88:91], v[152:155], v[40:43]\n"
    "ds_read_b64 v[128:129], v240 offset:2240\n"
    "ds_read_b64 v[130:131], v240 offset:2248\n"
    "v_mfma_f32_16x16x32_bf16 v[44:47], v[88:91], v[156:159], v[44:47]\n"
    "s_waitcnt vmcnt(10)\n"
    "s_barrier\n"
    "v_mfma_f32_16x16x32_bf16 v[48:51], v[88:91], v[160:163], v[48:51]\n"
    "s_waitcnt lgkmcnt(15)\n"
    "s_mov_b64 exec, s[94:95]\n"
    "buffer_store_dword v2, off, %[srdw], s67 sc1\n"
    "s_mov_b64 exec, -1\n"
    "v_mfma_f32_16x16x32_bf16 v[52:55], v[88:91], v[164:167], v[52:55]\n"
    "s_waitcnt vmcnt(10)\n"
    "s_barrier\n"
    "v_mfma_f32_16x16x32_bf16 v[40:43], v[84:87], v[156:159], v[40:43]\n"
    "ds_read_b64_tr_b16 a[132:133], v6 offset:12288\n"
    "ds_read_b64_tr_b16 a[134:135], v6 offset:13056\n"
    "v_mfma_f32_16x16x32_bf16 v[44:47], v[84:87], v[160:163], v[44:47]\n"
    "ds_read_b64_tr_b16 a[136:137], v6 offset:18432\n"
    "ds_read_b64_tr_b16 a[138:139], v6 offset:19200\n"
    "v_mfma_f32_16x16x32_bf16 v[48:51], v[84:87], v[164:167], v[48:51]\n"
    "s_waitcnt lgkmcnt(15)\n"
    "ds_read_b64_tr_b16 a[140:141], v7 offset:12288\n"
    "ds_read_b64_tr_b16 a[142:143], v7 offset:13056\n"
    "v_mfma_f32_16x16x32_bf16 v[52:55], v[84:87], v[168:171], v[52:55]\n"
    "s_add_u32 m0, s93, 0\n"
    "s_mov_b32 s68, 1474560\n"
    "buffer_load_dwordx4 v76, %[srdm], s68 offen lds\n"
    "v_mfma_f32_16x16x32_bf16 v[40:43], v[80:83], v[160:163], v[40:43]\n"
    "ds_read_b128 v[188:191], v241 offset:2240\n"
    "v_mfma_f32_16x16x32_bf16 v[44:47], v[80:83], v[164:167], v[44:47]\n"
    "buffer_load_dwordx4 v77, %[srdm], s68 offen offset:1024 lds\n"
    "ds_read_b64 v[124:125], v240 offset:1728\n"
    "v_mfma_f32_16x16x32_bf16 v[48:51], v[80:83], v[168:171], v[48:51]\n"
    "ds_read_b64 v[126:127], v240 offset:1736\n"
    "ds_read_b128 v[192:195], v241 offset:1728\n"
    "v_mfma_f32_16x16x32_bf16 v[52:55], v[80:83], v[172:175], v[52:55]\n"
    "s_waitcnt lgkmcnt(15)\n"
    "ds_read_b64 v[120:121], v240 offset:1216\n"
    "v_mfma_f32_16x16x32_bf16 v[52:55], v[140:143], v[176:179], v[52:55]\n"
    "ds_read_b64 v[122:123], v240 offset:1224\n"
    "ds_read_b128 v[196:199], v241 offset:1216\n"
    "v_mfma_f32_16x16x32_bf16 v[48:51], v[136:139], v[176:179], v[48:51]\n"
    "ds_read_b64 v[116:117], v240 offset:704\n"
    "v_mfma_f32_16x16x32_bf16 v[52:55], v[136:139], v[180:183], v[52:55]\n"
    "s_waitcnt lgkmcnt(15)\n"
    "buffer_load_dwordx4 v247, %[srdm], s68 offen offset:2048 lds\n"
    "v_mfma_f32_16x16x32_bf16 v[44:47], v[132:135], v[176:179], v[44:47]\n"
    "s_add_u32 s79, s47, 8192\n"
    "s_mov_b64 exec, 0xffff\n"
    "buffer_load_dword v79, v1, %[srdw], s79 offen sc1\n"
    "s_mov_b64 exec, -1\n"
    "v_mfma_f32_16x16x32_bf16 v[48:51], v[132:135], v[180:183], v[48:51]\n"
    "s_add_u32 s80, %[q], 6291456\n"
    "s_add_u32 s81, %[w1k], 147456\n"
    "s_add_u32 s82, %[q], 6946816\n"
    "v_mfma_f32_16x16x32_bf16 v[52:55], v[132:135], v[184:187], v[52:55]\n"
    "s_waitcnt lgkmcnt(14)\n"
    "s_add_u32 s83, %[w1k], 151552\n"
    "ds_read_b64 v[118:119], v240 offset:712\n"
    "v_mfma_f32_16x16x32_bf16 v[40:43], v[128:131], v[176:179], v[40:43]\n"
    "s_waitcnt vmcnt(11) lgkmcnt(9)\n"
    "s_barrier\n"
    "v_mfma_f32_16x16x32_bf16 v[44:47], v[128:131], v[180:183], v[44:47]\n"
    "ds_read_b64_tr_b16 a[144:145], v6 offset:24576\n"
    "ds_read_b64_tr_b16 a[146:147], v6 offset:25344\n"
    "v_mfma_f32_16x16x32_bf16 v[48:51], v[128:131], v[184:187], v[48:51]\n"
    "s_waitcnt lgkmcnt(10)\n"
    "ds_read_b64_tr_b16 a[148:149], v6 offset:30720\n"
    "ds_read_b64_tr_b16 a[150:151], v6 offset:31488\n"
    "v_mfma_f32_16x16x32_bf16 v[52:55], v[128:131], v[188:191], v[52:55]\n"
    "s_waitcnt lgkmcnt(10)\n"
    "ds_read_b64_tr_b16 a[152:153], v7 offset:24576\n"
    "ds_read_b64_tr_b16 a[154:155], v7 offset:25344\n"
    "v_mfma_f32_16x16x32_bf16 v[40:43], v[124:127], v[180:183], v[40:43]\n"
    "ds_read_b128 v[200:203], v241 offset:704\n"
    "v_mfma_f32_16x16x32_bf16 v[44:47], v[124:127], v[184:187], v[44:47]\n"
    "ds_read_b64 v[112:113], v240 offset:192\n"
    "ds_read_b64 v[114:115], v240 offset:200\n"
    "v_mfma_f32_16x16x32_bf16 v[48:51], v[124:127], v[188:191], v[48:51]\n"
    "s_waitcnt lgkmcnt(14)\n"
    "ds_read_b128 v[204:207], v241 offset:192\n"
    "v_mfma_f32_16x16x32_bf16 v[52:55], v[124:127], v[192:195], v[52:55]\n"
    "s_waitcnt lgkmcnt(13)\n"
    "s_add_u32 m0, s93, 12288\n"
    "s_mov_b32 s68, 1572864\n"
    "buffer_load_dwordx4 v76, %[srdm], s68 offen lds\n"
    "v_mfma_f32_16x16x32_bf16 v[40:43], v[120:123], v[184:187], v[40:43]\n"
    "ds_read_b64 v[108:109], v240 offset:3840\n"
    "ds_read_b64 v[110:111], v240 offset:3848\n"
    "v_mfma_f32_16x16x32_bf16 v[44:47], v[120:123], v[188:191], v[44:47]\n"
    "ds_read_b128 v[144:147], v241 offset:3840\n"
    "v_mfma_f32_16x16x32_bf16 v[48:51], v[120:123], v[192:195], v[48:51]\n"
    "s_waitcnt lgkmcnt(15)\n"
    "ds_read_b64 v[104:105], v240 offset:3328\n"
    "ds_read_b64 v[106:107], v240 offset:3336\n"
    "v_mfma_f32_16x16x32_bf16 v[52:55], v[120:123], v[196:199], v[52:55]\n"
    "s_waitcnt lgkmcnt(15)\n"
    "s_waitcnt lgkmcnt(9) vmcnt(9)\n"
    "s_barrier\n"
    "v_mfma_f32_16x16x32_bf16 v[40:43], v[116:119], v[188:191], v[40:43]\n"
    "buffer_load_dwordx4 v77, %[srdm], s68 offen offset:1024 lds\n"
    "ds_read_b64_tr_b16 a[156:157], v6 offset:36864\n"
    "v_mfma_f32_16x16x32_bf16 v[44:47], v[116:119], v[192:195], v[44:47]\n"
    "ds_read_b64_tr_b16 a[158:159], v6 offset:37632\n"
    "ds_read_b64_tr_b16 a[160:161], v6 offset:43008\n"
    "v_mfma_f32_16x16x32_bf16 v[48:51], v[116:119], v[196:199], v[48:51]\n"
    "s_waitcnt lgkmcnt(11)\n"
    "ds_read_b64_tr_b16 a[162:163], v6 offset:43776\n"
    "ds_read_b64_tr_b16 a[164:165], v7 offset:36864\n"
    "v_mfma_f32_16x16x32_bf16 v[52:55], v[116:119], v[200:203], v[52:55]\n"
    "s_waitcnt lgkmcnt(11)\n"
    "ds_read_b64_tr_b16 a[166:167], v7 offset:37632\n"
    "ds_read_b128 v[148:151], v241 offset:3328\n"
    "v_mfma_f32_16x16x32_bf16 v[40:43], v[112:115], v[192:195], v[40:43]\n"
    "ds_read_b64 v[100:101], v240 offset:2816\n"
    "v_mfma_f32_16x16x32_bf16 v[44:47], v[112:115], v[196:199], v[44:47]\n"
    "buffer_load_dwordx4 v247, %[srdm], s68 offen offset:2048 lds\n"
    "ds_read_b64 v[102:103], v240 offset:2824\n"
    "v_mfma_f32_16x16x32_bf16 v[48:51], v[112:115], v[200:203], v[48:51]\n"
    "s_waitcnt lgkmcnt(14)\n"
    "ds_read_b128 v[152:155], v241 offset:2816\n"
    "v_mfma_f32_16x16x32_bf16 v[52:55], v[112:115], v[204:207], v[52:55]\n"
    "s_waitcnt lgkmcnt(12)\n"
    "ds_read_b64 v[96:97], v240 offset:2304\n"
    "ds_read_b64 v[98:99], v240 offset:2312\n"
    "v_mfma_f32_16x16x32_bf16 v[52:55], v[108:111], v[144:147], v[52:55]\n"
    "s_waitcnt lgkmcnt(12)\n"
    "ds_read_b128 v[156:159], v241 offset:2304\n"
    "v_mfma_f32_16x16x32_bf16 v[48:51], v[104:107], v[144:147], v[48:51]\n"
    "s_waitcnt lgkmcnt(6)\n"
    "ds_read_b64 v[92:93], v240 offset:1792\n"
    "ds_read_b64 v[94:95], v240 offset:1800\n"
    "v_mfma_f32_16x16x32_bf16 v[52:55], v[104:107], v[148:151], v[52:55]\n"
    "s_waitcnt lgkmcnt(6)\n"
    "s_add_u32 m0, s93, 24576\n"
    "s_mov_b32 s68, 1671168\n"
    "buffer_load_dwordx4 v76, %[srdm], s68 offen lds\n"
    "v_mfma_f32_16x16x32_bf16 v[44:47], v[100:103], v[144:147], v[44:47]\n"
    "s_waitcnt vmcnt(9)\n"
    "s_barrier\n"
    "v_mfma_f32_16x16x32_bf16 v[48:51], v[100:103], v[148:151], v[48:51]\n"
    "s_waitcnt lgkmcnt(5)\n"
    "kx_a1:\n"
    "s_cbranch_execz kx_a5\n"
    "ds_read_b64_tr_b16 a[168:169], v6 offset:49152\n"
    "ds_read_b64_tr_b16 a[170:171], v6 offset:49920\n"
    "v_mfma_f32_16x16x32_bf16 v[52:55], v[100:103], v[152:155], v[52:55]\n"
    "s_waitcnt lgkmcnt(5)\n"
    "buffer_load_dwordx4 v77, %[srdm], s68 offen offset:1024 lds\n"
    "v_mfma_f32_16x16x32_bf16 v[40:43], v[96:99], v[144:147], v[40:43]\n"
    "ds_read_b64_tr_b16 a[172:173], v6 offset:55296\n"
    "ds_read_b64_tr_b16 a[174:175], v6 offset:56064\n"
    "v_mfma_f32_16x16x32_bf16 v[44:47], v[96:99], v[148:151], v[44:47]\n"
    "ds_read_b64_tr_b16 a[176:177], v7 offset:49152\n"
    "ds_read_b64_tr_b16 a[178:179], v7 offset:49920\n"
    "v_mfma_f32_16x16x32_bf16 v[48:51], v[96:99], v[152:155], v[48:51]\n"
    "s_waitcnt lgkmcnt(8)\n"
    "ds_read_b128 v[160:163], v241 offset:1792\n"
    "v_mfma_f32_16x16x32_bf16 v[52:55], v[96:99], v[156:159], v[52:55]\n"
    "s_waitcnt lgkmcnt(7)\n"
    "ds_read_b64 v[88:89], v240 offset:1280\n"
    "ds_read_b64 v[90:91], v240 offset:1288\n"
    "v_mfma_f32_16x16x32_bf16 v[40:43], v[92:95], v[148:151], v[40:43]\n"
    "buffer_load_dwordx4 v247, %[srdm], s68 offen offset:2048 lds\n"
    "ds_read_b128 v[164:167], v241 offset:1280\n"
    "v_mfma_f32_16x16x32_bf16 v[44:47], v[92:95], v[152:155], v[44:47]\n"
    "ds_read_b64 v[84:85], v240 offset:768\n"
    "ds_read_b64 v[86:87], v240 offset:776\n"
    "v_mfma_f32_16x16x32_bf16 v[48:51], v[92:95], v[156:159], v[48:51]\n"
    "s_waitcnt lgkmcnt(5)\n"
    "ds_read_b128 v[168:171], v241 offset:768\n"
    "s_add_u32 m0, s93, 36864\n"
    "s_mov_b32 s68, 1769472\n"
    "buffer_load_dwordx4 v76, %[srdm], s68 offen lds\n"
    "buffer_load_dwordx4 v77, %[srdm], s68 offen offset:1024 lds\n"
    "buffer_load_dwordx4 v247, %[srdm], s68 offen offset:2048 lds\n"
    "ds_read_b64 v[80:81], v240 offset:256\n"
    "ds_read_b64 v[82:83], v240 offset:264\n"
    "ds_read_b128 v[172:175], v241 offset:256\n"
    "ds_read_b64 v[140:141], v240 offset:3904\n"
    "v_mfma_f32_16x16x32_bf16 v[52:55], v[92:95], v[160:163], v[52:55]\n"
    "s_waitcnt lgkmcnt(8)\n"
    "v_mfma_f32_16x16x32_bf16 v[40:43], v[88:91], v[152:155], v[40:43]\n"
    "s_mov_b32 exec_hi, 0xff\n"
    "buffer_load_dword v246, v1, %[srdw], s78 offen sc1\n"
    "s_mov_b32 exec_hi, -1\n"
    "v_mfma_f32_16x16x32_bf16 v[44:47], v[88:91], v[156:159], v[44:47]\n"
    "ds_read_b64 v[142:143], v240 offset:3912\n"
    "ds_read_b128 v[176:179], v241 offset:3904\n"
    "v_mfma_f32_16x16x32_bf16 v[48:51], v[88:91], v[160:163], v[48:51]\n"
    "s_waitcnt lgkmcnt(9)\n"
    "ds_read_b64 v[136:137], v240 offset:3392\n"
    "v_mfma_f32_16x16x32_bf16 v[52:55], v[88:91], v[164:167], v[52:55]\n"
    "s_waitcnt lgkmcnt(8)\n"
    "ds_read_b64 v[138:139], v240 offset:3400\n"
    "ds_read_b128 v[180:183], v241 offset:3392\n"
    "v_mfma_f32_16x16x32_bf16 v[40:43], v[84:87], v[156:159], v[40:43]\n"
    "ds_read_b64 v[132:133], v240 offset:2880\n"
    "v_mfma_f32_16x16x32_bf16 v[44:47], v[84:87], v[160:163], v[44:47]\n"
    "ds_read_b64 v[134:135], v240 offset:2888\n"
    "ds_read_b128 v[184:187], v241 offset:2880\n"
    "v_mfma_f32_16x16x32_bf16 v[48:51], v[84:87], v[164:167], v[48:51]\n"
    "s_waitcnt lgkmcnt(12)\n"
    "s_waitcnt vmcnt(11)\n"
    "s_barrier\n"
    "v_mfma_f32_16x16x32_bf16 v[52:55], v[84:87], v[168:171], v[52:55]\n"
    "s_waitcnt lgkmcnt(10)\n"
    "ds_read_b64_tr_b16 a[180:181], v6 offset:0\n"
    "ds_read_b64_tr_b16 a[182:183], v6 offset:768\n"
    "v_mfma_f32_16x16x32_bf16 v[40:43], v[80:83], v[160:163], v[40:43]\n"
    "ds_read_b64_tr_b16 a[184:185], v6 offset:6144\n"
    "ds_read_b64_tr_b16 a[186:187], v6 offset:6912\n"
    "v_mfma_f32_16x16x32_bf16 v[44:47], v[80:83], v[164:167], v[44:47]\n"
    "ds_read_b64_tr_b16 a[188:189], v7 offset:0\n"
    "ds_read_b64_tr_b16 a[190:191], v7 offset:768\n"
    "v_mfma_f32_16x16x32_bf16 v[48:51], v[80:83], v[168:171], v[48:51]\n"
    "s_waitcnt lgkmcnt(15)\n"
    "ds_read_b64 v[128:129], v240 offset:2368\n"
    "ds_read_b64 v[130:131], v240 offset:2376\n"
    "v_mfma_f32_16x16x32_bf16 v[52:55], v[80:83], v[172:175], v[52:55]\n"
    "s_waitcnt lgkmcnt(14)\n"
    "ds_read_b128 v[188:191], v241 offset:2368\n"
    "v_mfma_f32_16x16x32_bf16 v[52:55], v[140:143], v[176:179], v[52:55]\n"
    "s_waitcnt lgkmcnt(13)\n"
    "ds_read_b64 v[124:125], v240 offset:1856\n"
    "ds_read_b64 v[126:127], v240 offset:1864\n"
    "v_mfma_f32_16x16x32_bf16 v[48:51], v[136:139], v[176:179], v[48:51]\n"
    "s_waitcnt lgkmcnt(14)\n"
    "ds_read_b128 v[192:195], v241 offset:1856\n"
    "v_mfma_f32_16x16x32_bf16 v[52:55], v[136:139], v[180:183], v[52:55]\n"
    "s_waitcnt lgkmcnt(13) vmcnt(10)\n"
    "v_cmp_ne_u32 vcc, %[tag], v79\n"
    "s_nop 0\n"
    "s_and_b32 vcc_lo, vcc_lo, 0xffff\n"
    "s_cbranch_scc1 L7_%=\n"
    "L8_%=:\n"
    "v_mfma_f32_16x16x32_bf16 v[44:47], v[132:135], v[176:179], v[44:47]\n"
    "ds_read_b64 v[120:121], v240 offset:1344\n"
    "ds_read_b64 v[122:123], v240 offset:1352\n"
    "v_mfma_f32_16x16x32_bf16 v[48:51], v[132:135], v[180:183], v[48:51]\n"
    "s_waitcnt lgkmcnt(14)\n"
    "s_waitcnt vmcnt(7) lgkmcnt(8)\n"
    "s_barrier\n"
    "v_mfma_f32_16x16x32_bf16 v[52:55], v[132:135], v[184:187], v[52:55]\n"
    "s_waitcnt lgkmcnt(6)\n"
    "ds_read_b64_tr_b16 a[192:193], v6 offset:12288\n"
    "ds_read_b64_tr_b16 a[194:195], v6 offset:13056\n"
    "v_mfma_f32_16x16x32_bf16 v[40:43], v[128:131], v[176:179], v[40:43]\n"
    "ds_read_b64_tr_b16 a[196:197], v6 offset:18432\n"
    "ds_read_b64_tr_b16 a[198:199], v6 offset:19200\n"
    "v_mfma_f32_16x16x32_bf16 v[44:47], v[128:131], v[180:183], v[44:47]\n"
    "ds_read_b64_tr_b16 a[200:201], v7 offset:12288\n"
    "ds_read_b64_tr_b16 a[202:203], v7 offset:13056\n"
    "v_mfma_f32_16x16x32_bf16 v[48:51], v[128:131], v[184:187], v[48:51]\n"
    "s_waitcnt lgkmcnt(11)\n"
    "ds_read_b128 v[196:199], v241 offset:1344\n"
    "v_mfma_f32_16x16x32_bf16 v[52:55], v[128:131], v[188:191], v[52:55]\n"
    "s_waitcnt lgkmcnt(10)\n"
    "ds_read_b64 v[116:117], v240 offset:832\n"
    "ds_read_b64 v[118:119], v240 offset:840\n"
    "v_mfma_f32_16x16x32_bf16 v[40:43], v[124:127], v[180:183], v[40:43]\n"
    "ds_read_b128 v[200:203], v241 offset:832\n"
    "v_mfma_f32_16x16x32_bf16 v[44:47], v[124:127], v[184:187], v[44:47]\n"
    "ds_read_b64 v[112:113], v240 offset:320\n"
    "ds_read_b64 v[114:115], v240 offset:328\n"
    "v_mfma_f32_16x16x32_bf16 v[48:51], v[124:127], v[188:191], v[48:51]\n"
    "s_waitcnt lgkmcnt(14)\n"
    "ds_read_b128 v[204:207], v241 offset:320\n"
    "v_mfma_f32_16x16x32_bf16 v[52:55], v[124:127], v[192:195], v[52:55]\n"
    "s_waitcnt lgkmcnt(13)\n"
    "ds_read_b64 v[108:109], v240 offset:3968\n"
    "ds_read_b64 v[110:111], v240 offset:3976\n"
    "v_mfma_f32_16x16x32_bf16 v[40:43], v[120:123], v[184:187], v[40:43]\n"
    "ds_read_b128 v[144:147], v241 offset:3968\n"
    "v_mfma_f32_16x16x32_bf16 v[44:47], v[120:123], v[188:191], v[44:47]\n"
    "s_waitcnt vmcnt(4) lgkmcnt(10)\n"
    "s_barrier\n"
    "v_mfma_f32_16x16x32_bf16 v[48:51], v[120:123], v[192:195], v[48:51]\n"
    "s_waitcnt lgkmcnt(9)\n"
    "buffer_load_dwordx4 v[224:227], v244, %[srdw], s80 offen sc1\n"
    "v_mfma_f32_16x16x32_bf16 v[52:55], v[120:123], v[196:199], v[52:55]\n"
    "s_waitcnt lgkmcnt(7)\n"
    "ds_read_b64_tr_b16 a[204:205], v6 offset:24576\n"
    "ds_read_b64_tr_b16 a[206:207], v6 offset:25344\n"
    "v_mfma_f32_16x16x32_bf16 v[40:43], v[116:119], v[188:191], v[40:43]\n"
    "ds_read_b64_tr_b16 a[208:209], v6 offset:30720\n"
    "ds_read_b64_tr_b16 a[210:211], v6 offset:31488\n"
    "v_mfma_f32_16x16x32_bf16 v[44:47], v[116:119], v[192:195], v[44:47]\n"
    "ds_read_b64_tr_b16 a[212:213], v7 offset:24576\n"
    "ds_read_b64_tr_b16 a[214:215], v7 offset:25344\n"
    "v_mfma_f32_16x16x32_bf16 v[48:51], v[116:119], v[196:199], v[48:51]\n"
    "s_waitcnt lgkmcnt(12)\n"
    "ds_read_b64 v[104:105], v240 offset:3456\n"
    "ds_read_b64 v[106:107], v240 offset:3464\n"
    "v_mfma_f32_16x16x32_bf16 v[52:55], v[116:119], v[200:203], v[52:55]\n"
    "s_waitcnt lgkmcnt(12)\n"
    "buffer_load_dwordx4 v[228:231], v0, %[srdw], s81 offen sc1\n"
    "v_mfma_f32_16x16x32_bf16 v[40:43], v[112:115], v[192:195], v[40:43]\n"
    "ds_read_b128 v[148:151], v241 offset:3456\n"
    "v_mfma_f32_16x16x32_bf16 v[44:47], v[112:115], v[196:199], v[44:47]\n"
    "ds_read_b64 v[100:101], v240 offset:2944\n"
    "ds_read_b64 v[102:103], v240 offset:2952\n"
    "v_mfma_f32_16x16x32_bf16 v[48:51], v[112:115], v[200:203], v[48:51]\n"
    "s_waitcnt lgkmcnt(14)\n"
    "ds_read_b128 v[152:155], v241 offset:2944\n"
    "v_mfma_f32_16x16x32_bf16 v[52:55], v[112:115], v[204:207], v[52:55]\n"
    "s_waitcnt lgkmcnt(12)\n"
    "ds_read_b64 v[96:97], v240 offset:2432\n"
    "ds_read_b64 v[98:99], v240 offset:2440\n"
    "v_mfma_f32_16x16x32_bf16 v[52:55], v[108:111], v[144:147], v[52:55]\n"
    "s_waitcnt lgkmcnt(6)\n"
    "buffer_load_dwordx4 v[232:235], v244, %[srdw], s82 offen sc1\n"
    "v_mfma_f32_16x16x32_bf16 v[48:51], v[104:107], v[144:147], v[48:51]\n"
    "s_waitcnt lgkmcnt(5)\n"
    "ds_read_b128 v[156:159], v241 offset:2432\n"
    "v_mfma_f32_16x16x32_bf16 v[52:55], v[104:107], v[148:151], v[52:55]\n"
    "s_waitcnt lgkmcnt(4)\n"
    "s_waitcnt vmcnt(4)\n"
    "s_barrier\n"
    "v_mfma_f32_16x16x32_bf16 v[44:47], v[100:103], v[144:147], v[44:47]\n"
    "buffer_load_dwordx4 v[236:239], v0, %[srdw], s83 offen sc1\n"
    "ds_read_b64_tr_b16 a[216:217], v6 offset:36864\n"
    "v_mfma_f32_16x16x32_bf16 v[48:51], v[100:103], v[148:151], v[48:51]\n"
    "s_waitcnt lgkmcnt(4)\n"
    "ds_read_b64_tr_b16 a[218:219], v6 offset:37632\n"
    "ds_read_b64_tr_b16 a[220:221], v6 offset:43008\n"
    "v_mfma_f32_16x16x32_bf16 v[52:55], v[100:103], v[152:155], v[52:55]\n"
    "s_waitcnt lgkmcnt(4)\n"
    "ds_read_b64_tr_b16 a[222:223], v6 offset:43776\n"
    "ds_read_b64_tr_b16 a[224:225], v7 offset:36864\n"
    "v_mfma_f32_16x16x32_bf16 v[40:43], v[96:99], v[144:147], v[40:43]\n"
    "ds_read_b64_tr_b16 a[226:227], v7 offset:37632\n"
    "ds_read_b64 v[92:93], v240 offset:1920\n"
    "v_mfma_f32_16x16x32_bf16 v[44:47], v[96:99], v[148:151], v[44:47]\n"
    "ds_read_b64 v[94:95], v240 offset:1928\n"
    "ds_read_b128 v[160:163], v241 offset:1920\n"
    "v_mfma_f32_16x16x32_bf16 v[48:51], v[96:99], v[152:155], v[48:51]\n"
    "s_waitcnt lgkmcnt(9)\n"
    "s_add_u32 m0, s93, 49152\n"
    "s_mov_b32 s68, 1867776\n"
    "buffer_load_dwordx4 v76, %[srdm], s68 offen lds\n"
    "v_mfma_f32_16x16x32_bf16 v[52:55], v[96:99], v[156:159], v[52:55]\n"
    "s_waitcnt lgkmcnt(1)\n"
    "ds_read_b64 v[88:89], v240 offset:1408\n"
    "ds_read_b64 v[90:91], v240 offset:1416\n"
    "buffer_load_dwordx4 v77, %[srdm], s68 offen offset:1024 lds\n"
    "buffer_load_dwordx4 v247, %[srdm], s68 offen offset:2048 lds\n"
    "ds_read_b128 v[164:167], v241 offset:1408\n"
    "ds_read_b64 v[84:85], v240 offset:896\n"
    "ds_read_b64 v[86:87], v240 offset:904\n"
    "ds_read_b128 v[168:171], v241 offset:896\n"
    "ds_read_b64 v[80:81], v240 offset:384\n"
    "ds_read_b64 v[82:83], v240 offset:392\n"
    "ds_read_b128 v[172:175], v241 offset:384\n"
    "ds_read_b64 v[140:141], v240 offset:4032\n"
    "ds_read_b64 v[142:143], v240 offset:4040\n"
    "ds_read_b128 v[176:179], v241 offset:4032\n"
    "ds_read_b64 v[136:137], v240 offset:3520\n"
    "ds_read_b64 v[138:139], v240 offset:3528\n"
    "ds_read_b128 v[180:183], v241 offset:3520\n"
    "ds_read_b64 v[132:133], v240 offset:3008\n"
    "v_mfma_f32_16x16x32_bf16 v[40:43], v[92:95], v[148:151], v[40:43]\n"
    "v_mfma_f32_16x16x32_bf16 v[44:47], v[92:95], v[152:155], v[44:47]\n"
    "v_mfma_f32_16x16x32_bf16 v[48:51], v[92:95], v[156:159], v[48:51]\n"
    "s_waitcnt lgkmcnt(15)\n"
    "v_mfma_f32_16x16x32_bf16 v[52:55], v[92:95], v[160:163], v[52:55]\n"
    "s_waitcnt lgkmcnt(14)\n"
    "v_mfma_f32_16x16x32_bf16 v[40:43], v[88:91], v[152:155], v[40:43]\n"
    "v_mfma_f32_16x16x32_bf16 v[44:47], v[88:91], v[156:159], v[44:47]\n"
    "ds_read_b64 v[134:135], v240 offset:3016\n"
    "v_mfma_f32_16x16x32_bf16 v[48:51], v[88:91], v[160:163], v[48:51]\n"
    "s_waitcnt lgkmcnt(14)\n"
    "ds_read_b128 v[184:187], v241 offset:3008\n"
    "v_mfma_f32_16x16x32_bf16 v[52:55], v[88:91], v[164:167], v[52:55]\n"
    "s_waitcnt lgkmcnt(13)\n"
    "ds_read_b64 v[128:129], v240 offset:2496\n"
    "ds_read_b64 v[130:131], v240 offset:2504\n"
    "v_mfma_f32_16x16x32_bf16 v[40:43], v[84:87], v[156:159], v[40:43]\n"
    "ds_read_b128 v[188:191], v241 offset:2496\n"
    "v_mfma_f32_16x16x32_bf16 v[44:47], v[84:87], v[160:163], v[44:47]\n"
    "ds_read_b64 v[124:125], v240 offset:1984\n"
    "ds_read_b64 v[126:127], v240 offset:1992\n"
    "v_mfma_f32_16x16x32_bf16 v[48:51], v[84:87], v[164:167], v[48:51]\n"
    "s_waitcnt lgkmcnt(15)\n"
    "ds_read_b128 v[192:195], v241 offset:1984\n"
    "v_mfma_f32_16x16x32_bf16 v[52:55], v[84:87], v[168:171], v[52:55]\n"
    "ds_read_b64 v[120:121], v240 offset:1472\n"
    "ds_read_b64 v[122:123], v240 offset:1480\n"
    "v_mfma_f32_16x16x32_bf16 v[40:43], v[80:83], v[160:163], v[40:43]\n"
    "ds_read_b128 v[196:199], v241 offset:1472\n"
    "v_mfma_f32_16x16x32_bf16 v[44:47], v[80:83], v[164:167], v[44:47]\n"
    "ds_read_b64 v[116:117], v240 offset:960\n"
    "ds_read_b64 v[118:119], v240 offset:968\n"
    "v_mfma_f32_16x16x32_bf16 v[48:51], v[80:83], v[168:171], v[48:51]\n"
    "s_waitcnt lgkmcnt(15)\n"
    "ds_read_b128 v[200:203], v241 offset:960\n"
    "v_mfma_f32_16x16x32_bf16 v[52:55], v[80:83], v[172:175], v[52:55]\n"
    "ds_read_b64 v[112:113], v240 offset:448\n"
    "ds_read_b64 v[114:115], v240 offset:456\n"
    "v_mfma_f32_16x16x32_bf16 v[52:55], v[140:143], v[176:179], v[52:55]\n"
    "ds_read_b128 v[204:207], v241 offset:448\n"
    "v_mfma_f32_16x16x32_bf16 v[48:51], v[136:139], v[176:179], v[48:51]\n"
    "s_waitcnt lgkmcnt(15)\n"
    "v_mfma_f32_16x16x32_bf16 v[52:55], v[136:139], v[180:183], v[52:55]\n"
    "v_mfma_f32_16x16x32_bf16 v[44:47], v[132:135], v[176:179], v[44:47]\n"
    "v_mfma_f32_16x16x32_bf16 v[48:51], v[132:135], v[180:183], v[48:51]\n"
    "v_mfma_f32_16x16x32_bf16 v[52:55], v[132:135], v[184:187], v[52:55]\n"
    "s_waitcnt lgkmcnt(13)\n"
    "v_mfma_f32_16x16x32_bf16 v[40:43], v[128:131], v[176:179], v[40:43]\n"
    "v_mfma_f32_16x16x32_bf16 v[44:47], v[128:131], v[180:183], v[44:47]\n"
    "v_mfma_f32_16x16x32_bf16 v[48:51], v[128:131], v[184:187], v[48:51]\n"
    "s_waitcnt lgkmcnt(12)\n"
    "v_mfma_f32_16x16x32_bf16 v[52:55], v[128:131], v[188:191], v[52:55]\n"
    "s_waitcnt lgkmcnt(10)\n"
    "v_mfma_f32_16x16x32_bf16 v[40:43], v[124:127], v[180:183], v[40:43]\n"
    "v_mfma_f32_16x16x32_bf16 v[44:47], v[124:127], v[184:187], v[44:47]\n"
    "v_mfma_f32_16x16x32_bf16 v[48:51], v[124:127], v[188:191], v[48:51]\n"
    "s_waitcnt lgkmcnt(9)\n"
    "v_mfma_f32_16x16x32_bf16 v[52:55], v[124:127], v[192:195], v[52:55]\n"
    "s_waitcnt lgkmcnt(7) vmcnt(6)\n"
    "v_cmp_ne_u32 vcc, %[tag], v246\n"
    "s_nop 0\n"
    "s_and_b32 vcc_hi, vcc_hi, 0xff\n"
    "s_cmp_lg_u64 vcc, 0\n"
    "s_cbranch_scc1 L5_%=\n"
    "L6_%=:\n"
    "v_mfma_f32_16x16x32_bf16 v[40:43], v[120:123], v[184:187], v[40:43]\n"
    "s_add_u32 m0, %[ldsx], 0\n"
    "s_add_u32 s68, %[x], 4980736\n"
    "buffer_load_dwordx4 v3, %[srdw], s68 offen sc1 lds\n"
    "v_mfma_f32_16x16x32_bf16 v[44:47], v[120:123], v[188:191], v[44:47]\n"
    "buffer_load_dwordx4 v3, %[srdw], s68 offen offset:1024 sc1 lds\n"
    "v_lshlrev_b32 v208, 16, v224\n"
    "v_mfma_f32_16x16x32_bf16 v[48:51], v[120:123], v[192:195], v[48:51]\n"
    "s_waitcnt lgkmcnt(6) vmcnt(7)\n"
    "v_and_b32 v209, 0xffff0000, v224\n"
    "v_fma_f32 v60, v208, v228, v229\n"
    "v_mfma_f32_16x16x32_bf16 v[52:55], v[120:123], v[196:199], v[52:55]\n"
    "s_waitcnt lgkmcnt(4)\n"
    "v_fma_f32 v61, v209, v228, v229\n"
    "v_lshlrev_b32 v210, 16, v225\n"
    "v_mfma_f32_16x16x32_bf16 v[40:43], v[116:119], v[188:191], v[40:43]\n"
    "v_and_b32 v211, 0xffff0000, v225\n"
    "v_fma_f32 v62, v210, v228, v229\n"
    "v_mfma_f32_16x16x32_bf16 v[44:47], v[116:119], v[192:195], v[44:47]\n"
    "v_fma_f32 v63, v211, v228, v229\n"
    "v_lshlrev_b32 v212, 16, v226\n"
    "v_mfma_f32_16x16x32_bf16 v[48:51], v[116:119], v[196:199], v[48:51]\n"
    "s_waitcnt lgkmcnt(3)\n"
    "buffer_load_dwordx4 v3, %[srdw], s68 offen offset:2048 sc1 lds\n"
    "v_mfma_f32_16x16x32_bf16 v[52:55], v[116:119], v[200:203], v[52:55]\n"
    "s_waitcnt lgkmcnt(1)\n"
    "s_waitcnt vmcnt(3)\n"
    "s_barrier\n"
    "v_mfma_f32_16x16x32_bf16 v[40:43], v[112:115], v[192:195], v[40:43]\n"
    "ds_read_b64_tr_b16 a[228:229], v6 offset:49152\n"
    "ds_read_b64_tr_b16 a[230:231], v6 offset:49920\n"
    "v_mfma_f32_16x16x32_bf16 v[44:47], v[112:115], v[196:199], v[44:47]\n"
    "ds_read_b64_tr_b16 a[232:233], v6 offset:55296\n"
    "ds_read_b64_tr_b16 a[234:235], v6 offset:56064\n"
    "v_mfma_f32_16x16x32_bf16 v[48:51], v[112:115], v[200:203], v[48:51]\n"
    "s_waitcnt lgkmcnt(4)\n"
    "buffer_load_dwordx4 v3, %[srdw], s68 offen offset:3072 sc1 lds\n"
    "v_mfma_f32_16x16x32_bf16 v[52:55], v[112:115], v[204:207], v[52:55]\n"
    "ds_read_b64_tr_b16 a[236:237], v7 offset:49152\n"
    "v_cvt_pk_bf16_f32 v40, v40, v41\n"
    "v_cvt_pk_bf16_f32 v41, v42, v43\n"
    "ds_write_b64 v242, v[40:41] offset:0\n"
    "v_cvt_pk_bf16_f32 v44, v44, v45\n"
    "v_cvt_pk_bf16_f32 v45, v46, v47\n"
    "ds_write_b64 v242, v[44:45] offset:512\n"
    "v_cvt_pk_bf16_f32 v48, v48, v49\n"
    "v_cvt_pk_bf16_f32 v49, v50, v51\n"
    "ds_write_b64 v242, v[48:49] offset:1024\n"
    "v_cvt_pk_bf16_f32 v52, v52, v53\n"
    "v_cvt_pk_bf16_f32 v53, v54, v55\n"
    "ds_write_b64 v242, v[52:53] offset:1536\n"
    "s_add_u32 m0, %[ldsx], 4096\n"
    "s_add_u32 s68, %[x], 4984832\n"
    "buffer_load_dwordx4 v3, %[srdw], s68 offen sc1 lds\n"
    "ds_read_b64_tr_b16 a[238:239], v7 offset:49920\n"
    "v_and_b32 v213, 0xffff0000, v226\n"
    "v_fma_f32 v64, v212, v230, v231\n"
    "v_fma_f32 v65, v213, v230, v231\n"
    "v_lshlrev_b32 v214, 16, v227\n"
    "v_and_b32 v215, 0xffff0000, v227\n"
    "v_fma_f32 v66, v214, v230, v231\n"
    "v_fma_f32 v67, v215, v230, v231\n"
    "v_lshlrev_b32 v216, 16, v232\n"
    "v_and_b32 v217, 0xffff0000, v232\n"
    "v_fma_f32 v68, v216, v236, v237\n"
    "v_fma_f32 v69, v217, v236, v237\n"
    "v_lshlrev_b32 v218, 16, v233\n"
    "v_and_b32 v219, 0xffff0000, v233\n"
    "v_fma_f32 v70, v218, v236, v237\n"
    "v_fma_f32 v71, v219, v236, v237\n"
    "v_lshlrev_b32 v220, 16, v234\n"
    "v_and_b32 v221, 0xffff0000, v234\n"
    "v_fma_f32 v72, v220, v238, v239\n"
    "v_fma_f32 v73, v221, v238, v239\n"
    "v_lshlrev_b32 v222, 16, v235\n"
    "v_and_b32 v223, 0xffff0000, v235\n"
    "v_fma_f32 v74, v222, v238, v239\n"
    "v_fma_f32 v75, v223, v238, v239\n"
    "s_add_u32 s84, %[fwd], 0\n"
    "s_mov_b64 exec, 0x1c7\n"
    "buffer_load_dword v79, v1, %[srdw], s84 offen sc1\n"
    "s_mov_b64 exec, -1\n"
    "s_bfe_u32 s85, %[dbg], 0xd\n"
    "s_mul_i32 s42, s85, -655360\n"
    "s_add_u32 s42, s42, %[x]\n"
    "s_mul_i32 s43, s85, -1572864\n"
    "s_add_u32 s43, s43, %[hst]\n"
    "s_mul_i32 s93, s85, -262144\n"
    "s_add_u32 s93, s93, %[f4p]\n"
    "s_waitcnt lgkmcnt(0)\n"
    "s_barrier\n"
    "ds_read_b64_tr_b16 v[96:97], v243 offset:0\n"
    "ds_read_b64_tr_b16 v[98:99], v243 offset:16\n"
    "ds_read_b64_tr_b16 v[104:105], v243 offset:1024\n"
    "ds_read_b64_tr_b16 v[106:107], v243 offset:1040\n"
    "s_add_u32 s87, %[q], 6291456\n"
    "s_add_u32 s88, %[q], 6946816\n"
    "s_add_u32 m0, %[ldsx], 21504\n"
    "s_add_u32 s68, s42, 5308416\n"
    "buffer_load_dwordx4 v3, %[srdw], s68 offen sc1 lds\n"
    "buffer_load_dwordx4 v3, %[srdw], s68 offen offset:1024 sc1 lds\n"
    "buffer_load_dwordx4 v3, %[srdw], s68 offen offset:2048 sc1 lds\n"
    "buffer_load_dwordx4 v3, %[srdw], s68 offen offset:3072 sc1 lds\n"
    "s_lshr_b32 s86, %[dbg], 3\n"
    "s_add_u32 s86, s86, %[dbg]\n"
    "s_mov_b32 s44, 0\n"
    "s_bfe_u32 s44, s44, 0x2000d\n"
    "s_add_u32 m0, %[ldsx], 25600\n"
    "s_add_u32 s68, s42, 5312512\n"
    "buffer_load_dwordx4 v3, %[srdw], s68 offen sc1 lds\n"
    "s_mul_i32 s44, s44, 3072\n"
    "s_mov_b32 s45, 8192\n"
    "s_bfe_u32 s45, s45, 0x2000d\n"
    "s_mul_i32 s45, s45, 3072\n"
    "s_waitcnt lgkmcnt(3)\n"
    "v_lshlrev_b32 v224, 16, v96\n"
    "v_and_b32 v228, 0xffff0000, v96\n"
    "v_fmac_f32 v208, v60, v224\n"
    "v_fmac_f32 v209, v61, v228\n"
    "v_cvt_pk_bf16_f32 v100, v208, v209\n"
    "v_lshlrev_b32 v225, 16, v97\n"
    "v_and_b32 v229, 0xffff0000, v97\n"
    "v_fmac_f32 v210, v62, v225\n"
    "v_fmac_f32 v211, v63, v229\n"
    "v_cvt_pk_bf16_f32 v101, v210, v211\n"
    "s_waitcnt lgkmcnt(2)\n"
    "v_lshlrev_b32 v226, 16, v98\n"
    "v_and_b32 v230, 0xffff0000, v98\n"
    "v_fmac_f32 v212, v64, v226\n"
    "v_fmac_f32 v213, v65, v230\n"
    "v_cvt_pk_bf16_f32 v102, v212, v213\n"
    "v_lshlrev_b32 v227, 16, v99\n"
    "v_and_b32 v231, 0xffff0000, v99\n"
    "v_fmac_f32 v214, v66, v227\n"
    "v_fmac_f32 v215, v67, v231\n"
    "v_cvt_pk_bf16_f32 v103, v214, v215\n"
    "buffer_store_dwordx4 v[100:103], v244, %[srdw], s87 offen sc1\n"
    "s_waitcnt lgkmcnt(1)\n"
    "v_lshlrev_b32 v232, 16, v104\n"
    "v_and_b32 v236, 0xffff0000, v104\n"
    "v_fmac_f32 v216, v68, v232\n"
    "v_fmac_f32 v217, v69, v236\n"
    "v_cvt_pk_bf16_f32 v108, v216, v217\n"
    "v_lshlrev_b32 v233, 16, v105\n"
    "v_and_b32 v237, 0xffff0000, v105\n"
    "v_fmac_f32 v218, v70, v233\n"
    "v_fmac_f32 v219, v71, v237\n"
    "v_cvt_pk_bf16_f32 v109, v218, v219\n"
    "s_waitcnt lgkmcnt(0)\n"
    "v_lshlrev_b32 v234, 16, v106\n"
    "v_and_b32 v238, 0xffff0000, v106\n"
    "v_fmac_f32 v220, v72, v234\n"
    "v_fmac_f32 v221, v73, v238\n"
    "v_cvt_pk_bf16_f32 v110, v220, v221\n"
    "v_lshlrev_b32 v235, 16, v107\n"
    "v_and_b32 v239, 0xffff0000, v107\n"
    "v_fmac_f32 v222, v74, v235\n"
    "v_fmac_f32 v223, v75, v239\n"
    "v_cvt_pk_bf16_f32 v111, v222, v223\n"
    "buffer_store_dwordx4 v[108:111], v244, %[srdw], s88 offen sc1\n"
    "s_mov_b32 s46, 16384\n"
    "s_bfe_u32 s46, s46, 0x2000d\n"
    "s_mul_i32 s46, s46, 3072\n"
    "s_mov_b32 s47, 24576\n"
    "s_bfe_u32 s47, s47, 0x2000d\n"
    "s_mul_i32 s47, s47, 3072\n"
    "LJOIN_%=:\n"
    "s_waitcnt vmcnt(8)\n"
    "s_barrier\n"
    "ds_read2st64_b64 v[24:27], v4 offset0:0 offset1:1\n"
    "ds_read2st64_b64 v[28:31], v4 offset0:2 offset1:3\n"
    "ds_read2st64_b64 v[32:35], v4 offset0:4 offset1:5\n"
    "ds_read2st64_b64 v[36:39], v4 offset0:6 offset1:7\n"
    "s_waitcnt lgkmcnt(3)\n"
    "v_mfma_f32_16x16x32_bf16 v[40:43], a[0:3], v[24:27], 0\n"
    "v_mfma_f32_16x16x32_bf16 v[44:47], a[4:7], v[24:27], 0\n"
    "v_mfma_f32_16x16x32_bf16 v[48:51], a[8:11], v[24:27], 0\n"
    "s_waitcnt lgkmcnt(2)\n"
    "ds_read2st64_b64 v[24:27], v5 offset0:0 offset1:1\n"
    "v_mfma_f32_16x16x32_bf16 v[40:43], a[12:15], v[28:31], v[40:43]\n"
    "v_mfma_f32_16x16x32_bf16 v[44:47], a[16:19], v[28:31], v[44:47]\n"
    "v_mfma_f32_16x16x32_bf16 v[48:51], a[20:23], v[28:31], v[48:51]\n"
    "s_waitcnt lgkmcnt(2)\n"
    "ds_read2st64_b64 v[28:31], v4 offset0:8 offset1:9\n"
    "v_mfma_f32_16x16x32_bf16 v[40:43], a[24:27], v[32:35], v[40:43]\n"
    "v_mfma_f32_16x16x32_bf16 v[44:47], a[28:31], v[32:35], v[44:47]\n"
    "s_lshl1_add_u32 s89, s46, %[wd]\n"
    "s_add_u32 s90, s89, s46\n"
    "s_add_u32 s91, s90, 8192\n"
    "v_mfma_f32_16x16x32_bf16 v[48:51], a[32:35], v[32:35], v[48:51]\n"
    "s_waitcnt lgkmcnt(2)\n"
    "ds_read2st64_b64 v[32:35], v5 offset0:2 offset1:3\n"
    "v_mfma_f32_16x16x32_bf16 v[40:43], a[36:39], v[36:39], v[40:43]\n"
    "s_add_u32 s46, s46, %[hld]\n"
    "v_mfma_f32_16x16x32_bf16 v[44:47], a[40:43], v[36:39], v[44:47]\n"
    "v_mfma_f32_16x16x32_bf16 v[48:51], a[44:47], v[36:39], v[48:51]\n"
    "s_waitcnt lgkmcnt(2)\n"
    "ds_read2st64_b64 v[36:39], v4 offset0:10 offset1:11\n"
    "v_lshrrev_b32 v90, 1, v0\n"
    "v_mfma_f32_16x16x32_bf16 v[52:55], v[20:23], v[24:27], 0\n"
    "buffer_load_dwordx2 v[88:89], v90, %[srdw], %[cs] offen sc1\n"
    "v_mfma_f32_16x16x32_bf16 v[56:59], v[24:27], v[24:27], 0\n"
    "s_waitcnt lgkmcnt(2)\n"
    "ds_read2st64_b64 v[24:27], v5 offset0:4 offset1:5\n"
    "v_mfma_f32_16x16x32_bf16 v[40:43], a[48:51], v[28:31], v[40:43]\n"
    "v_mfma_f32_16x16x32_bf16 v[44:47], a[52:55], v[28:31], v[44:47]\n"
    "v_mfma_f32_16x16x32_bf16 v[48:51], a[56:59], v[28:31], v[48:51]\n"
    "s_waitcnt lgkmcnt(2)\n"
    "ds_read2st64_b64 v[28:31], v4 offset0:12 offset1:13\n"
    "v_mfma_f32_16x16x32_bf16 v[52:55], v[20:23], v[32:35], v[52:55]\n"
    "v_mfma_f32_16x16x32_bf16 v[56:59], v[32:35], v[32:35], v[56:59]\n"
    "s_waitcnt lgkmcnt(2)\n"
    "ds_read2st64_b64 v[32:35], v5 offset0:6 offset1:7\n"
    "v_mfma_f32_16x16x32_bf16 v[40:43], a[60:63], v[36:39], v[40:43]\n"
    "s_add_u32 s92, %[f3p], 106496\n"
    "v_mfma_f32_16x16x32_bf16 v[44:47], a[64:67], v[36:39], v[44:47]\n"
    "v_mfma_f32_16x16x32_bf16 v[48:51], a[68:71], v[36:39], v[48:51]\n"
    "s_waitcnt lgkmcnt(2)\n"
    "ds_read2st64_b64 v[36:39], v4 offset0:14 offset1:15\n"
    "v_mfma_f32_16x16x32_bf16 v[52:55], v[20:23], v[24:27], v[52:55]\n"
    "v_mfma_f32_16x16x32_bf16 v[56:59], v[24:27], v[24:27], v[56:59]\n"
    "s_waitcnt lgkmcnt(2)\n"
    "ds_read2st64_b64 v[24:27], v5 offset0:8 offset1:9\n"
    "v_mfma_f32_16x16x32_bf16 v[40:43], a[72:75], v[28:31], v[40:43]\n"
    "s_waitcnt vmcnt(8)\n"
    "v_cmp_ne_u32 vcc, %[tag], v79\n"
    "s_nop 0\n"
    "s_and_b32 vcc_lo, vcc_lo, 0x1c7\n"
    "s_cbranch_scc1 L9_%=\n"
    "L10_%=:\n"
    "v_mfma_f32_16x16x32_bf16 v[44:47], a[76:79], v[28:31], v[44:47]\n"
    "s_add_u32 m0, %[ldswd], 12288\n"
    "s_add_u32 s68, s89, 36864\n"
    "buffer_load_dwordx4 v0, %[srdw], s68 offen sc1 lds\n"
    "v_mfma_f32_16x16x32_bf16 v[48:51], a[80:83], v[28:31], v[48:51]\n"
    "s_waitcnt lgkmcnt(2)\n"
    "ds_read2st64_b64 v[28:31], v4 offset0:16 offset1:17\n"
    "v_mfma_f32_16x16x32_bf16 v[52:55], v[20:23], v[32:35], v[52:55]\n"
    "buffer_load_dwordx4 v0, %[srdw], s68 offen offset:1024 sc1 lds\n"
    "v_mfma_f32_16x16x32_bf16 v[56:59], v[32:35], v[32:35], v[56:59]\n"
    "s_waitcnt lgkmcnt(2)\n"
    "ds_read2st64_b64 v[32:35], v4 offset0:18 offset1:19\n"
    "v_mfma_f32_16x16x32_bf16 v[40:43], a[84:87], v[36:39], v[40:43]\n"
    "s_waitcnt vmcnt(3)\n"
    "s_barrier\n"
    "v_mfma_f32_16x16x32_bf16 v[44:47], a[88:91], v[36:39], v[44:47]\n"
    "s_mov_b64 exec, s[94:95]\n"
    "buffer_store_dword v2, off, %[srdw], s92 sc1\n"
    "s_mov_b64 exec, -1\n"
    "v_mfma_f32_16x16x32_bf16 v[48:51], a[92:95], v[36:39], v[48:51]\n"
    "s_waitcnt lgkmcnt(2)\n"
    "ds_read2st64_b64 v[36:39], v4 offset0:20 offset1:21\n"
    "v_mfma_f32_16x16x32_bf16 v[52:55], v[20:23], v[24:27], v[52:55]\n"
    "s_add_u32 m0, %[ldsx], 43008\n"
    "s_add_u32 s68, %[x], 5636096\n"
    "buffer_load_dwordx4 v3, %[srdw], s68 offen sc1 lds\n"
    "v_mfma_f32_16x16x32_bf16 v[56:59], v[24:27], v[24:27], v[56:59]\n"
    "s_waitcnt lgkmcnt(2)\n"
    "ds_read2st64_b64 v[24:27], v4 offset0:22 offset1:23\n"
    "v_mfma_f32_16x16x32_bf16 v[40:43], a[96:99], v[28:31], v[40:43]\n"
    "v_mfma_f32_16x16x32_bf16 v[44:47], a[100:103], v[28:31], v[44:47]\n"
    "buffer_load_dwordx4 v3, %[srdw], s68 offen offset:1024 sc1 lds\n"
    "v_mfma_f32_16x16x32_bf16 v[48:51], a[104:107], v[28:31], v[48:51]\n"
    "s_waitcnt lgkmcnt(2)\n"
    "ds_read2st64_b64 v[28:31], v4 offset0:24 offset1:25\n"
    "v_cndmask_b32_e64 v53, v56, v57, s[48:49]\n"
    "v_mfma_f32_16x16x32_bf16 v[40:43], a[108:111], v[32:35], v[40:43]\n"
    "v_cndmask_b32_e64 v53, v53, v58, s[50:51]\n"
    "v_cndmask_b32_e64 v53, v53, v59, s[52:53]\n"
    "v_mfma_f32_16x16x32_bf16 v[44:47], a[112:115], v[32:35], v[44:47]\n"
    "ds_write_b64 v8, v[52:53] offset:0\n"
    "v_mfma_f32_16x16x32_bf16 v[48:51], a[116:119], v[32:35], v[48:51]\n"
    "s_waitcnt lgkmcnt(3)\n"
    "ds_read2st64_b64 v[112:115], v4 offset0:36 offset1:37\n"
    "ds_read2st64_b64 v[116:119], v4 offset0:38 offset1:39\n"
    "v_mfma_f32_16x16x32_bf16 v[40:43], a[120:123], v[36:39], v[40:43]\n"
    "s_add_u32 s56, %[fwd], 0\n"
    "ds_read2st64_b64 v[32:35], v4 offset0:26 offset1:27\n"
    "v_mfma_f32_16x16x32_bf16 v[44:47], a[124:127], v[36:39], v[44:47]\n"
    "buffer_load_dwordx4 v3, %[srdw], s68 offen offset:2048 sc1 lds\n"
    "v_mfma_f32_16x16x32_bf16 v[48:51], a[128:131], v[36:39], v[48:51]\n"
    "s_waitcnt lgkmcnt(5)\n"
    "ds_read2st64_b64 v[36:39], v4 offset0:28 offset1:29\n"
    "v_mfma_f32_16x16x32_bf16 v[40:43], a[132:135], v[24:27], v[40:43]\n"
    "s_mov_b64 exec, 0xfff\n"
    "buffer_load_dword v78, v1, %[srdw], s56 offen sc1\n"
    "s_mov_b64 exec, -1\n"
    "v_mfma_f32_16x16x32_bf16 v[44:47], a[136:139], v[24:27], v[44:47]\n"
    "v_mfma_f32_16x16x32_bf16 v[48:51], a[140:143], v[24:27], v[48:51]\n"
    "s_waitcnt lgkmcnt(5)\n"
    "ds_read2st64_b64 v[24:27], v4 offset0:30 offset1:31\n"
    "v_mfma_f32_16x16x32_bf16 v[40:43], a[144:147], v[28:31], v[40:43]\n"
    "v_mfma_f32_16x16x32_bf16 v[44:47], a[148:151], v[28:31], v[44:47]\n"
    "v_mfma_f32_16x16x32_bf16 v[48:51], a[152:155], v[28:31], v[48:51]\n"
    "s_waitcnt lgkmcnt(2)\n"
    "ds_read2st64_b64 v[28:31], v4 offset0:32 offset1:33\n"
    "v_mfma_f32_16x16x32_bf16 v[40:43], a[156:159], v[32:35], v[40:43]\n"
    "buffer_load_dwordx4 v3, %[srdw], s68 offen offset:3072 sc1 lds\n"
    "v_mfma_f32_16x16x32_bf16 v[44:47], a[160:163], v[32:35], v[44:47]\n"
    "v_mfma_f32_16x16x32_bf16 v[48:51], a[164:167], v[32:35], v[48:51]\n"
    "s_waitcnt lgkmcnt(2)\n"
    "ds_read2st64_b64 v[32:35], v4 offset0:34 offset1:35\n"
    "v_mfma_f32_16x16x32_bf16 v[40:43], a[168:171], v[36:39], v[40:43]\n"
    "s_add_u32 s57, s91, -4096\n"
    "v_mfma_f32_16x16x32_bf16 v[44:47], a[172:175], v[36:39], v[44:47]\n"
    "s_add_u32 m0, %[ldsx], 47104\n"
    "s_add_u32 s68, %[x], 5640192\n"
    "buffer_load_dwordx4 v3, %[srdw], s68 offen sc1 lds\n"
    "v_mfma_f32_16x16x32_bf16 v[48:51], a[176:179], v[36:39], v[48:51]\n"
    "s_waitcnt lgkmcnt(2)\n"
    "kx_a2:\n"
    "s_cbranch_execz kx_a6\n"
    "v_mfma_f32_16x16x32_bf16 v[40:43], a[180:183], v[24:27], v[40:43]\n"
    "v_mfma_f32_16x16x32_bf16 v[44:47], a[184:187], v[24:27], v[44:47]\n"
    "v_mfma_f32_16x16x32_bf16 v[48:51], a[188:191], v[24:27], v[48:51]\n"
    "s_waitcnt lgkmcnt(1)\n"
    "v_mfma_f32_16x16x32_bf16 v[40:43], a[192:195], v[28:31], v[40:43]\n"
    "s_add_u32 m0, %[ldswd], 14336\n"
    "s_add_u32 s68, s89, 38912\n"
    "buffer_load_dwordx4 v0, %[srdw], s68 offen sc1 lds\n"
    "v_mfma_f32_16x16x32_bf16 v[44:47], a[196:199], v[28:31], v[44:47]\n"
    "s_waitcnt lgkmcnt(0)\n"
    "s_barrier\n"
    "v_mfma_f32_16x16x32_bf16 v[48:51], a[200:203], v[28:31], v[48:51]\n"
    "ds_read2st64_b64 v[36:39], v4 offset0:42 offset1:43\n"
    "ds_read2st64_b64 v[24:27], v4 offset0:44 offset1:45\n"
    "v_mfma_f32_16x16x32_bf16 v[40:43], a[204:207], v[32:35], v[40:43]\n"
    "ds_read2st64_b64 v[28:31], v4 offset0:46 offset1:47\n"
    "ds_read_b128 v[52:55], v9 offset:0\n"
    "v_mfma_f32_16x16x32_bf16 v[44:47], a[208:211], v[32:35], v[44:47]\n"
    "ds_read_b128 v[56:59], v9 offset:16\n"
    "v_mfma_f32_16x16x32_bf16 v[48:51], a[212:215], v[32:35], v[48:51]\n"
    "ds_read2st64_b64 v[32:35], v4 offset0:48 offset1:49\n"
    "v_mfma_f32_16x16x32_bf16 v[40:43], a[216:219], v[112:115], v[40:43]\n"
    "s_add_u32 m0, %[ldsx], 0\n"
    "s_add_u32 s68, s42, 5963776\n"
    "buffer_load_dwordx4 v3, %[srdw], s68 offen sc1 lds\n"
    "v_mfma_f32_16x16x32_bf16 v[44:47], a[220:223], v[112:115], v[44:47]\n"
    "s_waitcnt vmcnt(11)\n"
    "v_cmp_ne_u32 vcc, %[tag], v89\n"
    "s_nop 0\n"
    "s_cmp_lg_u64 vcc, 0\n"
    "s_cbranch_scc1 L11_%=\n"
    "L12_%=:\n"
    "v_mfma_f32_16x16x32_bf16 v[48:51], a[224:227], v[112:115], v[48:51]\n"
    "buffer_load_dwordx4 v3, %[srdw], s68 offen offset:1024 sc1 lds\n"
    "v_mov_b32 v76, v88\n"
    "v_mfma_f32_16x16x32_bf16 v[40:43], a[228:231], v[116:119], v[40:43]\n"
    "v_mfma_f32_16x16x32_bf16 v[44:47], a[232:235], v[116:119], v[44:47]\n"
    "v_mfma_f32_16x16x32_bf16 v[48:51], a[236:239], v[116:119], v[48:51]\n"
    "s_waitcnt lgkmcnt(2)\n"
    "v_add_f32 v52, v52, v54\n"
    "v_add_f32 v53, v53, v55\n"
    "v_mfma_f32_16x16x32_bf16 v[60:63], a[0:3], v[36:39], 0\n"
    "s_waitcnt lgkmcnt(1)\n"
    "v_add_f32 v56, v56, v58\n"
    "v_add_f32 v57, v57, v59\n"
    "v_mfma_f32_16x16x32_bf16 v[64:67], a[4:7], v[36:39], 0\n"
    "s_add_u32 s59, %[yst], 2359296\n"
    "v_add_f32 v52, v52, v56\n"
    "v_add_f32 v53, v53, v57\n"
    "v_mfma_f32_16x16x32_bf16 v[68:71], a[8:11], v[36:39], 0\n"
    "s_add_u32 s60, %[yst], 2363392\n"
    "ds_read2st64_b64 v[36:39], v5 offset0:42 offset1:43\n"
    "v_mul_f32 v77, 0xbacccccd, v52\n"
    "v_mfma_f32_16x16x32_bf16 v[60:63], a[12:15], v[24:27], v[60:63]\n"
    "s_add_u32 s58, %[hst], 9568256\n"
    "v_mul_f32 v53, 0x3acccccd, v53\n"
    "v_fma_f32 v53, -v77, v77, v53\n"
    "v_mfma_f32_16x16x32_bf16 v[64:67], a[16:19], v[24:27], v[64:67]\n"
    "v_max_f32 v53, 0, v53\n"
    "v_add_f32 v73, 0x3727c5ac, v53\n"
    "v_mfma_f32_16x16x32_bf16 v[68:71], a[20:23], v[24:27], v[68:71]\n"
    "ds_read2st64_b64 v[24:27], v4 offset0:50 offset1:51\n"
    "buffer_load_dwordx4 v3, %[srdw], s68 offen offset:2048 sc1 lds\n"
    "v_mfma_f32_16x16x32_bf16 v[60:63], a[24:27], v[28:31], v[60:63]\n"
    "v_rsq_f32 v52, v73\n"
    "v_mfma_f32_16x16x32_bf16 v[64:67], a[28:31], v[28:31], v[64:67]\n"
    "v_mul_f32 v72, 0xbfb8aa3b, v52\n"
    "v_fmac_f32_dpp v48, v76, v77 row_newbcast:8 row_mask:0xf bank_mask:0xf\n"
    "v_mfma_f32_16x16x32_bf16 v[68:71], a[32:35], v[28:31], v[68:71]\n"
    "s_waitcnt lgkmcnt(2)\n"
    "ds_read2st64_b64 v[28:31], v5 offset0:44 offset1:45\n"
    "s_add_u32 s65, s57, -4096\n"
    "v_mfma_f32_16x16x32_bf16 v[60:63], a[36:39], v[32:35], v[60:63]\n"
    "s_add_u32 s63, %[yst], 2367488\n"
    "buffer_load_dwordx4 v3, %[srdw], s68 offen offset:3072 sc1 lds\n"
    "v_mfma_f32_16x16x32_bf16 v[64:67], a[40:43], v[32:35], v[64:67]\n"
    "s_add_u32 s64, %[yst], 2371584\n"
    "v_fmac_f32_dpp v49, v76, v77 row_newbcast:9 row_mask:0xf bank_mask:0xf\n"
    "v_fmac_f32_dpp v50, v76, v77 row_newbcast:10 row_mask:0xf bank_mask:0xf\n"
    "v_mfma_f32_16x16x32_bf16 v[68:71], a[44:47], v[32:35], v[68:71]\n"
    "s_waitcnt lgkmcnt(2)\n"
    "ds_read2st64_b64 v[32:35], v4 offset0:52 offset1:53\n"
    "v_fmac_f32_dpp v51, v76, v77 row_newbcast:11 row_mask:0xf bank_mask:0xf\n"
    "v_mfma_f32_16x16x32_bf16 v[52:55], v[20:23], v[36:39], 0\n"
    "s_add_u32 m0, %[ldsx], 4096\n"
    "s_add_u32 s68, s42, 5967872\n"
    "buffer_load_dwordx4 v3, %[srdw], s68 offen sc1 lds\n"
    "v_mfma_f32_16x16x32_bf16 v[56:59], v[36:39], v[36:39], 0\n"
    "s_waitcnt lgkmcnt(2)\n"
    "ds_read2st64_b64 v[36:39], v5 offset0:46 offset1:47\n"
    "v_fmac_f32_dpp v40, v76, v77 row_newbcast:0 row_mask:0xf bank_mask:0xf\n"
    "v_mfma_f32_16x16x32_bf16 v[60:63], a[48:51], v[24:27], v[60:63]\n"
    "v_permlane16_swap_b32 v48, v50\n"
    "v_mfma_f32_16x16x32_bf16 v[64:67], a[52:55], v[24:27], v[64:67]\n"
    "buffer_store_dwordx4 a[240:243], v245, %[srdw], s59 offen sc1\n"
    "v_mfma_f32_16x16x32_bf16 v[68:71], a[56:59], v[24:27], v[68:71]\n"
    "s_waitcnt lgkmcnt(2)\n"
    "ds_read2st64_b64 v[24:27], v4 offset0:54 offset1:55\n"
    "v_fmac_f32_dpp v44, v76, v77 row_newbcast:4 row_mask:0xf bank_mask:0xf\n"
    "v_mfma_f32_16x16x32_bf16 v[52:55], v[20:23], v[28:31], v[52:55]\n"
    "v_permlane16_swap_b32 v49, v51\n"
    "v_mfma_f32_16x16x32_bf16 v[56:59], v[28:31], v[28:31], v[56:59]\n"
    "s_waitcnt lgkmcnt(2)\n"
    "ds_read2st64_b64 v[28:31], v5 offset0:48 offset1:49\n"
    "v_mul_f32 v44, v40, v44\n"
    "v_mfma_f32_16x16x32_bf16 v[60:63], a[60:63], v[32:35], v[60:63]\n"
    "buffer_store_dwordx4 a[244:247], v245, %[srdw], s60 offen sc1\n"
    "v_mfma_f32_16x16x32_bf16 v[64:67], a[64:67], v[32:35], v[64:67]\n"
    "v_mul_f32 v40, v72, v40\n"
    "v_fmac_f32_dpp v41, v76, v77 row_newbcast:1 row_mask:0xf bank_mask:0xf\n"
    "v_mfma_f32_16x16x32_bf16 v[68:71], a[68:71], v[32:35], v[68:71]\n"
    "s_waitcnt lgkmcnt(2)\n"
    "ds_read2st64_b64 v[32:35], v4 offset0:56 offset1:57\n"
    "v_fmac_f32_dpp v45, v76, v77 row_newbcast:5 row_mask:0xf bank_mask:0xf\n"
    "v_mfma_f32_16x16x32_bf16 v[52:55], v[20:23], v[36:39], v[52:55]\n"
    "v_exp_f32 v40, v40\n"
    "v_mfma_f32_16x16x32_bf16 v[56:59], v[36:39], v[36:39], v[56:59]\n"
    "s_waitcnt lgkmcnt(2)\n"
    "ds_read2st64_b64 v[36:39], v5 offset0:50 offset1:51\n"
    "v_fma_f32 v40, v40, v73, v73\n"
    "v_mfma_f32_16x16x32_bf16 v[60:63], a[72:75], v[24:27], v[60:63]\n"
    "buffer_store_dwordx4 v[96:99], v245, %[srdw], s63 offen sc1\n"
    "v_mfma_f32_16x16x32_bf16 v[64:67], a[76:79], v[24:27], v[64:67]\n"
    "v_rcp_f32 v40, v40\n"
    "v_mfma_f32_16x16x32_bf16 v[68:71], a[80:83], v[24:27], v[68:71]\n"
    "s_waitcnt lgkmcnt(2) vmcnt(11)\n"
    "ds_read2st64_b64 v[24:27], v4 offset0:58 offset1:59\n"
    "v_mul_f32 v40, v40, v44\n"
    "v_mfma_f32_16x16x32_bf16 v[52:55], v[20:23], v[28:31], v[52:55]\n"
    "v_cmp_ne_u32 vcc, %[tag], v78\n"
    "s_nop 0\n"
    "s_and_b32 vcc_lo, vcc_lo, 0xfff\n"
    "s_cbranch_scc1 L13_%=\n"
    "L14_%=:\n"
    "v_mfma_f32_16x16x32_bf16 v[56:59], v[28:31], v[28:31], v[56:59]\n"
    "s_waitcnt lgkmcnt(2)\n"
    "ds_read2st64_b64 v[28:31], v4 offset0:60 offset1:61\n"
    "v_mul_f32 v45, v41, v45\n"
    "v_mfma_f32_16x16x32_bf16 v[60:63], a[84:87], v[32:35], v[60:63]\n"
    "buffer_store_dwordx4 v[104:107], v245, %[srdw], s64 offen sc1\n"
    "v_mfma_f32_16x16x32_bf16 v[64:67], a[88:91], v[32:35], v[64:67]\n"
    "v_mul_f32 v41, v72, v41\n"
    "v_fmac_f32_dpp v42, v76, v77 row_newbcast:2 row_mask:0xf bank_mask:0xf\n"
    "v_mfma_f32_16x16x32_bf16 v[68:71], a[92:95], v[32:35], v[68:71]\n"
    "s_waitcnt lgkmcnt(2)\n"
    "ds_read2st64_b64 v[32:35], v4 offset0:62 offset1:63\n"
    "v_fmac_f32_dpp v46, v76, v77 row_newbcast:6 row_mask:0xf bank_mask:0xf\n"
    "v_mfma_f32_16x16x32_bf16 v[52:55], v[20:23], v[36:39], v[52:55]\n"
    "s_add_u32 m0, %[ldswd], 15360\n"
    "s_add_u32 s68, s89, 39936\n"
    "buffer_load_dwordx4 v0, %[srdw], s68 offen sc1 lds\n"
    "v_mfma_f32_16x16x32_bf16 v[56:59], v[36:39], v[36:39], v[56:59]\n"
    "s_waitcnt lgkmcnt(2)\n"
    "ds_read2st64_b64 v[36:39], v4 offset0:64 offset1:65\n"
    "v_mul_f32 v46, v42, v46\n"
    "v_mfma_f32_16x16x32_bf16 v[60:63], a[96:99], v[24:27], v[60:63]\n"
    "s_lshl1_add_u32 s66, s44, %[wd]\n"
    "s_add_u32 s67, s66, s44\n"
    "s_add_u32 s78, s67, 8192\n"
    "v_mfma_f32_16x16x32_bf16 v[64:67], a[100:103], v[24:27], v[64:67]\n"
    "v_cndmask_b32_e64 v53, v56, v57, s[48:49]\n"
    "v_cndmask_b32_e64 v53, v53, v58, s[50:51]\n"
    "v_mfma_f32_16x16x32_bf16 v[68:71], a[104:107], v[24:27], v[68:71]\n"
    "s_waitcnt lgkmcnt(2)\n"
    "ds_read2st64_b64 v[24:27], v4 offset0:66 offset1:67\n"
    "v_cndmask_b32_e64 v53, v53, v59, s[52:53]\n"
    "v_mfma_f32_16x16x32_bf16 v[60:63], a[108:111], v[28:31], v[60:63]\n"
    "s_add_u32 s44, s44, %[hld]\n"
    "buffer_load_dwordx4 v0, %[srdw], s68 offen offset:1024 sc1 lds\n"
    "v_mfma_f32_16x16x32_bf16 v[64:67], a[112:115], v[28:31], v[64:67]\n"
    "ds_write_b64 v8, v[52:53] offset:512\n"
    "v_mfma_f32_16x16x32_bf16 v[68:71], a[116:119], v[28:31], v[68:71]\n"
    "s_waitcnt lgkmcnt(3)\n"
    "ds_read2st64_b64 v[112:115], v4 offset0:78 offset1:79\n"
    "ds_read2st64_b64 v[116:119], v4 offset0:80 offset1:81\n"
    "v_mfma_f32_16x16x32_bf16 v[60:63], a[120:123], v[32:35], v[60:63]\n"
    "ds_read2st64_b64 v[28:31], v4 offset0:68 offset1:69\n"
    "buffer_load_dwordx4 v0, %[srdw], s68 offen offset:2048 sc1 lds\n"
    "v_mfma_f32_16x16x32_bf16 v[64:67], a[124:127], v[32:35], v[64:67]\n"
    "v_exp_f32 v41, v41\n"
    "v_mfma_f32_16x16x32_bf16 v[68:71], a[128:131], v[32:35], v[68:71]\n"
    "s_waitcnt lgkmcnt(5)\n"
    "ds_read2st64_b64 v[32:35], v4 offset0:70 offset1:71\n"
    "v_fma_f32 v41, v41, v73, v73\n"
    "v_mfma_f32_16x16x32_bf16 v[60:63], a[132:135], v[36:39], v[60:63]\n"
    "v_rcp_f32 v41, v41\n"
    "v_mfma_f32_16x16x32_bf16 v[64:67], a[136:139], v[36:39], v[64:67]\n"
    "buffer_load_dwordx4 v[216:219], v0, %[srdw], s91 offen offset:0 sc1\n"
    "v_mul_f32 v41, v41, v45\n"
    "v_mfma_f32_16x16x32_bf16 v[68:71], a[140:143], v[36:39], v[68:71]\n"
    "s_waitcnt lgkmcnt(5)\n"
    "ds_read2st64_b64 v[36:39], v4 offset0:72 offset1:73\n"
    "v_cvt_pk_bf16_f32 v40, v40, v41\n"
    "v_mfma_f32_16x16x32_bf16 v[60:63], a[144:147], v[24:27], v[60:63]\n"
    "v_mul_f32 v42, v72, v42\n"
    "v_fmac_f32_dpp v43, v76, v77 row_newbcast:3 row_mask:0xf bank_mask:0xf\n"
    "v_mfma_f32_16x16x32_bf16 v[64:67], a[148:151], v[24:27], v[64:67]\n"
    "v_exp_f32 v42, v42\n"
    "v_mfma_f32_16x16x32_bf16 v[68:71], a[152:155], v[24:27], v[68:71]\n"
    "s_waitcnt lgkmcnt(2)\n"
    "ds_read2st64_b64 v[24:27], v4 offset0:74 offset1:75\n"
    "v_fma_f32 v42, v42, v73, v73\n"
    "v_mfma_f32_16x16x32_bf16 v[60:63], a[156:159], v[28:31], v[60:63]\n"
    "buffer_load_dwordx4 v[212:215], v0, %[srdw], s57 offen offset:3072 sc1\n"
    "v_fmac_f32_dpp v47, v76, v77 row_newbcast:7 row_mask:0xf bank_mask:0xf\n"
    "v_mfma_f32_16x16x32_bf16 v[64:67], a[160:163], v[28:31], v[64:67]\n"
    "v_rcp_f32 v42, v42\n"
    "v_mfma_f32_16x16x32_bf16 v[68:71], a[164:167], v[28:31], v[68:71]\n"
    "s_waitcnt lgkmcnt(2)\n"
    "ds_read2st64_b64 v[28:31], v4 offset0:76 offset1:77\n"
    "v_mul_f32 v42, v42, v46\n"
    "v_mfma_f32_16x16x32_bf16 v[60:63], a[168:171], v[32:35], v[60:63]\n"
    "v_mul_f32 v47, v43, v47\n"
    "v_mul_f32 v43, v72, v43\n"
    "v_mfma_f32_16x16x32_bf16 v[64:67], a[172:175], v[32:35], v[64:67]\n"
    "buffer_load_dwordx4 v[208:211], v0, %[srdw], s57 offen offset:2048 sc1\n"
    "v_mul_f32 v50, v48, v50\n"
    "v_mfma_f32_16x16x32_bf16 v[68:71], a[176:179], v[32:35], v[68:71]\n"
    "s_waitcnt lgkmcnt(2)\n"
    "v_exp_f32 v43, v43\n"
    "v_mfma_f32_16x16x32_bf16 v[60:63], a[180:183], v[36:39], v[60:63]\n"
    "s_lshr_b32 s79, %[ldsx], 3\n"
    "s_add_u32 s80, s79, 106496\n"
    "v_fma_f32 v43, v43, v73, v73\n"
    "v_mfma_f32_16x16x32_bf16 v[64:67], a[184:187], v[36:39], v[64:67]\n"
    "s_mov_b32 exec_hi, 0xff\n"
    "buffer_load_dword v80, v0, %[srdw], s80 offen sc1\n"
    "s_mov_b32 exec_hi, -1\n"
    "v_mfma_f32_16x16x32_bf16 v[68:71], a[188:191], v[36:39], v[68:71]\n"
    "s_waitcnt lgkmcnt(1)\n"
    "v_rcp_f32 v43, v43\n"
    "v_mfma_f32_16x16x32_bf16 v[60:63], a[192:195], v[24:27], v[60:63]\n"
    "v_mul_f32 v43, v43, v47\n"
    "v_cvt_pk_bf16_f32 v41, v42, v43\n"
    "v_mfma_f32_16x16x32_bf16 v[64:67], a[196:199], v[24:27], v[64:67]\n"
    "s_waitcnt lgkmcnt(0) vmcnt(17)\n"
    "s_barrier\n"
    "v_mfma_f32_16x16x32_bf16 v[68:71], a[200:203], v[24:27], v[68:71]\n"
    "ds_read2st64_b64 v[32:35], v4 offset0:84 offset1:85\n"
    "ds_read2st64_b64 v[36:39], v4 offset0:86 offset1:87\n"
    "v_mfma_f32_16x16x32_bf16 v[60:63], a[204:207], v[28:31], v[60:63]\n"
    "ds_read2st64_b64 v[24:27], v4 offset0:88 offset1:89\n"
    "ds_read_b128 v[52:55], v9 offset:512\n"
    "v_mfma_f32_16x16x32_bf16 v[64:67], a[208:211], v[28:31], v[64:67]\n"
    "ds_read_b128 v[56:59], v9 offset:528\n"
    "v_mul_f32 v48, v72, v48\n"
    "v_mfma_f32_16x16x32_bf16 v[68:71], a[212:215], v[28:31], v[68:71]\n"
    "ds_read2st64_b64 v[28:31], v4 offset0:90 offset1:91\n"
    "v_mul_f32 v51, v49, v51\n"
    "v_mfma_f32_16x16x32_bf16 v[60:63], a[216:219], v[112:115], v[60:63]\n"
    "v_exp_f32 v48, v48\n"
    "v_mfma_f32_16x16x32_bf16 v[64:67], a[220:223], v[112:115], v[64:67]\n"
    "v_fma_f32 v48, v48, v73, v73\n"
    "v_mul_f32 v49, v72, v49\n"
    "v_mfma_f32_16x16x32_bf16 v[68:71], a[224:227], v[112:115], v[68:71]\n"
    "v_rcp_f32 v48, v48\n"
    "v_mfma_f32_16x16x32_bf16 v[60:63], a[228:231], v[116:119], v[60:63]\n"
    "buffer_load_dwordx4 v[204:207], v0, %[srdw], s57 offen offset:1024 sc1\n"
    "v_mul_f32 v48, v48, v50\n"
    "v_mfma_f32_16x16x32_bf16 v[64:67], a[232:235], v[116:119], v[64:67]\n"
    "v_exp_f32 v49, v49\n"
    "v_mfma_f32_16x16x32_bf16 v[68:71], a[236:239], v[116:119], v[68:71]\n"
    "s_add_u32 s81, s43, 10354688\n"
    "v_fma_f32 v49, v49, v73, v73\n"
    "v_rcp_f32 v49, v49\n"
    "v_permlane32_swap_b32 v41, v43\n"
    "v_mul_f32 v49, v49, v51\n"
    "v_cvt_pk_bf16_f32 v42, v48, v49\n"
    "v_permlane16_swap_b32 v40, v41\n"
    "s_nop 0\n"
    "v_permlane16_swap_b32 v42, v43\n"
    "buffer_store_dwordx4 v[40:43], v10, %[srdw], s58 offen sc1\n"
    "s_waitcnt lgkmcnt(5)\n"
    "s_nop 0\n"
    "v_mfma_f32_16x16x32_bf16 v[40:43], a[0:3], v[32:35], 0\n"
    "s_waitcnt lgkmcnt(2)\n"
    "v_add_f32 v52, v52, v54\n"
    "v_add_f32 v53, v53, v55\n"
    "v_mfma_f32_16x16x32_bf16 v[44:47], a[4:7], v[32:35], 0\n"
    "buffer_load_dwordx4 v[200:203], v0, %[srdw], s57 offen offset:0 sc1\n"
    "v_mfma_f32_16x16x32_bf16 v[48:51], a[8:11], v[32:35], 0\n"
    "s_waitcnt lgkmcnt(1)\n"
    "ds_read2st64_b64 v[32:35], v5 offset0:84 offset1:85\n"
    "v_add_f32 v56, v56, v58\n"
    "v_mfma_f32_16x16x32_bf16 v[40:43], a[12:15], v[36:39], v[40:43]\n"
    "v_add_f32 v57, v57, v59\n"
    "v_add_f32 v52, v52, v56\n"
    "v_mfma_f32_16x16x32_bf16 v[44:47], a[16:19], v[36:39], v[44:47]\n"
    "v_add_f32 v53, v53, v57\n"
    "v_mul_f32 v77, 0xbacccccd, v52\n"
    "v_mfma_f32_16x16x32_bf16 v[48:51], a[20:23], v[36:39], v[48:51]\n"
    "ds_read2st64_b64 v[36:39], v4 offset0:92 offset1:93\n"
    "buffer_load_dwordx4 v[196:199], v0, %[srdw], s65 offen offset:3072 sc1\n"
    "v_mfma_f32_16x16x32_bf16 v[40:43], a[24:27], v[24:27], v[40:43]\n"
    "v_mul_f32 v53, 0x3acccccd, v53\n"
    "v_fma_f32 v53, -v77, v77, v53\n"
    "v_mfma_f32_16x16x32_bf16 v[44:47], a[28:31], v[24:27], v[44:47]\n"
    "v_max_f32 v53, 0, v53\n"
    "v_add_f32 v73, 0x3727c5ac, v53\n"
    "v_mfma_f32_16x16x32_bf16 v[48:51], a[32:35], v[24:27], v[48:51]\n"
    "s_waitcnt lgkmcnt(2)\n"
    "ds_read2st64_b64 v[24:27], v5 offset0:86 offset1:87\n"
    "v_fmac_f32_dpp v68, v76, v77 row_newbcast:8 row_mask:0xf bank_mask:0xf\n"
    "v_mfma_f32_16x16x32_bf16 v[40:43], a[36:39], v[28:31], v[40:43]\n"
    "buffer_load_dwordx4 v[192:195], v0, %[srdw], s65 offen offset:2048 sc1\n"
    "v_fmac_f32_dpp v69, v76, v77 row_newbcast:9 row_mask:0xf bank_mask:0xf\n"
    "v_mfma_f32_16x16x32_bf16 v[44:47], a[40:43], v[28:31], v[44:47]\n"
    "s_add_u32 s82, s78, -4096\n"
    "v_rsq_f32 v52, v73\n"
    "v_mfma_f32_16x16x32_bf16 v[48:51], a[44:47], v[28:31], v[48:51]\n"
    "ds_read2st64_b64 v[28:31], v4 offset0:94 offset1:95\n"
    "v_mul_f32 v72, 0xbfb8aa3b, v52\n"
    "s_waitcnt lgkmcnt(3)\n"
    "v_mfma_f32_16x16x32_bf16 v[52:55], v[20:23], v[32:35], 0\n"
    "v_fmac_f32_dpp v70, v76, v77 row_newbcast:10 row_mask:0xf bank_mask:0xf\n"
    "v_fmac_f32_dpp v71, v76, v77 row_newbcast:11 row_mask:0xf bank_mask:0xf\n"
    "v_mfma_f32_16x16x32_bf16 v[56:59], v[32:35], v[32:35], 0\n"
    "s_waitcnt lgkmcnt(2)\n"
    "ds_read2st64_b64 v[32:35], v5 offset0:88 offset1:89\n"
    "v_fmac_f32_dpp v60, v76, v77 row_newbcast:0 row_mask:0xf bank_mask:0xf\n"
    "v_mfma_f32_16x16x32_bf16 v[40:43], a[48:51], v[36:39], v[40:43]\n"
    "buffer_load_dwordx4 v[188:191], v0, %[srdw], s65 offen offset:1024 sc1\n"
    "v_fmac_f32_dpp v64, v76, v77 row_newbcast:4 row_mask:0xf bank_mask:0xf\n"
    "v_mfma_f32_16x16x32_bf16 v[44:47], a[52:55], v[36:39], v[44:47]\n"
    "v_permlane16_swap_b32 v68, v70\n"
    "v_mfma_f32_16x16x32_bf16 v[48:51], a[56:59], v[36:39], v[48:51]\n"
    "s_waitcnt lgkmcnt(2)\n"
    "ds_read2st64_b64 v[36:39], v4 offset0:96 offset1:97\n"
    "v_mul_f32 v64, v60, v64\n"
    "v_mfma_f32_16x16x32_bf16 v[52:55], v[20:23], v[24:27], v[52:55]\n"
    "v_permlane16_swap_b32 v69, v71\n"
    "v_mfma_f32_16x16x32_bf16 v[56:59], v[24:27], v[24:27], v[56:59]\n"
    "s_waitcnt lgkmcnt(2)\n"
    "ds_read2st64_b64 v[24:27], v5 offset0:90 offset1:91\n"
    "v_mul_f32 v60, v72, v60\n"
    "v_mfma_f32_16x16x32_bf16 v[40:43], a[60:63], v[28:31], v[40:43]\n"
    "buffer_load_dwordx4 v[184:187], v0, %[srdw], s65 offen offset:0 sc1\n"
    "v_fmac_f32_dpp v61, v76, v77 row_newbcast:1 row_mask:0xf bank_mask:0xf\n"
    "v_mfma_f32_16x16x32_bf16 v[44:47], a[64:67], v[28:31], v[44:47]\n"
    "v_exp_f32 v60, v60\n"
    "v_mfma_f32_16x16x32_bf16 v[48:51], a[68:71], v[28:31], v[48:51]\n"
    "s_waitcnt lgkmcnt(2)\n"
    "ds_read2st64_b64 v[28:31], v4 offset0:98 offset1:99\n"
    "v_fma_f32 v60, v60, v73, v73\n"
    "v_mfma_f32_16x16x32_bf16 v[52:55], v[20:23], v[32:35], v[52:55]\n"
    "s_add_u32 m0, %[ldswd], 0\n"
    "s_add_u32 s68, s66, 36864\n"
    "buffer_load_dwordx4 v0, %[srdw], s68 offen sc1 lds\n"
    "v_mfma_f32_16x16x32_bf16 v[56:59], v[32:35], v[32:35], v[56:59]\n"
    "s_waitcnt lgkmcnt(2)\n"
    "ds_read2st64_b64 v[32:35], v5 offset0:92 offset1:93\n"
    "v_fmac_f32_dpp v65, v76, v77 row_newbcast:5 row_mask:0xf bank_mask:0xf\n"
    "v_mfma_f32_16x16x32_bf16 v[40:43], a[72:75], v[36:39], v[40:43]\n"
    "v_rcp_f32 v60, v60\n"
    "v_mfma_f32_16x16x32_bf16 v[44:47], a[76:79], v[36:39], v[44:47]\n"
    "v_mul_f32 v60, v60, v64\n"
    "v_mul_f32 v65, v61, v65\n"
    "v_mfma_f32_16x16x32_bf16 v[48:51], a[80:83], v[36:39], v[48:51]\n"
    "s_waitcnt lgkmcnt(2)\n"
    "ds_read2st64_b64 v[36:39], v4 offset0:100 offset1:101\n"
    "v_mul_f32 v61, v72, v61\n"
    "v_mfma_f32_16x16x32_bf16 v[52:55], v[20:23], v[24:27], v[52:55]\n"
    "buffer_load_dwordx4 v0, %[srdw], s68 offen offset:1024 sc1 lds\n"
    "v_fmac_f32_dpp v62, v76, v77 row_newbcast:2 row_mask:0xf bank_mask:0xf\n"
    "v_mfma_f32_16x16x32_bf16 v[56:59], v[24:27], v[24:27], v[56:59]\n"
    "s_waitcnt lgkmcnt(2)\n"
    "ds_read2st64_b64 v[24:27], v4 offset0:102 offset1:103\n"
    "v_fmac_f32_dpp v66, v76, v77 row_newbcast:6 row_mask:0xf bank_mask:0xf\n"
    "v_mfma_f32_16x16x32_bf16 v[40:43], a[84:87], v[28:31], v[40:43]\n"
    "v_exp_f32 v61, v61\n"
    "v_mfma_f32_16x16x32_bf16 v[44:47], a[88:91], v[28:31], v[44:47]\n"
    "buffer_load_dwordx4 v0, %[srdw], s68 offen offset:2048 sc1 lds\n"
    "v_fma_f32 v61, v61, v73, v73\n"
    "v_mfma_f32_16x16x32_bf16 v[48:51], a[92:95], v[28:31], v[48:51]\n"
    "s_waitcnt lgkmcnt(2)\n"
    "ds_read2st64_b64 v[28:31], v4 offset0:104 offset1:105\n"
    "v_mul_f32 v66, v62, v66\n"
    "v_mfma_f32_16x16x32_bf16 v[52:55], v[20:23], v[32:35], v[52:55]\n"
    "v_rcp_f32 v61, v61\n"
    "v_mfma_f32_16x16x32_bf16 v[56:59], v[32:35], v[32:35], v[56:59]\n"
    "s_waitcnt lgkmcnt(2)\n"
    "ds_read2st64_b64 v[32:35], v4 offset0:106 offset1:107\n"
    "v_mul_f32 v61, v61, v65\n"
    "v_mfma_f32_16x16x32_bf16 v[40:43], a[96:99], v[36:39], v[40:43]\n"
    "v_cvt_pk_bf16_f32 v60, v60, v61\n"
    "v_mul_f32 v62, v72, v62\n"
    "v_mfma_f32_16x16x32_bf16 v[44:47], a[100:103], v[36:39], v[44:47]\n"
    "buffer_load_dwordx4 v0, %[srdw], s68 offen offset:3072 sc1 lds\n"
    "v_cndmask_b32_e64 v53, v56, v57, s[48:49]\n"
    "v_mfma_f32_16x16x32_bf16 v[48:51], a[104:107], v[36:39], v[48:51]\n"
    "s_waitcnt lgkmcnt(2)\n"
    "ds_read2st64_b64 v[36:39], v4 offset0:108 offset1:109\n"
    "v_cndmask_b32_e64 v53, v53, v58, s[50:51]\n"
    "v_mfma_f32_16x16x32_bf16 v[40:43], a[108:111], v[24:27], v[40:43]\n"
    "s_add_u32 s83, s82, -4096\n"
    "v_cndmask_b32_e64 v53, v53, v59, s[52:53]\n"
    "v_fmac_f32_dpp v63, v76, v77 row_newbcast:3 row_mask:0xf bank_mask:0xf\n"
    "v_mfma_f32_16x16x32_bf16 v[44:47], a[112:115], v[24:27], v[44:47]\n"
    "ds_write_b64 v8, v[52:53] offset:0\n"
    "v_mfma_f32_16x16x32_bf16 v[48:51], a[116:119], v[24:27], v[48:51]\n"
    "s_waitcnt lgkmcnt(3)\n"
    "ds_read2st64_b64 v[112:115], v4 offset0:120 offset1:121\n"
    "ds_read2st64_b64 v[116:119], v4 offset0:122 offset1:123\n"
    "v_mfma_f32_16x16x32_bf16 v[40:43], a[120:123], v[28:31], v[40:43]\n"
    "ds_read2st64_b64 v[24:27], v4 offset0:110 offset1:111\n"
    "buffer_load_dwordx4 v[144:147], v0, %[srdw], s78 offen offset:0 sc1\n"
    "v_mfma_f32_16x16x32_bf16 v[44:47], a[124:127], v[28:31], v[44:47]\n"
    "v_exp_f32 v62, v62\n"
    "v_mfma_f32_16x16x32_bf16 v[48:51], a[128:131], v[28:31], v[48:51]\n"
    "s_waitcnt lgkmcnt(5)\n"
    "ds_read2st64_b64 v[28:31], v4 offset0:112 offset1:113\n"
    "v_fma_f32 v62, v62, v73, v73\n"
    "v_mfma_f32_16x16x32_bf16 v[40:43], a[132:135], v[32:35], v[40:43]\n"
    "s_add_u32 m0, %[ldswd], 4096\n"
    "s_add_u32 s68, s66, 40960\n"
    "buffer_load_dwordx4 v0, %[srdw], s68 offen sc1 lds\n"
    "v_mfma_f32_16x16x32_bf16 v[44:47], a[136:139], v[32:35], v[44:47]\n"
    "v_rcp_f32 v62, v62\n"
    "v_mfma_f32_16x16x32_bf16 v[48:51], a[140:143], v[32:35], v[48:51]\n"
    "s_waitcnt lgkmcnt(5)\n"
    "ds_read2st64_b64 v[32:35], v4 offset0:114 offset1:115\n"
    "v_mul_f32 v62, v62, v66\n"
    "v_mfma_f32_16x16x32_bf16 v[40:43], a[144:147], v[36:39], v[40:43]\n"
    "v_fmac_f32_dpp v67, v76, v77 row_newbcast:7 row_mask:0xf bank_mask:0xf\n"
    "v_mul_f32 v67, v63, v67\n"
    "v_mfma_f32_16x16x32_bf16 v[44:47], a[148:151], v[36:39], v[44:47]\n"
    "buffer_load_dwordx4 v0, %[srdw], s68 offen offset:1024 sc1 lds\n"
    "v_mul_f32 v63, v72, v63\n"
    "v_mfma_f32_16x16x32_bf16 v[48:51], a[152:155], v[36:39], v[48:51]\n"
    "s_waitcnt lgkmcnt(2) vmcnt(14)\n"
    "ds_read2st64_b64 v[36:39], v4 offset0:116 offset1:117\n"
    "v_mul_f32 v70, v68, v70\n"
    "v_mfma_f32_16x16x32_bf16 v[40:43], a[156:159], v[24:27], v[40:43]\n"
    "v_cmp_ne_u32 vcc, %[tag], v80\n"
    "s_nop 0\n"
    "s_and_b32 vcc_hi, vcc_hi, 0xff\n"
    "s_cmp_lg_u64 vcc, 0\n"
    "s_cbranch_scc1 L15_%=\n"
    "L16_%=:\n"
    "v_mfma_f32_16x16x32_bf16 v[44:47], a[160:163], v[24:27], v[44:47]\n"
    "v_exp_f32 v63, v63\n"
    "v_mfma_f32_16x16x32_bf16 v[48:51], a[164:167], v[24:27], v[48:51]\n"
    "s_waitcnt lgkmcnt(2)\n"
    "ds_read2st64_b64 v[24:27], v4 offset0:118 offset1:119\n"
    "v_fma_f32 v63, v63, v73, v73\n"
    "v_mfma_f32_16x16x32_bf16 v[40:43], a[168:171], v[28:31], v[40:43]\n"
    "s_add_u32 m0, %[ldsx], 21504\n"
    "s_add_u32 s68, %[x], 6291456\n"
    "buffer_load_dwordx4 v3, %[srdw], s68 offen sc1 lds\n"
    "v_mfma_f32_16x16x32_bf16 v[44:47], a[172:175], v[28:31], v[44:47]\n"
    "v_rcp_f32 v63, v63\n"
    "v_mfma_f32_16x16x32_bf16 v[48:51], a[176:179], v[28:31], v[48:51]\n"
    "s_waitcnt lgkmcnt(2)\n"
    "v_mul_f32 v63, v63, v67\n"
    "v_cvt_pk_bf16_f32 v61, v62, v63\n"
    "v_mfma_f32_16x16x32_bf16 v[40:43], a[180:183], v[32:35], v[40:43]\n"
    "buffer_load_dwordx4 v3, %[srdw], s68 offen offset:1024 sc1 lds\n"
    "v_mul_f32 v68, v72, v68\n"
    "v_mfma_f32_16x16x32_bf16 v[44:47], a[184:187], v[32:35], v[44:47]\n"
    "v_exp_f32 v68, v68\n"
    "v_mfma_f32_16x16x32_bf16 v[48:51], a[188:191], v[32:35], v[48:51]\n"
    "s_waitcnt lgkmcnt(1)\n"
    "v_fma_f32 v68, v68, v73, v73\n"
    "v_mul_f32 v71, v69, v71\n"
    "v_mfma_f32_16x16x32_bf16 v[40:43], a[192:195], v[36:39], v[40:43]\n"
    "v_rcp_f32 v68, v68\n"
    "v_mfma_f32_16x16x32_bf16 v[44:47], a[196:199], v[36:39], v[44:47]\n"
    "s_waitcnt lgkmcnt(0)\n"
    "s_barrier\n"
    "v_mfma_f32_16x16x32_bf16 v[48:51], a[200:203], v[36:39], v[48:51]\n"
    "ds_read2st64_b64 v[28:31], v4 offset0:0 offset1:1\n"
    "ds_read2st64_b64 v[32:35], v4 offset0:2 offset1:3\n"
    "v_mfma_f32_16x16x32_bf16 v[40:43], a[204:207], v[24:27], v[40:43]\n"
    "ds_read2st64_b64 v[36:39], v4 offset0:4 offset1:5\n"
    "buffer_load_dwordx4 v3, %[srdw], s68 offen offset:2048 sc1 lds\n"
    "v_mfma_f32_16x16x32_bf16 v[44:47], a[208:211], v[24:27], v[44:47]\n"
    "s_lshl1_add_u32 s84, s45, %[wd]\n"
    "ds_read_b128 v[52:55], v9 offset:0\n"
    "ds_read_b128 v[56:59], v9 offset:16\n"
    "v_mfma_f32_16x16x32_bf16 v[48:51], a[212:215], v[24:27], v[48:51]\n"
    "s_add_u32 s85, s84, s45\n"
    "ds_read2st64_b64 v[24:27], v4 offset0:6 offset1:7\n"
    "s_add_u32 s86, s85, 8192\n"
    "v_mfma_f32_16x16x32_bf16 v[40:43], a[216:219], v[112:115], v[40:43]\n"
    "s_add_u32 s45, s45, %[hld]\n"
    "buffer_load_dwordx4 v3, %[srdw], s68 offen offset:3072 sc1 lds\n"
    "v_mfma_f32_16x16x32_bf16 v[44:47], a[220:223], v[112:115], v[44:47]\n"
    "v_mul_f32 v68, v68, v70\n"
    "v_mul_f32 v69, v72, v69\n"
    "v_mfma_f32_16x16x32_bf16 v[48:51], a[224:227], v[112:115], v[48:51]\n"
    "v_exp_f32 v69, v69\n"
    "v_mfma_f32_16x16x32_bf16 v[40:43], a[228:231], v[116:119], v[40:43]\n"
    "v_fma_f32 v69, v69, v73, v73\n"
    "v_mfma_f32_16x16x32_bf16 v[44:47], a[232:235], v[116:119], v[44:47]\n"
    "v_rcp_f32 v69, v69\n"
    "v_mfma_f32_16x16x32_bf16 v[48:51], a[236:239], v[116:119], v[48:51]\n"
    "s_add_u32 s87, %[hst], 11141120\n"
    "s_add_u32 m0, %[ldsx], 25600\n"
    "s_add_u32 s68, %[x], 6295552\n"
    "buffer_load_dwordx4 v3, %[srdw], s68 offen sc1 lds\n"
    "v_mul_f32 v69, v69, v71\n"
    "v_cvt_pk_bf16_f32 v62, v68, v69\n"
    "v_permlane32_swap_b32 v61, v63\n"
    "s_nop 1\n"
    "v_permlane16_swap_b32 v62, v63\n"
    "v_permlane16_swap_b32 v60, v61\n"
    "buffer_store_dwordx4 v[60:63], v10, %[srdw], s81 offen sc1\n"
    "s_waitcnt lgkmcnt(5)\n"
    "s_nop 0\n"
    "v_mfma_f32_16x16x32_bf16 v[60:63], a[0:3], v[28:31], 0\n"
    "s_waitcnt lgkmcnt(2)\n"
    "v_add_f32 v52, v52, v54\n"
    "v_add_f32 v53, v53, v55\n"
    "v_mfma_f32_16x16x32_bf16 v[64:67], a[4:7], v[28:31], 0\n"
    "s_waitcnt lgkmcnt(1)\n"
    "v_add_f32 v56, v56, v58\n"
    "v_add_f32 v57, v57, v59\n"
    "v_mfma_f32_16x16x32_bf16 v[68:71], a[8:11], v[28:31], 0\n"
    "ds_read2st64_b64 v[28:31], v5 offset0:0 offset1:1\n"
    "v_add_f32 v52, v52, v56\n"
    "v_mfma_f32_16x16x32_bf16 v[60:63], a[12:15], v[32:35], v[60:63]\n"
    "v_add_f32 v53, v53, v57\n"
    "v_mul_f32 v77, 0xbacccccd, v52\n"
    "v_mfma_f32_16x16x32_bf16 v[64:67], a[16:19], v[32:35], v[64:67]\n"
    "v_mul_f32 v53, 0x3acccccd, v53\n"
    "v_fma_f32 v53, -v77, v77, v53\n"
    "v_mfma_f32_16x16x32_bf16 v[68:71], a[20:23], v[32:35], v[68:71]\n"
    "ds_read2st64_b64 v[32:35], v4 offset0:8 offset1:9\n"
    "v_max_f32 v53, 0, v53\n"
    "v_mfma_f32_16x16x32_bf16 v[60:63], a[24:27], v[36:39], v[60:63]\n"
    "v_add_f32 v73, 0x3727c5ac, v53\n"
    "v_fmac_f32_dpp v48, v76, v77 row_newbcast:8 row_mask:0xf bank_mask:0xf\n"
    "v_mfma_f32_16x16x32_bf16 v[64:67], a[28:31], v[36:39], v[64:67]\n"
    "v_rsq_f32 v52, v73\n"
    "v_mfma_f32_16x16x32_bf16 v[68:71], a[32:35], v[36:39], v[68:71]\n"
    "s_waitcnt lgkmcnt(2)\n"
    "ds_read2st64_b64 v[36:39], v5 offset0:2 offset1:3\n"
    "v_mul_f32 v72, 0xbfb8aa3b, v52\n"
    "v_mfma_f32_16x16x32_bf16 v[60:63], a[36:39], v[24:27], v[60:63]\n"
    "s_add_u32 s88, %[hst], 14286848\n"
    "s_add_u32 m0, %[ldsx], 43008\n"
    "s_add_u32 s68, s42, 6619136\n"
    "buffer_load_dwordx4 v3, %[srdw], s68 offen sc1 lds\n"
    "v_mfma_f32_16x16x32_bf16 v[64:67], a[40:43], v[24:27], v[64:67]\n"
    "buffer_load_dwordx4 v3, %[srdw], s68 offen offset:1024 sc1 lds\n"
    "v_mov_b32 v120, -1\n"
    "v_mfma_f32_16x16x32_bf16 v[68:71], a[44:47], v[24:27], v[68:71]\n"
    "s_waitcnt lgkmcnt(2)\n"
    "ds_read2st64_b64 v[24:27], v4 offset0:10 offset1:11\n"
    "v_mov_b32 v121, -1\n"
    "v_mfma_f32_16x16x32_bf16 v[52:55], v[20:23], v[28:31], 0\n"
    "v_mov_b32 v122, -1\n"
    "v_mov_b32 v123, -1\n"
    "v_mfma_f32_16x16x32_bf16 v[56:59], v[28:31], v[28:31], 0\n"
    "s_waitcnt lgkmcnt(2)\n"
    "ds_read2st64_b64 v[28:31], v5 offset0:4 offset1:5\n"
    "v_fmac_f32_dpp v49, v76, v77 row_newbcast:9 row_mask:0xf bank_mask:0xf\n"
    "v_mfma_f32_16x16x32_bf16 v[60:63], a[48:51], v[32:35], v[60:63]\n"
    "buffer_load_dwordx4 v3, %[srdw], s68 offen offset:2048 sc1 lds\n"
    "v_fmac_f32_dpp v50, v76, v77 row_newbcast:10 row_mask:0xf bank_mask:0xf\n"
    "v_mfma_f32_16x16x32_bf16 v[64:67], a[52:55], v[32:35], v[64:67]\n"
    "v_fmac_f32_dpp v51, v76, v77 row_newbcast:11 row_mask:0xf bank_mask:0xf\n"
    "v_fmac_f32_dpp v40, v76, v77 row_newbcast:0 row_mask:0xf bank_mask:0xf\n"
    "v_mfma_f32_16x16x32_bf16 v[68:71], a[56:59], v[32:35], v[68:71]\n"
    "s_waitcnt lgkmcnt(2)\n"
    "ds_read2st64_b64 v[32:35], v4 offset0:12 offset1:13\n"
    "v_fmac_f32_dpp v44, v76, v77 row_newbcast:4 row_mask:0xf bank_mask:0xf\n"
    "v_mfma_f32_16x16x32_bf16 v[52:55], v[20:23], v[36:39], v[52:55]\n"
    "v_permlane16_swap_b32 v48, v50\n"
    "v_mfma_f32_16x16x32_bf16 v[56:59], v[36:39], v[36:39], v[56:59]\n"
    "s_waitcnt lgkmcnt(2)\n"
    "ds_read2st64_b64 v[36:39], v5 offset0:6 offset1:7\n"
    "v_mul_f32 v44, v40, v44\n"
    "v_mfma_f32_16x16x32_bf16 v[60:63], a[60:63], v[24:27], v[60:63]\n"
    "buffer_load_dwordx4 v3, %[srdw], s68 offen offset:3072 sc1 lds\n"
    "v_mul_f32 v40, v72, v40\n"
    "v_mfma_f32_16x16x32_bf16 v[64:67], a[64:67], v[24:27], v[64:67]\n"
    "v_permlane16_swap_b32 v49, v51\n"
    "v_mfma_f32_16x16x32_bf16 v[68:71], a[68:71], v[24:27], v[68:71]\n"
    "s_waitcnt lgkmcnt(2)\n"
    "ds_read2st64_b64 v[24:27], v4 offset0:14 offset1:15\n"
    "v_fmac_f32_dpp v41, v76, v77 row_newbcast:1 row_mask:0xf bank_mask:0xf\n"
    "v_mfma_f32_16x16x32_bf16 v[52:55], v[20:23], v[28:31], v[52:55]\n"
    "v_exp_f32 v40, v40\n"
    "v_mfma_f32_16x16x32_bf16 v[56:59], v[28:31], v[28:31], v[56:59]\n"
    "s_waitcnt lgkmcnt(2)\n"
    "ds_read2st64_b64 v[28:31], v5 offset0:8 offset1:9\n"
    "v_fma_f32 v40, v40, v73, v73\n"
    "v_mfma_f32_16x16x32_bf16 v[60:63], a[72:75], v[32:35], v[60:63]\n"
    "s_add_u32 m0, %[ldsx], 47104\n"
    "s_add_u32 s68, s42, 6623232\n"
    "buffer_load_dwordx4 v3, %[srdw], s68 offen sc1 lds\n"
    "v_mfma_f32_16x16x32_bf16 v[64:67], a[76:79], v[32:35], v[64:67]\n"
    "v_rcp_f32 v40, v40\n"
    "v_mfma_f32_16x16x32_bf16 v[68:71], a[80:83], v[32:35], v[68:71]\n"
    "s_waitcnt lgkmcnt(2)\n"
    "ds_read2st64_b64 v[32:35], v4 offset0:16 offset1:17\n"
    "v_mul_f32 v40, v40, v44\n"
    "v_mfma_f32_16x16x32_bf16 v[52:55], v[20:23], v[36:39], v[52:55]\n"
    "v_fmac_f32_dpp v45, v76, v77 row_newbcast:5 row_mask:0xf bank_mask:0xf\n"
    "v_mul_f32 v45, v41, v45\n"
    "v_mfma_f32_16x16x32_bf16 v[56:59], v[36:39], v[36:39], v[56:59]\n"
    "s_waitcnt lgkmcnt(2)\n"
    "ds_read2st64_b64 v[36:39], v4 offset0:18 offset1:19\n"
    "v_mul_f32 v41, v72, v41\n"
    "v_mfma_f32_16x16x32_bf16 v[60:63], a[84:87], v[24:27], v[60:63]\n"
    "buffer_store_dwordx4 v[120:123], v10, %[srdw], s88 offen sc1\n"
    "v_mfma_f32_16x16x32_bf16 v[64:67], a[88:91], v[24:27], v[64:67]\n"
    "v_exp_f32 v41, v41\n"
    "v_mfma_f32_16x16x32_bf16 v[68:71], a[92:95], v[24:27], v[68:71]\n"
    "s_waitcnt lgkmcnt(2)\n"
    "ds_read2st64_b64 v[24:27], v4 offset0:20 offset1:21\n"
    "v_fma_f32 v41, v41, v73, v73\n"
    "v_mfma_f32_16x16x32_bf16 v[52:55], v[20:23], v[28:31], v[52:55]\n"
    "kx_a3:\n"
    "s_cbranch_execz kx_a7\n"
    "buffer_load_dwordx4 v[140:143], v0, %[srdw], s82 offen offset:3072 sc1\n"
    "v_fmac_f32_dpp v42, v76, v77 row_newbcast:2 row_mask:0xf bank_mask:0xf\n"
    "v_mfma_f32_16x16x32_bf16 v[56:59], v[28:31], v[28:31], v[56:59]\n"
    "s_waitcnt lgkmcnt(2)\n"
    "ds_read2st64_b64 v[28:31], v4 offset0:22 offset1:23\n"
    "v_fmac_f32_dpp v46, v76, v77 row_newbcast:6 row_mask:0xf bank_mask:0xf\n"
    "v_mfma_f32_16x16x32_bf16 v[60:63], a[96:99], v[32:35], v[60:63]\n"
    "v_rcp_f32 v41, v41\n"
    "v_mfma_f32_16x16x32_bf16 v[64:67], a[100:103], v[32:35], v[64:67]\n"
    "buffer_load_dwordx4 v[136:139], v0, %[srdw], s82 offen offset:2048 sc1\n"
    "v_mul_f32 v41, v41, v45\n"
    "v_mfma_f32_16x16x32_bf16 v[68:71], a[104:107], v[32:35], v[68:71]\n"
    "s_waitcnt lgkmcnt(2)\n"
    "ds_read2st64_b64 v[32:35], v4 offset0:24 offset1:25\n"
    "v_cndmask_b32_e64 v53, v56, v57, s[48:49]\n"
    "v_mfma_f32_16x16x32_bf16 v[60:63], a[108:111], v[36:39], v[60:63]\n"
    "s_add_u32 s89, s86, -4096\n"
    "v_cndmask_b32_e64 v53, v53, v58, s[50:51]\n"
    "v_cndmask_b32_e64 v53, v53, v59, s[52:53]\n"
    "v_mfma_f32_16x16x32_bf16 v[64:67], a[112:115], v[36:39], v[64:67]\n"
    "ds_write_b64 v8, v[52:53] offset:512\n"
    "v_mfma_f32_16x16x32_bf16 v[68:71], a[116:119], v[36:39], v[68:71]\n"
    "s_waitcnt lgkmcnt(3)\n"
    "ds_read2st64_b64 v[112:115], v4 offset0:36 offset1:37\n"
    "ds_read2st64_b64 v[116:119], v4 offset0:38 offset1:39\n"
    "v_mfma_f32_16x16x32_bf16 v[60:63], a[120:123], v[24:27], v[60:63]\n"
    "s_add_u32 s90, s43, 15073280\n"
    "ds_read2st64_b64 v[36:39], v4 offset0:26 offset1:27\n"
    "v_cvt_pk_bf16_f32 v40, v40, v41\n"
    "v_mfma_f32_16x16x32_bf16 v[64:67], a[124:127], v[24:27], v[64:67]\n"
    "buffer_store_dwordx4 v[120:123], v10, %[srdw], s90 offen sc1\n"
    "v_mfma_f32_16x16x32_bf16 v[68:71], a[128:131], v[24:27], v[68:71]\n"
    "s_waitcnt lgkmcnt(5)\n"
    "ds_read2st64_b64 v[24:27], v4 offset0:28 offset1:29\n"
    "v_mul_f32 v46, v42, v46\n"
    "v_mfma_f32_16x16x32_bf16 v[60:63], a[132:135], v[28:31], v[60:63]\n"
    "buffer_load_dwordx4 v[132:135], v0, %[srdw], s82 offen offset:1024 sc1\n"
    "v_mul_f32 v42, v72, v42\n"
    "v_mfma_f32_16x16x32_bf16 v[64:67], a[136:139], v[28:31], v[64:67]\n"
    "v_exp_f32 v42, v42\n"
    "v_mfma_f32_16x16x32_bf16 v[68:71], a[140:143], v[28:31], v[68:71]\n"
    "s_waitcnt lgkmcnt(5)\n"
    "ds_read2st64_b64 v[28:31], v4 offset0:30 offset1:31\n"
    "v_fma_f32 v42, v42, v73, v73\n"
    "v_mfma_f32_16x16x32_bf16 v[60:63], a[144:147], v[32:35], v[60:63]\n"
    "buffer_load_dwordx4 v[128:131], v0, %[srdw], s82 offen offset:0 sc1\n"
    "v_fmac_f32_dpp v43, v76, v77 row_newbcast:3 row_mask:0xf bank_mask:0xf\n"
    "v_mfma_f32_16x16x32_bf16 v[64:67], a[148:151], v[32:35], v[64:67]\n"
    "v_rcp_f32 v42, v42\n"
    "v_mfma_f32_16x16x32_bf16 v[68:71], a[152:155], v[32:35], v[68:71]\n"
    "s_waitcnt lgkmcnt(2)\n"
    "ds_read2st64_b64 v[32:35], v4 offset0:32 offset1:33\n"
    "v_mul_f32 v42, v42, v46\n"
    "v_mfma_f32_16x16x32_bf16 v[60:63], a[156:159], v[36:39], v[60:63]\n"
    "v_fmac_f32_dpp v47, v76, v77 row_newbcast:7 row_mask:0xf bank_mask:0xf\n"
    "v_mul_f32 v47, v43, v47\n"
    "v_mfma_f32_16x16x32_bf16 v[64:67], a[160:163], v[36:39], v[64:67]\n"
    "v_mul_f32 v43, v72, v43\n"
    "v_mul_f32 v50, v48, v50\n"
    "v_mfma_f32_16x16x32_bf16 v[68:71], a[164:167], v[36:39], v[68:71]\n"
    "s_waitcnt lgkmcnt(2)\n"
    "ds_read2st64_b64 v[36:39], v4 offset0:34 offset1:35\n"
    "v_mul_f32 v48, v72, v48\n"
    "v_mfma_f32_16x16x32_bf16 v[60:63], a[168:171], v[24:27], v[60:63]\n"
    "buffer_load_dwordx4 a[252:255], v0, %[srdw], s83 offen offset:3072 sc1\n"
    "v_mul_f32 v51, v49, v51\n"
    "v_mfma_f32_16x16x32_bf16 v[64:67], a[172:175], v[24:27], v[64:67]\n"
    "v_exp_f32 v43, v43\n"
    "v_mfma_f32_16x16x32_bf16 v[68:71], a[176:179], v[24:27], v[68:71]\n"
    "s_waitcnt lgkmcnt(2)\n"
    "v_fma_f32 v43, v43, v73, v73\n"
    "v_mul_f32 v49, v72, v49\n"
    "v_mfma_f32_16x16x32_bf16 v[60:63], a[180:183], v[28:31], v[60:63]\n"
    "buffer_load_dwordx4 a[248:251], v0, %[srdw], s83 offen offset:2048 sc1\n"
    "v_mfma_f32_16x16x32_bf16 v[64:67], a[184:187], v[28:31], v[64:67]\n"
    "s_add_u32 s91, %[f4p], 1179648\n"
    "s_add_u32 s92, %[f3p], 122880\n"
    "v_mfma_f32_16x16x32_bf16 v[68:71], a[188:191], v[28:31], v[68:71]\n"
    "s_waitcnt lgkmcnt(1)\n"
    "v_rcp_f32 v43, v43\n"
    "v_mfma_f32_16x16x32_bf16 v[60:63], a[192:195], v[32:35], v[60:63]\n"
    "v_mul_f32 v43, v43, v47\n"
    "v_cvt_pk_bf16_f32 v41, v42, v43\n"
    "v_mfma_f32_16x16x32_bf16 v[64:67], a[196:199], v[32:35], v[64:67]\n"
    "buffer_load_dwordx4 a[244:247], v0, %[srdw], s83 offen offset:1024 sc1\n"
    "v_mfma_f32_16x16x32_bf16 v[68:71], a[200:203], v[32:35], v[68:71]\n"
    "s_waitcnt lgkmcnt(0)\n"
    "s_waitcnt vmcnt(15)\n"
    "s_barrier\n"
    "v_mfma_f32_16x16x32_bf16 v[60:63], a[204:207], v[36:39], v[60:63]\n"
    "ds_read2st64_b64 v[24:27], v4 offset0:42 offset1:43\n"
    "ds_read2st64_b64 v[28:31], v4 offset0:44 offset1:45\n"
    "v_mfma_f32_16x16x32_bf16 v[64:67], a[208:211], v[36:39], v[64:67]\n"
    "ds_read2st64_b64 v[32:35], v4 offset0:46 offset1:47\n"
    "ds_read_b128 v[52:55], v9 offset:512\n"
    "v_mfma_f32_16x16x32_bf16 v[68:71], a[212:215], v[36:39], v[68:71]\n"
    "s_add_u32 s56, s89, -4096\n"
    "ds_read2st64_b64 v[36:39], v4 offset0:48 offset1:49\n"
    "ds_read_b128 v[56:59], v9 offset:528\n"
    "v_mfma_f32_16x16x32_bf16 v[60:63], a[216:219], v[112:115], v[60:63]\n"
    "s_mov_b64 exec, s[94:95]\n"
    "buffer_store_dword v2, off, %[srdw], s91 sc1\n"
    "s_mov_b64 exec, -1\n"
    "v_mfma_f32_16x16x32_bf16 v[64:67], a[220:223], v[112:115], v[64:67]\n"
    "s_mov_b64 exec, s[94:95]\n"
    "buffer_store_dword v2, off, %[srdw], s92 sc1\n"
    "s_mov_b64 exec, -1\n"
    "v_mfma_f32_16x16x32_bf16 v[68:71], a[224:227], v[112:115], v[68:71]\n"
    "s_add_u32 m0, %[ldsx], 0\n"
    "s_add_u32 s68, %[x], 6946816\n"
    "buffer_load_dwordx4 v3, %[srdw], s68 offen sc1 lds\n"
    "v_mfma_f32_16x16x32_bf16 v[60:63], a[228:231], v[116:119], v[60:63]\n"
    "v_exp_f32 v48, v48\n"
    "v_mfma_f32_16x16x32_bf16 v[64:67], a[232:235], v[116:119], v[64:67]\n"
    "buffer_load_dwordx4 v3, %[srdw], s68 offen offset:1024 sc1 lds\n"
    "v_fma_f32 v48, v48, v73, v73\n"
    "v_mfma_f32_16x16x32_bf16 v[68:71], a[236:239], v[116:119], v[68:71]\n"
    "s_add_u32 s57, s43, 11927552\n"
    "v_rcp_f32 v48, v48\n"
    "v_exp_f32 v49, v49\n"
    "v_mul_f32 v48, v48, v50\n"
    "v_fma_f32 v49, v49, v73, v73\n"
    "v_rcp_f32 v49, v49\n"
    "v_permlane32_swap_b32 v41, v43\n"
    "v_mul_f32 v49, v49, v51\n"
    "v_cvt_pk_bf16_f32 v42, v48, v49\n"
    "v_permlane16_swap_b32 v40, v41\n"
    "s_nop 0\n"
    "v_permlane16_swap_b32 v42, v43\n"
    "buffer_store_dwordx4 v[40:43], v10, %[srdw], s87 offen sc1\n"
    "s_waitcnt lgkmcnt(5)\n"
    "s_nop 0\n"
    "v_mfma_f32_16x16x32_bf16 v[40:43], a[0:3], v[24:27], 0\n"
    "s_waitcnt lgkmcnt(2)\n"
    "v_add_f32 v52, v52, v54\n"
    "v_add_f32 v53, v53, v55\n"
    "v_mfma_f32_16x16x32_bf16 v[44:47], a[4:7], v[24:27], 0\n"
    "s_waitcnt lgkmcnt(0)\n"
    "v_add_f32 v56, v56, v58\n"
    "v_add_f32 v57, v57, v59\n"
    "v_mfma_f32_16x16x32_bf16 v[48:51], a[8:11], v[24:27], 0\n"
    "ds_read2st64_b64 v[24:27], v5 offset0:42 offset1:43\n"
    "v_add_f32 v52, v52, v56\n"
    "v_mfma_f32_16x16x32_bf16 v[40:43], a[12:15], v[28:31], v[40:43]\n"
    "v_add_f32 v53, v53, v57\n"
    "v_mul_f32 v77, 0xbacccccd, v52\n"
    "v_mfma_f32_16x16x32_bf16 v[44:47], a[16:19], v[28:31], v[44:47]\n"
    "v_mul_f32 v53, 0x3acccccd, v53\n"
    "v_fma_f32 v53, -v77, v77, v53\n"
    "v_fmac_f32_dpp v68, v76, v77 row_newbcast:8 row_mask:0xf bank_mask:0xf\n"
    "v_mfma_f32_16x16x32_bf16 v[48:51], a[20:23], v[28:31], v[48:51]\n"
    "ds_read2st64_b64 v[28:31], v4 offset0:50 offset1:51\n"
    "v_max_f32 v53, 0, v53\n"
    "v_fmac_f32_dpp v69, v76, v77 row_newbcast:9 row_mask:0xf bank_mask:0xf\n"
    "v_mfma_f32_16x16x32_bf16 v[40:43], a[24:27], v[32:35], v[40:43]\n"
    "v_add_f32 v73, 0x3727c5ac, v53\n"
    "buffer_load_dwordx4 v3, %[srdw], s68 offen offset:2048 sc1 lds\n"
    "v_fmac_f32_dpp v70, v76, v77 row_newbcast:10 row_mask:0xf bank_mask:0xf\n"
    "v_mfma_f32_16x16x32_bf16 v[44:47], a[28:31], v[32:35], v[44:47]\n"
    "v_rsq_f32 v52, v73\n"
    "v_fmac_f32_dpp v71, v76, v77 row_newbcast:11 row_mask:0xf bank_mask:0xf\n"
    "v_mfma_f32_16x16x32_bf16 v[48:51], a[32:35], v[32:35], v[48:51]\n"
    "ds_read2st64_b64 v[32:35], v5 offset0:44 offset1:45\n"
    "v_mul_f32 v72, 0xbfb8aa3b, v52\n"
    "v_fmac_f32_dpp v60, v76, v77 row_newbcast:0 row_mask:0xf bank_mask:0xf\n"
    "v_mfma_f32_16x16x32_bf16 v[40:43], a[36:39], v[36:39], v[40:43]\n"
    "s_add_u32 s58, %[xv], 15859712\n"
    "buffer_load_dwordx4 v3, %[srdw], s68 offen offset:3072 sc1 lds\n"
    "v_fmac_f32_dpp v64, v76, v77 row_newbcast:4 row_mask:0xf bank_mask:0xf\n"
    "v_mfma_f32_16x16x32_bf16 v[44:47], a[40:43], v[36:39], v[44:47]\n"
    "buffer_store_dwordx4 v[120:123], v18, %[srdw], s58 offen sc1\n"
    "v_mfma_f32_16x16x32_bf16 v[48:51], a[44:47], v[36:39], v[48:51]\n"
    "s_waitcnt lgkmcnt(2)\n"
    "ds_read2st64_b64 v[36:39], v4 offset0:52 offset1:53\n"
    "v_permlane16_swap_b32 v68, v70\n"
    "v_mfma_f32_16x16x32_bf16 v[52:55], v[20:23], v[24:27], 0\n"
    "v_permlane16_swap_b32 v69, v71\n"
    "v_mul_f32 v64, v60, v64\n"
    "v_mfma_f32_16x16x32_bf16 v[56:59], v[24:27], v[24:27], 0\n"
    "s_waitcnt lgkmcnt(2)\n"
    "ds_read2st64_b64 v[24:27], v5 offset0:46 offset1:47\n"
    "v_mul_f32 v60, v72, v60\n"
    "v_fmac_f32_dpp v61, v76, v77 row_newbcast:1 row_mask:0xf bank_mask:0xf\n"
    "v_mfma_f32_16x16x32_bf16 v[40:43], a[48:51], v[28:31], v[40:43]\n"
    "s_add_u32 m0, %[ldsx], 4096\n"
    "s_add_u32 s68, %[x], 6950912\n"
    "buffer_load_dwordx4 v3, %[srdw], s68 offen sc1 lds\n"
    "v_fmac_f32_dpp v65, v76, v77 row_newbcast:5 row_mask:0xf bank_mask:0xf\n"
    "v_mfma_f32_16x16x32_bf16 v[44:47], a[52:55], v[28:31], v[44:47]\n"
    "v_exp_f32 v60, v60\n"
    "v_mul_f32 v65, v61, v65\n"
    "v_mfma_f32_16x16x32_bf16 v[48:51], a[56:59], v[28:31], v[48:51]\n"
    "s_waitcnt lgkmcnt(2)\n"
    "ds_read2st64_b64 v[28:31], v4 offset0:54 offset1:55\n"
    "v_fma_f32 v60, v60, v73, v73\n"
    "v_mul_f32 v61, v72, v61\n"
    "v_mfma_f32_16x16x32_bf16 v[52:55], v[20:23], v[32:35], v[52:55]\n"
    "buffer_load_dwordx4 a[240:243], v0, %[srdw], s83 offen offset:0 sc1\n"
    "v_rcp_f32 v60, v60\n"
    "v_mfma_f32_16x16x32_bf16 v[56:59], v[32:35], v[32:35], v[56:59]\n"
    "s_waitcnt lgkmcnt(2)\n"
    "ds_read2st64_b64 v[32:35], v5 offset0:48 offset1:49\n"
    "v_mul_f32 v60, v60, v64\n"
    "v_fmac_f32_dpp v62, v76, v77 row_newbcast:2 row_mask:0xf bank_mask:0xf\n"
    "v_mfma_f32_16x16x32_bf16 v[40:43], a[60:63], v[36:39], v[40:43]\n"
    "s_lshl1_add_u32 s59, s47, %[wd]\n"
    "s_add_u32 s60, s59, s47\n"
    "v_exp_f32 v61, v61\n"
    "v_mfma_f32_16x16x32_bf16 v[44:47], a[64:67], v[36:39], v[44:47]\n"
    "s_add_u32 m0, %[ldswd], 6144\n"
    "s_add_u32 s68, s84, 36864\n"
    "buffer_load_dwordx4 v0, %[srdw], s68 offen sc1 lds\n"
    "v_fma_f32 v61, v61, v73, v73\n"
    "v_mfma_f32_16x16x32_bf16 v[48:51], a[68:71], v[36:39], v[48:51]\n"
    "s_waitcnt lgkmcnt(2)\n"
    "ds_read2st64_b64 v[36:39], v4 offset0:56 offset1:57\n"
    "s_add_u32 s47, s47, %[hld]\n"
    "v_fmac_f32_dpp v66, v76, v77 row_newbcast:6 row_mask:0xf bank_mask:0xf\n"
    "v_mfma_f32_16x16x32_bf16 v[52:55], v[20:23], v[24:27], v[52:55]\n"
    "v_rcp_f32 v61, v61\n"
    "v_mul_f32 v66, v62, v66\n"
    "v_mfma_f32_16x16x32_bf16 v[56:59], v[24:27], v[24:27], v[56:59]\n"
    "s_waitcnt lgkmcnt(2)\n"
    "ds_read2st64_b64 v[24:27], v5 offset0:50 offset1:51\n"
    "v_mul_f32 v61, v61, v65\n"
    "v_cvt_pk_bf16_f32 v60, v60, v61\n"
    "v_mfma_f32_16x16x32_bf16 v[40:43], a[72:75], v[28:31], v[40:43]\n"
    "buffer_load_dwordx4 v0, %[srdw], s68 offen offset:1024 sc1 lds\n"
    "v_mul_f32 v62, v72, v62\n"
    "v_fmac_f32_dpp v63, v76, v77 row_newbcast:3 row_mask:0xf bank_mask:0xf\n"
    "v_mfma_f32_16x16x32_bf16 v[44:47], a[76:79], v[28:31], v[44:47]\n"
    "v_exp_f32 v62, v62\n"
    "v_fmac_f32_dpp v67, v76, v77 row_newbcast:7 row_mask:0xf bank_mask:0xf\n"
    "v_mfma_f32_16x16x32_bf16 v[48:51], a[80:83], v[28:31], v[48:51]\n"
    "s_waitcnt lgkmcnt(2)\n"
    "ds_read2st64_b64 v[28:31], v4 offset0:58 offset1:59\n"
    "v_fma_f32 v62, v62, v73, v73\n"
    "v_mul_f32 v67, v63, v67\n"
    "v_mfma_f32_16x16x32_bf16 v[52:55], v[20:23], v[32:35], v[52:55]\n"
    "buffer_load_dwordx4 v0, %[srdw], s68 offen offset:2048 sc1 lds\n"
    "v_rcp_f32 v62, v62\n"
    "v_mfma_f32_16x16x32_bf16 v[56:59], v[32:35], v[32:35], v[56:59]\n"
    "s_waitcnt lgkmcnt(2)\n"
    "ds_read2st64_b64 v[32:35], v4 offset0:60 offset1:61\n"
    "v_mul_f32 v62, v62, v66\n"
    "v_mul_f32 v63, v72, v63\n"
    "v_mfma_f32_16x16x32_bf16 v[40:43], a[84:87], v[36:39], v[40:43]\n"
    "v_exp_f32 v63, v63\n"
    "v_mul_f32 v70, v68, v70\n"
    "v_mfma_f32_16x16x32_bf16 v[44:47], a[88:91], v[36:39], v[44:47]\n"
    "buffer_load_dwordx4 v0, %[srdw], s68 offen offset:3072 sc1 lds\n"
    "v_fma_f32 v63, v63, v73, v73\n"
    "v_mul_f32 v68, v72, v68\n"
    "v_mfma_f32_16x16x32_bf16 v[48:51], a[92:95], v[36:39], v[48:51]\n"
    "s_waitcnt lgkmcnt(2)\n"
    "ds_read2st64_b64 v[36:39], v4 offset0:62 offset1:63\n"
    "v_rcp_f32 v63, v63\n"
    "v_mfma_f32_16x16x32_bf16 v[52:55], v[20:23], v[24:27], v[52:55]\n"
    "v_mul_f32 v63, v63, v67\n"
    "v_cvt_pk_bf16_f32 v61, v62, v63\n"
    "v_mul_f32 v71, v69, v71\n"
    "v_mfma_f32_16x16x32_bf16 v[56:59], v[24:27], v[24:27], v[56:59]\n"
    "s_waitcnt lgkmcnt(2)\n"
    "ds_read2st64_b64 v[24:27], v4 offset0:64 offset1:65\n"
    "v_exp_f32 v68, v68\n"
    "v_mfma_f32_16x16x32_bf16 v[40:43], a[96:99], v[28:31], v[40:43]\n"
    "s_add_u32 m0, %[ldswd], 10240\n"
    "s_add_u32 s68, s84, 40960\n"
    "buffer_load_dwordx4 v0, %[srdw], s68 offen sc1 lds\n"
    "v_fma_f32 v68, v68, v73, v73\n"
    "v_mfma_f32_16x16x32_bf16 v[44:47], a[100:103], v[28:31], v[44:47]\n"
    "v_cndmask_b32_e64 v53, v56, v57, s[48:49]\n"
    "v_cndmask_b32_e64 v53, v53, v58, s[50:51]\n"
    "v_mul_f32 v69, v72, v69\n"
    "v_mfma_f32_16x16x32_bf16 v[48:51], a[104:107], v[28:31], v[48:51]\n"
    "s_waitcnt lgkmcnt(2)\n"
    "ds_read2st64_b64 v[28:31], v4 offset0:66 offset1:67\n"
    "v_cndmask_b32_e64 v53, v53, v59, s[52:53]\n"
    "v_mfma_f32_16x16x32_bf16 v[40:43], a[108:111], v[32:35], v[40:43]\n"
    "buffer_load_dwordx4 v0, %[srdw], s68 offen offset:1024 sc1 lds\n"
    "v_rcp_f32 v68, v68\n"
    "v_mfma_f32_16x16x32_bf16 v[44:47], a[112:115], v[32:35], v[44:47]\n"
    "ds_write_b64 v8, v[52:53] offset:0\n"
    "v_mfma_f32_16x16x32_bf16 v[48:51], a[116:119], v[32:35], v[48:51]\n"
    "s_waitcnt lgkmcnt(3)\n"
    "ds_read2st64_b64 v[112:115], v4 offset0:78 offset1:79\n"
    "ds_read2st64_b64 v[116:119], v4 offset0:80 offset1:81\n"
    "v_mul_f32 v68, v68, v70\n"
    "v_mfma_f32_16x16x32_bf16 v[40:43], a[120:123], v[36:39], v[40:43]\n"
    "ds_read2st64_b64 v[32:35], v4 offset0:68 offset1:69\n"
    "v_exp_f32 v69, v69\n"
    "v_mfma_f32_16x16x32_bf16 v[44:47], a[124:127], v[36:39], v[44:47]\n"
    "buffer_load_dwordx4 v[180:183], v0, %[srdw], s86 offen offset:0 sc1\n"
    "v_fma_f32 v69, v69, v73, v73\n"
    "v_mfma_f32_16x16x32_bf16 v[48:51], a[128:131], v[36:39], v[48:51]\n"
    "s_waitcnt lgkmcnt(5)\n"
    "ds_read2st64_b64 v[36:39], v4 offset0:70 offset1:71\n"
    "v_rcp_f32 v69, v69\n"
    "v_mfma_f32_16x16x32_bf16 v[40:43], a[132:135], v[24:27], v[40:43]\n"
    "v_mul_f32 v69, v69, v71\n"
    "v_cvt_pk_bf16_f32 v62, v68, v69\n"
    "v_mfma_f32_16x16x32_bf16 v[44:47], a[136:139], v[24:27], v[44:47]\n"
    "v_permlane32_swap_b32 v61, v63\n"
    "v_mfma_f32_16x16x32_bf16 v[48:51], a[140:143], v[24:27], v[48:51]\n"
    "s_waitcnt lgkmcnt(5)\n"
    "ds_read2st64_b64 v[24:27], v4 offset0:72 offset1:73\n"
    "v_permlane16_swap_b32 v62, v63\n"
    "v_mfma_f32_16x16x32_bf16 v[40:43], a[144:147], v[28:31], v[40:43]\n"
    "buffer_load_dwordx4 v[176:179], v0, %[srdw], s89 offen offset:3072 sc1\n"
    "v_permlane16_swap_b32 v60, v61\n"
    "v_mfma_f32_16x16x32_bf16 v[44:47], a[148:151], v[28:31], v[44:47]\n"
    "buffer_store_dwordx4 v[60:63], v10, %[srdw], s57 offen sc1\n"
    "v_mfma_f32_16x16x32_bf16 v[48:51], a[152:155], v[28:31], v[48:51]\n"
    "s_waitcnt lgkmcnt(2)\n"
    "ds_read2st64_b64 v[28:31], v4 offset0:74 offset1:75\n"
    "v_mfma_f32_16x16x32_bf16 v[40:43], a[156:159], v[32:35], v[40:43]\n"
    "s_cmp_eq_u32 %[dbg], 0\n"
    "s_cselect_b32 s61, 0, 0x40000000\n"
    "v_mfma_f32_16x16x32_bf16 v[44:47], a[160:163], v[32:35], v[44:47]\n"
    "s_add_u32 s62, s61, 0\n"
    "s_add_u32 s61, s60, 8192\n"
    "v_mfma_f32_16x16x32_bf16 v[48:51], a[164:167], v[32:35], v[48:51]\n"
    "s_waitcnt lgkmcnt(2)\n"
    "ds_read2st64_b64 v[32:35], v4 offset0:76 offset1:77\n"
    "v_mfma_f32_16x16x32_bf16 v[40:43], a[168:171], v[36:39], v[40:43]\n"
    "s_mov_b64 exec, s[94:95]\n"
    "buffer_store_dword v2, off, %[srdw], s62 sc1\n"
    "s_mov_b64 exec, -1\n"
    "v_mfma_f32_16x16x32_bf16 v[44:47], a[172:175], v[36:39], v[44:47]\n"
    "s_add_u32 s62, %[xv], 16515072\n"
    "buffer_load_dwordx4 v[172:175], v0, %[srdw], s89 offen offset:2048 sc1\n"
    "v_mfma_f32_16x16x32_bf16 v[48:51], a[176:179], v[36:39], v[48:51]\n"
    "s_waitcnt lgkmcnt(2)\n"
    "buffer_store_dwordx4 v[120:123], v18, %[srdw], s62 offen sc1\n"
    "v_mfma_f32_16x16x32_bf16 v[40:43], a[180:183], v[24:27], v[40:43]\n"
    "v_mfma_f32_16x16x32_bf16 v[44:47], a[184:187], v[24:27], v[44:47]\n"
    "s_add_u32 s63, s93, 1310720\n"
    "v_mfma_f32_16x16x32_bf16 v[48:51], a[188:191], v[24:27], v[48:51]\n"
    "s_waitcnt lgkmcnt(1)\n"
    "v_mfma_f32_16x16x32_bf16 v[40:43], a[192:195], v[28:31], v[40:43]\n"
    "s_waitcnt vmcnt(31) lgkmcnt(0)\n"
    "s_barrier\n"
    "v_mfma_f32_16x16x32_bf16 v[44:47], a[196:199], v[28:31], v[44:47]\n"
    "ds_read2st64_b64 v[36:39], v4 offset0:84 offset1:85\n"
    "ds_read2st64_b64 v[24:27], v4 offset0:86 offset1:87\n"
    "v_mfma_f32_16x16x32_bf16 v[48:51], a[200:203], v[28:31], v[48:51]\n"
    "ds_read2st64_b64 v[28:31], v4 offset0:88 offset1:89\n"
    "ds_read_b128 v[52:55], v9 offset:0\n"
    "v_mfma_f32_16x16x32_bf16 v[40:43], a[204:207], v[32:35], v[40:43]\n"
    "s_mov_b64 exec, s[94:95]\n"
    "buffer_store_dword v2, off, %[srdw], s63 sc1\n"
    "s_mov_b64 exec, -1\n"
    "v_mfma_f32_16x16x32_bf16 v[44:47], a[208:211], v[32:35], v[44:47]\n"
    "ds_read_b128 v[56:59], v9 offset:16\n"
    "v_mfma_f32_16x16x32_bf16 v[48:51], a[212:215], v[32:35], v[48:51]\n"
    "ds_read2st64_b64 v[32:35], v4 offset0:90 offset1:91\n"
    "v_mfma_f32_16x16x32_bf16 v[40:43], a[216:219], v[112:115], v[40:43]\n"
    "s_add_u32 m0, %[ldsx], 21504\n"
    "s_add_u32 s68, s42, 7274496\n"
    "buffer_load_dwordx4 v3, %[srdw], s68 offen sc1 lds\n"
    "v_mfma_f32_16x16x32_bf16 v[44:47], a[220:223], v[112:115], v[44:47]\n"
    "s_add_u32 s64, s61, -4096\n"
    "buffer_load_dwordx4 v3, %[srdw], s68 offen offset:1024 sc1 lds\n"
    "v_mfma_f32_16x16x32_bf16 v[48:51], a[224:227], v[112:115], v[48:51]\n"
    "v_mfma_f32_16x16x32_bf16 v[40:43], a[228:231], v[116:119], v[40:43]\n"
    "v_mfma_f32_16x16x32_bf16 v[44:47], a[232:235], v[116:119], v[44:47]\n"
    "v_mfma_f32_16x16x32_bf16 v[48:51], a[236:239], v[116:119], v[48:51]\n"
    "s_waitcnt lgkmcnt(2)\n"
    "v_add_f32 v52, v52, v54\n"
    "v_add_f32 v53, v53, v55\n"
    "v_mfma_f32_16x16x32_bf16 v[60:63], a[0:3], v[36:39], 0\n"
    "s_waitcnt lgkmcnt(1)\n"
    "v_add_f32 v56, v56, v58\n"
    "v_add_f32 v57, v57, v59\n"
    "v_mfma_f32_16x16x32_bf16 v[64:67], a[4:7], v[36:39], 0\n"
    "s_add_u32 s66, %[xv], 17170432\n"
    "v_add_f32 v52, v52, v56\n"
    "v_add_f32 v53, v53, v57\n"
    "v_mfma_f32_16x16x32_bf16 v[68:71], a[8:11], v[36:39], 0\n"
    "s_lshr_b32 s67, %[ryy], 14\n"
    "ds_read2st64_b64 v[36:39], v5 offset0:84 offset1:85\n"
    "v_mul_f32 v77, 0xbacccccd, v52\n"
    "v_mfma_f32_16x16x32_bf16 v[60:63], a[12:15], v[24:27], v[60:63]\n"
    "s_lshl_b32 s67, s67, 4\n"
    "v_mul_f32 v53, 0x3acccccd, v53\n"
    "v_fma_f32 v53, -v77, v77, v53\n"
    "v_mfma_f32_16x16x32_bf16 v[64:67], a[16:19], v[24:27], v[64:67]\n"
    "s_add_u32 s78, s67, 122880\n"
    "v_max_f32 v53, 0, v53\n"
    "v_add_f32 v73, 0x3727c5ac, v53\n"
    "v_mfma_f32_16x16x32_bf16 v[68:71], a[20:23], v[24:27], v[68:71]\n"
    "s_add_u32 s65, %[hst], 12713984\n"
    "ds_read2st64_b64 v[24:27], v4 offset0:92 offset1:93\n"
    "v_fmac_f32_dpp v48, v76, v77 row_newbcast:8 row_mask:0xf bank_mask:0xf\n"
    "v_mfma_f32_16x16x32_bf16 v[60:63], a[24:27], v[28:31], v[60:63]\n"
    "v_rsq_f32 v52, v73\n"
    "v_mfma_f32_16x16x32_bf16 v[64:67], a[28:31], v[28:31], v[64:67]\n"
    "v_mul_f32 v72, 0xbfb8aa3b, v52\n"
    "v_fmac_f32_dpp v49, v76, v77 row_newbcast:9 row_mask:0xf bank_mask:0xf\n"
    "v_mfma_f32_16x16x32_bf16 v[68:71], a[32:35], v[28:31], v[68:71]\n"
    "s_waitcnt lgkmcnt(2)\n"
    "ds_read2st64_b64 v[28:31], v5 offset0:86 offset1:87\n"
    "v_fmac_f32_dpp v50, v76, v77 row_newbcast:10 row_mask:0xf bank_mask:0xf\n"
    "v_mfma_f32_16x16x32_bf16 v[60:63], a[36:39], v[32:35], v[60:63]\n"
    "buffer_load_dwordx4 v3, %[srdw], s68 offen offset:2048 sc1 lds\n"
    "v_fmac_f32_dpp v51, v76, v77 row_newbcast:11 row_mask:0xf bank_mask:0xf\n"
    "v_mfma_f32_16x16x32_bf16 v[64:67], a[40:43], v[32:35], v[64:67]\n"
    "buffer_load_dwordx4 v3, %[srdw], s68 offen offset:3072 sc1 lds\n"
    "v_fmac_f32_dpp v40, v76, v77 row_newbcast:0 row_mask:0xf bank_mask:0xf\n"
    "v_mfma_f32_16x16x32_bf16 v[68:71], a[44:47], v[32:35], v[68:71]\n"
    "s_waitcnt lgkmcnt(2)\n"
    "ds_read2st64_b64 v[32:35], v4 offset0:94 offset1:95\n"
    "v_fmac_f32_dpp v44, v76, v77 row_newbcast:4 row_mask:0xf bank_mask:0xf\n"
    "v_mfma_f32_16x16x32_bf16 v[52:55], v[20:23], v[36:39], 0\n"
    "buffer_store_dwordx4 v[120:123], v18, %[srdw], s66 offen sc1\n"
    "v_mfma_f32_16x16x32_bf16 v[56:59], v[36:39], v[36:39], 0\n"
    "s_waitcnt lgkmcnt(2)\n"
    "ds_read2st64_b64 v[36:39], v5 offset0:88 offset1:89\n"
    "v_mul_f32 v44, v40, v44\n"
    "v_mfma_f32_16x16x32_bf16 v[60:63], a[48:51], v[24:27], v[60:63]\n"
    "s_mov_b64 exec, 0xfffff\n"
    "buffer_load_dword v82, v0, %[srdw], s78 offen sc1\n"
    "s_mov_b64 exec, -1\n"
    "v_mfma_f32_16x16x32_bf16 v[64:67], a[52:55], v[24:27], v[64:67]\n"
    "v_permlane16_swap_b32 v48, v50\n"
    "v_mfma_f32_16x16x32_bf16 v[68:71], a[56:59], v[24:27], v[68:71]\n"
    "s_waitcnt lgkmcnt(2)\n"
    "ds_read2st64_b64 v[24:27], v4 offset0:96 offset1:97\n"
    "v_mul_f32 v40, v72, v40\n"
    "v_mfma_f32_16x16x32_bf16 v[52:55], v[20:23], v[28:31], v[52:55]\n"
    "v_permlane16_swap_b32 v49, v51\n"
    "v_mfma_f32_16x16x32_bf16 v[56:59], v[28:31], v[28:31], v[56:59]\n"
    "s_waitcnt lgkmcnt(2)\n"
    "ds_read2st64_b64 v[28:31], v5 offset0:90 offset1:91\n"
    "v_fmac_f32_dpp v41, v76, v77 row_newbcast:1 row_mask:0xf bank_mask:0xf\n"
    "v_mfma_f32_16x16x32_bf16 v[60:63], a[60:63], v[32:35], v[60:63]\n"
    "v_exp_f32 v40, v40\n"
    "v_mfma_f32_16x16x32_bf16 v[64:67], a[64:67], v[32:35], v[64:67]\n"
    "v_fma_f32 v40, v40, v73, v73\n"
    "v_fmac_f32_dpp v45, v76, v77 row_newbcast:5 row_mask:0xf bank_mask:0xf\n"
    "v_mfma_f32_16x16x32_bf16 v[68:71], a[68:71], v[32:35], v[68:71]\n"
    "s_waitcnt lgkmcnt(2)\n"
    "ds_read2st64_b64 v[32:35], v4 offset0:98 offset1:99\n"
    "s_add_u32 s79, s64, -4096\n"
    "v_mfma_f32_16x16x32_bf16 v[52:55], v[20:23], v[36:39], v[52:55]\n"
    "v_rcp_f32 v40, v40\n"
    "v_mfma_f32_16x16x32_bf16 v[56:59], v[36:39], v[36:39], v[56:59]\n"
    "s_waitcnt lgkmcnt(2)\n"
    "ds_read2st64_b64 v[36:39], v5 offset0:92 offset1:93\n"
    "v_mul_f32 v40, v40, v44\n"
    "v_mfma_f32_16x16x32_bf16 v[60:63], a[72:75], v[24:27], v[60:63]\n"
    "s_add_u32 m0, %[ldsx], 25600\n"
    "s_add_u32 s68, s42, 7278592\n"
    "buffer_load_dwordx4 v3, %[srdw], s68 offen sc1 lds\n"
    "v_mfma_f32_16x16x32_bf16 v[64:67], a[76:79], v[24:27], v[64:67]\n"
    "v_mul_f32 v45, v41, v45\n"
    "v_mul_f32 v41, v72, v41\n"
    "v_mfma_f32_16x16x32_bf16 v[68:71], a[80:83], v[24:27], v[68:71]\n"
    "s_waitcnt lgkmcnt(2)\n"
    "ds_read2st64_b64 v[24:27], v4 offset0:100 offset1:101\n"
    "v_fmac_f32_dpp v42, v76, v77 row_newbcast:2 row_mask:0xf bank_mask:0xf\n"
    "v_mfma_f32_16x16x32_bf16 v[52:55], v[20:23], v[28:31], v[52:55]\n"
    "v_exp_f32 v41, v41\n"
    "v_mfma_f32_16x16x32_bf16 v[56:59], v[28:31], v[28:31], v[56:59]\n"
    "s_waitcnt lgkmcnt(2)\n"
    "ds_read2st64_b64 v[28:31], v4 offset0:102 offset1:103\n"
    "v_fma_f32 v41, v41, v73, v73\n"
    "v_mfma_f32_16x16x32_bf16 v[60:63], a[84:87], v[32:35], v[60:63]\n"
    "s_add_u32 s80, %[xv], 17825792\n"
    "buffer_load_dwordx4 v[168:171], v0, %[srdw], s89 offen offset:1024 sc1\n"
    "v_mfma_f32_16x16x32_bf16 v[64:67], a[88:91], v[32:35], v[64:67]\n"
    "buffer_store_dwordx4 v[120:123], v18, %[srdw], s80 offen sc1\n"
    "v_mfma_f32_16x16x32_bf16 v[68:71], a[92:95], v[32:35], v[68:71]\n"
    "s_waitcnt lgkmcnt(2)\n"
    "ds_read2st64_b64 v[32:35], v4 offset0:104 offset1:105\n"
    "v_fmac_f32_dpp v46, v76, v77 row_newbcast:6 row_mask:0xf bank_mask:0xf\n"
    "v_mfma_f32_16x16x32_bf16 v[52:55], v[20:23], v[36:39], v[52:55]\n"
    "v_rcp_f32 v41, v41\n"
    "v_mfma_f32_16x16x32_bf16 v[56:59], v[36:39], v[36:39], v[56:59]\n"
    "s_waitcnt lgkmcnt(2)\n"
    "ds_read2st64_b64 v[36:39], v4 offset0:106 offset1:107\n"
    "v_mul_f32 v41, v41, v45\n"
    "v_mfma_f32_16x16x32_bf16 v[60:63], a[96:99], v[24:27], v[60:63]\n"
    "v_cvt_pk_bf16_f32 v40, v40, v41\n"
    "v_mul_f32 v46, v42, v46\n"
    "v_mfma_f32_16x16x32_bf16 v[64:67], a[100:103], v[24:27], v[64:67]\n"
    "buffer_load_dwordx4 v[164:167], v0, %[srdw], s89 offen offset:0 sc1\n"
    "v_cndmask_b32_e64 v53, v56, v57, s[48:49]\n"
    "v_mfma_f32_16x16x32_bf16 v[68:71], a[104:107], v[24:27], v[68:71]\n"
    "s_waitcnt lgkmcnt(2)\n"
    "ds_read2st64_b64 v[24:27], v4 offset0:108 offset1:109\n"
    "v_cndmask_b32_e64 v53, v53, v58, s[50:51]\n"
    "v_mfma_f32_16x16x32_bf16 v[60:63], a[108:111], v[28:31], v[60:63]\n"
    "v_cndmask_b32_e64 v53, v53, v59, s[52:53]\n"
    "v_mul_f32 v42, v72, v42\n"
    "v_mfma_f32_16x16x32_bf16 v[64:67], a[112:115], v[28:31], v[64:67]\n"
    "ds_write_b64 v8, v[52:53] offset:512\n"
    "v_mfma_f32_16x16x32_bf16 v[68:71], a[116:119], v[28:31], v[68:71]\n"
    "s_waitcnt lgkmcnt(3)\n"
    "ds_read2st64_b64 v[112:115], v4 offset0:120 offset1:121\n"
    "ds_read2st64_b64 v[116:119], v4 offset0:122 offset1:123\n"
    "v_mfma_f32_16x16x32_bf16 v[60:63], a[120:123], v[32:35], v[60:63]\n"
    "ds_read2st64_b64 v[28:31], v4 offset0:110 offset1:111\n"
    "buffer_load_dwordx4 v[160:163], v0, %[srdw], s56 offen offset:3072 sc1\n"
    "v_mfma_f32_16x16x32_bf16 v[64:67], a[124:127], v[32:35], v[64:67]\n"
    "v_exp_f32 v42, v42\n"
    "v_mfma_f32_16x16x32_bf16 v[68:71], a[128:131], v[32:35], v[68:71]\n"
    "s_waitcnt lgkmcnt(5)\n"
    "ds_read2st64_b64 v[32:35], v4 offset0:112 offset1:113\n"
    "v_fma_f32 v42, v42, v73, v73\n"
    "v_mfma_f32_16x16x32_bf16 v[60:63], a[132:135], v[36:39], v[60:63]\n"
    "buffer_load_dwordx4 v[156:159], v0, %[srdw], s56 offen offset:2048 sc1\n"
    "v_fmac_f32_dpp v43, v76, v77 row_newbcast:3 row_mask:0xf bank_mask:0xf\n"
    "v_mfma_f32_16x16x32_bf16 v[64:67], a[136:139], v[36:39], v[64:67]\n"
    "v_rcp_f32 v42, v42\n"
    "v_mfma_f32_16x16x32_bf16 v[68:71], a[140:143], v[36:39], v[68:71]\n"
    "s_waitcnt lgkmcnt(5)\n"
    "ds_read2st64_b64 v[36:39], v4 offset0:114 offset1:115\n"
    "v_mul_f32 v42, v42, v46\n"
    "v_mfma_f32_16x16x32_bf16 v[60:63], a[144:147], v[24:27], v[60:63]\n"
    "v_fmac_f32_dpp v47, v76, v77 row_newbcast:7 row_mask:0xf bank_mask:0xf\n"
    "v_mul_f32 v47, v43, v47\n"
    "v_mfma_f32_16x16x32_bf16 v[64:67], a[148:151], v[24:27], v[64:67]\n"
    "buffer_load_dwordx4 v[152:155], v0, %[srdw], s56 offen offset:1024 sc1\n"
    "v_mul_f32 v43, v72, v43\n"
    "v_mfma_f32_16x16x32_bf16 v[68:71], a[152:155], v[24:27], v[68:71]\n"
    "s_waitcnt lgkmcnt(2)\n"
    "ds_read2st64_b64 v[24:27], v4 offset0:116 offset1:117\n"
    "v_mul_f32 v50, v48, v50\n"
    "v_mfma_f32_16x16x32_bf16 v[60:63], a[156:159], v[28:31], v[60:63]\n"
    "v_exp_f32 v43, v43\n"
    "v_mfma_f32_16x16x32_bf16 v[64:67], a[160:163], v[28:31], v[64:67]\n"
    "v_fma_f32 v43, v43, v73, v73\n"
    "v_mul_f32 v48, v72, v48\n"
    "v_mfma_f32_16x16x32_bf16 v[68:71], a[164:167], v[28:31], v[68:71]\n"
    "s_waitcnt lgkmcnt(2)\n"
    "ds_read2st64_b64 v[28:31], v4 offset0:118 offset1:119\n"
    "v_mul_f32 v51, v49, v51\n"
    "v_mfma_f32_16x16x32_bf16 v[60:63], a[168:171], v[32:35], v[60:63]\n"
    "buffer_load_dwordx4 v[148:151], v0, %[srdw], s56 offen offset:0 sc1\n"
    "v_mul_f32 v49, v72, v49\n"
    "v_mfma_f32_16x16x32_bf16 v[64:67], a[172:175], v[32:35], v[64:67]\n"
    "v_rcp_f32 v43, v43\n"
    "v_mfma_f32_16x16x32_bf16 v[68:71], a[176:179], v[32:35], v[68:71]\n"
    "s_waitcnt lgkmcnt(2)\n"
    "v_mul_f32 v43, v43, v47\n"
    "v_cvt_pk_bf16_f32 v41, v42, v43\n"
    "v_mfma_f32_16x16x32_bf16 v[60:63], a[180:183], v[36:39], v[60:63]\n"
    "v_exp_f32 v48, v48\n"
    "v_mfma_f32_16x16x32_bf16 v[64:67], a[184:187], v[36:39], v[64:67]\n"
    "s_add_u32 s81, %[f4p], 1180160\n"
    "s_add_u32 m0, %[ldswd], 18432\n"
    "s_add_u32 s68, s59, 36864\n"
    "buffer_load_dwordx4 v0, %[srdw], s68 offen sc1 lds\n"
    "v_mfma_f32_16x16x32_bf16 v[68:71], a[188:191], v[36:39], v[68:71]\n"
    "s_waitcnt lgkmcnt(1)\n"
    "s_add_u32 s82, s93, 1311232\n"
    "v_fma_f32 v48, v48, v73, v73\n"
    "v_mfma_f32_16x16x32_bf16 v[60:63], a[192:195], v[24:27], v[60:63]\n"
    "v_rcp_f32 v48, v48\n"
    "v_mfma_f32_16x16x32_bf16 v[64:67], a[196:199], v[24:27], v[64:67]\n"
    "s_waitcnt vmcnt(19) lgkmcnt(0)\n"
    "s_barrier\n"
    "v_mfma_f32_16x16x32_bf16 v[68:71], a[200:203], v[24:27], v[68:71]\n"
    "ds_read2st64_b64 v[32:35], v4 offset0:0 offset1:1\n"
    "ds_read2st64_b64 v[36:39], v4 offset0:2 offset1:3\n"
    "v_mfma_f32_16x16x32_bf16 v[60:63], a[204:207], v[28:31], v[60:63]\n"
    "ds_read2st64_b64 v[24:27], v4 offset0:4 offset1:5\n"
    "ds_read_b128 v[52:55], v9 offset:512\n"
    "v_mfma_f32_16x16x32_bf16 v[64:67], a[208:211], v[28:31], v[64:67]\n"
    "s_mov_b64 exec, s[94:95]\n"
    "buffer_store_dword v2, off, %[srdw], s81 sc1\n"
    "s_mov_b64 exec, -1\n"
    "v_mfma_f32_16x16x32_bf16 v[68:71], a[212:215], v[28:31], v[68:71]\n"
    "ds_read2st64_b64 v[28:31], v4 offset0:6 offset1:7\n"
    "ds_read_b128 v[56:59], v9 offset:528\n"
    "v_mfma_f32_16x16x32_bf16 v[60:63], a[216:219], v[112:115], v[60:63]\n"
    "s_mov_b64 exec, s[94:95]\n"
    "buffer_store_dword v2, off, %[srdw], s82 sc1\n"
    "s_mov_b64 exec, -1\n"
    "v_mfma_f32_16x16x32_bf16 v[64:67], a[220:223], v[112:115], v[64:67]\n"
    "buffer_load_dwordx4 v0, %[srdw], s68 offen offset:1024 sc1 lds\n"
    "v_mul_f32 v48, v48, v50\n"
    "v_mfma_f32_16x16x32_bf16 v[68:71], a[224:227], v[112:115], v[68:71]\n"
    "buffer_load_dwordx4 v0, %[srdw], s68 offen offset:2048 sc1 lds\n"
    "v_lshrrev_b32 v80, 3, v0\n"
    "v_mfma_f32_16x16x32_bf16 v[60:63], a[228:231], v[116:119], v[60:63]\n"
    "v_exp_f32 v49, v49\n"
    "v_mfma_f32_16x16x32_bf16 v[64:67], a[232:235], v[116:119], v[64:67]\n"
    "v_fma_f32 v49, v49, v73, v73\n"
    "v_and_b32 v80, 0x70, v80\n"
    "v_mfma_f32_16x16x32_bf16 v[68:71], a[236:239], v[116:119], v[68:71]\n"
    "s_waitcnt lgkmcnt(2)\n"
    "s_add_u32 s84, %[f4c], 1179648\n"
    "v_add_f32 v52, v52, v54\n"
    "s_add_u32 s83, s43, 13500416\n"
    "v_rcp_f32 v49, v49\n"
    "v_permlane32_swap_b32 v41, v43\n"
    "v_mul_f32 v49, v49, v51\n"
    "v_cvt_pk_bf16_f32 v42, v48, v49\n"
    "v_permlane16_swap_b32 v40, v41\n"
    "s_nop 0\n"
    "v_permlane16_swap_b32 v42, v43\n"
    "buffer_store_dwordx4 v[40:43], v10, %[srdw], s65 offen sc1\n"
    "s_nop 1\n"
    "v_mfma_f32_16x16x32_bf16 v[40:43], a[0:3], v[32:35], 0\n"
    "s_mov_b64 exec, 0xf\n"
    "buffer_load_dword v78, v1, %[srdw], s84 offen sc1\n"
    "s_mov_b64 exec, -1\n"
    "v_mfma_f32_16x16x32_bf16 v[44:47], a[4:7], v[32:35], 0\n"
    "buffer_load_dwordx4 v0, %[srdw], s68 offen offset:3072 sc1 lds\n"
    "kx_a4:\n"
    "s_cbranch_execz kx_a8\n"
    "v_add_f32 v53, v53, v55\n"
    "v_mfma_f32_16x16x32_bf16 v[48:51], a[8:11], v[32:35], 0\n"
    "s_waitcnt lgkmcnt(0)\n"
    "ds_read2st64_b64 v[32:35], v5 offset0:0 offset1:1\n"
    "v_add_f32 v56, v56, v58\n"
    "v_mfma_f32_16x16x32_bf16 v[40:43], a[12:15], v[36:39], v[40:43]\n"
    "v_add_f32 v57, v57, v59\n"
    "v_add_f32 v52, v52, v56\n"
    "v_mfma_f32_16x16x32_bf16 v[44:47], a[16:19], v[36:39], v[44:47]\n"
    "v_add_f32 v53, v53, v57\n"
    "v_mul_f32 v77, 0xbacccccd, v52\n"
    "v_mfma_f32_16x16x32_bf16 v[48:51], a[20:23], v[36:39], v[48:51]\n"
    "ds_read2st64_b64 v[36:39], v4 offset0:8 offset1:9\n"
    "v_mul_f32 v53, 0x3acccccd, v53\n"
    "v_fmac_f32_dpp v68, v76, v77 row_newbcast:8 row_mask:0xf bank_mask:0xf\n"
    "v_mfma_f32_16x16x32_bf16 v[40:43], a[24:27], v[24:27], v[40:43]\n"
    "s_add_u32 m0, %[ldswd], 22528\n"
    "s_add_u32 s68, s59, 40960\n"
    "buffer_load_dwordx4 v0, %[srdw], s68 offen sc1 lds\n"
    "v_fmac_f32_dpp v69, v76, v77 row_newbcast:9 row_mask:0xf bank_mask:0xf\n"
    "v_mfma_f32_16x16x32_bf16 v[44:47], a[28:31], v[24:27], v[44:47]\n"
    "v_fma_f32 v53, -v77, v77, v53\n"
    "v_max_f32 v53, 0, v53\n"
    "v_fmac_f32_dpp v70, v76, v77 row_newbcast:10 row_mask:0xf bank_mask:0xf\n"
    "v_mfma_f32_16x16x32_bf16 v[48:51], a[32:35], v[24:27], v[48:51]\n"
    "ds_read2st64_b64 v[24:27], v5 offset0:2 offset1:3\n"
    "v_add_f32 v73, 0x3727c5ac, v53\n"
    "v_fmac_f32_dpp v71, v76, v77 row_newbcast:11 row_mask:0xf bank_mask:0xf\n"
    "v_mfma_f32_16x16x32_bf16 v[40:43], a[36:39], v[28:31], v[40:43]\n"
    "buffer_load_dwordx4 v0, %[srdw], s68 offen offset:1024 sc1 lds\n"
    "v_xor_b32 v80, v80, v0\n"
    "v_fmac_f32_dpp v60, v76, v77 row_newbcast:0 row_mask:0xf bank_mask:0xf\n"
    "v_mfma_f32_16x16x32_bf16 v[44:47], a[40:43], v[28:31], v[44:47]\n"
    "v_rsq_f32 v52, v73\n"
    "v_fmac_f32_dpp v64, v76, v77 row_newbcast:4 row_mask:0xf bank_mask:0xf\n"
    "v_mfma_f32_16x16x32_bf16 v[48:51], a[44:47], v[28:31], v[48:51]\n"
    "ds_read2st64_b64 v[28:31], v4 offset0:10 offset1:11\n"
    "v_mul_f32 v72, 0xbfb8aa3b, v52\n"
    "v_mul_f32 v64, v60, v64\n"
    "s_waitcnt lgkmcnt(3)\n"
    "v_mfma_f32_16x16x32_bf16 v[52:55], v[20:23], v[32:35], 0\n"
    "buffer_load_dwordx4 v[252:255], v0, %[srdw], s61 offen offset:0 sc1\n"
    "v_lshrrev_b32 v81, 7, v80\n"
    "v_mul_f32 v60, v72, v60\n"
    "v_mfma_f32_16x16x32_bf16 v[56:59], v[32:35], v[32:35], 0\n"
    "s_waitcnt lgkmcnt(2)\n"
    "ds_read2st64_b64 v[32:35], v5 offset0:4 offset1:5\n"
    "v_mul_u32_u24 v81, 16256, v81\n"
    "v_fmac_f32_dpp v61, v76, v77 row_newbcast:1 row_mask:0xf bank_mask:0xf\n"
    "v_mfma_f32_16x16x32_bf16 v[40:43], a[48:51], v[36:39], v[40:43]\n"
    "v_add_u32 v81, v81, v80\n"
    "v_permlane16_swap_b32 v68, v70\n"
    "v_mfma_f32_16x16x32_bf16 v[44:47], a[52:55], v[36:39], v[44:47]\n"
    "v_permlane16_swap_b32 v69, v71\n"
    "v_fmac_f32_dpp v65, v76, v77 row_newbcast:5 row_mask:0xf bank_mask:0xf\n"
    "v_mfma_f32_16x16x32_bf16 v[48:51], a[56:59], v[36:39], v[48:51]\n"
    "s_waitcnt lgkmcnt(2)\n"
    "ds_read2st64_b64 v[36:39], v4 offset0:12 offset1:13\n"
    "v_exp_f32 v60, v60\n"
    "v_mfma_f32_16x16x32_bf16 v[52:55], v[20:23], v[24:27], v[52:55]\n"
    "buffer_load_dwordx4 v[248:251], v0, %[srdw], s64 offen offset:3072 sc1\n"
    "v_fma_f32 v60, v60, v73, v73\n"
    "v_mul_f32 v65, v61, v65\n"
    "v_mfma_f32_16x16x32_bf16 v[56:59], v[24:27], v[24:27], v[56:59]\n"
    "s_waitcnt lgkmcnt(2)\n"
    "ds_read2st64_b64 v[24:27], v5 offset0:6 offset1:7\n"
    "v_rcp_f32 v60, v60\n"
    "v_mfma_f32_16x16x32_bf16 v[40:43], a[60:63], v[28:31], v[40:43]\n"
    "s_waitcnt vmcnt(20)\n"
    "v_cmp_ne_u32 vcc, %[tag], v82\n"
    "s_nop 0\n"
    "s_and_b32 vcc_lo, vcc_lo, 0xfffff\n"
    "s_cbranch_scc1 L17_%=\n"
    "L18_%=:\n"
    "v_mfma_f32_16x16x32_bf16 v[44:47], a[64:67], v[28:31], v[44:47]\n"
    "s_add_u32 m0, %[ldsx], 43008\n"
    "s_add_u32 s68, %[ry], 4980736\n"
    "buffer_load_dwordx4 v80, %[srdw], s68 offen sc1 lds\n"
    "v_mul_f32 v60, v60, v64\n"
    "v_mfma_f32_16x16x32_bf16 v[48:51], a[68:71], v[28:31], v[48:51]\n"
    "s_waitcnt lgkmcnt(2)\n"
    "ds_read2st64_b64 v[28:31], v4 offset0:14 offset1:15\n"
    "v_mul_f32 v61, v72, v61\n"
    "v_fmac_f32_dpp v62, v76, v77 row_newbcast:2 row_mask:0xf bank_mask:0xf\n"
    "v_mfma_f32_16x16x32_bf16 v[52:55], v[20:23], v[32:35], v[52:55]\n"
    "v_exp_f32 v61, v61\n"
    "v_fmac_f32_dpp v66, v76, v77 row_newbcast:6 row_mask:0xf bank_mask:0xf\n"
    "v_mfma_f32_16x16x32_bf16 v[56:59], v[32:35], v[32:35], v[56:59]\n"
    "s_waitcnt lgkmcnt(2)\n"
    "ds_read2st64_b64 v[32:35], v5 offset0:8 offset1:9\n"
    "v_fma_f32 v61, v61, v73, v73\n"
    "v_mul_f32 v66, v62, v66\n"
    "v_mfma_f32_16x16x32_bf16 v[40:43], a[72:75], v[36:39], v[40:43]\n"
    "buffer_load_dwordx4 v80, %[srdw], s68 offen offset:1024 sc1 lds\n"
    "v_rcp_f32 v61, v61\n"
    "v_mfma_f32_16x16x32_bf16 v[44:47], a[76:79], v[36:39], v[44:47]\n"
    "v_mul_f32 v61, v61, v65\n"
    "v_cvt_pk_bf16_f32 v60, v60, v61\n"
    "v_mul_f32 v62, v72, v62\n"
    "v_mfma_f32_16x16x32_bf16 v[48:51], a[80:83], v[36:39], v[48:51]\n"
    "s_waitcnt lgkmcnt(2)\n"
    "ds_read2st64_b64 v[36:39], v4 offset0:16 offset1:17\n"
    "v_exp_f32 v62, v62\n"
    "v_mfma_f32_16x16x32_bf16 v[52:55], v[20:23], v[24:27], v[52:55]\n"
    "s_mov_b32 exec_hi, 0\n"
    "buffer_load_dwordx4 v80, %[srdw], s68 offen offset:2048 sc1 lds\n"
    "s_mov_b32 exec_hi, -1\n"
    "v_fma_f32 v62, v62, v73, v73\n"
    "v_mfma_f32_16x16x32_bf16 v[56:59], v[24:27], v[24:27], v[56:59]\n"
    "s_waitcnt lgkmcnt(2)\n"
    "ds_read2st64_b64 v[24:27], v4 offset0:18 offset1:19\n"
    "v_rcp_f32 v62, v62\n"
    "v_mfma_f32_16x16x32_bf16 v[40:43], a[84:87], v[28:31], v[40:43]\n"
    "s_add_u32 m0, %[ldsx], 45568\n"
    "s_add_u32 s68, %[ryy], 2359296\n"
    "buffer_load_dwordx4 v81, %[srdw], s68 offen sc1 lds\n"
    "v_mul_f32 v62, v62, v66\n"
    "v_mfma_f32_16x16x32_bf16 v[44:47], a[88:91], v[28:31], v[44:47]\n"
    "v_fmac_f32_dpp v63, v76, v77 row_newbcast:3 row_mask:0xf bank_mask:0xf\n"
    "v_fmac_f32_dpp v67, v76, v77 row_newbcast:7 row_mask:0xf bank_mask:0xf\n"
    "v_mul_f32 v67, v63, v67\n"
    "v_mfma_f32_16x16x32_bf16 v[48:51], a[92:95], v[28:31], v[48:51]\n"
    "s_waitcnt lgkmcnt(2)\n"
    "ds_read2st64_b64 v[28:31], v4 offset0:20 offset1:21\n"
    "v_mul_f32 v63, v72, v63\n"
    "v_mul_f32 v70, v68, v70\n"
    "v_mfma_f32_16x16x32_bf16 v[52:55], v[20:23], v[32:35], v[52:55]\n"
    "v_exp_f32 v63, v63\n"
    "v_mul_f32 v68, v72, v68\n"
    "v_mfma_f32_16x16x32_bf16 v[56:59], v[32:35], v[32:35], v[56:59]\n"
    "s_waitcnt lgkmcnt(2)\n"
    "ds_read2st64_b64 v[32:35], v4 offset0:22 offset1:23\n"
    "v_fma_f32 v63, v63, v73, v73\n"
    "v_mul_f32 v71, v69, v71\n"
    "v_mfma_f32_16x16x32_bf16 v[40:43], a[96:99], v[36:39], v[40:43]\n"
    "s_add_u32 m0, %[ldsx], 46592\n"
    "s_add_u32 s68, %[ryy], 2490368\n"
    "buffer_load_dwordx4 v81, %[srdw], s68 offen sc1 lds\n"
    "v_mul_f32 v69, v72, v69\n"
    "v_mfma_f32_16x16x32_bf16 v[44:47], a[100:103], v[36:39], v[44:47]\n"
    "v_cndmask_b32_e64 v53, v56, v57, s[48:49]\n"
    "v_cndmask_b32_e64 v53, v53, v58, s[50:51]\n"
    "v_mfma_f32_16x16x32_bf16 v[48:51], a[104:107], v[36:39], v[48:51]\n"
    "s_waitcnt lgkmcnt(2)\n"
    "ds_read2st64_b64 v[36:39], v4 offset0:24 offset1:25\n"
    "v_cndmask_b32_e64 v53, v53, v59, s[52:53]\n"
    "v_mfma_f32_16x16x32_bf16 v[40:43], a[108:111], v[24:27], v[40:43]\n"
    "ds_write_b64 v8, v[52:53] offset:0\n"
    "v_mfma_f32_16x16x32_bf16 v[44:47], a[112:115], v[24:27], v[44:47]\n"
    "s_add_u32 m0, %[ldsx], 47616\n"
    "s_add_u32 s68, %[ryy], 2621440\n"
    "s_mov_b32 exec_hi, 0\n"
    "buffer_load_dwordx4 v81, %[srdw], s68 offen sc1 lds\n"
    "s_mov_b32 exec_hi, -1\n"
    "v_mfma_f32_16x16x32_bf16 v[48:51], a[116:119], v[24:27], v[48:51]\n"
    "s_waitcnt lgkmcnt(3)\n"
    "ds_read2st64_b64 v[112:115], v4 offset0:36 offset1:37\n"
    "ds_read2st64_b64 v[116:119], v4 offset0:38 offset1:39\n"
    "v_mfma_f32_16x16x32_bf16 v[40:43], a[120:123], v[28:31], v[40:43]\n"
    "ds_read2st64_b64 v[24:27], v4 offset0:26 offset1:27\n"
    "buffer_load_dwordx4 v[244:247], v0, %[srdw], s64 offen offset:2048 sc1\n"
    "v_mfma_f32_16x16x32_bf16 v[44:47], a[124:127], v[28:31], v[44:47]\n"
    "v_rcp_f32 v63, v63\n"
    "v_mfma_f32_16x16x32_bf16 v[48:51], a[128:131], v[28:31], v[48:51]\n"
    "s_waitcnt lgkmcnt(5)\n"
    "ds_read2st64_b64 v[28:31], v4 offset0:28 offset1:29\n"
    "v_mul_f32 v63, v63, v67\n"
    "v_cvt_pk_bf16_f32 v61, v62, v63\n"
    "v_mfma_f32_16x16x32_bf16 v[40:43], a[132:135], v[32:35], v[40:43]\n"
    "buffer_load_dwordx4 v[240:243], v0, %[srdw], s64 offen offset:1024 sc1\n"
    "v_exp_f32 v68, v68\n"
    "v_mfma_f32_16x16x32_bf16 v[44:47], a[136:139], v[32:35], v[44:47]\n"
    "v_fma_f32 v68, v68, v73, v73\n"
    "v_rcp_f32 v68, v68\n"
    "v_mfma_f32_16x16x32_bf16 v[48:51], a[140:143], v[32:35], v[48:51]\n"
    "s_waitcnt lgkmcnt(5)\n"
    "ds_read2st64_b64 v[32:35], v4 offset0:30 offset1:31\n"
    "v_mul_f32 v68, v68, v70\n"
    "v_mfma_f32_16x16x32_bf16 v[40:43], a[144:147], v[36:39], v[40:43]\n"
    "v_exp_f32 v69, v69\n"
    "v_mfma_f32_16x16x32_bf16 v[44:47], a[148:151], v[36:39], v[44:47]\n"
    "buffer_load_dwordx4 v[236:239], v0, %[srdw], s64 offen offset:0 sc1\n"
    "v_fma_f32 v69, v69, v73, v73\n"
    "v_mfma_f32_16x16x32_bf16 v[48:51], a[152:155], v[36:39], v[48:51]\n"
    "s_waitcnt lgkmcnt(2)\n"
    "ds_read2st64_b64 v[36:39], v4 offset0:32 offset1:33\n"
    "v_rcp_f32 v69, v69\n"
    "v_mfma_f32_16x16x32_bf16 v[40:43], a[156:159], v[24:27], v[40:43]\n"
    "v_mul_f32 v69, v69, v71\n"
    "v_cvt_pk_bf16_f32 v62, v68, v69\n"
    "v_mfma_f32_16x16x32_bf16 v[44:47], a[160:163], v[24:27], v[44:47]\n"
    "v_permlane32_swap_b32 v61, v63\n"
    "v_mfma_f32_16x16x32_bf16 v[48:51], a[164:167], v[24:27], v[48:51]\n"
    "s_waitcnt lgkmcnt(2)\n"
    "ds_read2st64_b64 v[24:27], v4 offset0:34 offset1:35\n"
    "v_permlane16_swap_b32 v62, v63\n"
    "v_mfma_f32_16x16x32_bf16 v[40:43], a[168:171], v[28:31], v[40:43]\n"
    "buffer_load_dwordx4 v[232:235], v0, %[srdw], s79 offen offset:3072 sc1\n"
    "v_permlane16_swap_b32 v60, v61\n"
    "v_mfma_f32_16x16x32_bf16 v[44:47], a[172:175], v[28:31], v[44:47]\n"
    "buffer_store_dwordx4 v[60:63], v10, %[srdw], s83 offen sc1\n"
    "v_mfma_f32_16x16x32_bf16 v[48:51], a[176:179], v[28:31], v[48:51]\n"
    "s_waitcnt lgkmcnt(2)\n"
    "v_mfma_f32_16x16x32_bf16 v[40:43], a[180:183], v[32:35], v[40:43]\n"
    "v_mfma_f32_16x16x32_bf16 v[44:47], a[184:187], v[32:35], v[44:47]\n"
    "s_add_u32 s85, %[f4p], 1180672\n"
    "v_mfma_f32_16x16x32_bf16 v[48:51], a[188:191], v[32:35], v[48:51]\n"
    "s_waitcnt lgkmcnt(1)\n"
    "buffer_load_dwordx4 v[228:231], v0, %[srdw], s79 offen offset:2048 sc1\n"
    "v_mfma_f32_16x16x32_bf16 v[40:43], a[192:195], v[36:39], v[40:43]\n"
    "v_mfma_f32_16x16x32_bf16 v[44:47], a[196:199], v[36:39], v[44:47]\n"
    "v_mfma_f32_16x16x32_bf16 v[48:51], a[200:203], v[36:39], v[48:51]\n"
    "s_waitcnt lgkmcnt(0)\n"
    "v_mfma_f32_16x16x32_bf16 v[40:43], a[204:207], v[24:27], v[40:43]\n"
    "v_mfma_f32_16x16x32_bf16 v[44:47], a[208:211], v[24:27], v[44:47]\n"
    "buffer_load_dwordx4 v[224:227], v0, %[srdw], s79 offen offset:1024 sc1\n"
    "v_mfma_f32_16x16x32_bf16 v[48:51], a[212:215], v[24:27], v[48:51]\n"
    "v_mfma_f32_16x16x32_bf16 v[40:43], a[216:219], v[112:115], v[40:43]\n"
    "v_mfma_f32_16x16x32_bf16 v[44:47], a[220:223], v[112:115], v[44:47]\n"
    "v_mfma_f32_16x16x32_bf16 v[48:51], a[224:227], v[112:115], v[48:51]\n"
    "buffer_load_dwordx4 v[220:223], v0, %[srdw], s79 offen offset:0 sc1\n"
    "v_mfma_f32_16x16x32_bf16 v[40:43], a[228:231], v[116:119], v[40:43]\n"
    "s_add_u32 s88, s44, 9568256\n"
    "v_mfma_f32_16x16x32_bf16 v[44:47], a[232:235], v[116:119], v[44:47]\n"
    "v_mfma_f32_16x16x32_bf16 v[48:51], a[236:239], v[116:119], v[48:51]\n"
    "s_waitcnt vmcnt(19)\n"
    "v_cmp_ne_u32 vcc, %[tag], v78\n"
    "s_nop 0\n"
    "s_and_b32 vcc_lo, vcc_lo, 0xf\n"
    "s_cbranch_scc1 L19_%=\n"
    "L20_%=:\n"
    "s_barrier\n"
    "ds_read2st64_b64 v[28:31], v4 offset0:42 offset1:43\n"
    "ds_read2st64_b64 v[32:35], v4 offset0:44 offset1:45\n"
    "ds_read2st64_b64 v[36:39], v4 offset0:46 offset1:47\n"
    "ds_read2st64_b64 v[24:27], v4 offset0:48 offset1:49\n"
    "s_add_u32 s87, %[f4c], 1180160\n"
    "s_mov_b64 exec, 0xf\n"
    "buffer_load_dword v78, v1, %[srdw], s87 offen sc1\n"
    "s_mov_b64 exec, -1\n"
    "buffer_load_dwordx4 v[80:83], v0, %[srdw], s88 offen offset:0 sc1\n"
    "s_mov_b64 exec, s[94:95]\n"
    "buffer_store_dword v2, off, %[srdw], s85 sc1\n"
    "s_mov_b64 exec, -1\n"
    "ds_read_b128 v[52:55], v9 offset:0\n"
    "ds_read_b128 v[56:59], v9 offset:16\n"
    "s_add_u32 s86, %[hst], 14286848\n"
    "s_waitcnt lgkmcnt(5)\n"
    "v_mfma_f32_16x16x32_bf16 v[60:63], a[0:3], v[28:31], 0\n"
    "v_mfma_f32_16x16x32_bf16 v[64:67], a[4:7], v[28:31], 0\n"
    "buffer_load_dwordx4 v[84:87], v0, %[srdw], s88 offen offset:1024 sc1\n"
    "v_mfma_f32_16x16x32_bf16 v[68:71], a[8:11], v[28:31], 0\n"
    "s_waitcnt lgkmcnt(4)\n"
    "ds_read2st64_b64 v[28:31], v5 offset0:42 offset1:43\n"
    "v_mfma_f32_16x16x32_bf16 v[60:63], a[12:15], v[32:35], v[60:63]\n"
    "s_add_u32 s89, s45, 9568256\n"
    "buffer_load_dwordx4 v[88:91], v0, %[srdw], s88 offen offset:2048 sc1\n"
    "v_mfma_f32_16x16x32_bf16 v[64:67], a[16:19], v[32:35], v[64:67]\n"
    "s_waitcnt lgkmcnt(2)\n"
    "v_add_f32 v52, v52, v54\n"
    "v_add_f32 v53, v53, v55\n"
    "v_mfma_f32_16x16x32_bf16 v[68:71], a[20:23], v[32:35], v[68:71]\n"
    "s_waitcnt lgkmcnt(1)\n"
    "ds_read2st64_b64 v[32:35], v4 offset0:50 offset1:51\n"
    "v_add_f32 v56, v56, v58\n"
    "v_mfma_f32_16x16x32_bf16 v[60:63], a[24:27], v[36:39], v[60:63]\n"
    "buffer_load_dwordx4 v[92:95], v0, %[srdw], s89 offen offset:0 sc1\n"
    "v_add_f32 v57, v57, v59\n"
    "v_mfma_f32_16x16x32_bf16 v[64:67], a[28:31], v[36:39], v[64:67]\n"
    "v_add_f32 v52, v52, v56\n"
    "v_add_f32 v53, v53, v57\n"
    "v_mfma_f32_16x16x32_bf16 v[68:71], a[32:35], v[36:39], v[68:71]\n"
    "ds_read2st64_b64 v[36:39], v5 offset0:44 offset1:45\n"
    "v_mul_f32 v77, 0xbacccccd, v52\n"
    "v_mfma_f32_16x16x32_bf16 v[60:63], a[36:39], v[24:27], v[60:63]\n"
    "s_add_u32 s90, s46, 9568256\n"
    "v_mul_f32 v53, 0x3acccccd, v53\n"
    "v_fma_f32 v53, -v77, v77, v53\n"
    "v_fmac_f32_dpp v48, v76, v77 row_newbcast:8 row_mask:0xf bank_mask:0xf\n"
    "v_mfma_f32_16x16x32_bf16 v[64:67], a[40:43], v[24:27], v[64:67]\n"
    "buffer_load_dwordx4 v[96:99], v0, %[srdw], s89 offen offset:1024 sc1\n"
    "v_max_f32 v53, 0, v53\n"
    "v_fmac_f32_dpp v49, v76, v77 row_newbcast:9 row_mask:0xf bank_mask:0xf\n"
    "v_mfma_f32_16x16x32_bf16 v[68:71], a[44:47], v[24:27], v[68:71]\n"
    "ds_read2st64_b64 v[24:27], v4 offset0:52 offset1:53\n"
    "v_add_f32 v73, 0x3727c5ac, v53\n"
    "v_fmac_f32_dpp v50, v76, v77 row_newbcast:10 row_mask:0xf bank_mask:0xf\n"
    "v_rsq_f32 v52, v73\n"
    "s_nop 0\n"
    "v_mul_f32 v72, 0xbfb8aa3b, v52\n"
    "s_waitcnt lgkmcnt(3)\n"
    "v_mfma_f32_16x16x32_bf16 v[52:55], v[20:23], v[28:31], 0\n"
    "buffer_load_dwordx4 v[100:103], v0, %[srdw], s89 offen offset:2048 sc1\n"
    "v_fmac_f32_dpp v51, v76, v77 row_newbcast:11 row_mask:0xf bank_mask:0xf\n"
    "v_fmac_f32_dpp v40, v76, v77 row_newbcast:0 row_mask:0xf bank_mask:0xf\n"
    "v_mfma_f32_16x16x32_bf16 v[56:59], v[28:31], v[28:31], 0\n"
    "s_waitcnt lgkmcnt(2)\n"
    "ds_read2st64_b64 v[28:31], v5 offset0:46 offset1:47\n"
    "v_permlane16_swap_b32 v48, v50\n"
    "v_mfma_f32_16x16x32_bf16 v[60:63], a[48:51], v[32:35], v[60:63]\n"
    "v_permlane16_swap_b32 v49, v51\n"
    "v_fmac_f32_dpp v44, v76, v77 row_newbcast:4 row_mask:0xf bank_mask:0xf\n"
    "v_mfma_f32_16x16x32_bf16 v[64:67], a[52:55], v[32:35], v[64:67]\n"
    "s_add_u32 s91, s47, 9568256\n"
    "buffer_load_dwordx4 v[104:107], v0, %[srdw], s90 offen offset:0 sc1\n"
    "v_mul_f32 v44, v40, v44\n"
    "v_mfma_f32_16x16x32_bf16 v[68:71], a[56:59], v[32:35], v[68:71]\n"
    "s_waitcnt lgkmcnt(2)\n"
    "ds_read2st64_b64 v[32:35], v4 offset0:54 offset1:55\n"
    "v_mul_f32 v40, v72, v40\n"
    "v_fmac_f32_dpp v41, v76, v77 row_newbcast:1 row_mask:0xf bank_mask:0xf\n"
    "v_mfma_f32_16x16x32_bf16 v[52:55], v[20:23], v[36:39], v[52:55]\n"
    "v_exp_f32 v40, v40\n"
    "v_fmac_f32_dpp v45, v76, v77 row_newbcast:5 row_mask:0xf bank_mask:0xf\n"
    "v_mfma_f32_16x16x32_bf16 v[56:59], v[36:39], v[36:39], v[56:59]\n"
    "s_waitcnt lgkmcnt(2)\n"
    "buffer_load_dwordx4 v[108:111], v0, %[srdw], s90 offen offset:1024 sc1\n"
    "v_fma_f32 v40, v40, v73, v73\n"
    "v_mfma_f32_16x16x32_bf16 v[60:63], a[60:63], v[24:27], v[60:63]\n"
    "ds_read2st64_b64 v[36:39], v5 offset0:48 offset1:49\n"
    "v_rcp_f32 v40, v40\n"
    "v_mfma_f32_16x16x32_bf16 v[64:67], a[64:67], v[24:27], v[64:67]\n"
    "v_mul_f32 v40, v40, v44\n"
    "v_mul_f32 v45, v41, v45\n"
    "v_mul_f32 v41, v72, v41\n"
    "v_mfma_f32_16x16x32_bf16 v[68:71], a[68:71], v[24:27], v[68:71]\n"
    "s_waitcnt lgkmcnt(2)\n"
    "buffer_load_dwordx4 v[112:115], v0, %[srdw], s90 offen offset:2048 sc1\n"
    "v_fmac_f32_dpp v42, v76, v77 row_newbcast:2 row_mask:0xf bank_mask:0xf\n"
    "v_mfma_f32_16x16x32_bf16 v[52:55], v[20:23], v[28:31], v[52:55]\n"
    "ds_read2st64_b64 v[24:27], v4 offset0:56 offset1:57\n"
    "v_exp_f32 v41, v41\n"
    "v_mfma_f32_16x16x32_bf16 v[56:59], v[28:31], v[28:31], v[56:59]\n"
    "s_waitcnt lgkmcnt(2)\n"
    "ds_read2st64_b64 v[28:31], v5 offset0:50 offset1:51\n"
    "v_fma_f32 v41, v41, v73, v73\n"
    "v_fmac_f32_dpp v46, v76, v77 row_newbcast:6 row_mask:0xf bank_mask:0xf\n"
    "v_mfma_f32_16x16x32_bf16 v[60:63], a[72:75], v[32:35], v[60:63]\n"
    "v_rcp_f32 v41, v41\n"
    "v_mul_f32 v46, v42, v46\n"
    "v_mfma_f32_16x16x32_bf16 v[64:67], a[76:79], v[32:35], v[64:67]\n"
    "buffer_load_dwordx4 v[116:119], v0, %[srdw], s91 offen offset:0 sc1\n"
    "v_mul_f32 v41, v41, v45\n"
    "v_cvt_pk_bf16_f32 v40, v40, v41\n"
    "v_mfma_f32_16x16x32_bf16 v[68:71], a[80:83], v[32:35], v[68:71]\n"
    "s_waitcnt lgkmcnt(2)\n"
    "ds_read2st64_b64 v[32:35], v4 offset0:58 offset1:59\n"
    "v_min_u32 v40, -2, v40\n"
    "v_mul_f32 v42, v72, v42\n"
    "v_mfma_f32_16x16x32_bf16 v[52:55], v[20:23], v[36:39], v[52:55]\n"
    "v_exp_f32 v42, v42\n"
    "v_fmac_f32_dpp v43, v76, v77 row_newbcast:3 row_mask:0xf bank_mask:0xf\n"
    "v_mfma_f32_16x16x32_bf16 v[56:59], v[36:39], v[36:39], v[56:59]\n"
    "s_waitcnt lgkmcnt(2)\n"
    "buffer_load_dwordx4 v[120:123], v0, %[srdw], s91 offen offset:1024 sc1\n"
    "v_fma_f32 v42, v42, v73, v73\n"
    "v_mfma_f32_16x16x32_bf16 v[60:63], a[84:87], v[24:27], v[60:63]\n"
    "ds_read2st64_b64 v[36:39], v4 offset0:60 offset1:61\n"
    "v_rcp_f32 v42, v42\n"
    "v_mfma_f32_16x16x32_bf16 v[64:67], a[88:91], v[24:27], v[64:67]\n"
    "v_mul_f32 v42, v42, v46\n"
    "v_fmac_f32_dpp v47, v76, v77 row_newbcast:7 row_mask:0xf bank_mask:0xf\n"
    "v_mul_f32 v47, v43, v47\n"
    "v_mfma_f32_16x16x32_bf16 v[68:71], a[92:95], v[24:27], v[68:71]\n"
    "s_waitcnt lgkmcnt(2)\n"
    "buffer_load_dwordx4 v[124:127], v0, %[srdw], s91 offen offset:2048 sc1\n"
    "v_mul_f32 v43, v72, v43\n"
    "v_mfma_f32_16x16x32_bf16 v[52:55], v[20:23], v[28:31], v[52:55]\n"
    "ds_read2st64_b64 v[24:27], v4 offset0:62 offset1:63\n"
    "v_exp_f32 v43, v43\n"
    "v_mfma_f32_16x16x32_bf16 v[56:59], v[28:31], v[28:31], v[56:59]\n"
    "s_waitcnt lgkmcnt(2)\n"
    "ds_read2st64_b64 v[28:31], v4 offset0:64 offset1:65\n"
    "v_fma_f32 v43, v43, v73, v73\n"
    "v_mul_f32 v50, v48, v50\n"
    "v_mfma_f32_16x16x32_bf16 v[60:63], a[96:99], v[32:35], v[60:63]\n"
    "v_rcp_f32 v43, v43\n"
    "v_mul_f32 v48, v72, v48\n"
    "v_mfma_f32_16x16x32_bf16 v[64:67], a[100:103], v[32:35], v[64:67]\n"
    "v_cndmask_b32_e64 v53, v56, v57, s[48:49]\n"
    "v_cndmask_b32_e64 v53, v53, v58, s[50:51]\n"
    "v_mul_f32 v43, v43, v47\n"
    "v_mfma_f32_16x16x32_bf16 v[68:71], a[104:107], v[32:35], v[68:71]\n"
    "s_waitcnt lgkmcnt(2)\n"
    "ds_read2st64_b64 v[32:35], v4 offset0:66 offset1:67\n"
    "v_cndmask_b32_e64 v53, v53, v59, s[52:53]\n"
    "v_cvt_pk_bf16_f32 v41, v42, v43\n"
    "v_mfma_f32_16x16x32_bf16 v[60:63], a[108:111], v[36:39], v[60:63]\n"
    "ds_write_b64 v8, v[52:53] offset:512\n"
    "v_mfma_f32_16x16x32_bf16 v[64:67], a[112:115], v[36:39], v[64:67]\n"
    "v_min_u32 v41, -2, v41\n"
    "v_exp_f32 v48, v48\n"
    "v_mfma_f32_16x16x32_bf16 v[68:71], a[116:119], v[36:39], v[68:71]\n"
    "s_waitcnt lgkmcnt(3)\n"
    "ds_read2st64_b64 v[36:39], v4 offset0:68 offset1:69\n"
    "v_fma_f32 v48, v48, v73, v73\n"
    "v_mul_f32 v51, v49, v51\n"
    "v_mfma_f32_16x16x32_bf16 v[60:63], a[120:123], v[24:27], v[60:63]\n"
    "v_rcp_f32 v48, v48\n"
    "v_mul_f32 v49, v72, v49\n"
    "v_mfma_f32_16x16x32_bf16 v[64:67], a[124:127], v[24:27], v[64:67]\n"
    "v_mul_f32 v48, v48, v50\n"
    "v_exp_f32 v49, v49\n"
    "v_mfma_f32_16x16x32_bf16 v[68:71], a[128:131], v[24:27], v[68:71]\n"
    "s_waitcnt lgkmcnt(3)\n"
    "ds_read2st64_b64 v[24:27], v4 offset0:70 offset1:71\n"
    "v_fma_f32 v49, v49, v73, v73\n"
    "v_mfma_f32_16x16x32_bf16 v[60:63], a[132:135], v[28:31], v[60:63]\n"
    "s_add_u32 s92, s93, 1311744\n"
    "s_add_u32 s56, %[f6p], 163840\n"
    "v_rcp_f32 v49, v49\n"
    "v_mfma_f32_16x16x32_bf16 v[64:67], a[136:139], v[28:31], v[64:67]\n"
    "v_mul_f32 v49, v49, v51\n"
    "v_cvt_pk_bf16_f32 v42, v48, v49\n"
    "v_min_u32 v42, -2, v42\n"
    "v_mfma_f32_16x16x32_bf16 v[68:71], a[140:143], v[28:31], v[68:71]\n"
    "s_waitcnt lgkmcnt(3)\n"
    "ds_read2st64_b64 v[28:31], v4 offset0:72 offset1:73\n"
    "v_permlane32_swap_b32 v41, v43\n"
    "v_mfma_f32_16x16x32_bf16 v[60:63], a[144:147], v[32:35], v[60:63]\n"
    "v_mfma_f32_16x16x32_bf16 v[64:67], a[148:151], v[32:35], v[64:67]\n"
    "v_permlane16_swap_b32 v42, v43\n"
    "v_mfma_f32_16x16x32_bf16 v[68:71], a[152:155], v[32:35], v[68:71]\n"
    "s_waitcnt lgkmcnt(2)\n"
    "ds_read2st64_b64 v[32:35], v4 offset0:74 offset1:75\n"
    "v_permlane16_swap_b32 v40, v41\n"
    "v_mfma_f32_16x16x32_bf16 v[60:63], a[156:159], v[36:39], v[60:63]\n"
    "buffer_store_dwordx4 v[40:43], v10, %[srdw], s86 offen sc1\n"
    "v_mfma_f32_16x16x32_bf16 v[64:67], a[160:163], v[36:39], v[64:67]\n"
    "s_waitcnt vmcnt(18)\n"
    "s_barrier\n"
    "v_mfma_f32_16x16x32_bf16 v[68:71], a[164:167], v[36:39], v[68:71]\n"
    "s_waitcnt lgkmcnt(2)\n"
    "ds_read2st64_b64 v[36:39], v4 offset0:76 offset1:77\n"
    "ds_read_b128 v[52:55], v9 offset:512\n"
    "v_mfma_f32_16x16x32_bf16 v[60:63], a[168:171], v[24:27], v[60:63]\n"
    "s_waitcnt vmcnt(14)\n"
    "v_cmp_ne_u32 vcc, %[tag], v78\n"
    "s_nop 0\n"
    "s_and_b32 vcc_lo, vcc_lo, 0xf\n"
    "s_cbranch_scc1 L21_%=\n"
    "L22_%=:\n"
    "v_mfma_f32_16x16x32_bf16 v[64:67], a[172:175], v[24:27], v[64:67]\n"
    "s_mov_b64 exec, s[94:95]\n"
    "buffer_store_dword v2, off, %[srdw], s92 sc1\n"
    "s_mov_b64 exec, -1\n"
    "v_mfma_f32_16x16x32_bf16 v[68:71], a[176:179], v[24:27], v[68:71]\n"
    "s_waitcnt lgkmcnt(3)\n"
    "ds_read2st64_b64 v[24:27], v4 offset0:78 offset1:79\n"
    "ds_read_b128 v[56:59], v9 offset:528\n"
    "v_mfma_f32_16x16x32_bf16 v[60:63], a[180:183], v[28:31], v[60:63]\n"
    "s_mov_b64 exec, s[94:95]\n"
    "buffer_store_dword v2, off, %[srdw], s56 sc1\n"
    "s_mov_b64 exec, -1\n"
    "v_mfma_f32_16x16x32_bf16 v[64:67], a[184:187], v[28:31], v[64:67]\n"
    "v_mfma_f32_16x16x32_bf16 v[68:71], a[188:191], v[28:31], v[68:71]\n"
    "s_waitcnt lgkmcnt(4)\n"
    "ds_read2st64_b64 v[28:31], v4 offset0:80 offset1:81\n"
    "v_mfma_f32_16x16x32_bf16 v[60:63], a[192:195], v[32:35], v[60:63]\n"
    "s_waitcnt lgkmcnt(3)\n"
    "v_add_f32 v52, v52, v54\n"
    "v_add_f32 v53, v53, v55\n"
    "v_mfma_f32_16x16x32_bf16 v[64:67], a[196:199], v[32:35], v[64:67]\n"
    "v_mfma_f32_16x16x32_bf16 v[68:71], a[200:203], v[32:35], v[68:71]\n"
    "ds_read_b128 v[32:35], v11 offset:0\n"
    "v_mfma_f32_16x16x32_bf16 v[60:63], a[204:207], v[36:39], v[60:63]\n"
    "v_mfma_f32_16x16x32_bf16 v[64:67], a[208:211], v[36:39], v[64:67]\n"
    "s_waitcnt lgkmcnt(2)\n"
    "v_add_f32 v56, v56, v58\n"
    "v_add_f32 v57, v57, v59\n"
    "v_mfma_f32_16x16x32_bf16 v[68:71], a[212:215], v[36:39], v[68:71]\n"
    "ds_read_b128 v[36:39], v11 offset:1024\n"
    "v_add_f32 v52, v52, v56\n"
    "v_mfma_f32_16x16x32_bf16 v[60:63], a[216:219], v[24:27], v[60:63]\n"
    "v_add_f32 v53, v53, v57\n"
    "v_mul_f32 v3, 0xbacccccd, v52\n"
    "v_mfma_f32_16x16x32_bf16 v[64:67], a[220:223], v[24:27], v[64:67]\n"
    "v_mul_f32 v53, 0x3acccccd, v53\n"
    "v_fma_f32 v53, -v3, v3, v53\n"
    "v_mfma_f32_16x16x32_bf16 v[68:71], a[224:227], v[24:27], v[68:71]\n"
    "s_waitcnt lgkmcnt(2)\n"
    "ds_read_b128 v[24:27], v11 offset:2048\n"
    "v_max_f32 v53, 0, v53\n"
    "v_mfma_f32_16x16x32_bf16 v[60:63], a[228:231], v[28:31], v[60:63]\n"
    "v_add_f32 v5, 0x3727c5ac, v53\n"
    "v_mfma_f32_16x16x32_bf16 v[64:67], a[232:235], v[28:31], v[64:67]\n"
    "v_rsq_f32 v52, v5\n"
    "v_mfma_f32_16x16x32_bf16 v[68:71], a[236:239], v[28:31], v[68:71]\n"
    "s_waitcnt vmcnt(15)\n"
    "ds_read_b128 v[28:31], v11 offset:3072\n"
    "v_mul_f32 v8, 0xbfb8aa3b, v52\n"
    "s_add_u32 s57, s43, 15073280\n"
    "v_mfma_f32_16x16x32_bf16 v[40:43], a[240:243], v[80:83], 0\n"
    "s_waitcnt lgkmcnt(3)\n"
    "v_fmac_f32_dpp v60, v76, v3 row_newbcast:0 row_mask:0xf bank_mask:0xf\n"
    "v_fmac_f32_dpp v64, v76, v3 row_newbcast:4 row_mask:0xf bank_mask:0xf\n"
    "v_fmac_f32_dpp v68, v76, v3 row_newbcast:8 row_mask:0xf bank_mask:0xf\n"
    "v_mfma_f32_16x16x32_bf16 v[44:47], v[32:35], v[80:83], 0\n"
    "ds_read_b128 v[32:35], v11 offset:4096\n"
    "v_fmac_f32_dpp v69, v76, v3 row_newbcast:9 row_mask:0xf bank_mask:0xf\n"
    "v_fmac_f32_dpp v70, v76, v3 row_newbcast:10 row_mask:0xf bank_mask:0xf\n"
    "v_mfma_f32_16x16x32_bf16 v[48:51], a[244:247], v[80:83], 0\n"
    "s_waitcnt lgkmcnt(3)\n"
    "v_fmac_f32_dpp v71, v76, v3 row_newbcast:11 row_mask:0xf bank_mask:0xf\n"
    "v_permlane16_swap_b32 v68, v70\n"
    "v_mfma_f32_16x16x32_bf16 v[52:55], v[36:39], v[80:83], 0\n"
    "s_add_u32 s58, s44, 11141120\n"
    "ds_read_b128 v[36:39], v11 offset:5120\n"
    "v_permlane16_swap_b32 v69, v71\n"
    "v_mfma_f32_16x16x32_bf16 v[56:59], a[248:251], v[80:83], 0\n"
    "s_waitcnt vmcnt(13)\n"
    "buffer_load_dwordx4 v[80:83], v0, %[srdw], s58 offen offset:0 sc1\n"
    "v_mul_f32 v64, v60, v64\n"
    "v_mfma_f32_16x16x32_bf16 v[40:43], a[252:255], v[84:87], v[40:43]\n"
    "s_waitcnt lgkmcnt(3)\n"
    "v_mul_f32 v60, v8, v60\n"
    "v_exp_f32 v60, v60\n"
    "v_mfma_f32_16x16x32_bf16 v[44:47], v[24:27], v[84:87], v[44:47]\n"
    "ds_read_b128 v[24:27], v11 offset:6144\n"
    "v_fma_f32 v60, v60, v5, v5\n"
    "v_fmac_f32_dpp v61, v76, v3 row_newbcast:1 row_mask:0xf bank_mask:0xf\n"
    "v_mfma_f32_16x16x32_bf16 v[48:51], v[128:131], v[84:87], v[48:51]\n"
    "s_waitcnt lgkmcnt(3)\n"
    "v_rcp_f32 v60, v60\n"
    "v_fmac_f32_dpp v65, v76, v3 row_newbcast:5 row_mask:0xf bank_mask:0xf\n"
    "v_mfma_f32_16x16x32_bf16 v[52:55], v[28:31], v[84:87], v[52:55]\n"
    "ds_read_b128 v[28:31], v11 offset:7168\n"
    "v_mul_f32 v60, v60, v64\n"
    "v_mul_f32 v65, v61, v65\n"
    "v_mfma_f32_16x16x32_bf16 v[56:59], v[132:135], v[84:87], v[56:59]\n"
    "s_waitcnt vmcnt(13)\n"
    "buffer_load_dwordx4 v[84:87], v0, %[srdw], s58 offen offset:1024 sc1\n"
    "v_mul_f32 v61, v8, v61\n"
    "v_mfma_f32_16x16x32_bf16 v[40:43], v[136:139], v[88:91], v[40:43]\n"
    "s_waitcnt lgkmcnt(3)\n"
    "v_exp_f32 v61, v61\n"
    "v_fmac_f32_dpp v62, v76, v3 row_newbcast:2 row_mask:0xf bank_mask:0xf\n"
    "v_mfma_f32_16x16x32_bf16 v[44:47], v[32:35], v[88:91], v[44:47]\n"
    "ds_read_b128 v[32:35], v11 offset:8192\n"
    "v_fma_f32 v61, v61, v5, v5\n"
    "v_fmac_f32_dpp v66, v76, v3 row_newbcast:6 row_mask:0xf bank_mask:0xf\n"
    "v_mfma_f32_16x16x32_bf16 v[48:51], v[140:143], v[88:91], v[48:51]\n"
    "s_waitcnt lgkmcnt(3)\n"
    "v_rcp_f32 v61, v61\n"
    "v_mul_f32 v66, v62, v66\n"
    "v_mfma_f32_16x16x32_bf16 v[52:55], v[36:39], v[88:91], v[52:55]\n"
    "ds_read_b128 v[36:39], v11 offset:9216\n"
    "v_mul_f32 v61, v61, v65\n"
    "v_cvt_pk_bf16_f32 v60, v60, v61\n"
    "v_mfma_f32_16x16x32_bf16 v[56:59], v[144:147], v[88:91], v[56:59]\n"
    "s_waitcnt vmcnt(13)\n"
    "buffer_load_dwordx4 v[88:91], v0, %[srdw], s58 offen offset:2048 sc1\n"
    "v_min_u32 v60, -2, v60\n"
    "v_mfma_f32_16x16x32_bf16 v[40:43], v[148:151], v[92:95], v[40:43]\n"
    "s_waitcnt lgkmcnt(3)\n"
    "v_mul_f32 v62, v8, v62\n"
    "v_exp_f32 v62, v62\n"
    "v_mfma_f32_16x16x32_bf16 v[44:47], v[24:27], v[92:95], v[44:47]\n"
    "ds_read_b128 v[24:27], v11 offset:10240\n"
    "v_fma_f32 v62, v62, v5, v5\n"
    "v_fmac_f32_dpp v63, v76, v3 row_newbcast:3 row_mask:0xf bank_mask:0xf\n"
    "v_mfma_f32_16x16x32_bf16 v[48:51], v[152:155], v[92:95], v[48:51]\n"
    "s_waitcnt lgkmcnt(3)\n"
    "v_rcp_f32 v62, v62\n"
    "v_fmac_f32_dpp v67, v76, v3 row_newbcast:7 row_mask:0xf bank_mask:0xf\n"
    "v_mfma_f32_16x16x32_bf16 v[52:55], v[28:31], v[92:95], v[52:55]\n"
    "s_add_u32 s59, s45, 11141120\n"
    "ds_read_b128 v[28:31], v11 offset:11264\n"
    "v_mul_f32 v62, v62, v66\n"
    "v_mul_f32 v67, v63, v67\n"
    "v_mfma_f32_16x16x32_bf16 v[56:59], v[156:159], v[92:95], v[56:59]\n"
    "s_waitcnt vmcnt(13)\n"
    "buffer_load_dwordx4 v[92:95], v0, %[srdw], s59 offen offset:0 sc1\n"
    "v_mul_f32 v63, v8, v63\n"
    "v_mfma_f32_16x16x32_bf16 v[40:43], v[160:163], v[96:99], v[40:43]\n"
    "s_waitcnt lgkmcnt(3)\n"
    "v_exp_f32 v63, v63\n"
    "v_mul_f32 v70, v68, v70\n"
    "v_mfma_f32_16x16x32_bf16 v[44:47], v[32:35], v[96:99], v[44:47]\n"
    "ds_read_b128 v[32:35], v11 offset:12288\n"
    "v_fma_f32 v63, v63, v5, v5\n"
    "v_mul_f32 v68, v8, v68\n"
    "v_mfma_f32_16x16x32_bf16 v[48:51], v[164:167], v[96:99], v[48:51]\n"
    "s_waitcnt lgkmcnt(3)\n"
    "v_rcp_f32 v63, v63\n"
    "v_mul_f32 v71, v69, v71\n"
    "v_mfma_f32_16x16x32_bf16 v[52:55], v[36:39], v[96:99], v[52:55]\n"
    "ds_read_b128 v[36:39], v11 offset:13312\n"
    "v_mul_f32 v63, v63, v67\n"
    "v_cvt_pk_bf16_f32 v61, v62, v63\n"
    "v_mfma_f32_16x16x32_bf16 v[56:59], v[168:171], v[96:99], v[56:59]\n"
    "s_waitcnt vmcnt(13)\n"
    "s_add_u32 s60, %[f4c], 1180672\n"
    "v_min_u32 v61, -2, v61\n"
    "v_mul_f32 v69, v8, v69\n"
    "v_mfma_f32_16x16x32_bf16 v[40:43], v[172:175], v[100:103], v[40:43]\n"
    "s_waitcnt lgkmcnt(3)\n"
    "v_lshlrev_b32 v79, 3, v0\n"
    "s_mov_b64 exec, 0xf\n"
    "buffer_load_dword v79, v79, %[srdw], s60 offen sc1\n"
    "s_mov_b64 exec, -1\n"
    "v_mfma_f32_16x16x32_bf16 v[44:47], v[24:27], v[100:103], v[44:47]\n"
    "buffer_load_dwordx4 v[96:99], v0, %[srdw], s59 offen offset:1024 sc1\n"
    "ds_read_b128 v[24:27], v11 offset:14336\n"
    "v_mfma_f32_16x16x32_bf16 v[48:51], v[176:179], v[100:103], v[48:51]\n"
    "s_waitcnt lgkmcnt(3)\n"
    "v_exp_f32 v68, v68\n"
    "v_mfma_f32_16x16x32_bf16 v[52:55], v[28:31], v[100:103], v[52:55]\n"
    "ds_read_b128 v[28:31], v11 offset:15360\n"
    "v_fma_f32 v68, v68, v5, v5\n"
    "v_mfma_f32_16x16x32_bf16 v[56:59], v[180:183], v[100:103], v[56:59]\n"
    "s_waitcnt vmcnt(14)\n"
    "buffer_load_dwordx4 v[100:103], v0, %[srdw], s59 offen offset:2048 sc1\n"
    "v_mfma_f32_16x16x32_bf16 v[40:43], v[184:187], v[104:107], v[40:43]\n"
    "s_waitcnt lgkmcnt(3)\n"
    "v_rcp_f32 v68, v68\n"
    "v_mfma_f32_16x16x32_bf16 v[44:47], v[32:35], v[104:107], v[44:47]\n"
    "s_add_u32 s61, %[f6c], 163840\n"
    "ds_read_b128 v[32:35], v11 offset:16384\n"
    "v_mul_f32 v68, v68, v70\n"
    "v_mfma_f32_16x16x32_bf16 v[48:51], v[188:191], v[104:107], v[48:51]\n"
    "s_waitcnt lgkmcnt(3)\n"
    "v_exp_f32 v69, v69\n"
    "v_mfma_f32_16x16x32_bf16 v[52:55], v[36:39], v[104:107], v[52:55]\n"
    "s_add_u32 s62, s46, 11141120\n"
    "ds_read_b128 v[36:39], v11 offset:18432\n"
    "v_fma_f32 v69, v69, v5, v5\n"
    "v_mfma_f32_16x16x32_bf16 v[56:59], v[192:195], v[104:107], v[56:59]\n"
    "s_waitcnt vmcnt(14)\n"
    "buffer_load_dwordx4 v[104:107], v0, %[srdw], s62 offen offset:0 sc1\n"
    "kx_a5:\n"
    "s_cbranch_execz kx_a9\n"
    "v_mfma_f32_16x16x32_bf16 v[40:43], v[196:199], v[108:111], v[40:43]\n"
    "s_waitcnt lgkmcnt(3)\n"
    "v_rcp_f32 v69, v69\n"
    "v_mfma_f32_16x16x32_bf16 v[44:47], v[24:27], v[108:111], v[44:47]\n"
    "ds_read_b128 v[24:27], v11 offset:20480\n"
    "v_mul_f32 v69, v69, v71\n"
    "v_cvt_pk_bf16_f32 v62, v68, v69\n"
    "v_mfma_f32_16x16x32_bf16 v[48:51], v[200:203], v[108:111], v[48:51]\n"
    "s_waitcnt lgkmcnt(3)\n"
    "v_min_u32 v62, -2, v62\n"
    "v_permlane32_swap_b32 v61, v63\n"
    "v_mfma_f32_16x16x32_bf16 v[52:55], v[28:31], v[108:111], v[52:55]\n"
    "ds_read_b128 v[28:31], v11 offset:22528\n"
    "v_permlane16_swap_b32 v62, v63\n"
    "v_mfma_f32_16x16x32_bf16 v[56:59], v[204:207], v[108:111], v[56:59]\n"
    "s_waitcnt vmcnt(14)\n"
    "buffer_load_dwordx4 v[108:111], v0, %[srdw], s62 offen offset:1024 sc1\n"
    "v_mfma_f32_16x16x32_bf16 v[40:43], v[208:211], v[112:115], v[40:43]\n"
    "s_waitcnt vmcnt(14)\n"
    "v_permlane16_swap_b32 v60, v61\n"
    "v_mfma_f32_16x16x32_bf16 v[40:43], v[220:223], v[116:119], v[40:43]\n"
    "s_waitcnt vmcnt(13)\n"
    "buffer_store_dwordx4 v[60:63], v10, %[srdw], s57 offen sc1\n"
    "v_mfma_f32_16x16x32_bf16 v[40:43], v[232:235], v[120:123], v[40:43]\n"
    "s_waitcnt vmcnt(13)\n"
    "v_lshlrev_b32 v78, 3, v0\n"
    "s_mov_b64 exec, 0xf\n"
    "buffer_load_dword v78, v78, %[srdw], s61 offen sc1\n"
    "s_mov_b64 exec, -1\n"
    "v_mfma_f32_16x16x32_bf16 v[40:43], v[244:247], v[124:127], v[40:43]\n"
    "s_waitcnt lgkmcnt(3)\n"
    "ds_read_b64 v[62:63], v16 offset:43008\n"
    "ds_read_u16 v72, v17 offset:43008\n"
    "v_mfma_f32_16x16x32_bf16 v[44:47], v[32:35], v[112:115], v[44:47]\n"
    "s_waitcnt lgkmcnt(4)\n"
    "ds_read_b128 v[32:35], v11 offset:17408\n"
    "v_bfe_u32 v20, v0, 8, 1\n"
    "v_mfma_f32_16x16x32_bf16 v[44:47], v[36:39], v[116:119], v[44:47]\n"
    "s_waitcnt lgkmcnt(4)\n"
    "ds_read_b128 v[36:39], v11 offset:19456\n"
    "v_lshlrev_b32 v3, 9, v20\n"
    "v_mfma_f32_16x16x32_bf16 v[44:47], v[24:27], v[120:123], v[44:47]\n"
    "s_waitcnt lgkmcnt(4)\n"
    "ds_read_b128 v[24:27], v11 offset:21504\n"
    "v_bfe_u32 v21, v0, 5, 2\n"
    "v_mfma_f32_16x16x32_bf16 v[44:47], v[28:31], v[124:127], v[44:47]\n"
    "ds_read_b128 v[28:31], v11 offset:23552\n"
    "v_lshl_or_b32 v3, v21, 7, v3\n"
    "v_mfma_f32_16x16x32_bf16 v[48:51], v[212:215], v[112:115], v[48:51]\n"
    "ds_write_b128 v12, v[40:43] offset:0\n"
    "v_mfma_f32_16x16x32_bf16 v[48:51], v[224:227], v[116:119], v[48:51]\n"
    "v_bfe_u32 v20, v0, 7, 1\n"
    "v_lshl_or_b32 v3, v20, 6, v3\n"
    "v_mfma_f32_16x16x32_bf16 v[48:51], v[236:239], v[120:123], v[48:51]\n"
    "ds_write_b128 v12, v[44:47] offset:64\n"
    "v_mfma_f32_16x16x32_bf16 v[48:51], v[248:251], v[124:127], v[48:51]\n"
    "s_waitcnt lgkmcnt(5)\n"
    "v_bfe_u32 v21, v0, 9, 1\n"
    "v_lshl_or_b32 v3, v21, 5, v3\n"
    "v_mfma_f32_16x16x32_bf16 v[52:55], v[32:35], v[112:115], v[52:55]\n"
    "s_waitcnt lgkmcnt(4)\n"
    "ds_read_b128 v[32:35], v11 offset:0\n"
    "v_bfe_u32 v20, v0, 4, 1\n"
    "v_mfma_f32_16x16x32_bf16 v[52:55], v[36:39], v[116:119], v[52:55]\n"
    "s_waitcnt lgkmcnt(4)\n"
    "ds_read_b128 v[36:39], v11 offset:1024\n"
    "v_lshl_or_b32 v3, v20, 4, v3\n"
    "v_mfma_f32_16x16x32_bf16 v[52:55], v[24:27], v[120:123], v[52:55]\n"
    "s_waitcnt lgkmcnt(4)\n"
    "ds_read_b128 v[24:27], v11 offset:2048\n"
    "v_lshlrev_b32 v60, 16, v62\n"
    "v_mfma_f32_16x16x32_bf16 v[52:55], v[28:31], v[124:127], v[52:55]\n"
    "s_add_u32 s63, s47, 11141120\n"
    "ds_read_b128 v[28:31], v11 offset:3072\n"
    "v_and_b32 v61, 0xffff0000, v62\n"
    "v_mfma_f32_16x16x32_bf16 v[56:59], v[216:219], v[112:115], v[56:59]\n"
    "buffer_load_dwordx4 v[112:115], v0, %[srdw], s62 offen offset:2048 sc1\n"
    "v_lshlrev_b32 v62, 16, v63\n"
    "v_mfma_f32_16x16x32_bf16 v[56:59], v[228:231], v[116:119], v[56:59]\n"
    "buffer_load_dwordx4 v[116:119], v0, %[srdw], s63 offen offset:0 sc1\n"
    "v_and_b32 v63, 0xffff0000, v63\n"
    "v_mfma_f32_16x16x32_bf16 v[56:59], v[240:243], v[120:123], v[56:59]\n"
    "ds_write_b128 v12, v[52:55] offset:192\n"
    "v_mfma_f32_16x16x32_bf16 v[56:59], v[252:255], v[124:127], v[56:59]\n"
    "s_waitcnt vmcnt(12)\n"
    "s_add_u32 s64, %[gr], 655360\n"
    "v_lshlrev_b32 v72, 16, v72\n"
    "buffer_load_dwordx4 v[120:123], v0, %[srdw], s63 offen offset:1024 sc1\n"
    "buffer_load_dwordx4 v[124:127], v0, %[srdw], s63 offen offset:2048 sc1\n"
    "ds_write_b128 v12, v[48:51] offset:128\n"
    "v_mfma_f32_16x16x32_bf16 v[40:43], a[240:243], v[80:83], 0\n"
    "s_waitcnt lgkmcnt(5)\n"
    "ds_write_b128 v12, v[56:59] offset:256\n"
    "v_mfma_f32_16x16x32_bf16 v[44:47], v[32:35], v[80:83], 0\n"
    "ds_read_b128 v[32:35], v11 offset:4096\n"
    "v_mfma_f32_16x16x32_bf16 v[48:51], a[244:247], v[80:83], 0\n"
    "s_waitcnt lgkmcnt(6)\n"
    "s_waitcnt lgkmcnt(1)\n"
    "s_barrier\n"
    "ds_read_b128 v[4:7], v13 offset:0\n"
    "ds_read_b32 v75, v14 offset:0\n"
    "ds_read_b128 v[20:23], v13 offset:5376\n"
    "ds_read_b32 v1, v14 offset:5376\n"
    "ds_read_b128 v[64:67], v13 offset:10752\n"
    "ds_read_b32 v9, v14 offset:10752\n"
    "ds_read_b128 v[68:71], v13 offset:16128\n"
    "ds_read_b32 v10, v14 offset:16128\n"
    "v_mfma_f32_16x16x32_bf16 v[52:55], v[36:39], v[80:83], 0\n"
    "s_add_u32 s65, s44, 12713984\n"
    "ds_read_b128 v[36:39], v11 offset:5120\n"
    "v_mfma_f32_16x16x32_bf16 v[56:59], a[248:251], v[80:83], 0\n"
    "s_waitcnt vmcnt(13)\n"
    "v_mfma_f32_16x16x32_bf16 v[40:43], a[252:255], v[84:87], v[40:43]\n"
    "v_mfma_f32_16x16x32_bf16 v[44:47], v[24:27], v[84:87], v[44:47]\n"
    "ds_read_b128 v[24:27], v11 offset:6144\n"
    "v_mfma_f32_16x16x32_bf16 v[48:51], v[128:131], v[84:87], v[48:51]\n"
    "s_waitcnt lgkmcnt(9)\n"
    "v_add_f32 v60, v60, v4\n"
    "v_add_f32 v61, v61, v5\n"
    "v_add_f32 v62, v62, v6\n"
    "v_add_f32 v63, v63, v7\n"
    "s_waitcnt lgkmcnt(8)\n"
    "v_add_f32 v72, v72, v75\n"
    "s_waitcnt lgkmcnt(7)\n"
    "v_add_f32 v60, v60, v20\n"
    "v_add_f32 v61, v61, v21\n"
    "v_add_f32 v62, v62, v22\n"
    "v_mfma_f32_16x16x32_bf16 v[52:55], v[28:31], v[84:87], v[52:55]\n"
    "s_waitcnt lgkmcnt(6)\n"
    "v_add_f32 v63, v63, v23\n"
    "v_add_f32 v72, v72, v1\n"
    "s_waitcnt lgkmcnt(5)\n"
    "v_add_f32 v60, v60, v64\n"
    "v_add_f32 v61, v61, v65\n"
    "v_add_f32 v62, v62, v66\n"
    "v_add_f32 v63, v63, v67\n"
    "s_waitcnt lgkmcnt(4)\n"
    "v_add_f32 v72, v72, v9\n"
    "v_mov_b32 v5, %[tag]\n"
    "v_mfma_f32_16x16x32_bf16 v[56:59], v[132:135], v[84:87], v[56:59]\n"
    "s_waitcnt vmcnt(12) lgkmcnt(3)\n"
    "v_add_f32 v60, v60, v68\n"
    "v_add_f32 v61, v61, v69\n"
    "v_add_f32 v62, v62, v70\n"
    "v_add_f32 v63, v63, v71\n"
    "s_waitcnt lgkmcnt(2)\n"
    "v_add_f32 v72, v72, v10\n"
    "v_add_f32 v4, v60, v61\n"
    "v_mul_f32 v6, v60, v60\n"
    "v_fmac_f32 v6, v61, v61\n"
    "v_add_f32 v4, v4, v62\n"
    "v_mfma_f32_16x16x32_bf16 v[40:43], v[136:139], v[88:91], v[40:43]\n"
    "v_fmac_f32 v6, v62, v62\n"
    "v_add_f32 v4, v4, v63\n"
    "v_fmac_f32 v6, v63, v63\n"
    "v_add_f32 v4, v4, v72\n"
    "v_fmac_f32 v6, v72, v72\n"
    "v_mov_b32 v7, %[tag]\n"
    "v_add_f32_dpp v4, v4, v4 row_ror:8 row_mask:0xf bank_mask:0xf\n"
    "v_add_f32_dpp v6, v6, v6 row_ror:8 row_mask:0xf bank_mask:0xf\n"
    "v_mfma_f32_16x16x32_bf16 v[44:47], v[32:35], v[88:91], v[44:47]\n"
    "ds_read_b128 v[28:31], v11 offset:7168\n"
    "v_add_f32_dpp v4, v4, v4 row_ror:4 row_mask:0xf bank_mask:0xf\n"
    "v_add_f32_dpp v6, v6, v6 row_ror:4 row_mask:0xf bank_mask:0xf\n"
    "v_mfma_f32_16x16x32_bf16 v[48:51], v[140:143], v[88:91], v[48:51]\n"
    "s_waitcnt lgkmcnt(2)\n"
    "v_add_f32_dpp v4, v4, v4 row_ror:2 row_mask:0xf bank_mask:0xf\n"
    "v_add_f32_dpp v6, v6, v6 row_ror:2 row_mask:0xf bank_mask:0xf\n"
    "v_mfma_f32_16x16x32_bf16 v[52:55], v[36:39], v[88:91], v[52:55]\n"
    "ds_read_b128 v[32:35], v11 offset:8192\n"
    "v_add_f32_dpp v4, v4, v4 row_ror:1 row_mask:0xf bank_mask:0xf\n"
    "v_add_f32_dpp v6, v6, v6 row_ror:1 row_mask:0xf bank_mask:0xf\n"
    "buffer_store_dwordx4 v[4:7], v19, %[srdw], s64 offen sc1\n"
    "v_mfma_f32_16x16x32_bf16 v[56:59], v[144:147], v[88:91], v[56:59]\n"
    "s_waitcnt vmcnt(11)\n"
    "v_cmp_ne_u32 vcc, %[tag], v79\n"
    "s_nop 0\n"
    "s_and_b32 vcc_lo, vcc_lo, 0xf\n"
    "s_cbranch_scc1 L23_%=\n"
    "L24_%=:\n"
    "v_mfma_f32_16x16x32_bf16 v[40:43], v[148:151], v[92:95], v[40:43]\n"
    "s_waitcnt lgkmcnt(2)\n"
    "buffer_load_dwordx4 v[80:83], v0, %[srdw], s65 offen offset:0 sc1\n"
    "v_mfma_f32_16x16x32_bf16 v[44:47], v[24:27], v[92:95], v[44:47]\n"
    "buffer_load_dwordx4 v[84:87], v0, %[srdw], s65 offen offset:1024 sc1\n"
    "ds_read_b128 v[36:39], v11 offset:9216\n"
    "v_mfma_f32_16x16x32_bf16 v[48:51], v[152:155], v[92:95], v[48:51]\n"
    "s_waitcnt lgkmcnt(2)\n"
    "ds_read_b128 v[24:27], v11 offset:10240\n"
    "s_add_u32 s66, s45, 12713984\n"
    "v_mfma_f32_16x16x32_bf16 v[52:55], v[28:31], v[92:95], v[52:55]\n"
    "ds_read_b128 v[28:31], v11 offset:11264\n"
    "v_mfma_f32_16x16x32_bf16 v[56:59], v[156:159], v[92:95], v[56:59]\n"
    "s_waitcnt vmcnt(12)\n"
    "v_mfma_f32_16x16x32_bf16 v[40:43], v[160:163], v[96:99], v[40:43]\n"
    "s_waitcnt lgkmcnt(3)\n"
    "buffer_load_dwordx4 v[88:91], v0, %[srdw], s65 offen offset:2048 sc1\n"
    "v_mfma_f32_16x16x32_bf16 v[44:47], v[32:35], v[96:99], v[44:47]\n"
    "s_barrier\n"
    "ds_read_b128 v[32:35], v11 offset:12288\n"
    "v_mfma_f32_16x16x32_bf16 v[48:51], v[164:167], v[96:99], v[48:51]\n"
    "s_waitcnt lgkmcnt(3)\n"
    "v_mfma_f32_16x16x32_bf16 v[52:55], v[36:39], v[96:99], v[52:55]\n"
    "buffer_load_dwordx4 v[92:95], v0, %[srdw], s66 offen offset:0 sc1\n"
    "ds_read_b128 v[36:39], v11 offset:13312\n"
    "v_mfma_f32_16x16x32_bf16 v[56:59], v[168:171], v[96:99], v[56:59]\n"
    "s_waitcnt vmcnt(13)\n"
    "v_mfma_f32_16x16x32_bf16 v[40:43], v[172:175], v[100:103], v[40:43]\n"
    "s_waitcnt lgkmcnt(3)\n"
    "v_mfma_f32_16x16x32_bf16 v[44:47], v[24:27], v[100:103], v[44:47]\n"
    "ds_read_b128 v[24:27], v11 offset:14336\n"
    "v_mfma_f32_16x16x32_bf16 v[48:51], v[176:179], v[100:103], v[48:51]\n"
    "s_waitcnt lgkmcnt(3)\n"
    "buffer_load_dwordx4 v[96:99], v0, %[srdw], s66 offen offset:1024 sc1\n"
    "v_mfma_f32_16x16x32_bf16 v[52:55], v[28:31], v[100:103], v[52:55]\n"
    "ds_read_b128 v[28:31], v11 offset:15360\n"
    "v_mfma_f32_16x16x32_bf16 v[56:59], v[180:183], v[100:103], v[56:59]\n"
    "s_waitcnt vmcnt(13)\n"
    "v_mfma_f32_16x16x32_bf16 v[40:43], v[184:187], v[104:107], v[40:43]\n"
    "s_waitcnt lgkmcnt(3)\n"
    "v_mfma_f32_16x16x32_bf16 v[44:47], v[32:35], v[104:107], v[44:47]\n"
    "buffer_load_dwordx4 v[100:103], v0, %[srdw], s66 offen offset:2048 sc1\n"
    "ds_read_b128 v[32:35], v11 offset:16384\n"
    "v_mfma_f32_16x16x32_bf16 v[48:51], v[188:191], v[104:107], v[48:51]\n"
    "s_waitcnt lgkmcnt(3)\n"
    "s_add_u32 s67, s46, 12713984\n"
    "v_mfma_f32_16x16x32_bf16 v[52:55], v[36:39], v[104:107], v[52:55]\n"
    "ds_read_b128 v[36:39], v11 offset:18432\n"
    "v_mfma_f32_16x16x32_bf16 v[56:59], v[192:195], v[104:107], v[56:59]\n"
    "s_waitcnt vmcnt(13)\n"
    "v_mfma_f32_16x16x32_bf16 v[40:43], v[196:199], v[108:111], v[40:43]\n"
    "s_waitcnt lgkmcnt(3)\n"
    "buffer_load_dwordx4 v[104:107], v0, %[srdw], s67 offen offset:0 sc1\n"
    "v_mfma_f32_16x16x32_bf16 v[44:47], v[24:27], v[108:111], v[44:47]\n"
    "ds_read_b128 v[24:27], v11 offset:20480\n"
    "v_mfma_f32_16x16x32_bf16 v[48:51], v[200:203], v[108:111], v[48:51]\n"
    "s_waitcnt lgkmcnt(3)\n"
    "v_mfma_f32_16x16x32_bf16 v[52:55], v[28:31], v[108:111], v[52:55]\n"
    "ds_read_b128 v[28:31], v11 offset:22528\n"
    "v_mfma_f32_16x16x32_bf16 v[56:59], v[204:207], v[108:111], v[56:59]\n"
    "s_waitcnt vmcnt(11)\n"
    "buffer_load_dwordx4 v[108:111], v0, %[srdw], s67 offen offset:1024 sc1\n"
    "ds_read_b64 v[66:67], v16 offset:48128\n"
    "ds_read_u16 v73, v17 offset:48128\n"
    "v_cmp_ne_u32 vcc, %[tag], v78\n"
    "s_nop 0\n"
    "s_and_b32 vcc_lo, vcc_lo, 0xf\n"
    "s_cbranch_scc1 L25_%=\n"
    "L26_%=:\n"
    "v_mfma_f32_16x16x32_bf16 v[40:43], v[208:211], v[112:115], v[40:43]\n"
    "s_waitcnt vmcnt(11) lgkmcnt(1)\n"
    "v_lshlrev_b32 v64, 16, v66\n"
    "v_and_b32 v65, 0xffff0000, v66\n"
    "v_lshlrev_b32 v66, 16, v67\n"
    "v_and_b32 v67, 0xffff0000, v67\n"
    "s_waitcnt lgkmcnt(0)\n"
    "v_lshlrev_b32 v73, 16, v73\n"
    "v_mfma_f32_16x16x32_bf16 v[40:43], v[220:223], v[116:119], v[40:43]\n"
    "s_waitcnt vmcnt(10)\n"
    "v_mfma_f32_16x16x32_bf16 v[40:43], v[232:235], v[120:123], v[40:43]\n"
    "s_waitcnt vmcnt(9)\n"
    "v_mfma_f32_16x16x32_bf16 v[40:43], v[244:247], v[124:127], v[40:43]\n"
    "v_mfma_f32_16x16x32_bf16 v[44:47], v[32:35], v[112:115], v[44:47]\n"
    "ds_read_b128 v[32:35], v11 offset:17408\n"
    "v_mfma_f32_16x16x32_bf16 v[44:47], v[36:39], v[116:119], v[44:47]\n"
    "ds_read_b128 v[36:39], v11 offset:19456\n"
    "v_mfma_f32_16x16x32_bf16 v[44:47], v[24:27], v[120:123], v[44:47]\n"
    "ds_read_b128 v[24:27], v11 offset:21504\n"
    "v_mfma_f32_16x16x32_bf16 v[44:47], v[28:31], v[124:127], v[44:47]\n"
    "ds_read_b128 v[28:31], v11 offset:23552\n"
    "v_mfma_f32_16x16x32_bf16 v[48:51], v[212:215], v[112:115], v[48:51]\n"
    "ds_write_b128 v12, v[40:43] offset:0\n"
    "v_mfma_f32_16x16x32_bf16 v[48:51], v[224:227], v[116:119], v[48:51]\n"
    "v_mfma_f32_16x16x32_bf16 v[48:51], v[236:239], v[120:123], v[48:51]\n"
    "v_mfma_f32_16x16x32_bf16 v[48:51], v[248:251], v[124:127], v[48:51]\n"
    "s_waitcnt lgkmcnt(4)\n"
    "v_mfma_f32_16x16x32_bf16 v[52:55], v[32:35], v[112:115], v[52:55]\n"
    "s_waitcnt lgkmcnt(3)\n"
    "ds_read_b128 v[32:35], v11 offset:0\n"
    "v_mfma_f32_16x16x32_bf16 v[52:55], v[36:39], v[116:119], v[52:55]\n"
    "s_waitcnt lgkmcnt(3)\n"
    "ds_read_b128 v[36:39], v11 offset:1024\n"
    "v_mfma_f32_16x16x32_bf16 v[52:55], v[24:27], v[120:123], v[52:55]\n"
    "s_waitcnt lgkmcnt(3)\n"
    "ds_read_b128 v[24:27], v11 offset:2048\n"
    "v_mfma_f32_16x16x32_bf16 v[52:55], v[28:31], v[124:127], v[52:55]\n"
    "s_add_u32 s78, s47, 12713984\n"
    "ds_read_b128 v[28:31], v11 offset:3072\n"
    "v_mfma_f32_16x16x32_bf16 v[56:59], v[216:219], v[112:115], v[56:59]\n"
    "buffer_load_dwordx4 v[112:115], v0, %[srdw], s67 offen offset:2048 sc1\n"
    "v_mfma_f32_16x16x32_bf16 v[56:59], v[228:231], v[116:119], v[56:59]\n"
    "buffer_load_dwordx4 v[116:119], v0, %[srdw], s78 offen offset:0 sc1\n"
    "v_mfma_f32_16x16x32_bf16 v[56:59], v[240:243], v[120:123], v[56:59]\n"
    "ds_write_b128 v12, v[44:47] offset:64\n"
    "v_mfma_f32_16x16x32_bf16 v[56:59], v[252:255], v[124:127], v[56:59]\n"
    "s_waitcnt vmcnt(9)\n"
    "ds_write_b128 v12, v[52:55] offset:192\n"
    "ds_read_b64 v[8:9], v16 offset:45568\n"
    "ds_read_u16 v10, v17 offset:45568\n"
    "s_add_u32 s80, %[gr], 655360\n"
    "buffer_load_dwordx4 v[76:79], v15, %[srdw], s80 offen sc1\n"
    "s_add_u32 s81, %[xv], 15859712\n"
    "buffer_load_dwordx4 v[120:123], v0, %[srdw], s78 offen offset:1024 sc1\n"
    "ds_write_b128 v12, v[56:59] offset:256\n"
    "v_mfma_f32_16x16x32_bf16 v[40:43], a[240:243], v[80:83], 0\n"
    "s_waitcnt lgkmcnt(8)\n"
    "s_add_u32 s79, %[gr], 786432\n"
    "v_mfma_f32_16x16x32_bf16 v[44:47], v[32:35], v[80:83], 0\n"
    "ds_read_b128 v[32:35], v11 offset:4096\n"
    "ds_write_b128 v12, v[48:51] offset:128\n"
    "v_mfma_f32_16x16x32_bf16 v[48:51], a[244:247], v[80:83], 0\n"
    "s_waitcnt lgkmcnt(3)\n"
    "v_lshlrev_b32 v10, 16, v10\n"
    "s_add_u32 s82, s44, 14286848\n"
    "v_mfma_f32_16x16x32_bf16 v[52:55], v[36:39], v[80:83], 0\n"
    "ds_read_b128 v[36:39], v11 offset:5120\n"
    "v_mfma_f32_16x16x32_bf16 v[56:59], a[248:251], v[80:83], 0\n"
    "s_waitcnt vmcnt(10)\n"
    "v_mfma_f32_16x16x32_bf16 v[40:43], a[252:255], v[84:87], v[40:43]\n"
    "v_mfma_f32_16x16x32_bf16 v[44:47], v[24:27], v[84:87], v[44:47]\n"
    "buffer_load_dwordx4 v[124:127], v0, %[srdw], s78 offen offset:2048 sc1\n"
    "ds_read_b128 v[24:27], v11 offset:6144\n"
    "v_mfma_f32_16x16x32_bf16 v[48:51], v[128:131], v[84:87], v[48:51]\n"
    "v_mfma_f32_16x16x32_bf16 v[52:55], v[28:31], v[84:87], v[52:55]\n"
    "s_waitcnt lgkmcnt(2)\n"
    "s_barrier\n"
    "ds_read_b128 v[4:7], v13 offset:0\n"
    "ds_read_b32 v75, v14 offset:0\n"
    "ds_read_b128 v[20:23], v13 offset:5376\n"
    "ds_read_b32 v1, v14 offset:5376\n"
    "ds_read_b128 v[68:71], v13 offset:10752\n"
    "ds_read_b32 v74, v14 offset:10752\n"
    "v_mfma_f32_16x16x32_bf16 v[56:59], v[132:135], v[84:87], v[56:59]\n"
    "s_waitcnt vmcnt(10)\n"
    "buffer_load_dwordx4 v[80:83], v0, %[srdw], s82 offen offset:0 sc1\n"
    "v_mfma_f32_16x16x32_bf16 v[40:43], v[136:139], v[88:91], v[40:43]\n"
    "ds_read_b128 v[28:31], v11 offset:7168\n"
    "v_mfma_f32_16x16x32_bf16 v[44:47], v[32:35], v[88:91], v[44:47]\n"
    "ds_read_b128 v[32:35], v11 offset:8192\n"
    "v_mfma_f32_16x16x32_bf16 v[48:51], v[140:143], v[88:91], v[48:51]\n"
    "s_waitcnt lgkmcnt(7)\n"
    "v_add_f32 v64, v64, v4\n"
    "v_add_f32 v65, v65, v5\n"
    "v_add_f32 v66, v66, v6\n"
    "v_add_f32 v67, v67, v7\n"
    "s_waitcnt lgkmcnt(6)\n"
    "v_add_f32 v73, v73, v75\n"
    "ds_read_b128 v[4:7], v13 offset:16128\n"
    "ds_read_b32 v75, v14 offset:16128\n"
    "s_waitcnt lgkmcnt(7)\n"
    "v_add_f32 v64, v64, v20\n"
    "v_mfma_f32_16x16x32_bf16 v[52:55], v[36:39], v[88:91], v[52:55]\n"
    "v_add_f32 v65, v65, v21\n"
    "v_add_f32 v66, v66, v22\n"
    "v_add_f32 v67, v67, v23\n"
    "s_waitcnt lgkmcnt(6)\n"
    "v_add_f32 v73, v73, v1\n"
    "s_waitcnt lgkmcnt(5)\n"
    "v_add_f32 v64, v64, v68\n"
    "v_add_f32 v65, v65, v69\n"
    "v_add_f32 v66, v66, v70\n"
    "v_add_f32 v67, v67, v71\n"
    "v_mfma_f32_16x16x32_bf16 v[56:59], v[144:147], v[88:91], v[56:59]\n"
    "s_waitcnt vmcnt(10) lgkmcnt(4)\n"
    "v_add_f32 v73, v73, v74\n"
    "ds_read_b128 v[36:39], v11 offset:9216\n"
    "v_mfma_f32_16x16x32_bf16 v[40:43], v[148:151], v[92:95], v[40:43]\n"
    "s_waitcnt lgkmcnt(2)\n"
    "v_add_f32 v64, v64, v4\n"
    "v_add_f32 v65, v65, v5\n"
    "v_add_f32 v66, v66, v6\n"
    "v_add_f32 v67, v67, v7\n"
    "s_waitcnt lgkmcnt(1)\n"
    "v_add_f32 v73, v73, v75\n"
    "v_add_f32 v4, v64, v65\n"
    "v_mul_f32 v6, v64, v64\n"
    "v_fmac_f32 v6, v65, v65\n"
    "v_add_f32 v4, v4, v66\n"
    "v_mfma_f32_16x16x32_bf16 v[44:47], v[24:27], v[92:95], v[44:47]\n"
    "v_fmac_f32 v6, v66, v66\n"
    "v_add_f32 v4, v4, v67\n"
    "v_fmac_f32 v6, v67, v67\n"
    "v_add_f32 v4, v4, v73\n"
    "v_fmac_f32 v6, v73, v73\n"
    "v_mov_b32 v5, %[tag]\n"
    "v_mov_b32 v7, %[tag]\n"
    "v_add_f32_dpp v4, v4, v4 row_ror:8 row_mask:0xf bank_mask:0xf\n"
    "v_add_f32_dpp v6, v6, v6 row_ror:8 row_mask:0xf bank_mask:0xf\n"
    "v_mfma_f32_16x16x32_bf16 v[48:51], v[152:155], v[92:95], v[48:51]\n"
    "s_add_u32 s83, s45, 14286848\n"
    "v_add_f32_dpp v4, v4, v4 row_ror:4 row_mask:0xf bank_mask:0xf\n"
    "v_add_f32_dpp v6, v6, v6 row_ror:4 row_mask:0xf bank_mask:0xf\n"
    "v_mfma_f32_16x16x32_bf16 v[52:55], v[28:31], v[92:95], v[52:55]\n"
    "buffer_load_dwordx4 v[84:87], v0, %[srdw], s82 offen offset:1024 sc1\n"
    "v_add_f32_dpp v4, v4, v4 row_ror:2 row_mask:0xf bank_mask:0xf\n"
    "v_add_f32_dpp v6, v6, v6 row_ror:2 row_mask:0xf bank_mask:0xf\n"
    "v_mfma_f32_16x16x32_bf16 v[56:59], v[156:159], v[92:95], v[56:59]\n"
    "s_waitcnt vmcnt(10)\n"
    "v_add_f32_dpp v4, v4, v4 row_ror:1 row_mask:0xf bank_mask:0xf\n"
    "v_add_f32_dpp v6, v6, v6 row_ror:1 row_mask:0xf bank_mask:0xf\n"
    "buffer_store_dwordx4 v[4:7], v19, %[srdw], s79 offen sc1\n"
    "v_mfma_f32_16x16x32_bf16 v[40:43], v[160:163], v[96:99], v[40:43]\n"
    "buffer_load_dwordx4 v[88:91], v0, %[srdw], s82 offen offset:2048 sc1\n"
    "ds_read_b128 v[24:27], v11 offset:10240\n"
    "v_mfma_f32_16x16x32_bf16 v[44:47], v[32:35], v[96:99], v[44:47]\n"
    "s_barrier\n"
    "ds_read_b128 v[28:31], v11 offset:11264\n"
    "ds_read_b128 v[32:35], v11 offset:12288\n"
    "v_mfma_f32_16x16x32_bf16 v[48:51], v[164:167], v[96:99], v[48:51]\n"
    "s_waitcnt lgkmcnt(3)\n"
    "v_mfma_f32_16x16x32_bf16 v[52:55], v[36:39], v[96:99], v[52:55]\n"
    "buffer_load_dwordx4 v[92:95], v0, %[srdw], s83 offen offset:0 sc1\n"
    "ds_read_b128 v[36:39], v11 offset:13312\n"
    "v_mfma_f32_16x16x32_bf16 v[56:59], v[168:171], v[96:99], v[56:59]\n"
    "s_waitcnt vmcnt(12)\n"
    "v_mfma_f32_16x16x32_bf16 v[40:43], v[172:175], v[100:103], v[40:43]\n"
    "s_waitcnt lgkmcnt(3)\n"
    "buffer_load_dwordx4 v[96:99], v0, %[srdw], s83 offen offset:1024 sc1\n"
    "v_mfma_f32_16x16x32_bf16 v[44:47], v[24:27], v[100:103], v[44:47]\n"
    "ds_read_b128 v[24:27], v11 offset:14336\n"
    "v_mfma_f32_16x16x32_bf16 v[48:51], v[176:179], v[100:103], v[48:51]\n"
    "s_waitcnt lgkmcnt(3)\n"
    "v_mfma_f32_16x16x32_bf16 v[52:55], v[28:31], v[100:103], v[52:55]\n"
    "ds_read_b128 v[28:31], v11 offset:15360\n"
    "v_mfma_f32_16x16x32_bf16 v[56:59], v[180:183], v[100:103], v[56:59]\n"
    "s_waitcnt vmcnt(12)\n"
    "buffer_load_dwordx4 v[100:103], v0, %[srdw], s83 offen offset:2048 sc1\n"
    "v_mfma_f32_16x16x32_bf16 v[40:43], v[184:187], v[104:107], v[40:43]\n"
    "s_waitcnt lgkmcnt(3)\n"
    "v_mfma_f32_16x16x32_bf16 v[44:47], v[32:35], v[104:107], v[44:47]\n"
    "ds_read_b128 v[32:35], v11 offset:16384\n"
    "v_mfma_f32_16x16x32_bf16 v[48:51], v[188:191], v[104:107], v[48:51]\n"
    "s_waitcnt lgkmcnt(3)\n"
    "s_add_u32 s84, s46, 14286848\n"
    "v_mfma_f32_16x16x32_bf16 v[52:55], v[36:39], v[104:107], v[52:55]\n"
    "ds_read_b128 v[36:39], v11 offset:18432\n"
    "v_mfma_f32_16x16x32_bf16 v[56:59], v[192:195], v[104:107], v[56:59]\n"
    "s_waitcnt vmcnt(12)\n"
    "buffer_load_dwordx4 v[104:107], v0, %[srdw], s84 offen offset:0 sc1\n"
    "v_mfma_f32_16x16x32_bf16 v[40:43], v[196:199], v[108:111], v[40:43]\n"
    "s_waitcnt lgkmcnt(3)\n"
    "v_mfma_f32_16x16x32_bf16 v[44:47], v[24:27], v[108:111], v[44:47]\n"
    "ds_read_b128 v[24:27], v11 offset:20480\n"
    "v_mfma_f32_16x16x32_bf16 v[48:51], v[200:203], v[108:111], v[48:51]\n"
    "s_waitcnt lgkmcnt(3)\n"
    "v_mfma_f32_16x16x32_bf16 v[52:55], v[28:31], v[108:111], v[52:55]\n"
    "ds_read_b128 v[28:31], v11 offset:22528\n"
    "v_mfma_f32_16x16x32_bf16 v[56:59], v[204:207], v[108:111], v[56:59]\n"
    "s_waitcnt vmcnt(12)\n"
    "buffer_load_dwordx4 v[108:111], v0, %[srdw], s84 offen offset:1024 sc1\n"
    "ds_read_b64 v[70:71], v16 offset:53248\n"
    "ds_read_u16 v74, v17 offset:53248\n"
    "v_mfma_f32_16x16x32_bf16 v[40:43], v[208:211], v[112:115], v[40:43]\n"
    "s_waitcnt vmcnt(12)\n"
    "s_waitcnt lgkmcnt(1)\n"
    "v_lshlrev_b32 v68, 16, v70\n"
    "v_and_b32 v69, 0xffff0000, v70\n"
    "v_mfma_f32_16x16x32_bf16 v[40:43], v[220:223], v[116:119], v[40:43]\n"
    "s_waitcnt vmcnt(10) lgkmcnt(0)\n"
    "v_lshlrev_b32 v70, 16, v71\n"
    "v_and_b32 v71, 0xffff0000, v71\n"
    "v_lshlrev_b32 v74, 16, v74\n"
    "v_mfma_f32_16x16x32_bf16 v[40:43], v[232:235], v[120:123], v[40:43]\n"
    "s_waitcnt vmcnt(9)\n"
    "v_cmp_ne_u32 vcc, %[tag], v77\n"
    "v_cmp_ne_u32 s[68:69], %[tag], v79\n"
    "s_nop 0\n"
    "s_or_b64 vcc, vcc, s[68:69]\n"
    "s_or_b32 s68, vcc_lo, vcc_hi\n"
    "s_and_b32 s68, s68, 0xff00ff\n"
    "s_cbranch_scc1 L27_%=\n"
    "L28_%=:\n"
    "v_add_f32_dpp v76, v76, v76 row_ror:8 row_mask:0xf bank_mask:0xf\n"
    "v_add_f32_dpp v78, v78, v78 row_ror:8 row_mask:0xf bank_mask:0xf\n"
    "v_lshlrev_b32 v77, 16, v8\n"
    "v_mfma_f32_16x16x32_bf16 v[40:43], v[244:247], v[124:127], v[40:43]\n"
    "v_add_f32_dpp v76, v76, v76 row_ror:4 row_mask:0xf bank_mask:0xf\n"
    "v_add_f32_dpp v78, v78, v78 row_ror:4 row_mask:0xf bank_mask:0xf\n"
    "v_mfma_f32_16x16x32_bf16 v[44:47], v[32:35], v[112:115], v[44:47]\n"
    "ds_read_b128 v[32:35], v11 offset:17408\n"
    "v_add_f32_dpp v76, v76, v76 row_ror:2 row_mask:0xf bank_mask:0xf\n"
    "v_add_f32_dpp v78, v78, v78 row_ror:2 row_mask:0xf bank_mask:0xf\n"
    "v_mfma_f32_16x16x32_bf16 v[44:47], v[36:39], v[116:119], v[44:47]\n"
    "ds_read_b128 v[36:39], v11 offset:19456\n"
    "v_add_f32_dpp v76, v76, v76 row_ror:1 row_mask:0xf bank_mask:0xf\n"
    "v_add_f32_dpp v78, v78, v78 row_ror:1 row_mask:0xf bank_mask:0xf\n"
    "v_mul_f32 v76, 0x3acccccd, v76\n"
    "v_mul_f32 v78, 0x3acccccd, v78\n"
    "v_fma_f32 v78, -v76, v76, v78\n"
    "v_max_f32 v78, 0, v78\n"
    "v_add_f32 v78, 0x3727c5ac, v78\n"
    "v_rsq_f32 v78, v78\n"
    "v_mfma_f32_16x16x32_bf16 v[44:47], v[24:27], v[120:123], v[44:47]\n"
    "ds_read_b128 v[24:27], v11 offset:21504\n"
    "v_mul_f32 v76, v76, v78\n"
    "v_fma_f32 v79, v60, v78, -v76\n"
    "v_fmac_f32 v60, v79, v77\n"
    "v_and_b32 v77, 0xffff0000, v8\n"
    "v_fma_f32 v79, v61, v78, -v76\n"
    "v_fmac_f32 v61, v79, v77\n"
    "v_lshlrev_b32 v77, 16, v9\n"
    "v_fma_f32 v79, v62, v78, -v76\n"
    "v_fmac_f32 v62, v79, v77\n"
    "v_mfma_f32_16x16x32_bf16 v[44:47], v[28:31], v[124:127], v[44:47]\n"
    "v_and_b32 v77, 0xffff0000, v9\n"
    "v_fma_f32 v79, v63, v78, -v76\n"
    "v_fmac_f32 v63, v79, v77\n"
    "v_fma_f32 v79, v72, v78, -v76\n"
    "v_fmac_f32 v72, v79, v10\n"
    "v_cvt_pk_bf16_f32 v76, v60, v61\n"
    "v_cvt_pk_bf16_f32 v77, v62, v63\n"
    "v_mov_b32_dpp v79, v72 quad_perm:[1,0,3,2] row_mask:0xf bank_mask:0xf\n"
    "v_cvt_pk_bf16_f32 v78, v72, v79\n"
    "v_min_u32 v76, -2, v76\n"
    "v_mfma_f32_16x16x32_bf16 v[48:51], v[212:215], v[112:115], v[48:51]\n"
    "v_min_u32 v77, -2, v77\n"
    "v_min_u32 v78, -2, v78\n"
    "v_mfma_f32_16x16x32_bf16 v[48:51], v[224:227], v[116:119], v[48:51]\n"
    "ds_read_b128 v[28:31], v11 offset:23552\n"
    "v_mov_b32_dpp v79, v78 quad_perm:[2,2,2,2] row_mask:0xf bank_mask:0xf\n"
    "v_permlane16_swap_b32 v76, v78\n"
    "v_mfma_f32_16x16x32_bf16 v[48:51], v[236:239], v[120:123], v[48:51]\n"
    "v_permlane16_swap_b32 v77, v79\n"
    "buffer_store_dwordx4 v[76:79], v18, %[srdw], s81 offen sc1\n"
    "v_mfma_f32_16x16x32_bf16 v[48:51], v[248:251], v[124:127], v[48:51]\n"
    "s_waitcnt lgkmcnt(3)\n"
    "ds_write_b128 v12, v[40:43] offset:0\n"
    "v_mfma_f32_16x16x32_bf16 v[52:55], v[32:35], v[112:115], v[52:55]\n"
    "s_waitcnt lgkmcnt(3)\n"
    "ds_read_b128 v[32:35], v11 offset:0\n"
    "v_mfma_f32_16x16x32_bf16 v[52:55], v[36:39], v[116:119], v[52:55]\n"
    "s_waitcnt lgkmcnt(3)\n"
    "ds_read_b128 v[36:39], v11 offset:1024\n"
    "v_mfma_f32_16x16x32_bf16 v[52:55], v[24:27], v[120:123], v[52:55]\n"
    "s_waitcnt lgkmcnt(3)\n"
    "ds_read_b128 v[24:27], v11 offset:2048\n"
    "v_mfma_f32_16x16x32_bf16 v[52:55], v[28:31], v[124:127], v[52:55]\n"
    "s_add_u32 s85, s47, 14286848\n"
    "ds_read_b128 v[28:31], v11 offset:3072\n"
    "v_mfma_f32_16x16x32_bf16 v[56:59], v[216:219], v[112:115], v[56:59]\n"
    "buffer_load_dwordx4 v[112:115], v0, %[srdw], s84 offen offset:2048 sc1\n"
    "v_mfma_f32_16x16x32_bf16 v[56:59], v[228:231], v[116:119], v[56:59]\n"
    "ds_write_b128 v12, v[44:47] offset:64\n"
    "v_mfma_f32_16x16x32_bf16 v[56:59], v[240:243], v[120:123], v[56:59]\n"
    "buffer_load_dwordx4 v[116:119], v0, %[srdw], s85 offen offset:0 sc1\n"
    "v_mfma_f32_16x16x32_bf16 v[56:59], v[252:255], v[124:127], v[56:59]\n"
    "s_waitcnt vmcnt(11)\n"
    "ds_write_b128 v12, v[52:55] offset:192\n"
    "v_mfma_f32_16x16x32_bf16 v[40:43], a[240:243], v[80:83], 0\n"
    "s_waitcnt lgkmcnt(5)\n"
    "s_add_u32 s86, %[gr], 917504\n"
    "s_add_u32 s87, %[gr], 786432\n"
    "v_mfma_f32_16x16x32_bf16 v[44:47], v[32:35], v[80:83], 0\n"
    "s_lshr_b32 s88, %[w1k], 7\n"
    "ds_write_b128 v12, v[56:59] offset:256\n"
    "ds_write_b128 v12, v[48:51] offset:128\n"
    "v_mfma_f32_16x16x32_bf16 v[48:51], a[244:247], v[80:83], 0\n"
    "s_waitcnt lgkmcnt(6)\n"
    "buffer_load_dwordx4 v[120:123], v0, %[srdw], s85 offen offset:1024 sc1\n"
    "v_mfma_f32_16x16x32_bf16 v[52:55], v[36:39], v[80:83], 0\n"
    "buffer_load_dwordx4 v[124:127], v0, %[srdw], s85 offen offset:2048 sc1\n"
    "ds_read_b128 v[32:35], v11 offset:4096\n"
    "v_mfma_f32_16x16x32_bf16 v[56:59], a[248:251], v[80:83], 0\n"
    "s_waitcnt vmcnt(12)\n"
    "s_waitcnt lgkmcnt(1)\n"
    "s_barrier\n"
    "v_mfma_f32_16x16x32_bf16 v[40:43], a[252:255], v[84:87], v[40:43]\n"
    "ds_read_b128 v[4:7], v13 offset:0\n"
    "ds_read_b32 v75, v14 offset:0\n"
    "v_mfma_f32_16x16x32_bf16 v[44:47], v[24:27], v[84:87], v[44:47]\n"
    "ds_read_b128 v[20:23], v13 offset:5376\n"
    "ds_read_b32 v1, v14 offset:5376\n"
    "v_mfma_f32_16x16x32_bf16 v[48:51], v[128:131], v[84:87], v[48:51]\n"
    "ds_read_b128 v[36:39], v11 offset:5120\n"
    "ds_read_b128 v[24:27], v11 offset:6144\n"
    "v_mfma_f32_16x16x32_bf16 v[52:55], v[28:31], v[84:87], v[52:55]\n"
    "ds_read_b128 v[28:31], v11 offset:7168\n"
    "buffer_load_dwordx4 v[76:79], v15, %[srdw], s87 offen sc1\n"
    "v_mfma_f32_16x16x32_bf16 v[56:59], v[132:135], v[84:87], v[56:59]\n"
    "s_waitcnt vmcnt(11)\n"
    "ds_read_b64 v[82:83], v16 offset:58368\n"
    "ds_read_u16 v84, v17 offset:58368\n"
    "v_mfma_f32_16x16x32_bf16 v[40:43], v[136:139], v[88:91], v[40:43]\n"
    "s_waitcnt lgkmcnt(9)\n"
    "ds_read_b64 v[86:87], v16 offset:50688\n"
    "ds_read_u16 v85, v17 offset:50688\n"
    "v_mfma_f32_16x16x32_bf16 v[44:47], v[32:35], v[88:91], v[44:47]\n"
    "s_waitcnt lgkmcnt(10)\n"
    "v_add_f32 v68, v68, v4\n"
    "v_add_f32 v69, v69, v5\n"
    "v_mfma_f32_16x16x32_bf16 v[48:51], v[140:143], v[88:91], v[48:51]\n"
    "s_waitcnt lgkmcnt(6)\n"
    "v_add_f32 v70, v70, v6\n"
    "v_add_f32 v71, v71, v7\n"
    "v_add_f32 v74, v74, v75\n"
    "ds_read_b128 v[4:7], v13 offset:10752\n"
    "ds_read_b32 v75, v14 offset:10752\n"
    "v_add_f32 v68, v68, v20\n"
    "v_add_f32 v69, v69, v21\n"
    "v_mfma_f32_16x16x32_bf16 v[52:55], v[36:39], v[88:91], v[52:55]\n"
    "s_add_u32 s89, %[xv], 16515072\n"
    "v_add_f32 v70, v70, v22\n"
    "v_add_f32 v71, v71, v23\n"
    "v_mfma_f32_16x16x32_bf16 v[56:59], v[144:147], v[88:91], v[56:59]\n"
    "s_waitcnt vmcnt(10)\n"
    "ds_read_b128 v[20:23], v13 offset:16128\n"
    "v_add_f32 v74, v74, v1\n"
    "v_mfma_f32_16x16x32_bf16 v[40:43], v[148:151], v[92:95], v[40:43]\n"
    "s_waitcnt lgkmcnt(8)\n"
    "ds_read_b32 v1, v14 offset:16128\n"
    "ds_read_b128 v[32:35], v11 offset:8192\n"
    "v_mfma_f32_16x16x32_bf16 v[44:47], v[24:27], v[92:95], v[44:47]\n"
    "ds_read_b128 v[36:39], v11 offset:9216\n"
    "ds_read_b64 v[88:89], v16 offset:55808\n"
    "v_mfma_f32_16x16x32_bf16 v[48:51], v[152:155], v[92:95], v[48:51]\n"
    "s_waitcnt lgkmcnt(6)\n"
    "v_add_f32 v68, v68, v4\n"
    "v_add_f32 v69, v69, v5\n"
    "v_mfma_f32_16x16x32_bf16 v[52:55], v[28:31], v[92:95], v[52:55]\n"
    "v_add_f32 v70, v70, v6\n"
    "v_add_f32 v71, v71, v7\n"
    "v_mfma_f32_16x16x32_bf16 v[56:59], v[156:159], v[92:95], v[56:59]\n"
    "s_waitcnt vmcnt(9) lgkmcnt(5)\n"
    "v_add_f32 v74, v74, v75\n"
    "v_mov_b32 v5, %[tag]\n"
    "v_mfma_f32_16x16x32_bf16 v[40:43], v[160:163], v[96:99], v[40:43]\n"
    "s_waitcnt lgkmcnt(2)\n"
    "v_add_f32 v68, v68, v20\n"
    "v_add_f32 v69, v69, v21\n"
    "v_add_f32 v70, v70, v22\n"
    "v_add_f32 v71, v71, v23\n"
    "v_add_f32 v74, v74, v1\n"
    "v_add_f32 v4, v68, v69\n"
    "v_mul_f32 v6, v68, v68\n"
    "kx_a6:\n"
    "s_cbranch_execz kx_a10\n"
    "v_mfma_f32_16x16x32_bf16 v[44:47], v[32:35], v[96:99], v[44:47]\n"
    "v_fmac_f32 v6, v69, v69\n"
    "v_add_f32 v4, v4, v70\n"
    "v_mfma_f32_16x16x32_bf16 v[48:51], v[164:167], v[96:99], v[48:51]\n"
    "s_waitcnt lgkmcnt(1)\n"
    "v_fmac_f32 v6, v70, v70\n"
    "v_add_f32 v4, v4, v71\n"
    "v_mfma_f32_16x16x32_bf16 v[52:55], v[36:39], v[96:99], v[52:55]\n"
    "v_fmac_f32 v6, v71, v71\n"
    "v_add_f32 v4, v4, v74\n"
    "v_mfma_f32_16x16x32_bf16 v[56:59], v[168:171], v[96:99], v[56:59]\n"
    "s_waitcnt vmcnt(8)\n"
    "v_fmac_f32 v6, v74, v74\n"
    "v_mov_b32 v7, %[tag]\n"
    "v_mfma_f32_16x16x32_bf16 v[40:43], v[172:175], v[100:103], v[40:43]\n"
    "v_add_f32_dpp v4, v4, v4 row_ror:8 row_mask:0xf bank_mask:0xf\n"
    "v_add_f32_dpp v6, v6, v6 row_ror:8 row_mask:0xf bank_mask:0xf\n"
    "ds_read_b128 v[24:27], v11 offset:10240\n"
    "v_add_f32_dpp v4, v4, v4 row_ror:4 row_mask:0xf bank_mask:0xf\n"
    "v_add_f32_dpp v6, v6, v6 row_ror:4 row_mask:0xf bank_mask:0xf\n"
    "ds_read_u16 v90, v17 offset:55808\n"
    "v_add_f32_dpp v4, v4, v4 row_ror:2 row_mask:0xf bank_mask:0xf\n"
    "v_add_f32_dpp v6, v6, v6 row_ror:2 row_mask:0xf bank_mask:0xf\n"
    "ds_read_b128 v[28:31], v11 offset:11264\n"
    "v_add_f32_dpp v4, v4, v4 row_ror:1 row_mask:0xf bank_mask:0xf\n"
    "v_add_f32_dpp v6, v6, v6 row_ror:1 row_mask:0xf bank_mask:0xf\n"
    "buffer_store_dwordx4 v[4:7], v19, %[srdw], s86 offen sc1\n"
    "ds_read_b64 v[92:93], v16 offset:60928\n"
    "ds_read_u16 v91, v17 offset:60928\n"
    "ds_read_b128 v[32:35], v11 offset:12288\n"
    "ds_read_b128 v[36:39], v11 offset:13312\n"
    "v_lshlrev_b32 v85, 16, v85\n"
    "v_lshlrev_b32 v1, 3, v0\n"
    "v_bfe_u32 v22, v0, 8, 2\n"
    "v_lshlrev_b32 v4, 7, v22\n"
    "v_bfe_u32 v23, v0, 4, 3\n"
    "v_lshl_or_b32 v4, v23, 4, v4\n"
    "v_bfe_u32 v22, v0, 7, 1\n"
    "v_lshl_or_b32 v4, v22, 3, v4\n"
    "v_add_u32 v5, %[ldsx], v4\n"
    "v_mov_b32 v20, 0x3f803f80\n"
    "v_mov_b32 v21, 0x3f803f80\n"
    "s_waitcnt lgkmcnt(6)\n"
    "v_mfma_f32_16x16x32_bf16 v[44:47], v[24:27], v[100:103], v[44:47]\n"
    "ds_read_b128 v[24:27], v11 offset:14336\n"
    "v_mov_b32 v22, 0x3f803f80\n"
    "v_mfma_f32_16x16x32_bf16 v[48:51], v[176:179], v[100:103], v[48:51]\n"
    "s_waitcnt lgkmcnt(5)\n"
    "v_mov_b32 v23, 0x3f803f80\n"
    "v_bfe_u32 v75, v0, 4, 4\n"
    "v_mfma_f32_16x16x32_bf16 v[52:55], v[28:31], v[100:103], v[52:55]\n"
    "ds_read_b128 v[28:31], v11 offset:15360\n"
    "v_lshlrev_b32 v9, 5, v75\n"
    "v_mfma_f32_16x16x32_bf16 v[56:59], v[180:183], v[100:103], v[56:59]\n"
    "s_waitcnt vmcnt(8)\n"
    "s_waitcnt lgkmcnt(4)\n"
    "s_barrier\n"
    "v_mfma_f32_16x16x32_bf16 v[40:43], v[184:187], v[104:107], v[40:43]\n"
    "s_waitcnt lgkmcnt(3)\n"
    "v_add_u32 v9, 20480, v9\n"
    "v_add_u32 v8, s88, v9\n"
    "v_mfma_f32_16x16x32_bf16 v[44:47], v[32:35], v[104:107], v[44:47]\n"
    "ds_read_b128 v[32:35], v11 offset:16384\n"
    "v_mov_b32 v10, v0\n"
    "v_mfma_f32_16x16x32_bf16 v[48:51], v[188:191], v[104:107], v[48:51]\n"
    "s_waitcnt lgkmcnt(3)\n"
    "s_mov_b32 exec_lo, 0xff0ffff0\n"
    "s_mov_b32 exec_hi, 0x0ffff0ff\n"
    "v_mov_b32 v8, 0x30000\n"
    "s_mov_b64 exec, -1\n"
    "v_mfma_f32_16x16x32_bf16 v[52:55], v[36:39], v[104:107], v[52:55]\n"
    "ds_read_b128 v[36:39], v11 offset:18432\n"
    "v_lshlrev_b32 v80, 16, v82\n"
    "v_mfma_f32_16x16x32_bf16 v[56:59], v[192:195], v[104:107], v[56:59]\n"
    "s_waitcnt vmcnt(7)\n"
    "s_mov_b32 exec_lo, 0\n"
    "s_mov_b32 exec_hi, 0xffff0000\n"
    "v_mov_b32 v10, 0x7ffffff0\n"
    "s_mov_b64 exec, -1\n"
    "v_mfma_f32_16x16x32_bf16 v[40:43], v[196:199], v[108:111], v[40:43]\n"
    "s_waitcnt lgkmcnt(3)\n"
    "v_and_b32 v81, 0xffff0000, v82\n"
    "v_lshlrev_b32 v82, 16, v83\n"
    "v_mfma_f32_16x16x32_bf16 v[44:47], v[24:27], v[108:111], v[44:47]\n"
    "ds_read_b128 v[24:27], v11 offset:20480\n"
    "v_and_b32 v83, 0xffff0000, v83\n"
    "v_mfma_f32_16x16x32_bf16 v[48:51], v[200:203], v[108:111], v[48:51]\n"
    "s_waitcnt lgkmcnt(3)\n"
    "v_lshlrev_b32 v84, 16, v84\n"
    "v_mfma_f32_16x16x32_bf16 v[52:55], v[28:31], v[108:111], v[52:55]\n"
    "ds_read_b128 v[28:31], v11 offset:22528\n"
    "v_mfma_f32_16x16x32_bf16 v[56:59], v[204:207], v[108:111], v[56:59]\n"
    "s_waitcnt vmcnt(5)\n"
    "s_add_u32 m0, %[ldsx], 21504\n"
    "s_add_u32 s68, %[x], 15859712\n"
    "buffer_load_dwordx4 v3, %[srdw], s68 offen sc1 lds\n"
    "buffer_load_dwordx4 v3, %[srdw], s68 offen offset:1024 sc1 lds\n"
    "buffer_load_dwordx4 v3, %[srdw], s68 offen offset:2048 sc1 lds\n"
    "buffer_load_dwordx4 v3, %[srdw], s68 offen offset:3072 sc1 lds\n"
    "s_add_u32 m0, %[ldsx], 25600\n"
    "s_add_u32 s68, %[x], 15863808\n"
    "buffer_load_dwordx4 v3, %[srdw], s68 offen sc1 lds\n"
    "v_mfma_f32_16x16x32_bf16 v[40:43], v[208:211], v[112:115], v[40:43]\n"
    "s_waitcnt vmcnt(9)\n"
    "v_mfma_f32_16x16x32_bf16 v[40:43], v[220:223], v[116:119], v[40:43]\n"
    "s_waitcnt vmcnt(8)\n"
    "v_mfma_f32_16x16x32_bf16 v[40:43], v[232:235], v[120:123], v[40:43]\n"
    "s_waitcnt vmcnt(7)\n"
    "v_mfma_f32_16x16x32_bf16 v[40:43], v[244:247], v[124:127], v[40:43]\n"
    "s_waitcnt lgkmcnt(3)\n"
    "v_mfma_f32_16x16x32_bf16 v[44:47], v[32:35], v[112:115], v[44:47]\n"
    "s_waitcnt lgkmcnt(2)\n"
    "s_add_u32 m0, %[ldsx], 43008\n"
    "s_add_u32 s68, s42, 16187392\n"
    "buffer_load_dwordx4 v3, %[srdw], s68 offen sc1 lds\n"
    "v_mfma_f32_16x16x32_bf16 v[44:47], v[36:39], v[116:119], v[44:47]\n"
    "s_waitcnt lgkmcnt(1)\n"
    "buffer_load_dwordx4 v3, %[srdw], s68 offen offset:1024 sc1 lds\n"
    "v_mfma_f32_16x16x32_bf16 v[44:47], v[24:27], v[120:123], v[44:47]\n"
    "s_waitcnt lgkmcnt(0)\n"
    "buffer_load_dwordx4 v3, %[srdw], s68 offen offset:2048 sc1 lds\n"
    "v_mfma_f32_16x16x32_bf16 v[44:47], v[28:31], v[124:127], v[44:47]\n"
    "v_cmp_u_f32 vcc, v40, v40\n"
    "s_nop 0\n"
    "s_cmp_lg_u64 vcc, 0\n"
    "s_cbranch_scc1 L31_%=\n"
    "L32_%=:\n"
    "v_mfma_f32_16x16x32_bf16 v[48:51], v[212:215], v[112:115], v[48:51]\n"
    "ds_write_b128 v12, v[40:43] offset:0\n"
    "v_mfma_f32_16x16x32_bf16 v[48:51], v[224:227], v[116:119], v[48:51]\n"
    "buffer_load_dwordx4 v3, %[srdw], s68 offen offset:3072 sc1 lds\n"
    "ds_read_b128 v[32:35], v11 offset:17408\n"
    "v_mfma_f32_16x16x32_bf16 v[48:51], v[236:239], v[120:123], v[48:51]\n"
    "ds_write_b128 v12, v[44:47] offset:64\n"
    "v_mfma_f32_16x16x32_bf16 v[48:51], v[248:251], v[124:127], v[48:51]\n"
    "s_waitcnt lgkmcnt(1)\n"
    "s_add_u32 m0, %[ldsx], 47104\n"
    "s_add_u32 s68, s42, 16191488\n"
    "buffer_load_dwordx4 v3, %[srdw], s68 offen sc1 lds\n"
    "ds_read_b128 v[36:39], v11 offset:19456\n"
    "ds_read_b128 v[24:27], v11 offset:21504\n"
    "ds_read_b128 v[28:31], v11 offset:23552\n"
    "s_waitcnt vmcnt(11)\n"
    "v_cmp_ne_u32 vcc, %[tag], v77\n"
    "v_cmp_ne_u32 s[68:69], %[tag], v79\n"
    "s_nop 0\n"
    "s_or_b64 vcc, vcc, s[68:69]\n"
    "s_or_b32 s68, vcc_lo, vcc_hi\n"
    "s_and_b32 s68, s68, 0xff00ff\n"
    "s_cbranch_scc1 L29_%=\n"
    "L30_%=:\n"
    "v_add_f32_dpp v76, v76, v76 row_ror:8 row_mask:0xf bank_mask:0xf\n"
    "v_mfma_f32_16x16x32_bf16 v[52:55], v[32:35], v[112:115], v[52:55]\n"
    "s_waitcnt lgkmcnt(2)\n"
    "ds_write_b128 v12, v[48:51] offset:128\n"
    "v_add_f32_dpp v78, v78, v78 row_ror:8 row_mask:0xf bank_mask:0xf\n"
    "v_add_f32_dpp v76, v76, v76 row_ror:4 row_mask:0xf bank_mask:0xf\n"
    "v_lshlrev_b32 v77, 16, v86\n"
    "v_add_f32_dpp v78, v78, v78 row_ror:4 row_mask:0xf bank_mask:0xf\n"
    "v_add_f32_dpp v76, v76, v76 row_ror:2 row_mask:0xf bank_mask:0xf\n"
    "s_nop 0\n"
    "v_add_f32_dpp v78, v78, v78 row_ror:2 row_mask:0xf bank_mask:0xf\n"
    "v_mfma_f32_16x16x32_bf16 v[52:55], v[36:39], v[116:119], v[52:55]\n"
    "s_waitcnt lgkmcnt(2)\n"
    "v_add_f32_dpp v76, v76, v76 row_ror:1 row_mask:0xf bank_mask:0xf\n"
    "v_add_f32_dpp v78, v78, v78 row_ror:1 row_mask:0xf bank_mask:0xf\n"
    "v_mfma_f32_16x16x32_bf16 v[52:55], v[24:27], v[120:123], v[52:55]\n"
    "s_waitcnt lgkmcnt(1)\n"
    "v_mul_f32 v76, 0x3acccccd, v76\n"
    "v_mul_f32 v78, 0x3acccccd, v78\n"
    "v_mfma_f32_16x16x32_bf16 v[52:55], v[28:31], v[124:127], v[52:55]\n"
    "v_fma_f32 v78, -v76, v76, v78\n"
    "v_max_f32 v78, 0, v78\n"
    "v_mfma_f32_16x16x32_bf16 v[56:59], v[216:219], v[112:115], v[56:59]\n"
    "v_add_f32 v78, 0x3727c5ac, v78\n"
    "v_mfma_f32_16x16x32_bf16 v[56:59], v[228:231], v[116:119], v[56:59]\n"
    "v_rsq_f32 v78, v78\n"
    "v_mfma_f32_16x16x32_bf16 v[56:59], v[240:243], v[120:123], v[56:59]\n"
    "v_mul_f32 v76, v76, v78\n"
    "v_fma_f32 v79, v64, v78, -v76\n"
    "v_mfma_f32_16x16x32_bf16 v[56:59], v[252:255], v[124:127], v[56:59]\n"
    "s_add_u32 s91, %[gr], 917504\n"
    "ds_write_b128 v12, v[52:55] offset:192\n"
    "v_fmac_f32 v64, v79, v77\n"
    "v_and_b32 v77, 0xffff0000, v86\n"
    "v_fma_f32 v79, v65, v78, -v76\n"
    "v_fmac_f32 v65, v79, v77\n"
    "v_lshlrev_b32 v77, 16, v87\n"
    "v_fma_f32 v79, v66, v78, -v76\n"
    "ds_write_b128 v12, v[56:59] offset:256\n"
    "v_fmac_f32 v66, v79, v77\n"
    "v_and_b32 v77, 0xffff0000, v87\n"
    "v_fma_f32 v79, v67, v78, -v76\n"
    "v_fmac_f32 v67, v79, v77\n"
    "v_fma_f32 v79, v73, v78, -v76\n"
    "v_fmac_f32 v73, v79, v85\n"
    "v_cvt_pk_bf16_f32 v76, v64, v65\n"
    "s_waitcnt lgkmcnt(0)\n"
    "s_barrier\n"
    "ds_read_b128 v[96:99], v13 offset:0\n"
    "ds_read_b32 v100, v14 offset:0\n"
    "ds_read_b128 v[112:115], v13 offset:5376\n"
    "ds_read_b32 v101, v14 offset:5376\n"
    "ds_read_b128 v[116:119], v13 offset:10752\n"
    "ds_read_b32 v102, v14 offset:10752\n"
    "ds_read_b128 v[120:123], v13 offset:16128\n"
    "ds_read_b32 v103, v14 offset:16128\n"
    "v_cvt_pk_bf16_f32 v77, v66, v67\n"
    "v_mov_b32_dpp v79, v73 quad_perm:[1,0,3,2] row_mask:0xf bank_mask:0xf\n"
    "v_cvt_pk_bf16_f32 v78, v73, v79\n"
    "v_min_u32 v76, -2, v76\n"
    "v_min_u32 v77, -2, v77\n"
    "v_min_u32 v78, -2, v78\n"
    "buffer_load_dwordx4 v[104:107], v15, %[srdw], s91 offen sc1\n"
    "v_lshlrev_b32 v90, 16, v90\n"
    "v_mov_b32_dpp v79, v78 quad_perm:[2,2,2,2] row_mask:0xf bank_mask:0xf\n"
    "v_permlane16_swap_b32 v76, v78\n"
    "s_add_u32 s92, %[xv], 17170432\n"
    "v_permlane16_swap_b32 v77, v79\n"
    "buffer_store_dwordx4 v[76:79], v18, %[srdw], s89 offen sc1\n"
    "s_waitcnt lgkmcnt(7)\n"
    "v_add_f32 v80, v80, v96\n"
    "v_add_f32 v81, v81, v97\n"
    "v_add_f32 v82, v82, v98\n"
    "v_add_f32 v83, v83, v99\n"
    "s_waitcnt lgkmcnt(6)\n"
    "v_add_f32 v84, v84, v100\n"
    "s_waitcnt lgkmcnt(5)\n"
    "v_add_f32 v80, v80, v112\n"
    "v_add_f32 v81, v81, v113\n"
    "v_add_f32 v82, v82, v114\n"
    "v_add_f32 v83, v83, v115\n"
    "s_waitcnt lgkmcnt(4)\n"
    "v_add_f32 v84, v84, v101\n"
    "s_waitcnt lgkmcnt(3)\n"
    "v_add_f32 v80, v80, v116\n"
    "v_add_f32 v81, v81, v117\n"
    "v_add_f32 v82, v82, v118\n"
    "v_add_f32 v83, v83, v119\n"
    "s_waitcnt lgkmcnt(2)\n"
    "v_add_f32 v84, v84, v102\n"
    "s_waitcnt lgkmcnt(1)\n"
    "v_add_f32 v80, v80, v120\n"
    "v_add_f32 v81, v81, v121\n"
    "v_add_f32 v82, v82, v122\n"
    "v_add_f32 v83, v83, v123\n"
    "s_waitcnt lgkmcnt(0)\n"
    "v_add_f32 v84, v84, v103\n"
    "v_add_f32 v96, v80, v81\n"
    "v_mul_f32 v98, v80, v80\n"
    "v_fmac_f32 v98, v81, v81\n"
    "v_add_f32 v96, v96, v82\n"
    "v_fmac_f32 v98, v82, v82\n"
    "v_add_f32 v96, v96, v83\n"
    "v_fmac_f32 v98, v83, v83\n"
    "v_add_f32 v96, v96, v84\n"
    "v_fmac_f32 v98, v84, v84\n"
    "v_mov_b32 v97, %[tag]\n"
    "v_mov_b32 v99, %[tag]\n"
    "v_add_f32_dpp v96, v96, v96 row_ror:8 row_mask:0xf bank_mask:0xf\n"
    "v_add_f32_dpp v98, v98, v98 row_ror:8 row_mask:0xf bank_mask:0xf\n"
    "s_add_u32 s90, %[gr], 1048576\n"
    "v_add_f32_dpp v96, v96, v96 row_ror:4 row_mask:0xf bank_mask:0xf\n"
    "v_add_f32_dpp v98, v98, v98 row_ror:4 row_mask:0xf bank_mask:0xf\n"
    "s_nop 0\n"
    "v_add_f32_dpp v96, v96, v96 row_ror:2 row_mask:0xf bank_mask:0xf\n"
    "v_add_f32_dpp v98, v98, v98 row_ror:2 row_mask:0xf bank_mask:0xf\n"
    "s_nop 0\n"
    "v_add_f32_dpp v96, v96, v96 row_ror:1 row_mask:0xf bank_mask:0xf\n"
    "s_waitcnt vmcnt(11)\n"
    "ds_read2st64_b64 v[32:35], v5 offset0:42 offset1:43\n"
    "v_add_f32_dpp v98, v98, v98 row_ror:1 row_mask:0xf bank_mask:0xf\n"
    "buffer_store_dwordx4 v[96:99], v19, %[srdw], s90 offen sc1\n"
    "s_waitcnt vmcnt(11)\n"
    "ds_read2st64_b64 v[36:39], v5 offset0:44 offset1:45\n"
    "s_waitcnt lgkmcnt(1)\n"
    "v_mfma_f32_16x16x32_bf16 v[52:55], v[20:23], v[32:35], 0\n"
    "v_mfma_f32_16x16x32_bf16 v[56:59], v[32:35], v[32:35], 0\n"
    "s_waitcnt lgkmcnt(0) vmcnt(10)\n"
    "ds_read2st64_b64 v[24:27], v5 offset0:46 offset1:47\n"
    "v_mfma_f32_16x16x32_bf16 v[52:55], v[20:23], v[36:39], v[52:55]\n"
    "v_mfma_f32_16x16x32_bf16 v[56:59], v[36:39], v[36:39], v[56:59]\n"
    "s_waitcnt lgkmcnt(0) vmcnt(9)\n"
    "ds_read2st64_b64 v[28:31], v5 offset0:48 offset1:49\n"
    "v_mfma_f32_16x16x32_bf16 v[52:55], v[20:23], v[24:27], v[52:55]\n"
    "v_mfma_f32_16x16x32_bf16 v[56:59], v[24:27], v[24:27], v[56:59]\n"
    "s_waitcnt lgkmcnt(0) vmcnt(8)\n"
    "ds_read2st64_b64 v[32:35], v5 offset0:50 offset1:51\n"
    "v_mfma_f32_16x16x32_bf16 v[52:55], v[20:23], v[28:31], v[52:55]\n"
    "v_mfma_f32_16x16x32_bf16 v[56:59], v[28:31], v[28:31], v[56:59]\n"
    "s_waitcnt lgkmcnt(0)\n"
    "v_mfma_f32_16x16x32_bf16 v[52:55], v[20:23], v[32:35], v[52:55]\n"
    "v_mfma_f32_16x16x32_bf16 v[56:59], v[32:35], v[32:35], v[56:59]\n"
    "v_cvt_pk_bf16_f32 v76, v60, v61\n"
    "v_cvt_pk_bf16_f32 v77, v62, v63\n"
    "s_nop 4\n"
    "v_cmp_u_f32 vcc, v52, v52\n"
    "s_nop 0\n"
    "s_cmp_lg_u64 vcc, 0\n"
    "s_cbranch_scc0 L36_%=\n"
    " s_mov_b32 s98, m0\n"
    " s_mov_b32 s99, s68\n"
    " s_waitcnt lgkmcnt(0)\n"
    " L35_%=_l:\n"
    " s_sub_u32 s54, s54, 1\n"
    " s_cselect_b32 s54, 0, s54\n"
    " s_cbranch_scc1 L35_%=_x\n"
    " v_mov_b32 v94, 0\n"
    " s_add_u32 m0, %[ldsx], 21504\n"
    " s_add_u32 s68, %[x], 15859712\n"
    " buffer_load_dwordx4 v3, %[srdw], s68 offen offset:0 sc1 lds\n"
    " buffer_load_dwordx4 v3, %[srdw], s68 offen offset:1024 sc1 lds\n"
    " buffer_load_dwordx4 v3, %[srdw], s68 offen offset:2048 sc1 lds\n"
    " buffer_load_dwordx4 v3, %[srdw], s68 offen offset:3072 sc1 lds\n"
    " s_add_u32 m0, %[ldsx], 25600\n"
    " s_add_u32 s68, %[x], 15863808\n"
    " buffer_load_dwordx4 v3, %[srdw], s68 offen offset:0 sc1 lds\n"
    " s_waitcnt vmcnt(0)\n"
    " ds_read2st64_b64 v[24:27], v5 offset0:42 offset1:43\n"
    " ds_read2st64_b64 v[28:31], v5 offset0:44 offset1:45\n"
    " ds_read2st64_b64 v[32:35], v5 offset0:46 offset1:47\n"
    " ds_read2st64_b64 v[36:39], v5 offset0:48 offset1:49\n"
    " ds_read2st64_b64 v[124:127], v5 offset0:50 offset1:51\n"
    " s_waitcnt lgkmcnt(4)\n"
    " v_mfma_f32_16x16x32_bf16 v[52:55], v[20:23], v[24:27], 0\n"
    " v_max3_u32 v94, v24, v25, v94\n"
    " v_mfma_f32_16x16x32_bf16 v[56:59], v[24:27], v[24:27], 0\n"
    " v_max3_u32 v94, v26, v27, v94\n"
    " s_waitcnt lgkmcnt(3)\n"
    " v_mfma_f32_16x16x32_bf16 v[52:55], v[20:23], v[28:31], v[52:55]\n"
    " v_max3_u32 v94, v28, v29, v94\n"
    " v_mfma_f32_16x16x32_bf16 v[56:59], v[28:31], v[28:31], v[56:59]\n"
    " v_max3_u32 v94, v30, v31, v94\n"
    " s_waitcnt lgkmcnt(2)\n"
    " v_mfma_f32_16x16x32_bf16 v[52:55], v[20:23], v[32:35], v[52:55]\n"
    " v_max3_u32 v94, v32, v33, v94\n"
    " v_mfma_f32_16x16x32_bf16 v[56:59], v[32:35], v[32:35], v[56:59]\n"
    " v_max3_u32 v94, v34, v35, v94\n"
    " s_waitcnt lgkmcnt(1)\n"
    " v_mfma_f32_16x16x32_bf16 v[52:55], v[20:23], v[36:39], v[52:55]\n"
    " v_max3_u32 v94, v36, v37, v94\n"
    " v_mfma_f32_16x16x32_bf16 v[56:59], v[36:39], v[36:39], v[56:59]\n"
    " v_max3_u32 v94, v38, v39, v94\n"
    " s_waitcnt lgkmcnt(0)\n"
    " v_mfma_f32_16x16x32_bf16 v[52:55], v[20:23], v[124:127], v[52:55]\n"
    " v_max3_u32 v94, v124, v125, v94\n"
    " v_mfma_f32_16x16x32_bf16 v[56:59], v[124:127], v[124:127], v[56:59]\n"
    " v_max3_u32 v94, v126, v127, v94\n"
    " v_cmp_eq_u32 vcc, -1, v94\n"
    " s_nop 1\n"
    " s_cmp_lg_u64 vcc, 0\n"
    " s_cbranch_scc1 L35_%=_l\n"
    " L35_%=_x:\n"
    " s_mov_b32 m0, s98\n"
    " s_mov_b32 s68, s99\n"
    " s_nop 7\n"
    "L36_%=:\n"
    "v_cndmask_b32_e64 v53, v56, v57, s[48:49]\n"
    "v_cndmask_b32_e64 v53, v53, v58, s[50:51]\n"
    "v_cndmask_b32_e64 v53, v53, v59, s[52:53]\n"
    "ds_write_b64 v8, v[52:53] offset:21504\n"
    "v_mov_b32 v75, v72\n"
    "v_cvt_pk_bf16_f32 v124, v64, v65\n"
    "v_cvt_pk_bf16_f32 v125, v66, v67\n"
    "v_mov_b32 v79, v73\n"
    "s_waitcnt lgkmcnt(0)\n"
    "s_barrier\n"
    "ds_read2st64_b64 v[36:39], v4 offset0:42 offset1:43\n"
    "ds_read2st64_b64 v[24:27], v4 offset0:44 offset1:45\n"
    "ds_read2st64_b64 v[28:31], v4 offset0:46 offset1:47\n"
    "ds_read2st64_b64 v[32:35], v4 offset0:48 offset1:49\n"
    "s_waitcnt lgkmcnt(3)\n"
    "v_mfma_f32_16x16x32_bf16 v[40:43], a[0:3], v[36:39], 0\n"
    "v_mfma_f32_16x16x32_bf16 v[44:47], a[4:7], v[36:39], 0\n"
    "v_mfma_f32_16x16x32_bf16 v[48:51], a[8:11], v[36:39], 0\n"
    "s_waitcnt lgkmcnt(2)\n"
    "ds_read2st64_b64 v[36:39], v4 offset0:50 offset1:51\n"
    "v_mfma_f32_16x16x32_bf16 v[40:43], a[12:15], v[24:27], v[40:43]\n"
    "v_mfma_f32_16x16x32_bf16 v[44:47], a[16:19], v[24:27], v[44:47]\n"
    "v_mfma_f32_16x16x32_bf16 v[48:51], a[20:23], v[24:27], v[48:51]\n"
    "s_waitcnt lgkmcnt(2)\n"
    "ds_read2st64_b64 v[24:27], v4 offset0:52 offset1:53\n"
    "v_mfma_f32_16x16x32_bf16 v[40:43], a[24:27], v[28:31], v[40:43]\n"
    "v_mfma_f32_16x16x32_bf16 v[44:47], a[28:31], v[28:31], v[44:47]\n"
    "v_mfma_f32_16x16x32_bf16 v[48:51], a[32:35], v[28:31], v[48:51]\n"
    "s_waitcnt lgkmcnt(2)\n"
    "ds_read2st64_b64 v[28:31], v4 offset0:54 offset1:55\n"
    "v_mfma_f32_16x16x32_bf16 v[40:43], a[36:39], v[32:35], v[40:43]\n"
    "v_lshrrev_b32 v98, 1, v0\n"
    "v_mfma_f32_16x16x32_bf16 v[44:47], a[40:43], v[32:35], v[44:47]\n"
    "buffer_load_dwordx2 v[96:97], v98, %[srdw], %[cs] offen sc1\n"
    "v_mfma_f32_16x16x32_bf16 v[48:51], a[44:47], v[32:35], v[48:51]\n"
    "s_waitcnt lgkmcnt(2)\n"
    "ds_read2st64_b64 v[32:35], v4 offset0:56 offset1:57\n"
    "v_mfma_f32_16x16x32_bf16 v[40:43], a[48:51], v[36:39], v[40:43]\n"
    "v_mfma_f32_16x16x32_bf16 v[44:47], a[52:55], v[36:39], v[44:47]\n"
    "v_mfma_f32_16x16x32_bf16 v[48:51], a[56:59], v[36:39], v[48:51]\n"
    "s_waitcnt lgkmcnt(2) vmcnt(8)\n"
    "ds_read2st64_b64 v[36:39], v5 offset0:84 offset1:85\n"
    "v_mfma_f32_16x16x32_bf16 v[40:43], a[60:63], v[24:27], v[40:43]\n"
    "s_waitcnt vmcnt(3)\n"
    "v_cmp_ne_u32 vcc, %[tag], v105\n"
    "v_cmp_ne_u32 s[68:69], %[tag], v107\n"
    "s_nop 0\n"
    "s_or_b64 vcc, vcc, s[68:69]\n"
    "s_or_b32 s68, vcc_lo, vcc_hi\n"
    "s_and_b32 s68, s68, 0xff00ff\n"
    "s_cbranch_scc1 L33_%=\n"
    "L34_%=:\n"
    "v_mfma_f32_16x16x32_bf16 v[44:47], a[64:67], v[24:27], v[44:47]\n"
    "v_add_f32_dpp v104, v104, v104 row_ror:8 row_mask:0xf bank_mask:0xf\n"
    "v_add_f32_dpp v106, v106, v106 row_ror:8 row_mask:0xf bank_mask:0xf\n"
    "v_mfma_f32_16x16x32_bf16 v[48:51], a[68:71], v[24:27], v[48:51]\n"
    "s_waitcnt lgkmcnt(2)\n"
    "ds_read2st64_b64 v[24:27], v4 offset0:58 offset1:59\n"
    "v_add_f32_dpp v104, v104, v104 row_ror:4 row_mask:0xf bank_mask:0xf\n"
    "v_mfma_f32_16x16x32_bf16 v[40:43], a[72:75], v[28:31], v[40:43]\n"
    "v_add_f32_dpp v106, v106, v106 row_ror:4 row_mask:0xf bank_mask:0xf\n"
    "v_add_f32_dpp v104, v104, v104 row_ror:2 row_mask:0xf bank_mask:0xf\n"
    "v_mfma_f32_16x16x32_bf16 v[44:47], a[76:79], v[28:31], v[44:47]\n"
    "v_add_f32_dpp v106, v106, v106 row_ror:2 row_mask:0xf bank_mask:0xf\n"
    "v_add_f32_dpp v104, v104, v104 row_ror:1 row_mask:0xf bank_mask:0xf\n"
    "v_mfma_f32_16x16x32_bf16 v[48:51], a[80:83], v[28:31], v[48:51]\n"
    "s_waitcnt lgkmcnt(2)\n"
    "ds_read2st64_b64 v[28:31], v5 offset0:86 offset1:87\n"
    "v_add_f32_dpp v106, v106, v106 row_ror:1 row_mask:0xf bank_mask:0xf\n"
    "v_mfma_f32_16x16x32_bf16 v[40:43], a[84:87], v[32:35], v[40:43]\n"
    "s_add_u32 m0, %[ldsx], 0\n"
    "s_add_u32 s68, %[x], 16515072\n"
    "buffer_load_dwordx4 v3, %[srdw], s68 offen sc1 lds\n"
    "v_mfma_f32_16x16x32_bf16 v[44:47], a[88:91], v[32:35], v[44:47]\n"
    "buffer_load_dwordx4 v3, %[srdw], s68 offen offset:1024 sc1 lds\n"
    "v_mul_f32 v104, 0x3acccccd, v104\n"
    "v_mfma_f32_16x16x32_bf16 v[48:51], a[92:95], v[32:35], v[48:51]\n"
    "s_waitcnt lgkmcnt(2)\n"
    "ds_read2st64_b64 v[32:35], v4 offset0:60 offset1:61\n"
    "v_mul_f32 v106, 0x3acccccd, v106\n"
    "v_mfma_f32_16x16x32_bf16 v[52:55], v[20:23], v[36:39], 0\n"
    "v_fma_f32 v106, -v104, v104, v106\n"
    "v_max_f32 v106, 0, v106\n"
    "v_mfma_f32_16x16x32_bf16 v[56:59], v[36:39], v[36:39], 0\n"
    "s_waitcnt lgkmcnt(2)\n"
    "ds_read2st64_b64 v[36:39], v5 offset0:88 offset1:89\n"
    "v_add_f32 v106, 0x3727c5ac, v106\n"
    "v_mfma_f32_16x16x32_bf16 v[40:43], a[96:99], v[24:27], v[40:43]\n"
    "v_rsq_f32 v106, v106\n"
    "v_mfma_f32_16x16x32_bf16 v[44:47], a[100:103], v[24:27], v[44:47]\n"
    "buffer_load_dwordx4 v3, %[srdw], s68 offen offset:2048 sc1 lds\n"
    "v_mul_f32 v104, v104, v106\n"
    "v_mfma_f32_16x16x32_bf16 v[48:51], a[104:107], v[24:27], v[48:51]\n"
    "s_waitcnt lgkmcnt(2)\n"
    "ds_read2st64_b64 v[24:27], v4 offset0:62 offset1:63\n"
    "v_lshlrev_b32 v105, 16, v88\n"
    "v_mfma_f32_16x16x32_bf16 v[52:55], v[20:23], v[28:31], v[52:55]\n"
    "v_fma_f32 v107, v68, v106, -v104\n"
    "v_fmac_f32 v68, v107, v105\n"
    "v_mfma_f32_16x16x32_bf16 v[56:59], v[28:31], v[28:31], v[56:59]\n"
    "s_waitcnt lgkmcnt(2)\n"
    "ds_read2st64_b64 v[28:31], v5 offset0:90 offset1:91\n"
    "v_and_b32 v105, 0xffff0000, v88\n"
    "v_mfma_f32_16x16x32_bf16 v[40:43], a[108:111], v[32:35], v[40:43]\n"
    "buffer_load_dwordx4 v3, %[srdw], s68 offen offset:3072 sc1 lds\n"
    "v_fma_f32 v107, v69, v106, -v104\n"
    "v_mfma_f32_16x16x32_bf16 v[44:47], a[112:115], v[32:35], v[44:47]\n"
    "v_fmac_f32 v69, v107, v105\n"
    "v_lshlrev_b32 v105, 16, v89\n"
    "v_mfma_f32_16x16x32_bf16 v[48:51], a[116:119], v[32:35], v[48:51]\n"
    "s_waitcnt lgkmcnt(2)\n"
    "ds_read2st64_b64 v[32:35], v4 offset0:64 offset1:65\n"
    "v_fma_f32 v107, v70, v106, -v104\n"
    "v_mfma_f32_16x16x32_bf16 v[52:55], v[20:23], v[36:39], v[52:55]\n"
    "v_fmac_f32 v70, v107, v105\n"
    "v_and_b32 v105, 0xffff0000, v89\n"
    "v_mfma_f32_16x16x32_bf16 v[56:59], v[36:39], v[36:39], v[56:59]\n"
    "s_waitcnt lgkmcnt(2)\n"
    "ds_read2st64_b64 v[36:39], v5 offset0:92 offset1:93\n"
    "v_fma_f32 v107, v71, v106, -v104\n"
    "v_mfma_f32_16x16x32_bf16 v[40:43], a[120:123], v[24:27], v[40:43]\n"
    "s_add_u32 m0, %[ldsx], 4096\n"
    "s_add_u32 s68, %[x], 16519168\n"
    "buffer_load_dwordx4 v3, %[srdw], s68 offen sc1 lds\n"
    "v_mfma_f32_16x16x32_bf16 v[44:47], a[124:127], v[24:27], v[44:47]\n"
    "v_fmac_f32 v71, v107, v105\n"
    "v_fma_f32 v107, v74, v106, -v104\n"
    "v_mfma_f32_16x16x32_bf16 v[48:51], a[128:131], v[24:27], v[48:51]\n"
    "s_waitcnt lgkmcnt(2)\n"
    "ds_read2st64_b64 v[24:27], v4 offset0:66 offset1:67\n"
    "v_fmac_f32 v74, v107, v90\n"
    "v_mfma_f32_16x16x32_bf16 v[52:55], v[20:23], v[28:31], v[52:55]\n"
    "ds_read2st64_b64 v[112:115], v4 offset0:78 offset1:79\n"
    "ds_read2st64_b64 v[116:119], v4 offset0:80 offset1:81\n"
    "v_mfma_f32_16x16x32_bf16 v[56:59], v[28:31], v[28:31], v[56:59]\n"
    "s_waitcnt lgkmcnt(4)\n"
    "ds_read2st64_b64 v[28:31], v4 offset0:68 offset1:69\n"
    "v_cvt_pk_bf16_f32 v104, v68, v69\n"
    "v_mfma_f32_16x16x32_bf16 v[40:43], a[132:135], v[32:35], v[40:43]\n"
    "v_cvt_pk_bf16_f32 v105, v70, v71\n"
    "v_mov_b32_dpp v107, v74 quad_perm:[1,0,3,2] row_mask:0xf bank_mask:0xf\n"
    "v_mfma_f32_16x16x32_bf16 v[44:47], a[136:139], v[32:35], v[44:47]\n"
    "v_cvt_pk_bf16_f32 v106, v74, v107\n"
    "v_min_u32 v104, -2, v104\n"
    "v_mfma_f32_16x16x32_bf16 v[48:51], a[140:143], v[32:35], v[48:51]\n"
    "s_waitcnt lgkmcnt(4)\n"
    "ds_read2st64_b64 v[32:35], v4 offset0:70 offset1:71\n"
    "v_min_u32 v105, -2, v105\n"
    "v_mfma_f32_16x16x32_bf16 v[52:55], v[20:23], v[36:39], v[52:55]\n"
    "v_min_u32 v106, -2, v106\n"
    "v_cvt_pk_bf16_f32 v126, v68, v69\n"
    "v_mfma_f32_16x16x32_bf16 v[56:59], v[36:39], v[36:39], v[56:59]\n"
    "s_waitcnt lgkmcnt(4)\n"
    "ds_read2st64_b64 v[36:39], v4 offset0:72 offset1:73\n"
    "v_mov_b32_dpp v107, v106 quad_perm:[2,2,2,2] row_mask:0xf bank_mask:0xf\n"
    "v_mfma_f32_16x16x32_bf16 v[40:43], a[144:147], v[24:27], v[40:43]\n"
    "v_permlane16_swap_b32 v104, v106\n"
    "v_mfma_f32_16x16x32_bf16 v[44:47], a[148:151], v[24:27], v[44:47]\n"
    "v_cmp_u_f32 vcc, v52, v52\n"
    "s_nop 0\n"
    "s_cmp_lg_u64 vcc, 0\n"
    "s_cbranch_scc1 L39_%=\n"
    "L40_%=:\n"
    "v_mfma_f32_16x16x32_bf16 v[48:51], a[152:155], v[24:27], v[48:51]\n"
    "s_waitcnt lgkmcnt(2)\n"
    "ds_read2st64_b64 v[24:27], v4 offset0:74 offset1:75\n"
    "v_cndmask_b32_e64 v53, v56, v57, s[48:49]\n"
    "v_mfma_f32_16x16x32_bf16 v[40:43], a[156:159], v[28:31], v[40:43]\n"
    "v_cndmask_b32_e64 v53, v53, v58, s[50:51]\n"
    "v_cndmask_b32_e64 v53, v53, v59, s[52:53]\n"
    "v_mfma_f32_16x16x32_bf16 v[44:47], a[160:163], v[28:31], v[44:47]\n"
    "ds_write_b64 v8, v[52:53] offset:43008\n"
    "v_mfma_f32_16x16x32_bf16 v[48:51], a[164:167], v[28:31], v[48:51]\n"
    "s_waitcnt lgkmcnt(3)\n"
    "ds_read2st64_b64 v[28:31], v4 offset0:76 offset1:77\n"
    "v_cvt_pk_bf16_f32 v127, v70, v71\n"
    "v_mfma_f32_16x16x32_bf16 v[40:43], a[168:171], v[32:35], v[40:43]\n"
    "v_permlane16_swap_b32 v105, v107\n"
    "v_mfma_f32_16x16x32_bf16 v[44:47], a[172:175], v[32:35], v[44:47]\n"
    "buffer_store_dwordx4 v[104:107], v18, %[srdw], s92 offen sc1\n"
    "v_mfma_f32_16x16x32_bf16 v[48:51], a[176:179], v[32:35], v[48:51]\n"
    "s_waitcnt lgkmcnt(3)\n"
    "v_mfma_f32_16x16x32_bf16 v[40:43], a[180:183], v[36:39], v[40:43]\n"
    "v_mfma_f32_16x16x32_bf16 v[44:47], a[184:187], v[36:39], v[44:47]\n"
    "ds_read_b128 v[52:55], v9 offset:21504\n"
    "ds_read_b128 v[56:59], v9 offset:21520\n"
    "v_mfma_f32_16x16x32_bf16 v[48:51], a[188:191], v[36:39], v[48:51]\n"
    "s_waitcnt lgkmcnt(4) vmcnt(6)\n"
    "v_cmp_ne_u32 vcc, %[tag], v97\n"
    "s_nop 0\n"
    "s_cmp_lg_u64 vcc, 0\n"
    "s_cbranch_scc1 L37_%=\n"
    "L38_%=:\n"
    "v_mfma_f32_16x16x32_bf16 v[40:43], a[192:195], v[24:27], v[40:43]\n"
    "s_waitcnt lgkmcnt(2)\n"
    "s_barrier\n"
    "v_mfma_f32_16x16x32_bf16 v[44:47], a[196:199], v[24:27], v[44:47]\n"
    "ds_read2st64_b64 v[32:35], v4 offset0:84 offset1:85\n"
    "ds_read2st64_b64 v[36:39], v4 offset0:86 offset1:87\n"
    "v_mfma_f32_16x16x32_bf16 v[48:51], a[200:203], v[24:27], v[48:51]\n"
    "s_waitcnt lgkmcnt(3)\n"
    "ds_read2st64_b64 v[24:27], v4 offset0:88 offset1:89\n"
    "v_add_f32 v52, v52, v54\n"
    "v_mfma_f32_16x16x32_bf16 v[40:43], a[204:207], v[28:31], v[40:43]\n"
    "s_waitcnt lgkmcnt(3)\n"
    "v_add_f32 v53, v53, v55\n"
    "v_add_f32 v56, v56, v58\n"
    "v_mfma_f32_16x16x32_bf16 v[44:47], a[208:211], v[28:31], v[44:47]\n"
    "s_add_u32 m0, %[ldsx], 21504\n"
    "s_add_u32 s68, s42, 16842752\n"
    "buffer_load_dwordx4 v3, %[srdw], s68 offen sc1 lds\n"
    "v_mfma_f32_16x16x32_bf16 v[48:51], a[212:215], v[28:31], v[48:51]\n"
    "ds_read2st64_b64 v[28:31], v4 offset0:90 offset1:91\n"
    "v_add_f32 v57, v57, v59\n"
    "v_mfma_f32_16x16x32_bf16 v[40:43], a[216:219], v[112:115], v[40:43]\n"
    "s_add_u32 s56, %[gr], 1048576\n"
    "v_add_f32 v52, v52, v56\n"
    "v_add_f32 v53, v53, v57\n"
    "v_mfma_f32_16x16x32_bf16 v[44:47], a[220:223], v[112:115], v[44:47]\n"
    "s_add_u32 s57, %[xv], 17825792\n"
    "v_mul_f32 v7, 0xbacccccd, v52\n"
    "v_mul_f32 v53, 0x3acccccd, v53\n"
    "v_mfma_f32_16x16x32_bf16 v[48:51], a[224:227], v[112:115], v[48:51]\n"
    "buffer_load_dwordx4 v[108:111], v15, %[srdw], s56 offen sc1\n"
    "v_fma_f32 v53, -v7, v7, v53\n"
    "v_mfma_f32_16x16x32_bf16 v[40:43], a[228:231], v[116:119], v[40:43]\n"
    "v_max_f32 v53, 0, v53\n"
    "v_add_f32 v73, 0x3727c5ac, v53\n"
    "v_mfma_f32_16x16x32_bf16 v[44:47], a[232:235], v[116:119], v[44:47]\n"
    "v_rsq_f32 v52, v73\n"
    "v_mfma_f32_16x16x32_bf16 v[48:51], a[236:239], v[116:119], v[48:51]\n"
    "s_waitcnt lgkmcnt(3)\n"
    "v_mul_f32 v72, 0xbfb8aa3b, v52\n"
    "v_lshlrev_b32 v91, 16, v91\n"
    "v_mfma_f32_16x16x32_bf16 v[60:63], a[0:3], v[32:35], 0\n"
    "s_add_u32 s58, %[hst], 9568256\n"
    "v_mov_b32 v6, v96\n"
    "v_mfma_f32_16x16x32_bf16 v[64:67], a[4:7], v[32:35], 0\n"
    "v_mfma_f32_16x16x32_bf16 v[68:71], a[8:11], v[32:35], 0\n"
    "s_waitcnt lgkmcnt(2)\n"
    "ds_read2st64_b64 v[32:35], v4 offset0:92 offset1:93\n"
    "v_fmac_f32_dpp v48, v6, v7 row_newbcast:8 row_mask:0xf bank_mask:0xf\n"
    "v_mfma_f32_16x16x32_bf16 v[60:63], a[12:15], v[36:39], v[60:63]\n"
    "v_fmac_f32_dpp v49, v6, v7 row_newbcast:9 row_mask:0xf bank_mask:0xf\n"
    "v_fmac_f32_dpp v50, v6, v7 row_newbcast:10 row_mask:0xf bank_mask:0xf\n"
    "v_mfma_f32_16x16x32_bf16 v[64:67], a[16:19], v[36:39], v[64:67]\n"
    "v_fmac_f32_dpp v51, v6, v7 row_newbcast:11 row_mask:0xf bank_mask:0xf\n"
    "v_fmac_f32_dpp v40, v6, v7 row_newbcast:0 row_mask:0xf bank_mask:0xf\n"
    "v_mfma_f32_16x16x32_bf16 v[68:71], a[20:23], v[36:39], v[68:71]\n"
    "s_waitcnt lgkmcnt(2)\n"
    "ds_read2st64_b64 v[36:39], v4 offset0:94 offset1:95\n"
    "v_fmac_f32_dpp v44, v6, v7 row_newbcast:4 row_mask:0xf bank_mask:0xf\n"
    "v_mfma_f32_16x16x32_bf16 v[60:63], a[24:27], v[24:27], v[60:63]\n"
    "buffer_load_dwordx4 v3, %[srdw], s68 offen offset:1024 sc1 lds\n"
    "v_mul_f32 v44, v40, v44\n"
    "v_mfma_f32_16x16x32_bf16 v[64:67], a[28:31], v[24:27], v[64:67]\n"
    "v_permlane16_swap_b32 v48, v50\n"
    "v_mfma_f32_16x16x32_bf16 v[68:71], a[32:35], v[24:27], v[68:71]\n"
    "s_waitcnt lgkmcnt(2)\n"
    "ds_read2st64_b64 v[24:27], v4 offset0:96 offset1:97\n"
    "v_mul_f32 v40, v72, v40\n"
    "v_mfma_f32_16x16x32_bf16 v[60:63], a[36:39], v[28:31], v[60:63]\n"
    "v_permlane16_swap_b32 v49, v51\n"
    "v_mfma_f32_16x16x32_bf16 v[64:67], a[40:43], v[28:31], v[64:67]\n"
    "v_exp_f32 v40, v40\n"
    "v_mfma_f32_16x16x32_bf16 v[68:71], a[44:47], v[28:31], v[68:71]\n"
    "s_waitcnt lgkmcnt(2)\n"
    "ds_read2st64_b64 v[28:31], v4 offset0:98 offset1:99\n"
    "v_fma_f32 v40, v40, v73, v73\n"
    "v_mfma_f32_16x16x32_bf16 v[60:63], a[48:51], v[32:35], v[60:63]\n"
    "v_rcp_f32 v40, v40\n"
    "v_mfma_f32_16x16x32_bf16 v[64:67], a[52:55], v[32:35], v[64:67]\n"
    "v_mul_f32 v40, v40, v44\n"
    "v_fmac_f32_dpp v41, v6, v7 row_newbcast:1 row_mask:0xf bank_mask:0xf\n"
    "v_mfma_f32_16x16x32_bf16 v[68:71], a[56:59], v[32:35], v[68:71]\n"
    "s_waitcnt lgkmcnt(2)\n"
    "buffer_load_dwordx4 v3, %[srdw], s68 offen offset:2048 sc1 lds\n"
    "v_mfma_f32_16x16x32_bf16 v[60:63], a[60:63], v[36:39], v[60:63]\n"
    "kx_a7:\n"
    "s_cbranch_execz kx_ret\n"
    "s_waitcnt vmcnt(9)\n"
    "ds_read2st64_b64 v[32:35], v5 offset0:0 offset1:1\n"
    "v_fmac_f32_dpp v45, v6, v7 row_newbcast:5 row_mask:0xf bank_mask:0xf\n"
    "v_mfma_f32_16x16x32_bf16 v[64:67], a[64:67], v[36:39], v[64:67]\n"
    "v_mul_f32 v45, v41, v45\n"
    "v_mul_f32 v41, v72, v41\n"
    "v_mfma_f32_16x16x32_bf16 v[68:71], a[68:71], v[36:39], v[68:71]\n"
    "s_waitcnt lgkmcnt(2)\n"
    "ds_read2st64_b64 v[36:39], v4 offset0:100 offset1:101\n"
    "v_fmac_f32_dpp v42, v6, v7 row_newbcast:2 row_mask:0xf bank_mask:0xf\n"
    "v_mfma_f32_16x16x32_bf16 v[60:63], a[72:75], v[24:27], v[60:63]\n"
    "v_exp_f32 v41, v41\n"
    "v_mfma_f32_16x16x32_bf16 v[64:67], a[76:79], v[24:27], v[64:67]\n"
    "s_add_u32 s59, %[hst], 18481152\n"
    "v_mov_b32 v100, -1\n"
    "v_mov_b32 v101, -1\n"
    "v_mfma_f32_16x16x32_bf16 v[68:71], a[80:83], v[24:27], v[68:71]\n"
    "s_waitcnt lgkmcnt(2) vmcnt(8)\n"
    "ds_read2st64_b64 v[24:27], v5 offset0:2 offset1:3\n"
    "v_mov_b32 v102, -1\n"
    "v_mfma_f32_16x16x32_bf16 v[60:63], a[84:87], v[28:31], v[60:63]\n"
    "v_mov_b32 v103, -1\n"
    "v_fma_f32 v41, v41, v73, v73\n"
    "v_mfma_f32_16x16x32_bf16 v[64:67], a[88:91], v[28:31], v[64:67]\n"
    "v_rcp_f32 v41, v41\n"
    "v_mfma_f32_16x16x32_bf16 v[68:71], a[92:95], v[28:31], v[68:71]\n"
    "s_waitcnt lgkmcnt(2)\n"
    "buffer_load_dwordx4 v3, %[srdw], s68 offen offset:3072 sc1 lds\n"
    "v_mfma_f32_16x16x32_bf16 v[52:55], v[20:23], v[32:35], 0\n"
    "ds_read2st64_b64 v[28:31], v4 offset0:102 offset1:103\n"
    "v_mul_f32 v41, v41, v45\n"
    "v_mfma_f32_16x16x32_bf16 v[56:59], v[32:35], v[32:35], 0\n"
    "s_waitcnt lgkmcnt(2) vmcnt(8)\n"
    "ds_read2st64_b64 v[32:35], v5 offset0:4 offset1:5\n"
    "v_cvt_pk_bf16_f32 v40, v40, v41\n"
    "v_mfma_f32_16x16x32_bf16 v[60:63], a[96:99], v[36:39], v[60:63]\n"
    "v_fmac_f32_dpp v46, v6, v7 row_newbcast:6 row_mask:0xf bank_mask:0xf\n"
    "v_mul_f32 v46, v42, v46\n"
    "v_mfma_f32_16x16x32_bf16 v[64:67], a[100:103], v[36:39], v[64:67]\n"
    "v_mul_f32 v42, v72, v42\n"
    "v_fmac_f32_dpp v43, v6, v7 row_newbcast:3 row_mask:0xf bank_mask:0xf\n"
    "v_mfma_f32_16x16x32_bf16 v[68:71], a[104:107], v[36:39], v[68:71]\n"
    "s_waitcnt lgkmcnt(2)\n"
    "ds_read2st64_b64 v[36:39], v4 offset0:104 offset1:105\n"
    "v_fmac_f32_dpp v47, v6, v7 row_newbcast:7 row_mask:0xf bank_mask:0xf\n"
    "v_mfma_f32_16x16x32_bf16 v[52:55], v[20:23], v[24:27], v[52:55]\n"
    "v_exp_f32 v42, v42\n"
    "v_mfma_f32_16x16x32_bf16 v[56:59], v[24:27], v[24:27], v[56:59]\n"
    "s_waitcnt lgkmcnt(2) vmcnt(7)\n"
    "ds_read2st64_b64 v[24:27], v5 offset0:6 offset1:7\n"
    "v_fma_f32 v42, v42, v73, v73\n"
    "v_mfma_f32_16x16x32_bf16 v[60:63], a[108:111], v[28:31], v[60:63]\n"
    "s_add_u32 m0, %[ldsx], 25600\n"
    "s_add_u32 s68, s42, 16846848\n"
    "buffer_load_dwordx4 v3, %[srdw], s68 offen sc1 lds\n"
    "v_mfma_f32_16x16x32_bf16 v[64:67], a[112:115], v[28:31], v[64:67]\n"
    "v_rcp_f32 v42, v42\n"
    "v_mfma_f32_16x16x32_bf16 v[68:71], a[116:119], v[28:31], v[68:71]\n"
    "s_waitcnt lgkmcnt(2)\n"
    "ds_read2st64_b64 v[28:31], v4 offset0:106 offset1:107\n"
    "v_mul_f32 v42, v42, v46\n"
    "v_mfma_f32_16x16x32_bf16 v[52:55], v[20:23], v[32:35], v[52:55]\n"
    "v_mul_f32 v47, v43, v47\n"
    "v_mul_f32 v43, v72, v43\n"
    "v_mfma_f32_16x16x32_bf16 v[56:59], v[32:35], v[32:35], v[56:59]\n"
    "s_waitcnt lgkmcnt(2) vmcnt(7)\n"
    "ds_read2st64_b64 v[32:35], v5 offset0:8 offset1:9\n"
    "v_mul_f32 v50, v48, v50\n"
    "v_mfma_f32_16x16x32_bf16 v[60:63], a[120:123], v[36:39], v[60:63]\n"
    "v_exp_f32 v43, v43\n"
    "v_mfma_f32_16x16x32_bf16 v[64:67], a[124:127], v[36:39], v[64:67]\n"
    "v_fma_f32 v43, v43, v73, v73\n"
    "v_mul_f32 v48, v72, v48\n"
    "v_mfma_f32_16x16x32_bf16 v[68:71], a[128:131], v[36:39], v[68:71]\n"
    "s_waitcnt lgkmcnt(2)\n"
    "ds_read2st64_b64 v[36:39], v4 offset0:108 offset1:109\n"
    "v_mul_f32 v51, v49, v51\n"
    "v_mfma_f32_16x16x32_bf16 v[52:55], v[20:23], v[24:27], v[52:55]\n"
    "ds_read2st64_b64 v[112:115], v4 offset0:120 offset1:121\n"
    "ds_read2st64_b64 v[116:119], v4 offset0:122 offset1:123\n"
    "v_mfma_f32_16x16x32_bf16 v[56:59], v[24:27], v[24:27], v[56:59]\n"
    "s_waitcnt lgkmcnt(4)\n"
    "ds_read2st64_b64 v[24:27], v4 offset0:110 offset1:111\n"
    "v_mul_f32 v49, v72, v49\n"
    "v_mfma_f32_16x16x32_bf16 v[60:63], a[132:135], v[28:31], v[60:63]\n"
    "buffer_store_dwordx4 v[100:103], v10, %[srdw], s59 offen sc1\n"
    "v_mfma_f32_16x16x32_bf16 v[64:67], a[136:139], v[28:31], v[64:67]\n"
    "v_rcp_f32 v43, v43\n"
    "v_mfma_f32_16x16x32_bf16 v[68:71], a[140:143], v[28:31], v[68:71]\n"
    "s_waitcnt lgkmcnt(4)\n"
    "ds_read2st64_b64 v[28:31], v4 offset0:112 offset1:113\n"
    "v_mul_f32 v43, v43, v47\n"
    "v_mfma_f32_16x16x32_bf16 v[52:55], v[20:23], v[32:35], v[52:55]\n"
    "v_cvt_pk_bf16_f32 v41, v42, v43\n"
    "v_mfma_f32_16x16x32_bf16 v[56:59], v[32:35], v[32:35], v[56:59]\n"
    "s_waitcnt lgkmcnt(4)\n"
    "ds_read2st64_b64 v[32:35], v4 offset0:114 offset1:115\n"
    "v_mfma_f32_16x16x32_bf16 v[60:63], a[144:147], v[36:39], v[60:63]\n"
    "v_exp_f32 v48, v48\n"
    "v_mfma_f32_16x16x32_bf16 v[64:67], a[148:151], v[36:39], v[64:67]\n"
    "v_fma_f32 v48, v48, v73, v73\n"
    "v_mfma_f32_16x16x32_bf16 v[68:71], a[152:155], v[36:39], v[68:71]\n"
    "s_waitcnt lgkmcnt(2)\n"
    "v_cmp_u_f32 vcc, v52, v52\n"
    "s_nop 0\n"
    "s_cmp_lg_u64 vcc, 0\n"
    "s_cbranch_scc1 L43_%=\n"
    "L44_%=:\n"
    "v_mfma_f32_16x16x32_bf16 v[60:63], a[156:159], v[24:27], v[60:63]\n"
    "s_add_u32 s60, s43, 19267584\n"
    "ds_read2st64_b64 v[36:39], v4 offset0:116 offset1:117\n"
    "v_cndmask_b32_e64 v53, v56, v57, s[48:49]\n"
    "v_mfma_f32_16x16x32_bf16 v[64:67], a[160:163], v[24:27], v[64:67]\n"
    "v_cndmask_b32_e64 v53, v53, v58, s[50:51]\n"
    "v_cndmask_b32_e64 v53, v53, v59, s[52:53]\n"
    "v_mfma_f32_16x16x32_bf16 v[68:71], a[164:167], v[24:27], v[68:71]\n"
    "s_waitcnt lgkmcnt(2)\n"
    "ds_write_b64 v8, v[52:53] offset:0\n"
    "v_mfma_f32_16x16x32_bf16 v[60:63], a[168:171], v[28:31], v[60:63]\n"
    "ds_read2st64_b64 v[24:27], v4 offset0:118 offset1:119\n"
    "v_mfma_f32_16x16x32_bf16 v[64:67], a[172:175], v[28:31], v[64:67]\n"
    "buffer_store_dwordx4 v[100:103], v10, %[srdw], s60 offen sc1\n"
    "v_mfma_f32_16x16x32_bf16 v[68:71], a[176:179], v[28:31], v[68:71]\n"
    "s_waitcnt lgkmcnt(3)\n"
    "v_rcp_f32 v48, v48\n"
    "v_mfma_f32_16x16x32_bf16 v[60:63], a[180:183], v[32:35], v[60:63]\n"
    "v_mul_f32 v48, v48, v50\n"
    "v_mfma_f32_16x16x32_bf16 v[64:67], a[184:187], v[32:35], v[64:67]\n"
    "ds_read_b128 v[52:55], v9 offset:43008\n"
    "ds_read_b128 v[56:59], v9 offset:43024\n"
    "v_mfma_f32_16x16x32_bf16 v[68:71], a[188:191], v[32:35], v[68:71]\n"
    "s_waitcnt lgkmcnt(4)\n"
    "v_exp_f32 v49, v49\n"
    "v_mfma_f32_16x16x32_bf16 v[60:63], a[192:195], v[36:39], v[60:63]\n"
    "v_fma_f32 v49, v49, v73, v73\n"
    "v_mfma_f32_16x16x32_bf16 v[64:67], a[196:199], v[36:39], v[64:67]\n"
    "s_waitcnt lgkmcnt(2)\n"
    "s_barrier\n"
    "v_mfma_f32_16x16x32_bf16 v[68:71], a[200:203], v[36:39], v[68:71]\n"
    "ds_read2st64_b64 v[28:31], v4 offset0:0 offset1:1\n"
    "ds_read2st64_b64 v[32:35], v4 offset0:2 offset1:3\n"
    "v_mfma_f32_16x16x32_bf16 v[60:63], a[204:207], v[24:27], v[60:63]\n"
    "s_waitcnt lgkmcnt(3)\n"
    "ds_read2st64_b64 v[36:39], v4 offset0:4 offset1:5\n"
    "v_add_f32 v52, v52, v54\n"
    "v_mfma_f32_16x16x32_bf16 v[64:67], a[208:211], v[24:27], v[64:67]\n"
    "s_add_u32 m0, %[ldsx], 43008\n"
    "s_add_u32 s68, %[x], 17170432\n"
    "buffer_load_dwordx4 v3, %[srdw], s68 offen sc1 lds\n"
    "v_mfma_f32_16x16x32_bf16 v[68:71], a[212:215], v[24:27], v[68:71]\n"
    "ds_read2st64_b64 v[24:27], v4 offset0:6 offset1:7\n"
    "v_add_f32 v53, v53, v55\n"
    "v_mfma_f32_16x16x32_bf16 v[60:63], a[216:219], v[112:115], v[60:63]\n"
    "s_waitcnt lgkmcnt(4)\n"
    "v_add_f32 v56, v56, v58\n"
    "v_add_f32 v57, v57, v59\n"
    "v_mfma_f32_16x16x32_bf16 v[64:67], a[220:223], v[112:115], v[64:67]\n"
    "v_add_f32 v52, v52, v56\n"
    "v_add_f32 v53, v53, v57\n"
    "v_mfma_f32_16x16x32_bf16 v[68:71], a[224:227], v[112:115], v[68:71]\n"
    "v_mul_f32 v7, 0xbacccccd, v52\n"
    "v_mul_f32 v53, 0x3acccccd, v53\n"
    "v_mfma_f32_16x16x32_bf16 v[60:63], a[228:231], v[116:119], v[60:63]\n"
    "buffer_load_dwordx4 v3, %[srdw], s68 offen offset:1024 sc1 lds\n"
    "v_fma_f32 v53, -v7, v7, v53\n"
    "v_mfma_f32_16x16x32_bf16 v[64:67], a[232:235], v[116:119], v[64:67]\n"
    "v_max_f32 v53, 0, v53\n"
    "v_add_f32 v73, 0x3727c5ac, v53\n"
    "v_mfma_f32_16x16x32_bf16 v[68:71], a[236:239], v[116:119], v[68:71]\n"
    "s_add_u32 s61, s43, 10354688\n"
    "v_rsq_f32 v52, v73\n"
    "v_rcp_f32 v49, v49\n"
    "v_permlane32_swap_b32 v41, v43\n"
    "v_mul_f32 v49, v49, v51\n"
    "v_cvt_pk_bf16_f32 v42, v48, v49\n"
    "v_permlane16_swap_b32 v40, v41\n"
    "s_nop 0\n"
    "v_permlane16_swap_b32 v42, v43\n"
    "buffer_store_dwordx4 v[40:43], v10, %[srdw], s58 offen sc1\n"
    "s_waitcnt lgkmcnt(3)\n"
    "s_nop 0\n"
    "v_mfma_f32_16x16x32_bf16 v[40:43], a[0:3], v[28:31], 0\n"
    "s_waitcnt vmcnt(9)\n"
    "v_mul_f32 v72, 0xbfb8aa3b, v52\n"
    "v_fmac_f32_dpp v68, v6, v7 row_newbcast:8 row_mask:0xf bank_mask:0xf\n"
    "v_mfma_f32_16x16x32_bf16 v[44:47], a[4:7], v[28:31], 0\n"
    "v_cmp_ne_u32 vcc, %[tag], v109\n"
    "v_cmp_ne_u32 s[68:69], %[tag], v111\n"
    "s_nop 0\n"
    "s_or_b64 vcc, vcc, s[68:69]\n"
    "s_or_b32 s68, vcc_lo, vcc_hi\n"
    "s_and_b32 s68, s68, 0xff00ff\n"
    "s_cbranch_scc1 L41_%=\n"
    "L42_%=:\n"
    "v_mfma_f32_16x16x32_bf16 v[48:51], a[8:11], v[28:31], 0\n"
    "s_waitcnt lgkmcnt(2)\n"
    "ds_read2st64_b64 v[28:31], v4 offset0:8 offset1:9\n"
    "v_add_f32_dpp v108, v108, v108 row_ror:8 row_mask:0xf bank_mask:0xf\n"
    "v_mfma_f32_16x16x32_bf16 v[40:43], a[12:15], v[32:35], v[40:43]\n"
    "v_add_f32_dpp v110, v110, v110 row_ror:8 row_mask:0xf bank_mask:0xf\n"
    "v_add_f32_dpp v108, v108, v108 row_ror:4 row_mask:0xf bank_mask:0xf\n"
    "v_mfma_f32_16x16x32_bf16 v[44:47], a[16:19], v[32:35], v[44:47]\n"
    "s_add_u32 m0, %[ldsx], 45056\n"
    "s_add_u32 s68, %[x], 17172480\n"
    "buffer_load_dwordx4 v3, %[srdw], s68 offen sc1 lds\n"
    "v_mfma_f32_16x16x32_bf16 v[48:51], a[20:23], v[32:35], v[48:51]\n"
    "s_waitcnt lgkmcnt(2)\n"
    "ds_read2st64_b64 v[32:35], v4 offset0:10 offset1:11\n"
    "v_add_f32_dpp v110, v110, v110 row_ror:4 row_mask:0xf bank_mask:0xf\n"
    "v_mfma_f32_16x16x32_bf16 v[40:43], a[24:27], v[36:39], v[40:43]\n"
    "v_add_f32_dpp v108, v108, v108 row_ror:2 row_mask:0xf bank_mask:0xf\n"
    "v_add_f32_dpp v110, v110, v110 row_ror:2 row_mask:0xf bank_mask:0xf\n"
    "v_mfma_f32_16x16x32_bf16 v[44:47], a[28:31], v[36:39], v[44:47]\n"
    "v_add_f32_dpp v108, v108, v108 row_ror:1 row_mask:0xf bank_mask:0xf\n"
    "v_add_f32_dpp v110, v110, v110 row_ror:1 row_mask:0xf bank_mask:0xf\n"
    "v_mfma_f32_16x16x32_bf16 v[48:51], a[32:35], v[36:39], v[48:51]\n"
    "s_waitcnt lgkmcnt(2)\n"
    "ds_read2st64_b64 v[36:39], v4 offset0:12 offset1:13\n"
    "v_mul_f32 v108, 0x3acccccd, v108\n"
    "v_mfma_f32_16x16x32_bf16 v[40:43], a[36:39], v[24:27], v[40:43]\n"
    "s_add_u32 s62, %[hst], 20054016\n"
    "v_mul_f32 v110, 0x3acccccd, v110\n"
    "v_fma_f32 v110, -v108, v108, v110\n"
    "v_mfma_f32_16x16x32_bf16 v[44:47], a[40:43], v[24:27], v[44:47]\n"
    "v_max_f32 v110, 0, v110\n"
    "v_add_f32 v110, 0x3727c5ac, v110\n"
    "v_mfma_f32_16x16x32_bf16 v[48:51], a[44:47], v[24:27], v[48:51]\n"
    "s_waitcnt lgkmcnt(2)\n"
    "ds_read2st64_b64 v[24:27], v4 offset0:14 offset1:15\n"
    "v_lshlrev_b32 v109, 16, v92\n"
    "v_mfma_f32_16x16x32_bf16 v[40:43], a[48:51], v[28:31], v[40:43]\n"
    "v_rsq_f32 v110, v110\n"
    "v_mfma_f32_16x16x32_bf16 v[44:47], a[52:55], v[28:31], v[44:47]\n"
    "buffer_load_dwordx4 v3, %[srdw], s68 offen offset:1024 sc1 lds\n"
    "v_mul_f32 v108, v108, v110\n"
    "v_mfma_f32_16x16x32_bf16 v[48:51], a[56:59], v[28:31], v[48:51]\n"
    "s_waitcnt lgkmcnt(2)\n"
    "ds_read2st64_b64 v[28:31], v5 offset0:42 offset1:43\n"
    "v_fma_f32 v111, v80, v110, -v108\n"
    "v_mfma_f32_16x16x32_bf16 v[40:43], a[60:63], v[32:35], v[40:43]\n"
    "v_fmac_f32 v80, v111, v109\n"
    "v_and_b32 v109, 0xffff0000, v92\n"
    "v_mfma_f32_16x16x32_bf16 v[44:47], a[64:67], v[32:35], v[44:47]\n"
    "v_fma_f32 v111, v81, v110, -v108\n"
    "v_fmac_f32 v81, v111, v109\n"
    "v_mfma_f32_16x16x32_bf16 v[48:51], a[68:71], v[32:35], v[48:51]\n"
    "s_waitcnt lgkmcnt(2)\n"
    "ds_read2st64_b64 v[32:35], v4 offset0:16 offset1:17\n"
    "v_lshlrev_b32 v109, 16, v93\n"
    "v_mfma_f32_16x16x32_bf16 v[40:43], a[72:75], v[36:39], v[40:43]\n"
    "v_fma_f32 v111, v82, v110, -v108\n"
    "v_fmac_f32 v82, v111, v109\n"
    "v_mfma_f32_16x16x32_bf16 v[44:47], a[76:79], v[36:39], v[44:47]\n"
    "v_and_b32 v109, 0xffff0000, v93\n"
    "v_fma_f32 v111, v83, v110, -v108\n"
    "v_mfma_f32_16x16x32_bf16 v[48:51], a[80:83], v[36:39], v[48:51]\n"
    "s_waitcnt lgkmcnt(2) vmcnt(10)\n"
    "ds_read2st64_b64 v[36:39], v5 offset0:44 offset1:45\n"
    "v_fmac_f32 v83, v111, v109\n"
    "v_mfma_f32_16x16x32_bf16 v[40:43], a[84:87], v[24:27], v[40:43]\n"
    "v_fma_f32 v111, v84, v110, -v108\n"
    "v_fmac_f32 v84, v111, v91\n"
    "v_mfma_f32_16x16x32_bf16 v[44:47], a[88:91], v[24:27], v[44:47]\n"
    "buffer_load_dwordx4 v3, %[srdw], s68 offen offset:2048 sc1 lds\n"
    "v_cvt_pk_bf16_f32 v108, v80, v81\n"
    "v_mfma_f32_16x16x32_bf16 v[48:51], a[92:95], v[24:27], v[48:51]\n"
    "s_waitcnt lgkmcnt(2)\n"
    "ds_read2st64_b64 v[24:27], v4 offset0:18 offset1:19\n"
    "v_cvt_pk_bf16_f32 v109, v82, v83\n"
    "v_mfma_f32_16x16x32_bf16 v[52:55], v[20:23], v[28:31], 0\n"
    "v_mov_b32_dpp v111, v84 quad_perm:[1,0,3,2] row_mask:0xf bank_mask:0xf\n"
    "v_cvt_pk_bf16_f32 v110, v84, v111\n"
    "v_mfma_f32_16x16x32_bf16 v[56:59], v[28:31], v[28:31], 0\n"
    "s_waitcnt lgkmcnt(2) vmcnt(10)\n"
    "ds_read2st64_b64 v[28:31], v5 offset0:46 offset1:47\n"
    "v_min_u32 v108, -2, v108\n"
    "v_mfma_f32_16x16x32_bf16 v[40:43], a[96:99], v[32:35], v[40:43]\n"
    "v_min_u32 v109, -2, v109\n"
    "v_min_u32 v110, -2, v110\n"
    "v_mfma_f32_16x16x32_bf16 v[44:47], a[100:103], v[32:35], v[44:47]\n"
    "v_fmac_f32_dpp v69, v6, v7 row_newbcast:9 row_mask:0xf bank_mask:0xf\n"
    "v_mov_b32_dpp v111, v110 quad_perm:[2,2,2,2] row_mask:0xf bank_mask:0xf\n"
    "v_mfma_f32_16x16x32_bf16 v[48:51], a[104:107], v[32:35], v[48:51]\n"
    "s_waitcnt lgkmcnt(2)\n"
    "ds_read2st64_b64 v[32:35], v4 offset0:20 offset1:21\n"
    "v_fmac_f32_dpp v70, v6, v7 row_newbcast:10 row_mask:0xf bank_mask:0xf\n"
    "v_mfma_f32_16x16x32_bf16 v[52:55], v[20:23], v[36:39], v[52:55]\n"
    "v_permlane16_swap_b32 v108, v110\n"
    "v_mfma_f32_16x16x32_bf16 v[56:59], v[36:39], v[36:39], v[56:59]\n"
    "s_waitcnt lgkmcnt(2) vmcnt(9)\n"
    "ds_read2st64_b64 v[36:39], v5 offset0:48 offset1:49\n"
    "v_fmac_f32_dpp v71, v6, v7 row_newbcast:11 row_mask:0xf bank_mask:0xf\n"
    "v_mfma_f32_16x16x32_bf16 v[40:43], a[108:111], v[24:27], v[40:43]\n"
    "buffer_store_dwordx4 v[100:103], v10, %[srdw], s62 offen sc1\n"
    "v_mfma_f32_16x16x32_bf16 v[44:47], a[112:115], v[24:27], v[44:47]\n"
    "v_permlane16_swap_b32 v109, v111\n"
    "v_mfma_f32_16x16x32_bf16 v[48:51], a[116:119], v[24:27], v[48:51]\n"
    "s_waitcnt lgkmcnt(2)\n"
    "ds_read2st64_b64 v[24:27], v4 offset0:22 offset1:23\n"
    "v_fmac_f32_dpp v60, v6, v7 row_newbcast:0 row_mask:0xf bank_mask:0xf\n"
    "v_mfma_f32_16x16x32_bf16 v[52:55], v[20:23], v[28:31], v[52:55]\n"
    "buffer_store_dwordx4 v[108:111], v18, %[srdw], s57 offen sc1\n"
    "v_mfma_f32_16x16x32_bf16 v[56:59], v[28:31], v[28:31], v[56:59]\n"
    "s_waitcnt lgkmcnt(2) vmcnt(10)\n"
    "ds_read2st64_b64 v[28:31], v5 offset0:50 offset1:51\n"
    "v_fmac_f32_dpp v64, v6, v7 row_newbcast:4 row_mask:0xf bank_mask:0xf\n"
    "v_mfma_f32_16x16x32_bf16 v[40:43], a[120:123], v[32:35], v[40:43]\n"
    "v_permlane16_swap_b32 v68, v70\n"
    "v_mfma_f32_16x16x32_bf16 v[44:47], a[124:127], v[32:35], v[44:47]\n"
    "v_permlane16_swap_b32 v69, v71\n"
    "v_mfma_f32_16x16x32_bf16 v[48:51], a[128:131], v[32:35], v[48:51]\n"
    "s_waitcnt lgkmcnt(2)\n"
    "ds_read2st64_b64 v[32:35], v4 offset0:24 offset1:25\n"
    "v_mul_f32 v64, v60, v64\n"
    "v_mfma_f32_16x16x32_bf16 v[52:55], v[20:23], v[36:39], v[52:55]\n"
    "s_add_u32 s63, s43, 20840448\n"
    "ds_read2st64_b64 v[112:115], v4 offset0:36 offset1:37\n"
    "ds_read2st64_b64 v[116:119], v4 offset0:38 offset1:39\n"
    "v_mfma_f32_16x16x32_bf16 v[56:59], v[36:39], v[36:39], v[56:59]\n"
    "s_waitcnt lgkmcnt(4)\n"
    "ds_read2st64_b64 v[36:39], v4 offset0:26 offset1:27\n"
    "v_mul_f32 v60, v72, v60\n"
    "v_mfma_f32_16x16x32_bf16 v[40:43], a[132:135], v[24:27], v[40:43]\n"
    "v_exp_f32 v60, v60\n"
    "v_mfma_f32_16x16x32_bf16 v[44:47], a[136:139], v[24:27], v[44:47]\n"
    "v_fma_f32 v60, v60, v73, v73\n"
    "v_fmac_f32_dpp v61, v6, v7 row_newbcast:1 row_mask:0xf bank_mask:0xf\n"
    "v_mfma_f32_16x16x32_bf16 v[48:51], a[140:143], v[24:27], v[48:51]\n"
    "s_waitcnt lgkmcnt(4)\n"
    "ds_read2st64_b64 v[24:27], v4 offset0:28 offset1:29\n"
    "v_fmac_f32_dpp v65, v6, v7 row_newbcast:5 row_mask:0xf bank_mask:0xf\n"
    "v_mfma_f32_16x16x32_bf16 v[52:55], v[20:23], v[28:31], v[52:55]\n"
    "buffer_store_dwordx4 v[100:103], v10, %[srdw], s63 offen sc1\n"
    "v_mfma_f32_16x16x32_bf16 v[56:59], v[28:31], v[28:31], v[56:59]\n"
    "s_waitcnt lgkmcnt(4)\n"
    "ds_read2st64_b64 v[28:31], v4 offset0:30 offset1:31\n"
    "v_mul_f32 v65, v61, v65\n"
    "v_mfma_f32_16x16x32_bf16 v[40:43], a[144:147], v[32:35], v[40:43]\n"
    "v_rcp_f32 v60, v60\n"
    "v_mfma_f32_16x16x32_bf16 v[44:47], a[148:151], v[32:35], v[44:47]\n"
    "v_cmp_u_f32 vcc, v52, v52\n"
    "s_nop 0\n"
    "s_cmp_lg_u64 vcc, 0\n"
    "s_cbranch_scc1 L45_%=\n"
    "L46_%=:\n"
    "v_mfma_f32_16x16x32_bf16 v[48:51], a[152:155], v[32:35], v[48:51]\n"
    "s_waitcnt lgkmcnt(2)\n"
    "ds_read2st64_b64 v[32:35], v4 offset0:32 offset1:33\n"
    "v_cndmask_b32_e64 v53, v56, v57, s[48:49]\n"
    "v_mfma_f32_16x16x32_bf16 v[40:43], a[156:159], v[36:39], v[40:43]\n"
    "v_cndmask_b32_e64 v53, v53, v58, s[50:51]\n"
    "v_cndmask_b32_e64 v53, v53, v59, s[52:53]\n"
    "v_mfma_f32_16x16x32_bf16 v[44:47], a[160:163], v[36:39], v[44:47]\n"
    "ds_write_b64 v8, v[52:53] offset:21504\n"
    "v_mfma_f32_16x16x32_bf16 v[48:51], a[164:167], v[36:39], v[48:51]\n"
    "s_waitcnt lgkmcnt(3)\n"
    "ds_read2st64_b64 v[36:39], v4 offset0:34 offset1:35\n"
    "v_mul_f32 v60, v60, v64\n"
    "v_mfma_f32_16x16x32_bf16 v[40:43], a[168:171], v[24:27], v[40:43]\n"
    "v_mul_f32 v61, v72, v61\n"
    "v_fmac_f32_dpp v62, v6, v7 row_newbcast:2 row_mask:0xf bank_mask:0xf\n"
    "v_mfma_f32_16x16x32_bf16 v[44:47], a[172:175], v[24:27], v[44:47]\n"
    "v_exp_f32 v61, v61\n"
    "v_mfma_f32_16x16x32_bf16 v[48:51], a[176:179], v[24:27], v[48:51]\n"
    "s_waitcnt lgkmcnt(3)\n"
    "v_fma_f32 v61, v61, v73, v73\n"
    "v_fmac_f32_dpp v66, v6, v7 row_newbcast:6 row_mask:0xf bank_mask:0xf\n"
    "v_mfma_f32_16x16x32_bf16 v[40:43], a[180:183], v[28:31], v[40:43]\n"
    "v_rcp_f32 v61, v61\n"
    "v_mfma_f32_16x16x32_bf16 v[44:47], a[184:187], v[28:31], v[44:47]\n"
    "ds_read_b128 v[52:55], v9 offset:0\n"
    "ds_read_b128 v[56:59], v9 offset:16\n"
    "v_mfma_f32_16x16x32_bf16 v[48:51], a[188:191], v[28:31], v[48:51]\n"
    "s_waitcnt lgkmcnt(4)\n"
    "v_mul_f32 v61, v61, v65\n"
    "v_cvt_pk_bf16_f32 v60, v60, v61\n"
    "v_mfma_f32_16x16x32_bf16 v[40:43], a[192:195], v[32:35], v[40:43]\n"
    "v_mul_f32 v66, v62, v66\n"
    "v_mul_f32 v62, v72, v62\n"
    "v_mfma_f32_16x16x32_bf16 v[44:47], a[196:199], v[32:35], v[44:47]\n"
    "s_waitcnt lgkmcnt(2)\n"
    "s_barrier\n"
    "v_mfma_f32_16x16x32_bf16 v[48:51], a[200:203], v[32:35], v[48:51]\n"
    "ds_read2st64_b64 v[24:27], v4 offset0:42 offset1:43\n"
    "ds_read2st64_b64 v[28:31], v4 offset0:44 offset1:45\n"
    "v_mfma_f32_16x16x32_bf16 v[40:43], a[204:207], v[36:39], v[40:43]\n"
    "s_waitcnt lgkmcnt(3)\n"
    "ds_read2st64_b64 v[32:35], v4 offset0:46 offset1:47\n"
    "v_add_f32 v52, v52, v54\n"
    "v_mfma_f32_16x16x32_bf16 v[44:47], a[208:211], v[36:39], v[44:47]\n"
    "s_add_u32 m0, %[ldsx], 0\n"
    "s_add_u32 s68, s42, 17498112\n"
    "buffer_load_dwordx4 v3, %[srdw], s68 offen sc1 lds\n"
    "v_mfma_f32_16x16x32_bf16 v[48:51], a[212:215], v[36:39], v[48:51]\n"
    "ds_read2st64_b64 v[36:39], v4 offset0:48 offset1:49\n"
    "v_add_f32 v53, v53, v55\n"
    "v_mfma_f32_16x16x32_bf16 v[40:43], a[216:219], v[112:115], v[40:43]\n"
    "s_waitcnt lgkmcnt(4)\n"
    "v_add_f32 v56, v56, v58\n"
    "v_add_f32 v57, v57, v59\n"
    "v_mfma_f32_16x16x32_bf16 v[44:47], a[220:223], v[112:115], v[44:47]\n"
    "v_add_f32 v52, v52, v56\n"
    "v_add_f32 v53, v53, v57\n"
    "v_mfma_f32_16x16x32_bf16 v[48:51], a[224:227], v[112:115], v[48:51]\n"
    "v_mul_f32 v53, 0x3acccccd, v53\n"
    "v_fmac_f32_dpp v63, v6, v7 row_newbcast:3 row_mask:0xf bank_mask:0xf\n"
    "v_mfma_f32_16x16x32_bf16 v[40:43], a[228:231], v[116:119], v[40:43]\n"
    "buffer_load_dwordx4 v3, %[srdw], s68 offen offset:1024 sc1 lds\n"
    "v_fmac_f32_dpp v67, v6, v7 row_newbcast:7 row_mask:0xf bank_mask:0xf\n"
    "v_mfma_f32_16x16x32_bf16 v[44:47], a[232:235], v[116:119], v[44:47]\n"
    "v_mul_f32 v7, 0xbacccccd, v52\n"
    "v_fma_f32 v53, -v7, v7, v53\n"
    "v_mfma_f32_16x16x32_bf16 v[48:51], a[236:239], v[116:119], v[48:51]\n"
    "s_add_u32 s64, %[hst], 11141120\n"
    "v_max_f32 v53, 0, v53\n"
    "v_mul_f32 v67, v63, v67\n"
    "v_exp_f32 v62, v62\n"
    "v_mul_f32 v63, v72, v63\n"
    "v_fma_f32 v62, v62, v73, v73\n"
    "v_rcp_f32 v62, v62\n"
    "v_exp_f32 v63, v63\n"
    "v_mul_f32 v62, v62, v66\n"
    "v_fma_f32 v63, v63, v73, v73\n"
    "v_rcp_f32 v63, v63\n"
    "v_mul_f32 v70, v68, v70\n"
    "v_mul_f32 v63, v63, v67\n"
    "v_cvt_pk_bf16_f32 v61, v62, v63\n"
    "v_mul_f32 v68, v72, v68\n"
    "v_exp_f32 v68, v68\n"
    "v_mul_f32 v71, v69, v71\n"
    "v_fma_f32 v68, v68, v73, v73\n"
    "v_rcp_f32 v68, v68\n"
    "v_mul_f32 v69, v72, v69\n"
    "v_mul_f32 v68, v68, v70\n"
    "v_exp_f32 v69, v69\n"
    "v_permlane32_swap_b32 v61, v63\n"
    "v_fma_f32 v69, v69, v73, v73\n"
    "v_rcp_f32 v69, v69\n"
    "v_permlane16_swap_b32 v60, v61\n"
    "v_mul_f32 v69, v69, v71\n"
    "v_cvt_pk_bf16_f32 v62, v68, v69\n"
    "s_nop 1\n"
    "v_permlane16_swap_b32 v62, v63\n"
    "buffer_store_dwordx4 v[60:63], v10, %[srdw], s61 offen sc1\n"
    "s_waitcnt lgkmcnt(3)\n"
    "s_nop 0\n"
    "v_mfma_f32_16x16x32_bf16 v[60:63], a[0:3], v[24:27], 0\n"
    "buffer_load_dwordx4 v3, %[srdw], s68 offen offset:2048 sc1 lds\n"
    "v_add_f32 v73, 0x3727c5ac, v53\n"
    "v_mfma_f32_16x16x32_bf16 v[64:67], a[4:7], v[24:27], 0\n"
    "v_rsq_f32 v52, v73\n"
    "v_mfma_f32_16x16x32_bf16 v[68:71], a[8:11], v[24:27], 0\n"
    "s_waitcnt lgkmcnt(2)\n"
    "ds_read2st64_b64 v[24:27], v4 offset0:50 offset1:51\n"
    "v_mul_f32 v72, 0xbfb8aa3b, v52\n"
    "v_mfma_f32_16x16x32_bf16 v[60:63], a[12:15], v[28:31], v[60:63]\n"
    "v_fmac_f32_dpp v48, v6, v7 row_newbcast:8 row_mask:0xf bank_mask:0xf\n"
    "v_fmac_f32_dpp v49, v6, v7 row_newbcast:9 row_mask:0xf bank_mask:0xf\n"
    "v_mfma_f32_16x16x32_bf16 v[64:67], a[16:19], v[28:31], v[64:67]\n"
    "v_fmac_f32_dpp v50, v6, v7 row_newbcast:10 row_mask:0xf bank_mask:0xf\n"
    "v_fmac_f32_dpp v51, v6, v7 row_newbcast:11 row_mask:0xf bank_mask:0xf\n"
    "v_mfma_f32_16x16x32_bf16 v[68:71], a[20:23], v[28:31], v[68:71]\n"
    "s_waitcnt lgkmcnt(2)\n"
    "ds_read2st64_b64 v[28:31], v4 offset0:52 offset1:53\n"
    "v_fmac_f32_dpp v40, v6, v7 row_newbcast:0 row_mask:0xf bank_mask:0xf\n"
    "v_mfma_f32_16x16x32_bf16 v[60:63], a[24:27], v[32:35], v[60:63]\n"
    "v_permlane16_swap_b32 v48, v50\n"
    "v_mfma_f32_16x16x32_bf16 v[64:67], a[28:31], v[32:35], v[64:67]\n"
    "buffer_load_dwordx4 v3, %[srdw], s68 offen offset:3072 sc1 lds\n"
    "v_fmac_f32_dpp v44, v6, v7 row_newbcast:4 row_mask:0xf bank_mask:0xf\n"
    "v_mfma_f32_16x16x32_bf16 v[68:71], a[32:35], v[32:35], v[68:71]\n"
    "s_waitcnt lgkmcnt(2)\n"
    "ds_read2st64_b64 v[32:35], v4 offset0:54 offset1:55\n"
    "v_mul_f32 v44, v40, v44\n"
    "v_mfma_f32_16x16x32_bf16 v[60:63], a[36:39], v[36:39], v[60:63]\n"
    "v_permlane16_swap_b32 v49, v51\n"
    "v_mfma_f32_16x16x32_bf16 v[64:67], a[40:43], v[36:39], v[64:67]\n"
    "v_mul_f32 v40, v72, v40\n"
    "v_fmac_f32_dpp v41, v6, v7 row_newbcast:1 row_mask:0xf bank_mask:0xf\n"
    "v_mfma_f32_16x16x32_bf16 v[68:71], a[44:47], v[36:39], v[68:71]\n"
    "s_waitcnt lgkmcnt(2)\n"
    "ds_read2st64_b64 v[36:39], v4 offset0:56 offset1:57\n"
    "v_fmac_f32_dpp v45, v6, v7 row_newbcast:5 row_mask:0xf bank_mask:0xf\n"
    "v_mfma_f32_16x16x32_bf16 v[60:63], a[48:51], v[24:27], v[60:63]\n"
    "v_exp_f32 v40, v40\n"
    "v_mfma_f32_16x16x32_bf16 v[64:67], a[52:55], v[24:27], v[64:67]\n"
    "v_fma_f32 v40, v40, v73, v73\n"
    "v_mul_f32 v45, v41, v45\n"
    "v_mfma_f32_16x16x32_bf16 v[68:71], a[56:59], v[24:27], v[68:71]\n"
    "s_waitcnt lgkmcnt(2) vmcnt(13)\n"
    "ds_read2st64_b64 v[24:27], v5 offset0:84 offset1:85\n"
    "v_mul_f32 v41, v72, v41\n"
    "v_mfma_f32_16x16x32_bf16 v[60:63], a[60:63], v[28:31], v[60:63]\n"
    "v_rcp_f32 v40, v40\n"
    "v_mfma_f32_16x16x32_bf16 v[64:67], a[64:67], v[28:31], v[64:67]\n"
    "s_add_u32 m0, %[ldsx], 4096\n"
    "s_add_u32 s68, s42, 17502208\n"
    "buffer_load_dwordx4 v3, %[srdw], s68 offen sc1 lds\n"
    "v_mfma_f32_16x16x32_bf16 v[68:71], a[68:71], v[28:31], v[68:71]\n"
    "s_waitcnt lgkmcnt(2)\n"
    "ds_read2st64_b64 v[28:31], v4 offset0:58 offset1:59\n"
    "v_mul_f32 v40, v40, v44\n"
    "v_mfma_f32_16x16x32_bf16 v[60:63], a[72:75], v[32:35], v[60:63]\n"
    "v_exp_f32 v41, v41\n"
    "v_mfma_f32_16x16x32_bf16 v[64:67], a[76:79], v[32:35], v[64:67]\n"
    "v_fma_f32 v41, v41, v73, v73\n"
    "v_fmac_f32_dpp v42, v6, v7 row_newbcast:2 row_mask:0xf bank_mask:0xf\n"
    "v_mfma_f32_16x16x32_bf16 v[68:71], a[80:83], v[32:35], v[68:71]\n"
    "s_waitcnt lgkmcnt(2) vmcnt(13)\n"
    "ds_read2st64_b64 v[32:35], v5 offset0:86 offset1:87\n"
    "v_fmac_f32_dpp v46, v6, v7 row_newbcast:6 row_mask:0xf bank_mask:0xf\n"
    "v_mfma_f32_16x16x32_bf16 v[60:63], a[84:87], v[36:39], v[60:63]\n"
    "v_rcp_f32 v41, v41\n"
    "v_mfma_f32_16x16x32_bf16 v[64:67], a[88:91], v[36:39], v[64:67]\n"
    "v_mul_f32 v41, v41, v45\n"
    "v_cvt_pk_bf16_f32 v40, v40, v41\n"
    "v_mfma_f32_16x16x32_bf16 v[68:71], a[92:95], v[36:39], v[68:71]\n"
    "s_waitcnt lgkmcnt(2)\n"
    "ds_read2st64_b64 v[36:39], v4 offset0:60 offset1:61\n"
    "v_mul_f32 v46, v42, v46\n"
    "v_mfma_f32_16x16x32_bf16 v[52:55], v[20:23], v[24:27], 0\n"
    "v_mul_f32 v42, v72, v42\n"
    "v_fmac_f32_dpp v43, v6, v7 row_newbcast:3 row_mask:0xf bank_mask:0xf\n"
    "v_mfma_f32_16x16x32_bf16 v[56:59], v[24:27], v[24:27], 0\n"
    "s_waitcnt lgkmcnt(2) vmcnt(11)\n"
    "ds_read2st64_b64 v[24:27], v5 offset0:88 offset1:89\n"
    "v_fmac_f32_dpp v47, v6, v7 row_newbcast:7 row_mask:0xf bank_mask:0xf\n"
    "v_mfma_f32_16x16x32_bf16 v[60:63], a[96:99], v[28:31], v[60:63]\n"
    "v_exp_f32 v42, v42\n"
    "v_mfma_f32_16x16x32_bf16 v[64:67], a[100:103], v[28:31], v[64:67]\n"
    "v_fma_f32 v42, v42, v73, v73\n"
    "v_mul_f32 v47, v43, v47\n"
    "v_mfma_f32_16x16x32_bf16 v[68:71], a[104:107], v[28:31], v[68:71]\n"
    "s_waitcnt lgkmcnt(2)\n"
    "ds_read2st64_b64 v[28:31], v4 offset0:62 offset1:63\n"
    "v_mul_f32 v43, v72, v43\n"
    "v_mfma_f32_16x16x32_bf16 v[52:55], v[20:23], v[32:35], v[52:55]\n"
    "v_rcp_f32 v42, v42\n"
    "v_mfma_f32_16x16x32_bf16 v[56:59], v[32:35], v[32:35], v[56:59]\n"
    "s_waitcnt lgkmcnt(2) vmcnt(10)\n"
    "ds_read2st64_b64 v[32:35], v5 offset0:90 offset1:91\n"
    "v_mul_f32 v42, v42, v46\n"
    "v_mfma_f32_16x16x32_bf16 v[60:63], a[108:111], v[36:39], v[60:63]\n"
    "v_exp_f32 v43, v43\n"
    "v_mfma_f32_16x16x32_bf16 v[64:67], a[112:115], v[36:39], v[64:67]\n"
    "v_fma_f32 v43, v43, v73, v73\n"
    "v_mul_f32 v50, v48, v50\n"
    "v_mfma_f32_16x16x32_bf16 v[68:71], a[116:119], v[36:39], v[68:71]\n"
    "s_waitcnt lgkmcnt(2)\n"
    "ds_read2st64_b64 v[36:39], v4 offset0:64 offset1:65\n"
    "v_mul_f32 v48, v72, v48\n"
    "v_mfma_f32_16x16x32_bf16 v[52:55], v[20:23], v[24:27], v[52:55]\n"
    "v_rcp_f32 v43, v43\n"
    "v_mfma_f32_16x16x32_bf16 v[56:59], v[24:27], v[24:27], v[56:59]\n"
    "s_waitcnt lgkmcnt(2) vmcnt(9)\n"
    "ds_read2st64_b64 v[24:27], v5 offset0:92 offset1:93\n"
    "v_mul_f32 v43, v43, v47\n"
    "v_mfma_f32_16x16x32_bf16 v[60:63], a[120:123], v[28:31], v[60:63]\n"
    "v_cvt_pk_bf16_f32 v41, v42, v43\n"
    "v_mul_f32 v51, v49, v51\n"
    "v_mfma_f32_16x16x32_bf16 v[64:67], a[124:127], v[28:31], v[64:67]\n"
    "v_exp_f32 v48, v48\n"
    "v_mfma_f32_16x16x32_bf16 v[68:71], a[128:131], v[28:31], v[68:71]\n"
    "s_waitcnt lgkmcnt(2)\n"
    "ds_read2st64_b64 v[28:31], v4 offset0:66 offset1:67\n"
    "v_fma_f32 v48, v48, v73, v73\n"
    "v_mfma_f32_16x16x32_bf16 v[52:55], v[20:23], v[32:35], v[52:55]\n"
    "ds_read2st64_b64 v[112:115], v4 offset0:78 offset1:79\n"
    "ds_read2st64_b64 v[116:119], v4 offset0:80 offset1:81\n"
    "v_mfma_f32_16x16x32_bf16 v[56:59], v[32:35], v[32:35], v[56:59]\n"
    "s_waitcnt lgkmcnt(4)\n"
    "ds_read2st64_b64 v[32:35], v4 offset0:68 offset1:69\n"
    "v_mul_f32 v49, v72, v49\n"
    "v_mfma_f32_16x16x32_bf16 v[60:63], a[132:135], v[36:39], v[60:63]\n"
    "v_rcp_f32 v48, v48\n"
    "v_mfma_f32_16x16x32_bf16 v[64:67], a[136:139], v[36:39], v[64:67]\n"
    "v_mul_f32 v48, v48, v50\n"
    "v_cvt_pk_bf16_f32 v16, v80, v81\n"
    "v_mfma_f32_16x16x32_bf16 v[68:71], a[140:143], v[36:39], v[68:71]\n"
    "s_waitcnt lgkmcnt(4)\n"
    "ds_read2st64_b64 v[36:39], v4 offset0:70 offset1:71\n"
    "v_cvt_pk_bf16_f32 v17, v82, v83\n"
    "v_mfma_f32_16x16x32_bf16 v[52:55], v[20:23], v[24:27], v[52:55]\n"
    "v_exp_f32 v49, v49\n"
    "v_mfma_f32_16x16x32_bf16 v[56:59], v[24:27], v[24:27], v[56:59]\n"
    "s_waitcnt lgkmcnt(4)\n"
    "ds_read2st64_b64 v[24:27], v4 offset0:72 offset1:73\n"
    "v_fma_f32 v49, v49, v73, v73\n"
    "v_mfma_f32_16x16x32_bf16 v[60:63], a[144:147], v[28:31], v[60:63]\n"
    "v_rcp_f32 v49, v49\n"
    "v_mfma_f32_16x16x32_bf16 v[64:67], a[148:151], v[28:31], v[64:67]\n"
    "v_cmp_u_f32 vcc, v52, v52\n"
    "s_nop 0\n"
    "s_cmp_lg_u64 vcc, 0\n"
    "s_cbranch_scc1 L47_%=\n"
    "L48_%=:\n"
    "v_mfma_f32_16x16x32_bf16 v[68:71], a[152:155], v[28:31], v[68:71]\n"
    "s_waitcnt lgkmcnt(2)\n"
    "ds_read2st64_b64 v[28:31], v4 offset0:74 offset1:75\n"
    "v_cndmask_b32_e64 v53, v56, v57, s[48:49]\n"
    "v_mfma_f32_16x16x32_bf16 v[60:63], a[156:159], v[32:35], v[60:63]\n"
    "v_cndmask_b32_e64 v53, v53, v58, s[50:51]\n"
    "v_cndmask_b32_e64 v53, v53, v59, s[52:53]\n"
    "v_mfma_f32_16x16x32_bf16 v[64:67], a[160:163], v[32:35], v[64:67]\n"
    "ds_write_b64 v8, v[52:53] offset:43008\n"
    "v_mfma_f32_16x16x32_bf16 v[68:71], a[164:167], v[32:35], v[68:71]\n"
    "s_waitcnt lgkmcnt(3)\n"
    "ds_read2st64_b64 v[32:35], v4 offset0:76 offset1:77\n"
    "v_mul_f32 v49, v49, v51\n"
    "v_mfma_f32_16x16x32_bf16 v[60:63], a[168:171], v[36:39], v[60:63]\n"
    "v_cvt_pk_bf16_f32 v42, v48, v49\n"
    "v_mov_b32 v15, v84\n"
    "v_mfma_f32_16x16x32_bf16 v[64:67], a[172:175], v[36:39], v[64:67]\n"
    "v_permlane32_swap_b32 v41, v43\n"
    "v_mfma_f32_16x16x32_bf16 v[68:71], a[176:179], v[36:39], v[68:71]\n"
    "s_waitcnt lgkmcnt(3)\n"
    "v_permlane16_swap_b32 v42, v43\n"
    "v_mfma_f32_16x16x32_bf16 v[60:63], a[180:183], v[24:27], v[60:63]\n"
    "v_permlane16_swap_b32 v40, v41\n"
    "v_mfma_f32_16x16x32_bf16 v[64:67], a[184:187], v[24:27], v[64:67]\n"
    "s_add_u32 s65, %[f4p], 1441792\n"
    "buffer_store_dwordx4 v[40:43], v10, %[srdw], s64 offen sc1\n"
    "v_mfma_f32_16x16x32_bf16 v[68:71], a[188:191], v[24:27], v[68:71]\n"
    "s_waitcnt lgkmcnt(2)\n"
    "ds_read_b128 v[52:55], v9 offset:21504\n"
    "ds_read_b128 v[56:59], v9 offset:21520\n"
    "v_mfma_f32_16x16x32_bf16 v[60:63], a[192:195], v[28:31], v[60:63]\n"
    "s_waitcnt lgkmcnt(2)\n"
    "s_barrier\n"
    "v_mfma_f32_16x16x32_bf16 v[64:67], a[196:199], v[28:31], v[64:67]\n"
    "kx_a8:\n"
    "s_cbranch_execz kx_ret\n"
    "ds_read2st64_b64 v[36:39], v4 offset0:84 offset1:85\n"
    "ds_read2st64_b64 v[24:27], v4 offset0:86 offset1:87\n"
    "v_mfma_f32_16x16x32_bf16 v[68:71], a[200:203], v[28:31], v[68:71]\n"
    "ds_read2st64_b64 v[28:31], v4 offset0:88 offset1:89\n"
    "v_mfma_f32_16x16x32_bf16 v[60:63], a[204:207], v[32:35], v[60:63]\n"
    "s_mov_b64 exec, s[94:95]\n"
    "buffer_store_dword v2, off, %[srdw], s65 sc1\n"
    "s_mov_b64 exec, -1\n"
    "v_mfma_f32_16x16x32_bf16 v[64:67], a[208:211], v[32:35], v[64:67]\n"
    "s_add_u32 m0, %[ldsx], 21504\n"
    "s_add_u32 s68, %[x], 17825792\n"
    "buffer_load_dwordx4 v3, %[srdw], s68 offen sc1 lds\n"
    "v_mfma_f32_16x16x32_bf16 v[68:71], a[212:215], v[32:35], v[68:71]\n"
    "s_waitcnt lgkmcnt(4)\n"
    "ds_read2st64_b64 v[32:35], v4 offset0:90 offset1:91\n"
    "v_add_f32 v52, v52, v54\n"
    "v_mfma_f32_16x16x32_bf16 v[60:63], a[216:219], v[112:115], v[60:63]\n"
    "s_waitcnt lgkmcnt(4)\n"
    "v_add_f32 v53, v53, v55\n"
    "v_add_f32 v56, v56, v58\n"
    "v_mfma_f32_16x16x32_bf16 v[64:67], a[220:223], v[112:115], v[64:67]\n"
    "v_add_f32 v57, v57, v59\n"
    "v_add_f32 v52, v52, v56\n"
    "v_mfma_f32_16x16x32_bf16 v[68:71], a[224:227], v[112:115], v[68:71]\n"
    "v_add_f32 v53, v53, v57\n"
    "v_mul_f32 v7, 0xbacccccd, v52\n"
    "v_mfma_f32_16x16x32_bf16 v[60:63], a[228:231], v[116:119], v[60:63]\n"
    "buffer_load_dwordx4 v3, %[srdw], s68 offen offset:1024 sc1 lds\n"
    "v_mul_f32 v53, 0x3acccccd, v53\n"
    "v_mfma_f32_16x16x32_bf16 v[64:67], a[232:235], v[116:119], v[64:67]\n"
    "v_fma_f32 v53, -v7, v7, v53\n"
    "v_max_f32 v53, 0, v53\n"
    "v_mfma_f32_16x16x32_bf16 v[68:71], a[236:239], v[116:119], v[68:71]\n"
    "s_waitcnt lgkmcnt(3)\n"
    "v_add_f32 v73, 0x3727c5ac, v53\n"
    "v_fmac_f32_dpp v60, v6, v7 row_newbcast:0 row_mask:0xf bank_mask:0xf\n"
    "v_fmac_f32_dpp v61, v6, v7 row_newbcast:1 row_mask:0xf bank_mask:0xf\n"
    "v_mfma_f32_16x16x32_bf16 v[40:43], a[0:3], v[36:39], 0\n"
    "s_add_u32 s66, s43, 11927552\n"
    "v_rsq_f32 v52, v73\n"
    "v_fmac_f32_dpp v64, v6, v7 row_newbcast:4 row_mask:0xf bank_mask:0xf\n"
    "v_mfma_f32_16x16x32_bf16 v[44:47], a[4:7], v[36:39], 0\n"
    "v_mul_f32 v72, 0xbfb8aa3b, v52\n"
    "v_fmac_f32_dpp v68, v6, v7 row_newbcast:8 row_mask:0xf bank_mask:0xf\n"
    "v_fmac_f32_dpp v69, v6, v7 row_newbcast:9 row_mask:0xf bank_mask:0xf\n"
    "v_mfma_f32_16x16x32_bf16 v[48:51], a[8:11], v[36:39], 0\n"
    "s_waitcnt lgkmcnt(2)\n"
    "ds_read2st64_b64 v[36:39], v4 offset0:92 offset1:93\n"
    "v_fmac_f32_dpp v70, v6, v7 row_newbcast:10 row_mask:0xf bank_mask:0xf\n"
    "v_fmac_f32_dpp v71, v6, v7 row_newbcast:11 row_mask:0xf bank_mask:0xf\n"
    "v_mfma_f32_16x16x32_bf16 v[40:43], a[12:15], v[24:27], v[40:43]\n"
    "v_permlane16_swap_b32 v68, v70\n"
    "v_mul_f32 v64, v60, v64\n"
    "v_mfma_f32_16x16x32_bf16 v[44:47], a[16:19], v[24:27], v[44:47]\n"
    "v_permlane16_swap_b32 v69, v71\n"
    "v_mul_f32 v60, v72, v60\n"
    "v_mfma_f32_16x16x32_bf16 v[48:51], a[20:23], v[24:27], v[48:51]\n"
    "s_waitcnt lgkmcnt(2)\n"
    "buffer_load_dwordx4 v3, %[srdw], s68 offen offset:2048 sc1 lds\n"
    "v_fmac_f32_dpp v65, v6, v7 row_newbcast:5 row_mask:0xf bank_mask:0xf\n"
    "v_mfma_f32_16x16x32_bf16 v[40:43], a[24:27], v[28:31], v[40:43]\n"
    "ds_read2st64_b64 v[24:27], v4 offset0:94 offset1:95\n"
    "v_exp_f32 v60, v60\n"
    "v_mfma_f32_16x16x32_bf16 v[44:47], a[28:31], v[28:31], v[44:47]\n"
    "v_fma_f32 v60, v60, v73, v73\n"
    "v_rcp_f32 v60, v60\n"
    "v_mfma_f32_16x16x32_bf16 v[48:51], a[32:35], v[28:31], v[48:51]\n"
    "s_waitcnt lgkmcnt(2)\n"
    "ds_read2st64_b64 v[28:31], v4 offset0:96 offset1:97\n"
    "v_mul_f32 v60, v60, v64\n"
    "v_mul_f32 v65, v61, v65\n"
    "v_mfma_f32_16x16x32_bf16 v[40:43], a[36:39], v[32:35], v[40:43]\n"
    "v_mul_f32 v61, v72, v61\n"
    "v_exp_f32 v61, v61\n"
    "v_mfma_f32_16x16x32_bf16 v[44:47], a[40:43], v[32:35], v[44:47]\n"
    "v_fma_f32 v61, v61, v73, v73\n"
    "v_rcp_f32 v61, v61\n"
    "v_mfma_f32_16x16x32_bf16 v[48:51], a[44:47], v[32:35], v[48:51]\n"
    "s_waitcnt lgkmcnt(2)\n"
    "ds_read2st64_b64 v[32:35], v4 offset0:98 offset1:99\n"
    "v_mul_f32 v61, v61, v65\n"
    "v_cvt_pk_bf16_f32 v60, v60, v61\n"
    "v_mfma_f32_16x16x32_bf16 v[40:43], a[48:51], v[36:39], v[40:43]\n"
    "buffer_load_dwordx4 v3, %[srdw], s68 offen offset:3072 sc1 lds\n"
    "v_fmac_f32_dpp v62, v6, v7 row_newbcast:2 row_mask:0xf bank_mask:0xf\n"
    "v_fmac_f32_dpp v66, v6, v7 row_newbcast:6 row_mask:0xf bank_mask:0xf\n"
    "v_mfma_f32_16x16x32_bf16 v[44:47], a[52:55], v[36:39], v[44:47]\n"
    "v_mul_f32 v66, v62, v66\n"
    "v_mul_f32 v62, v72, v62\n"
    "v_fmac_f32_dpp v63, v6, v7 row_newbcast:3 row_mask:0xf bank_mask:0xf\n"
    "v_mfma_f32_16x16x32_bf16 v[48:51], a[56:59], v[36:39], v[48:51]\n"
    "s_waitcnt lgkmcnt(2) vmcnt(11)\n"
    "ds_read2st64_b64 v[36:39], v5 offset0:0 offset1:1\n"
    "v_exp_f32 v62, v62\n"
    "v_mfma_f32_16x16x32_bf16 v[40:43], a[60:63], v[24:27], v[40:43]\n"
    "v_fma_f32 v62, v62, v73, v73\n"
    "v_rcp_f32 v62, v62\n"
    "v_mfma_f32_16x16x32_bf16 v[44:47], a[64:67], v[24:27], v[44:47]\n"
    "v_mul_f32 v62, v62, v66\n"
    "v_fmac_f32_dpp v67, v6, v7 row_newbcast:7 row_mask:0xf bank_mask:0xf\n"
    "v_mul_f32 v67, v63, v67\n"
    "v_mfma_f32_16x16x32_bf16 v[48:51], a[68:71], v[24:27], v[48:51]\n"
    "s_waitcnt lgkmcnt(2)\n"
    "ds_read2st64_b64 v[24:27], v4 offset0:100 offset1:101\n"
    "v_mul_f32 v63, v72, v63\n"
    "v_mul_f32 v70, v68, v70\n"
    "v_mfma_f32_16x16x32_bf16 v[40:43], a[72:75], v[28:31], v[40:43]\n"
    "v_exp_f32 v63, v63\n"
    "v_mul_f32 v68, v72, v68\n"
    "v_mfma_f32_16x16x32_bf16 v[44:47], a[76:79], v[28:31], v[44:47]\n"
    "s_add_u32 m0, %[ldsx], 25600\n"
    "s_add_u32 s68, %[x], 17829888\n"
    "buffer_load_dwordx4 v3, %[srdw], s68 offen sc1 lds\n"
    "v_fma_f32 v63, v63, v73, v73\n"
    "v_mfma_f32_16x16x32_bf16 v[48:51], a[80:83], v[28:31], v[48:51]\n"
    "s_waitcnt lgkmcnt(2) vmcnt(11)\n"
    "ds_read2st64_b64 v[28:31], v5 offset0:2 offset1:3\n"
    "v_rcp_f32 v63, v63\n"
    "v_mfma_f32_16x16x32_bf16 v[40:43], a[84:87], v[32:35], v[40:43]\n"
    "v_mul_f32 v63, v63, v67\n"
    "v_cvt_pk_bf16_f32 v61, v62, v63\n"
    "v_mul_f32 v71, v69, v71\n"
    "v_mfma_f32_16x16x32_bf16 v[44:47], a[88:91], v[32:35], v[44:47]\n"
    "v_exp_f32 v68, v68\n"
    "v_mul_f32 v69, v72, v69\n"
    "v_mfma_f32_16x16x32_bf16 v[48:51], a[92:95], v[32:35], v[48:51]\n"
    "s_waitcnt lgkmcnt(2)\n"
    "ds_read2st64_b64 v[32:35], v4 offset0:102 offset1:103\n"
    "v_fma_f32 v68, v68, v73, v73\n"
    "v_mfma_f32_16x16x32_bf16 v[52:55], v[20:23], v[36:39], 0\n"
    "v_rcp_f32 v68, v68\n"
    "v_mfma_f32_16x16x32_bf16 v[56:59], v[36:39], v[36:39], 0\n"
    "s_waitcnt lgkmcnt(2) vmcnt(9)\n"
    "ds_read2st64_b64 v[36:39], v5 offset0:4 offset1:5\n"
    "v_mul_f32 v68, v68, v70\n"
    "v_mfma_f32_16x16x32_bf16 v[40:43], a[96:99], v[24:27], v[40:43]\n"
    "v_exp_f32 v69, v69\n"
    "v_mfma_f32_16x16x32_bf16 v[44:47], a[100:103], v[24:27], v[44:47]\n"
    "v_fma_f32 v69, v69, v73, v73\n"
    "v_rcp_f32 v69, v69\n"
    "v_mfma_f32_16x16x32_bf16 v[48:51], a[104:107], v[24:27], v[48:51]\n"
    "s_waitcnt lgkmcnt(2)\n"
    "ds_read2st64_b64 v[24:27], v4 offset0:104 offset1:105\n"
    "v_mul_f32 v69, v69, v71\n"
    "v_cvt_pk_bf16_f32 v62, v68, v69\n"
    "v_mfma_f32_16x16x32_bf16 v[52:55], v[20:23], v[28:31], v[52:55]\n"
    "v_permlane32_swap_b32 v61, v63\n"
    "v_mfma_f32_16x16x32_bf16 v[56:59], v[28:31], v[28:31], v[56:59]\n"
    "s_waitcnt lgkmcnt(2) vmcnt(8)\n"
    "ds_read2st64_b64 v[28:31], v5 offset0:6 offset1:7\n"
    "v_permlane16_swap_b32 v62, v63\n"
    "v_mfma_f32_16x16x32_bf16 v[40:43], a[108:111], v[32:35], v[40:43]\n"
    "v_permlane16_swap_b32 v60, v61\n"
    "v_mfma_f32_16x16x32_bf16 v[44:47], a[112:115], v[32:35], v[44:47]\n"
    "buffer_store_dwordx4 v[60:63], v10, %[srdw], s66 offen sc1\n"
    "v_mfma_f32_16x16x32_bf16 v[48:51], a[116:119], v[32:35], v[48:51]\n"
    "s_waitcnt lgkmcnt(2)\n"
    "ds_read2st64_b64 v[32:35], v4 offset0:106 offset1:107\n"
    "v_mfma_f32_16x16x32_bf16 v[52:55], v[20:23], v[36:39], v[52:55]\n"
    "v_mfma_f32_16x16x32_bf16 v[56:59], v[36:39], v[36:39], v[56:59]\n"
    "s_waitcnt lgkmcnt(2) vmcnt(8)\n"
    "ds_read2st64_b64 v[36:39], v5 offset0:8 offset1:9\n"
    "v_mfma_f32_16x16x32_bf16 v[40:43], a[120:123], v[24:27], v[40:43]\n"
    "v_mfma_f32_16x16x32_bf16 v[44:47], a[124:127], v[24:27], v[44:47]\n"
    "v_mfma_f32_16x16x32_bf16 v[48:51], a[128:131], v[24:27], v[48:51]\n"
    "s_waitcnt lgkmcnt(2)\n"
    "ds_read2st64_b64 v[24:27], v4 offset0:108 offset1:109\n"
    "v_mfma_f32_16x16x32_bf16 v[52:55], v[20:23], v[28:31], v[52:55]\n"
    "ds_read2st64_b64 v[112:115], v4 offset0:120 offset1:121\n"
    "ds_read2st64_b64 v[116:119], v4 offset0:122 offset1:123\n"
    "v_mfma_f32_16x16x32_bf16 v[56:59], v[28:31], v[28:31], v[56:59]\n"
    "s_waitcnt lgkmcnt(4)\n"
    "ds_read2st64_b64 v[28:31], v4 offset0:110 offset1:111\n"
    "v_mfma_f32_16x16x32_bf16 v[40:43], a[132:135], v[32:35], v[40:43]\n"
    "v_mfma_f32_16x16x32_bf16 v[44:47], a[136:139], v[32:35], v[44:47]\n"
    "v_mfma_f32_16x16x32_bf16 v[48:51], a[140:143], v[32:35], v[48:51]\n"
    "s_waitcnt lgkmcnt(4)\n"
    "ds_read2st64_b64 v[32:35], v4 offset0:112 offset1:113\n"
    "v_mfma_f32_16x16x32_bf16 v[52:55], v[20:23], v[36:39], v[52:55]\n"
    "v_mfma_f32_16x16x32_bf16 v[56:59], v[36:39], v[36:39], v[56:59]\n"
    "s_waitcnt lgkmcnt(4)\n"
    "ds_read2st64_b64 v[36:39], v4 offset0:114 offset1:115\n"
    "v_mfma_f32_16x16x32_bf16 v[40:43], a[144:147], v[24:27], v[40:43]\n"
    "v_mfma_f32_16x16x32_bf16 v[44:47], a[148:151], v[24:27], v[44:47]\n"
    "v_mfma_f32_16x16x32_bf16 v[48:51], a[152:155], v[24:27], v[48:51]\n"
    "s_waitcnt lgkmcnt(2)\n"
    "ds_read2st64_b64 v[24:27], v4 offset0:116 offset1:117\n"
    "v_mfma_f32_16x16x32_bf16 v[40:43], a[156:159], v[28:31], v[40:43]\n"
    "v_cmp_u_f32 vcc, v52, v52\n"
    "s_nop 0\n"
    "s_cmp_lg_u64 vcc, 0\n"
    "s_cbranch_scc1 L49_%=\n"
    "L50_%=:\n"
    "v_mfma_f32_16x16x32_bf16 v[44:47], a[160:163], v[28:31], v[44:47]\n"
    "v_cndmask_b32_e64 v53, v56, v57, s[48:49]\n"
    "v_cndmask_b32_e64 v53, v53, v58, s[50:51]\n"
    "v_mfma_f32_16x16x32_bf16 v[48:51], a[164:167], v[28:31], v[48:51]\n"
    "s_waitcnt lgkmcnt(2)\n"
    "ds_read2st64_b64 v[28:31], v4 offset0:118 offset1:119\n"
    "v_cndmask_b32_e64 v53, v53, v59, s[52:53]\n"
    "v_mfma_f32_16x16x32_bf16 v[40:43], a[168:171], v[32:35], v[40:43]\n"
    "ds_write_b64 v8, v[52:53] offset:0\n"
    "v_mfma_f32_16x16x32_bf16 v[44:47], a[172:175], v[32:35], v[44:47]\n"
    "v_mfma_f32_16x16x32_bf16 v[48:51], a[176:179], v[32:35], v[48:51]\n"
    "s_waitcnt lgkmcnt(3)\n"
    "v_mfma_f32_16x16x32_bf16 v[40:43], a[180:183], v[36:39], v[40:43]\n"
    "v_mfma_f32_16x16x32_bf16 v[44:47], a[184:187], v[36:39], v[44:47]\n"
    "s_add_u32 s67, s93, 1572864\n"
    "ds_read_b128 v[52:55], v9 offset:43008\n"
    "ds_read_b128 v[56:59], v9 offset:43024\n"
    "v_mfma_f32_16x16x32_bf16 v[48:51], a[188:191], v[36:39], v[48:51]\n"
    "s_waitcnt lgkmcnt(4)\n"
    "v_mfma_f32_16x16x32_bf16 v[40:43], a[192:195], v[24:27], v[40:43]\n"
    "v_mfma_f32_16x16x32_bf16 v[44:47], a[196:199], v[24:27], v[44:47]\n"
    "s_waitcnt lgkmcnt(2)\n"
    "s_barrier\n"
    "v_mfma_f32_16x16x32_bf16 v[48:51], a[200:203], v[24:27], v[48:51]\n"
    "ds_read2st64_b64 v[32:35], v4 offset0:0 offset1:1\n"
    "ds_read2st64_b64 v[36:39], v4 offset0:2 offset1:3\n"
    "v_mfma_f32_16x16x32_bf16 v[40:43], a[204:207], v[28:31], v[40:43]\n"
    "ds_read2st64_b64 v[24:27], v4 offset0:4 offset1:5\n"
    "v_mfma_f32_16x16x32_bf16 v[44:47], a[208:211], v[28:31], v[44:47]\n"
    "s_add_u32 m0, %[ldsx], 43008\n"
    "s_add_u32 s68, s42, 18153472\n"
    "buffer_load_dwordx4 v3, %[srdw], s68 offen sc1 lds\n"
    "v_mfma_f32_16x16x32_bf16 v[48:51], a[212:215], v[28:31], v[48:51]\n"
    "ds_read2st64_b64 v[28:31], v4 offset0:6 offset1:7\n"
    "v_mfma_f32_16x16x32_bf16 v[40:43], a[216:219], v[112:115], v[40:43]\n"
    "s_waitcnt lgkmcnt(5)\n"
    "s_mov_b64 exec, s[94:95]\n"
    "buffer_store_dword v2, off, %[srdw], s67 sc1\n"
    "s_mov_b64 exec, -1\n"
    "v_mfma_f32_16x16x32_bf16 v[44:47], a[220:223], v[112:115], v[44:47]\n"
    "v_add_f32 v52, v52, v54\n"
    "v_add_f32 v53, v53, v55\n"
    "v_mfma_f32_16x16x32_bf16 v[48:51], a[224:227], v[112:115], v[48:51]\n"
    "s_waitcnt lgkmcnt(4)\n"
    "v_add_f32 v56, v56, v58\n"
    "v_add_f32 v57, v57, v59\n"
    "v_mfma_f32_16x16x32_bf16 v[40:43], a[228:231], v[116:119], v[40:43]\n"
    "buffer_load_dwordx4 v3, %[srdw], s68 offen offset:1024 sc1 lds\n"
    "v_add_f32 v52, v52, v56\n"
    "v_mfma_f32_16x16x32_bf16 v[44:47], a[232:235], v[116:119], v[44:47]\n"
    "v_add_f32 v53, v53, v57\n"
    "v_mul_f32 v7, 0xbacccccd, v52\n"
    "v_mfma_f32_16x16x32_bf16 v[48:51], a[236:239], v[116:119], v[48:51]\n"
    "s_waitcnt lgkmcnt(3)\n"
    "v_mul_f32 v53, 0x3acccccd, v53\n"
    "v_fma_f32 v53, -v7, v7, v53\n"
    "v_mfma_f32_16x16x32_bf16 v[60:63], a[0:3], v[32:35], 0\n"
    "s_add_u32 s78, %[hst], 18481152\n"
    "v_max_f32 v53, 0, v53\n"
    "v_add_f32 v73, 0x3727c5ac, v53\n"
    "v_mfma_f32_16x16x32_bf16 v[64:67], a[4:7], v[32:35], 0\n"
    "v_rsq_f32 v52, v73\n"
    "v_mfma_f32_16x16x32_bf16 v[68:71], a[8:11], v[32:35], 0\n"
    "s_waitcnt lgkmcnt(2)\n"
    "ds_read2st64_b64 v[32:35], v4 offset0:8 offset1:9\n"
    "v_mul_f32 v72, 0xbfb8aa3b, v52\n"
    "v_mfma_f32_16x16x32_bf16 v[60:63], a[12:15], v[36:39], v[60:63]\n"
    "v_fmac_f32_dpp v48, v6, v7 row_newbcast:8 row_mask:0xf bank_mask:0xf\n"
    "v_fmac_f32_dpp v49, v6, v7 row_newbcast:9 row_mask:0xf bank_mask:0xf\n"
    "v_mfma_f32_16x16x32_bf16 v[64:67], a[16:19], v[36:39], v[64:67]\n"
    "v_fmac_f32_dpp v50, v6, v7 row_newbcast:10 row_mask:0xf bank_mask:0xf\n"
    "v_fmac_f32_dpp v51, v6, v7 row_newbcast:11 row_mask:0xf bank_mask:0xf\n"
    "v_mfma_f32_16x16x32_bf16 v[68:71], a[20:23], v[36:39], v[68:71]\n"
    "s_waitcnt lgkmcnt(2)\n"
    "ds_read2st64_b64 v[36:39], v4 offset0:10 offset1:11\n"
    "v_fmac_f32_dpp v40, v6, v7 row_newbcast:0 row_mask:0xf bank_mask:0xf\n"
    "v_mfma_f32_16x16x32_bf16 v[60:63], a[24:27], v[24:27], v[60:63]\n"
    "buffer_load_dwordx4 v3, %[srdw], s68 offen offset:2048 sc1 lds\n"
    "v_fmac_f32_dpp v44, v6, v7 row_newbcast:4 row_mask:0xf bank_mask:0xf\n"
    "v_mfma_f32_16x16x32_bf16 v[64:67], a[28:31], v[24:27], v[64:67]\n"
    "v_permlane16_swap_b32 v48, v50\n"
    "v_mfma_f32_16x16x32_bf16 v[68:71], a[32:35], v[24:27], v[68:71]\n"
    "s_waitcnt lgkmcnt(2)\n"
    "ds_read2st64_b64 v[24:27], v4 offset0:12 offset1:13\n"
    "v_mul_f32 v44, v40, v44\n"
    "v_mfma_f32_16x16x32_bf16 v[60:63], a[36:39], v[28:31], v[60:63]\n"
    "v_permlane16_swap_b32 v49, v51\n"
    "v_mfma_f32_16x16x32_bf16 v[64:67], a[40:43], v[28:31], v[64:67]\n"
    "v_mul_f32 v40, v72, v40\n"
    "v_fmac_f32_dpp v41, v6, v7 row_newbcast:1 row_mask:0xf bank_mask:0xf\n"
    "v_mfma_f32_16x16x32_bf16 v[68:71], a[44:47], v[28:31], v[68:71]\n"
    "s_waitcnt lgkmcnt(2)\n"
    "ds_read2st64_b64 v[28:31], v4 offset0:14 offset1:15\n"
    "v_fmac_f32_dpp v45, v6, v7 row_newbcast:5 row_mask:0xf bank_mask:0xf\n"
    "v_mfma_f32_16x16x32_bf16 v[60:63], a[48:51], v[32:35], v[60:63]\n"
    "v_exp_f32 v40, v40\n"
    "v_mfma_f32_16x16x32_bf16 v[64:67], a[52:55], v[32:35], v[64:67]\n"
    "v_fma_f32 v40, v40, v73, v73\n"
    "v_mul_f32 v45, v41, v45\n"
    "v_mfma_f32_16x16x32_bf16 v[68:71], a[56:59], v[32:35], v[68:71]\n"
    "s_waitcnt lgkmcnt(2) vmcnt(9)\n"
    "ds_read2st64_b64 v[32:35], v5 offset0:42 offset1:43\n"
    "v_mul_f32 v41, v72, v41\n"
    "v_mfma_f32_16x16x32_bf16 v[60:63], a[60:63], v[36:39], v[60:63]\n"
    "buffer_load_dwordx4 v3, %[srdw], s68 offen offset:3072 sc1 lds\n"
    "v_fmac_f32_dpp v42, v6, v7 row_newbcast:2 row_mask:0xf bank_mask:0xf\n"
    "v_mfma_f32_16x16x32_bf16 v[64:67], a[64:67], v[36:39], v[64:67]\n"
    "v_rcp_f32 v40, v40\n"
    "v_mfma_f32_16x16x32_bf16 v[68:71], a[68:71], v[36:39], v[68:71]\n"
    "s_waitcnt lgkmcnt(2)\n"
    "ds_read2st64_b64 v[36:39], v4 offset0:16 offset1:17\n"
    "v_mul_f32 v40, v40, v44\n"
    "v_mfma_f32_16x16x32_bf16 v[60:63], a[72:75], v[24:27], v[60:63]\n"
    "v_exp_f32 v41, v41\n"
    "v_mfma_f32_16x16x32_bf16 v[64:67], a[76:79], v[24:27], v[64:67]\n"
    "v_fma_f32 v41, v41, v73, v73\n"
    "v_fmac_f32_dpp v46, v6, v7 row_newbcast:6 row_mask:0xf bank_mask:0xf\n"
    "v_mfma_f32_16x16x32_bf16 v[68:71], a[80:83], v[24:27], v[68:71]\n"
    "s_waitcnt lgkmcnt(2) vmcnt(9)\n"
    "ds_read2st64_b64 v[24:27], v5 offset0:44 offset1:45\n"
    "v_mul_f32 v46, v42, v46\n"
    "v_mfma_f32_16x16x32_bf16 v[60:63], a[84:87], v[28:31], v[60:63]\n"
    "v_rcp_f32 v41, v41\n"
    "v_mfma_f32_16x16x32_bf16 v[64:67], a[88:91], v[28:31], v[64:67]\n"
    "v_mul_f32 v41, v41, v45\n"
    "v_cvt_pk_bf16_f32 v40, v40, v41\n"
    "v_mfma_f32_16x16x32_bf16 v[68:71], a[92:95], v[28:31], v[68:71]\n"
    "s_waitcnt lgkmcnt(2)\n"
    "ds_read2st64_b64 v[28:31], v4 offset0:18 offset1:19\n"
    "v_min_u32 v40, -2, v40\n"
    "v_mfma_f32_16x16x32_bf16 v[52:55], v[20:23], v[32:35], 0\n"
    "s_add_u32 m0, %[ldsx], 47104\n"
    "s_add_u32 s68, s42, 18157568\n"
    "buffer_load_dwordx4 v3, %[srdw], s68 offen sc1 lds\n"
    "v_mfma_f32_16x16x32_bf16 v[56:59], v[32:35], v[32:35], 0\n"
    "s_waitcnt lgkmcnt(2) vmcnt(9)\n"
    "ds_read2st64_b64 v[32:35], v5 offset0:46 offset1:47\n"
    "v_mul_f32 v42, v72, v42\n"
    "v_mfma_f32_16x16x32_bf16 v[60:63], a[96:99], v[36:39], v[60:63]\n"
    "v_exp_f32 v42, v42\n"
    "v_mfma_f32_16x16x32_bf16 v[64:67], a[100:103], v[36:39], v[64:67]\n"
    "v_fma_f32 v42, v42, v73, v73\n"
    "v_fmac_f32_dpp v43, v6, v7 row_newbcast:3 row_mask:0xf bank_mask:0xf\n"
    "v_mfma_f32_16x16x32_bf16 v[68:71], a[104:107], v[36:39], v[68:71]\n"
    "s_waitcnt lgkmcnt(2)\n"
    "ds_read2st64_b64 v[36:39], v4 offset0:20 offset1:21\n"
    "v_fmac_f32_dpp v47, v6, v7 row_newbcast:7 row_mask:0xf bank_mask:0xf\n"
    "v_mfma_f32_16x16x32_bf16 v[52:55], v[20:23], v[24:27], v[52:55]\n"
    "v_rcp_f32 v42, v42\n"
    "v_mfma_f32_16x16x32_bf16 v[56:59], v[24:27], v[24:27], v[56:59]\n"
    "s_waitcnt lgkmcnt(2) vmcnt(8)\n"
    "ds_read2st64_b64 v[24:27], v5 offset0:48 offset1:49\n"
    "v_mul_f32 v42, v42, v46\n"
    "v_mfma_f32_16x16x32_bf16 v[60:63], a[108:111], v[28:31], v[60:63]\n"
    "v_mul_f32 v47, v43, v47\n"
    "v_mul_f32 v43, v72, v43\n"
    "v_mfma_f32_16x16x32_bf16 v[64:67], a[112:115], v[28:31], v[64:67]\n"
    "v_exp_f32 v43, v43\n"
    "v_mfma_f32_16x16x32_bf16 v[68:71], a[116:119], v[28:31], v[68:71]\n"
    "s_waitcnt lgkmcnt(2)\n"
    "ds_read2st64_b64 v[28:31], v4 offset0:22 offset1:23\n"
    "v_fma_f32 v43, v43, v73, v73\n"
    "v_mfma_f32_16x16x32_bf16 v[52:55], v[20:23], v[32:35], v[52:55]\n"
    "v_rcp_f32 v43, v43\n"
    "v_mfma_f32_16x16x32_bf16 v[56:59], v[32:35], v[32:35], v[56:59]\n"
    "s_waitcnt lgkmcnt(2) vmcnt(7)\n"
    "ds_read2st64_b64 v[32:35], v5 offset0:50 offset1:51\n"
    "v_mul_f32 v43, v43, v47\n"
    "v_mfma_f32_16x16x32_bf16 v[60:63], a[120:123], v[36:39], v[60:63]\n"
    "v_cvt_pk_bf16_f32 v41, v42, v43\n"
    "v_min_u32 v41, -2, v41\n"
    "v_mfma_f32_16x16x32_bf16 v[64:67], a[124:127], v[36:39], v[64:67]\n"
    "v_mul_f32 v50, v48, v50\n"
    "v_mul_f32 v48, v72, v48\n"
    "v_mfma_f32_16x16x32_bf16 v[68:71], a[128:131], v[36:39], v[68:71]\n"
    "s_waitcnt lgkmcnt(2)\n"
    "ds_read2st64_b64 v[36:39], v4 offset0:24 offset1:25\n"
    "v_mul_f32 v51, v49, v51\n"
    "v_mfma_f32_16x16x32_bf16 v[52:55], v[20:23], v[24:27], v[52:55]\n"
    "ds_read2st64_b64 v[112:115], v4 offset0:36 offset1:37\n"
    "ds_read2st64_b64 v[116:119], v4 offset0:38 offset1:39\n"
    "v_mfma_f32_16x16x32_bf16 v[56:59], v[24:27], v[24:27], v[56:59]\n"
    "s_waitcnt lgkmcnt(4)\n"
    "ds_read2st64_b64 v[24:27], v4 offset0:26 offset1:27\n"
    "v_mul_f32 v49, v72, v49\n"
    "v_mfma_f32_16x16x32_bf16 v[60:63], a[132:135], v[28:31], v[60:63]\n"
    "v_exp_f32 v48, v48\n"
    "v_mfma_f32_16x16x32_bf16 v[64:67], a[136:139], v[28:31], v[64:67]\n"
    "v_fma_f32 v48, v48, v73, v73\n"
    "v_mfma_f32_16x16x32_bf16 v[68:71], a[140:143], v[28:31], v[68:71]\n"
    "s_waitcnt lgkmcnt(4)\n"
    "ds_read2st64_b64 v[28:31], v4 offset0:28 offset1:29\n"
    "v_mfma_f32_16x16x32_bf16 v[52:55], v[20:23], v[32:35], v[52:55]\n"
    "v_rcp_f32 v48, v48\n"
    "v_mfma_f32_16x16x32_bf16 v[56:59], v[32:35], v[32:35], v[56:59]\n"
    "s_waitcnt lgkmcnt(4)\n"
    "ds_read2st64_b64 v[32:35], v4 offset0:30 offset1:31\n"
    "v_mul_f32 v48, v48, v50\n"
    "v_mfma_f32_16x16x32_bf16 v[60:63], a[144:147], v[36:39], v[60:63]\n"
    "v_exp_f32 v49, v49\n"
    "v_mfma_f32_16x16x32_bf16 v[64:67], a[148:151], v[36:39], v[64:67]\n"
    "v_cmp_u_f32 vcc, v52, v52\n"
    "s_nop 0\n"
    "s_cmp_lg_u64 vcc, 0\n"
    "s_cbranch_scc1 L51_%=\n"
    "L52_%=:\n"
    "v_mfma_f32_16x16x32_bf16 v[68:71], a[152:155], v[36:39], v[68:71]\n"
    "s_waitcnt lgkmcnt(2)\n"
    "ds_read2st64_b64 v[36:39], v4 offset0:32 offset1:33\n"
    "v_cndmask_b32_e64 v53, v56, v57, s[48:49]\n"
    "v_mfma_f32_16x16x32_bf16 v[60:63], a[156:159], v[24:27], v[60:63]\n"
    "v_cndmask_b32_e64 v53, v53, v58, s[50:51]\n"
    "v_cndmask_b32_e64 v53, v53, v59, s[52:53]\n"
    "v_mfma_f32_16x16x32_bf16 v[64:67], a[160:163], v[24:27], v[64:67]\n"
    "ds_write_b64 v8, v[52:53] offset:21504\n"
    "v_mfma_f32_16x16x32_bf16 v[68:71], a[164:167], v[24:27], v[68:71]\n"
    "s_waitcnt lgkmcnt(3)\n"
    "ds_read2st64_b64 v[24:27], v4 offset0:34 offset1:35\n"
    "v_fma_f32 v49, v49, v73, v73\n"
    "v_mfma_f32_16x16x32_bf16 v[60:63], a[168:171], v[28:31], v[60:63]\n"
    "v_rcp_f32 v49, v49\n"
    "v_mfma_f32_16x16x32_bf16 v[64:67], a[172:175], v[28:31], v[64:67]\n"
    "v_mul_f32 v49, v49, v51\n"
    "v_cvt_pk_bf16_f32 v42, v48, v49\n"
    "v_mfma_f32_16x16x32_bf16 v[68:71], a[176:179], v[28:31], v[68:71]\n"
    "s_waitcnt lgkmcnt(3)\n"
    "v_min_u32 v42, -2, v42\n"
    "v_mfma_f32_16x16x32_bf16 v[60:63], a[180:183], v[32:35], v[60:63]\n"
    "v_permlane32_swap_b32 v41, v43\n"
    "v_mfma_f32_16x16x32_bf16 v[64:67], a[184:187], v[32:35], v[64:67]\n"
    "s_add_u32 s79, %[f4p], 1442304\n"
    "s_add_u32 s80, s93, 1573376\n"
    "ds_read_b128 v[52:55], v9 offset:0\n"
    "v_mfma_f32_16x16x32_bf16 v[68:71], a[188:191], v[32:35], v[68:71]\n"
    "s_waitcnt lgkmcnt(3)\n"
    "ds_read_b128 v[56:59], v9 offset:16\n"
    "v_mfma_f32_16x16x32_bf16 v[60:63], a[192:195], v[36:39], v[60:63]\n"
    "v_permlane16_swap_b32 v42, v43\n"
    "v_mfma_f32_16x16x32_bf16 v[64:67], a[196:199], v[36:39], v[64:67]\n"
    "s_add_u32 s81, %[f4c], 1441792\n"
    "s_waitcnt lgkmcnt(2) vmcnt(6)\n"
    "s_barrier\n"
    "v_mfma_f32_16x16x32_bf16 v[68:71], a[200:203], v[36:39], v[68:71]\n"
    "ds_read2st64_b64 v[28:31], v4 offset0:42 offset1:43\n"
    "ds_read2st64_b64 v[32:35], v4 offset0:44 offset1:45\n"
    "v_mfma_f32_16x16x32_bf16 v[60:63], a[204:207], v[24:27], v[60:63]\n"
    "s_mov_b64 exec, 0xf\n"
    "buffer_load_dword v78, v1, %[srdw], s81 offen sc1\n"
    "s_mov_b64 exec, -1\n"
    "v_mfma_f32_16x16x32_bf16 v[64:67], a[208:211], v[24:27], v[64:67]\n"
    "ds_read2st64_b64 v[36:39], v4 offset0:46 offset1:47\n"
    "v_mfma_f32_16x16x32_bf16 v[68:71], a[212:215], v[24:27], v[68:71]\n"
    "ds_read2st64_b64 v[24:27], v4 offset0:48 offset1:49\n"
    "v_mfma_f32_16x16x32_bf16 v[60:63], a[216:219], v[112:115], v[60:63]\n"
    "s_mov_b64 exec, s[94:95]\n"
    "buffer_store_dword v2, off, %[srdw], s79 sc1\n"
    "s_mov_b64 exec, -1\n"
    "v_mfma_f32_16x16x32_bf16 v[64:67], a[220:223], v[112:115], v[64:67]\n"
    "s_waitcnt lgkmcnt(5)\n"
    "s_mov_b64 exec, s[94:95]\n"
    "buffer_store_dword v2, off, %[srdw], s80 sc1\n"
    "s_mov_b64 exec, -1\n"
    "v_mfma_f32_16x16x32_bf16 v[68:71], a[224:227], v[112:115], v[68:71]\n"
    "v_add_f32 v52, v52, v54\n"
    "v_add_f32 v53, v53, v55\n"
    "v_mfma_f32_16x16x32_bf16 v[60:63], a[228:231], v[116:119], v[60:63]\n"
    "s_waitcnt lgkmcnt(4)\n"
    "v_add_f32 v56, v56, v58\n"
    "v_add_f32 v57, v57, v59\n"
    "v_mfma_f32_16x16x32_bf16 v[64:67], a[232:235], v[116:119], v[64:67]\n"
    "v_add_f32 v52, v52, v56\n"
    "v_add_f32 v53, v53, v57\n"
    "v_mfma_f32_16x16x32_bf16 v[68:71], a[236:239], v[116:119], v[68:71]\n"
    "s_add_u32 s82, s43, 19267584\n"
    "v_mul_f32 v7, 0xbacccccd, v52\n"
    "v_mul_f32 v53, 0x3acccccd, v53\n"
    "v_permlane16_swap_b32 v40, v41\n"
    "buffer_store_dwordx4 v[40:43], v10, %[srdw], s78 offen sc1\n"
    "s_waitcnt lgkmcnt(3)\n"
    "s_nop 0\n"
    "v_mfma_f32_16x16x32_bf16 v[40:43], a[0:3], v[28:31], 0\n"
    "v_fma_f32 v53, -v7, v7, v53\n"
    "v_max_f32 v53, 0, v53\n"
    "v_fmac_f32_dpp v68, v6, v7 row_newbcast:8 row_mask:0xf bank_mask:0xf\n"
    "v_mfma_f32_16x16x32_bf16 v[44:47], a[4:7], v[28:31], 0\n"
    "v_add_f32 v73, 0x3727c5ac, v53\n"
    "v_fmac_f32_dpp v69, v6, v7 row_newbcast:9 row_mask:0xf bank_mask:0xf\n"
    "v_fmac_f32_dpp v70, v6, v7 row_newbcast:10 row_mask:0xf bank_mask:0xf\n"
    "v_mfma_f32_16x16x32_bf16 v[48:51], a[8:11], v[28:31], 0\n"
    "s_waitcnt lgkmcnt(2)\n"
    "ds_read2st64_b64 v[28:31], v4 offset0:50 offset1:51\n"
    "v_fmac_f32_dpp v71, v6, v7 row_newbcast:11 row_mask:0xf bank_mask:0xf\n"
    "v_fmac_f32_dpp v60, v6, v7 row_newbcast:0 row_mask:0xf bank_mask:0xf\n"
    "v_mfma_f32_16x16x32_bf16 v[40:43], a[12:15], v[32:35], v[40:43]\n"
    "v_rsq_f32 v52, v73\n"
    "v_fmac_f32_dpp v64, v6, v7 row_newbcast:4 row_mask:0xf bank_mask:0xf\n"
    "v_mfma_f32_16x16x32_bf16 v[44:47], a[16:19], v[32:35], v[44:47]\n"
    "v_mul_f32 v72, 0xbfb8aa3b, v52\n"
    "v_permlane16_swap_b32 v68, v70\n"
    "v_mfma_f32_16x16x32_bf16 v[48:51], a[20:23], v[32:35], v[48:51]\n"
    "s_waitcnt lgkmcnt(2)\n"
    "ds_read2st64_b64 v[32:35], v4 offset0:52 offset1:53\n"
    "v_permlane16_swap_b32 v69, v71\n"
    "v_mfma_f32_16x16x32_bf16 v[40:43], a[24:27], v[36:39], v[40:43]\n"
    "v_mul_f32 v64, v60, v64\n"
    "v_mul_f32 v60, v72, v60\n"
    "v_fmac_f32_dpp v61, v6, v7 row_newbcast:1 row_mask:0xf bank_mask:0xf\n"
    "v_mfma_f32_16x16x32_bf16 v[44:47], a[28:31], v[36:39], v[44:47]\n"
    "v_exp_f32 v60, v60\n"
    "v_fmac_f32_dpp v65, v6, v7 row_newbcast:5 row_mask:0xf bank_mask:0xf\n"
    "v_mfma_f32_16x16x32_bf16 v[48:51], a[32:35], v[36:39], v[48:51]\n"
    "s_waitcnt lgkmcnt(2)\n"
    "ds_read2st64_b64 v[36:39], v4 offset0:54 offset1:55\n"
    "v_fma_f32 v60, v60, v73, v73\n"
    "v_mul_f32 v65, v61, v65\n"
    "v_mfma_f32_16x16x32_bf16 v[40:43], a[36:39], v[24:27], v[40:43]\n"
    "v_rcp_f32 v60, v60\n"
    "v_mul_f32 v61, v72, v61\n"
    "v_mfma_f32_16x16x32_bf16 v[44:47], a[40:43], v[24:27], v[44:47]\n"
    "v_mul_f32 v60, v60, v64\n"
    "v_exp_f32 v61, v61\n"
    "v_mfma_f32_16x16x32_bf16 v[48:51], a[44:47], v[24:27], v[48:51]\n"
    "s_waitcnt lgkmcnt(2)\n"
    "ds_read2st64_b64 v[24:27], v4 offset0:56 offset1:57\n"
    "v_fma_f32 v61, v61, v73, v73\n"
    "v_fmac_f32_dpp v62, v6, v7 row_newbcast:2 row_mask:0xf bank_mask:0xf\n"
    "v_mfma_f32_16x16x32_bf16 v[40:43], a[48:51], v[28:31], v[40:43]\n"
    "v_rcp_f32 v61, v61\n"
    "v_fmac_f32_dpp v66, v6, v7 row_newbcast:6 row_mask:0xf bank_mask:0xf\n"
    "v_mfma_f32_16x16x32_bf16 v[44:47], a[52:55], v[28:31], v[44:47]\n"
    "v_mul_f32 v61, v61, v65\n"
    "v_cvt_pk_bf16_f32 v60, v60, v61\n"
    "v_min_u32 v60, -2, v60\n"
    "v_mfma_f32_16x16x32_bf16 v[48:51], a[56:59], v[28:31], v[48:51]\n"
    "s_waitcnt lgkmcnt(2) vmcnt(9)\n"
    "ds_read2st64_b64 v[28:31], v5 offset0:84 offset1:85\n"
    "v_mul_f32 v66, v62, v66\n"
    "v_mul_f32 v62, v72, v62\n"
    "v_mfma_f32_16x16x32_bf16 v[40:43], a[60:63], v[32:35], v[40:43]\n"
    "v_exp_f32 v62, v62\n"
    "v_fmac_f32_dpp v63, v6, v7 row_newbcast:3 row_mask:0xf bank_mask:0xf\n"
    "v_mfma_f32_16x16x32_bf16 v[44:47], a[64:67], v[32:35], v[44:47]\n"
    "v_fma_f32 v62, v62, v73, v73\n"
    "v_rcp_f32 v62, v62\n"
    "v_mfma_f32_16x16x32_bf16 v[48:51], a[68:71], v[32:35], v[48:51]\n"
    "s_waitcnt lgkmcnt(2)\n"
    "ds_read2st64_b64 v[32:35], v4 offset0:58 offset1:59\n"
    "v_mul_f32 v62, v62, v66\n"
    "v_fmac_f32_dpp v67, v6, v7 row_newbcast:7 row_mask:0xf bank_mask:0xf\n"
    "v_mfma_f32_16x16x32_bf16 v[40:43], a[72:75], v[36:39], v[40:43]\n"
    "v_mul_f32 v67, v63, v67\n"
    "v_mul_f32 v63, v72, v63\n"
    "v_mul_f32 v70, v68, v70\n"
    "v_mfma_f32_16x16x32_bf16 v[44:47], a[76:79], v[36:39], v[44:47]\n"
    "v_exp_f32 v63, v63\n"
    "v_mul_f32 v68, v72, v68\n"
    "v_mfma_f32_16x16x32_bf16 v[48:51], a[80:83], v[36:39], v[48:51]\n"
    "s_waitcnt lgkmcnt(2) vmcnt(7)\n"
    "ds_read2st64_b64 v[36:39], v5 offset0:86 offset1:87\n"
    "v_fma_f32 v63, v63, v73, v73\n"
    "v_mul_f32 v71, v69, v71\n"
    "v_mfma_f32_16x16x32_bf16 v[40:43], a[84:87], v[24:27], v[40:43]\n"
    "v_rcp_f32 v63, v63\n"
    "v_mul_f32 v69, v72, v69\n"
    "v_mfma_f32_16x16x32_bf16 v[44:47], a[88:91], v[24:27], v[44:47]\n"
    "v_mul_f32 v63, v63, v67\n"
    "v_cvt_pk_bf16_f32 v61, v62, v63\n"
    "v_min_u32 v61, -2, v61\n"
    "v_mfma_f32_16x16x32_bf16 v[48:51], a[92:95], v[24:27], v[48:51]\n"
    "s_waitcnt lgkmcnt(2)\n"
    "ds_read2st64_b64 v[24:27], v4 offset0:60 offset1:61\n"
    "v_exp_f32 v68, v68\n"
    "v_mfma_f32_16x16x32_bf16 v[52:55], v[20:23], v[28:31], 0\n"
    "v_fma_f32 v68, v68, v73, v73\n"
    "v_rcp_f32 v68, v68\n"
    "v_mfma_f32_16x16x32_bf16 v[56:59], v[28:31], v[28:31], 0\n"
    "s_waitcnt lgkmcnt(2) vmcnt(6)\n"
    "ds_read2st64_b64 v[28:31], v5 offset0:88 offset1:89\n"
    "v_mul_f32 v68, v68, v70\n"
    "v_mfma_f32_16x16x32_bf16 v[40:43], a[96:99], v[32:35], v[40:43]\n"
    "v_exp_f32 v69, v69\n"
    "v_mfma_f32_16x16x32_bf16 v[44:47], a[100:103], v[32:35], v[44:47]\n"
    "v_fma_f32 v69, v69, v73, v73\n"
    "v_rcp_f32 v69, v69\n"
    "v_mfma_f32_16x16x32_bf16 v[48:51], a[104:107], v[32:35], v[48:51]\n"
    "s_waitcnt lgkmcnt(2)\n"
    "ds_read2st64_b64 v[32:35], v4 offset0:62 offset1:63\n"
    "v_mul_f32 v69, v69, v71\n"
    "v_cvt_pk_bf16_f32 v62, v68, v69\n"
    "v_mfma_f32_16x16x32_bf16 v[52:55], v[20:23], v[36:39], v[52:55]\n"
    "v_min_u32 v62, -2, v62\n"
    "v_permlane32_swap_b32 v61, v63\n"
    "v_mfma_f32_16x16x32_bf16 v[56:59], v[36:39], v[36:39], v[56:59]\n"
    "s_waitcnt lgkmcnt(2) vmcnt(5)\n"
    "ds_read2st64_b64 v[36:39], v5 offset0:90 offset1:91\n"
    "v_permlane16_swap_b32 v62, v63\n"
    "v_mfma_f32_16x16x32_bf16 v[40:43], a[108:111], v[24:27], v[40:43]\n"
    "v_permlane16_swap_b32 v60, v61\n"
    "v_mfma_f32_16x16x32_bf16 v[44:47], a[112:115], v[24:27], v[44:47]\n"
    "buffer_store_dwordx4 v[60:63], v10, %[srdw], s82 offen sc1\n"
    "v_mfma_f32_16x16x32_bf16 v[48:51], a[116:119], v[24:27], v[48:51]\n"
    "s_waitcnt lgkmcnt(2)\n"
    "ds_read2st64_b64 v[24:27], v4 offset0:64 offset1:65\n"
    "v_mfma_f32_16x16x32_bf16 v[52:55], v[20:23], v[28:31], v[52:55]\n"
    "v_mfma_f32_16x16x32_bf16 v[56:59], v[28:31], v[28:31], v[56:59]\n"
    "s_waitcnt lgkmcnt(2) vmcnt(5)\n"
    "ds_read2st64_b64 v[28:31], v5 offset0:92 offset1:93\n"
    "v_mfma_f32_16x16x32_bf16 v[40:43], a[120:123], v[32:35], v[40:43]\n"
    "v_mfma_f32_16x16x32_bf16 v[44:47], a[124:127], v[32:35], v[44:47]\n"
    "v_mfma_f32_16x16x32_bf16 v[48:51], a[128:131], v[32:35], v[48:51]\n"
    "s_waitcnt lgkmcnt(2)\n"
    "ds_read2st64_b64 v[32:35], v4 offset0:66 offset1:67\n"
    "v_mfma_f32_16x16x32_bf16 v[52:55], v[20:23], v[36:39], v[52:55]\n"
    "ds_read2st64_b64 v[112:115], v4 offset0:78 offset1:79\n"
    "ds_read2st64_b64 v[116:119], v4 offset0:80 offset1:81\n"
    "v_mfma_f32_16x16x32_bf16 v[56:59], v[36:39], v[36:39], v[56:59]\n"
    "s_waitcnt lgkmcnt(4)\n"
    "ds_read2st64_b64 v[36:39], v4 offset0:68 offset1:69\n"
    "v_mfma_f32_16x16x32_bf16 v[40:43], a[132:135], v[24:27], v[40:43]\n"
    "v_mfma_f32_16x16x32_bf16 v[44:47], a[136:139], v[24:27], v[44:47]\n"
    "v_mfma_f32_16x16x32_bf16 v[48:51], a[140:143], v[24:27], v[48:51]\n"
    "s_waitcnt lgkmcnt(4)\n"
    "ds_read2st64_b64 v[24:27], v4 offset0:70 offset1:71\n"
    "v_mfma_f32_16x16x32_bf16 v[52:55], v[20:23], v[28:31], v[52:55]\n"
    "v_mfma_f32_16x16x32_bf16 v[56:59], v[28:31], v[28:31], v[56:59]\n"
    "s_waitcnt lgkmcnt(4)\n"
    "ds_read2st64_b64 v[28:31], v4 offset0:72 offset1:73\n"
    "s_add_u32 s83, s44, 9568256\n"
    "v_mfma_f32_16x16x32_bf16 v[40:43], a[144:147], v[32:35], v[40:43]\n"
    "v_mfma_f32_16x16x32_bf16 v[44:47], a[148:151], v[32:35], v[44:47]\n"
    "v_mfma_f32_16x16x32_bf16 v[48:51], a[152:155], v[32:35], v[48:51]\n"
    "s_waitcnt lgkmcnt(2)\n"
    "v_cmp_u_f32 vcc, v52, v52\n"
    "s_nop 0\n"
    "s_cmp_lg_u64 vcc, 0\n"
    "kx_a9:\n"
    "s_cbranch_execz kx_ret\n"
    "s_cbranch_scc1 L55_%=\n"
    "L56_%=:\n"
    "v_mfma_f32_16x16x32_bf16 v[40:43], a[156:159], v[36:39], v[40:43]\n"
    "s_waitcnt vmcnt(4)\n"
    "v_cmp_ne_u32 vcc, %[tag], v78\n"
    "s_nop 0\n"
    "s_and_b32 vcc_lo, vcc_lo, 0xf\n"
    "s_cbranch_scc1 L53_%=\n"
    "L54_%=:\n"
    "v_mfma_f32_16x16x32_bf16 v[44:47], a[160:163], v[36:39], v[44:47]\n"
    "s_add_u32 s84, %[f4c], 1442304\n"
    "v_cndmask_b32_e64 v53, v56, v57, s[48:49]\n"
    "v_cndmask_b32_e64 v53, v53, v58, s[50:51]\n"
    "v_mfma_f32_16x16x32_bf16 v[48:51], a[164:167], v[36:39], v[48:51]\n"
    "s_waitcnt lgkmcnt(1)\n"
    "v_cndmask_b32_e64 v53, v53, v59, s[52:53]\n"
    "s_add_u32 s85, s45, 9568256\n"
    "v_mfma_f32_16x16x32_bf16 v[40:43], a[168:171], v[24:27], v[40:43]\n"
    "ds_write_b64 v8, v[52:53] offset:43008\n"
    "v_mfma_f32_16x16x32_bf16 v[44:47], a[172:175], v[24:27], v[44:47]\n"
    "buffer_load_dwordx4 v[80:83], v0, %[srdw], s83 offen offset:0 sc1\n"
    "ds_read2st64_b64 v[32:35], v4 offset0:74 offset1:75\n"
    "v_mfma_f32_16x16x32_bf16 v[48:51], a[176:179], v[24:27], v[48:51]\n"
    "s_waitcnt lgkmcnt(2)\n"
    "s_mov_b64 exec, 0xf\n"
    "buffer_load_dword v3, v1, %[srdw], s84 offen sc1\n"
    "s_mov_b64 exec, -1\n"
    "v_mfma_f32_16x16x32_bf16 v[40:43], a[180:183], v[28:31], v[40:43]\n"
    "buffer_load_dwordx4 v[84:87], v0, %[srdw], s83 offen offset:1024 sc1\n"
    "ds_read2st64_b64 v[36:39], v4 offset0:76 offset1:77\n"
    "v_mfma_f32_16x16x32_bf16 v[44:47], a[184:187], v[28:31], v[44:47]\n"
    "ds_read_b128 v[52:55], v9 offset:21504\n"
    "ds_read_b128 v[56:59], v9 offset:21520\n"
    "v_mfma_f32_16x16x32_bf16 v[48:51], a[188:191], v[28:31], v[48:51]\n"
    "s_waitcnt lgkmcnt(3)\n"
    "s_add_u32 s86, s46, 9568256\n"
    "buffer_load_dwordx4 v[88:91], v0, %[srdw], s83 offen offset:2048 sc1\n"
    "buffer_load_dwordx4 v[92:95], v0, %[srdw], s85 offen offset:0 sc1\n"
    "buffer_load_dwordx4 v[96:99], v0, %[srdw], s85 offen offset:1024 sc1\n"
    "buffer_load_dwordx4 v[100:103], v0, %[srdw], s85 offen offset:2048 sc1\n"
    "v_mfma_f32_16x16x32_bf16 v[40:43], a[192:195], v[32:35], v[40:43]\n"
    "v_mfma_f32_16x16x32_bf16 v[44:47], a[196:199], v[32:35], v[44:47]\n"
    "v_mfma_f32_16x16x32_bf16 v[48:51], a[200:203], v[32:35], v[48:51]\n"
    "s_waitcnt lgkmcnt(1)\n"
    "s_barrier\n"
    "ds_read2st64_b64 v[24:27], v4 offset0:84 offset1:85\n"
    "v_mfma_f32_16x16x32_bf16 v[40:43], a[204:207], v[36:39], v[40:43]\n"
    "ds_read2st64_b64 v[28:31], v4 offset0:86 offset1:87\n"
    "ds_read2st64_b64 v[32:35], v4 offset0:88 offset1:89\n"
    "v_mfma_f32_16x16x32_bf16 v[44:47], a[208:211], v[36:39], v[44:47]\n"
    "v_add_f32 v52, v52, v54\n"
    "v_add_f32 v53, v53, v55\n"
    "v_mfma_f32_16x16x32_bf16 v[48:51], a[212:215], v[36:39], v[48:51]\n"
    "s_waitcnt lgkmcnt(3)\n"
    "ds_read2st64_b64 v[36:39], v4 offset0:90 offset1:91\n"
    "v_add_f32 v56, v56, v58\n"
    "v_mfma_f32_16x16x32_bf16 v[40:43], a[216:219], v[112:115], v[40:43]\n"
    "v_add_f32 v57, v57, v59\n"
    "v_add_f32 v52, v52, v56\n"
    "v_mfma_f32_16x16x32_bf16 v[44:47], a[220:223], v[112:115], v[44:47]\n"
    "v_add_f32 v53, v53, v57\n"
    "v_mul_f32 v7, 0xbacccccd, v52\n"
    "v_mfma_f32_16x16x32_bf16 v[48:51], a[224:227], v[112:115], v[48:51]\n"
    "v_mul_f32 v53, 0x3acccccd, v53\n"
    "v_fma_f32 v53, -v7, v7, v53\n"
    "v_mfma_f32_16x16x32_bf16 v[40:43], a[228:231], v[116:119], v[40:43]\n"
    "v_max_f32 v53, 0, v53\n"
    "v_add_f32 v73, 0x3727c5ac, v53\n"
    "v_mfma_f32_16x16x32_bf16 v[44:47], a[232:235], v[116:119], v[44:47]\n"
    "v_rsq_f32 v52, v73\n"
    "v_mfma_f32_16x16x32_bf16 v[48:51], a[236:239], v[116:119], v[48:51]\n"
    "s_waitcnt lgkmcnt(3)\n"
    "v_mul_f32 v72, 0xbfb8aa3b, v52\n"
    "v_mov_b32 v20, v124\n"
    "v_fmac_f32_dpp v40, v6, v7 row_newbcast:0 row_mask:0xf bank_mask:0xf\n"
    "v_mfma_f32_16x16x32_bf16 v[60:63], a[0:3], v[24:27], 0\n"
    "s_add_u32 s87, %[hst], 20054016\n"
    "v_mov_b32 v21, v125\n"
    "v_mov_b32 v22, v126\n"
    "v_fmac_f32_dpp v48, v6, v7 row_newbcast:8 row_mask:0xf bank_mask:0xf\n"
    "v_mfma_f32_16x16x32_bf16 v[64:67], a[4:7], v[24:27], 0\n"
    "s_add_u32 s88, s47, 9568256\n"
    "v_mov_b32 v23, v127\n"
    "v_fmac_f32_dpp v49, v6, v7 row_newbcast:9 row_mask:0xf bank_mask:0xf\n"
    "v_fmac_f32_dpp v50, v6, v7 row_newbcast:10 row_mask:0xf bank_mask:0xf\n"
    "v_mfma_f32_16x16x32_bf16 v[68:71], a[8:11], v[24:27], 0\n"
    "s_waitcnt lgkmcnt(2)\n"
    "buffer_load_dwordx4 v[104:107], v0, %[srdw], s86 offen offset:0 sc1\n"
    "v_fmac_f32_dpp v51, v6, v7 row_newbcast:11 row_mask:0xf bank_mask:0xf\n"
    "v_mfma_f32_16x16x32_bf16 v[60:63], a[12:15], v[28:31], v[60:63]\n"
    "s_add_u32 s89, s44, 11141120\n"
    "ds_read2st64_b64 v[24:27], v4 offset0:92 offset1:93\n"
    "v_permlane16_swap_b32 v48, v50\n"
    "v_mfma_f32_16x16x32_bf16 v[64:67], a[16:19], v[28:31], v[64:67]\n"
    "v_permlane16_swap_b32 v49, v51\n"
    "v_fmac_f32_dpp v44, v6, v7 row_newbcast:4 row_mask:0xf bank_mask:0xf\n"
    "v_mfma_f32_16x16x32_bf16 v[68:71], a[20:23], v[28:31], v[68:71]\n"
    "s_waitcnt lgkmcnt(2)\n"
    "buffer_load_dwordx4 v[108:111], v0, %[srdw], s86 offen offset:1024 sc1\n"
    "v_mul_f32 v44, v40, v44\n"
    "v_mfma_f32_16x16x32_bf16 v[60:63], a[24:27], v[32:35], v[60:63]\n"
    "ds_read2st64_b64 v[28:31], v4 offset0:94 offset1:95\n"
    "v_mul_f32 v40, v72, v40\n"
    "v_fmac_f32_dpp v41, v6, v7 row_newbcast:1 row_mask:0xf bank_mask:0xf\n"
    "v_mfma_f32_16x16x32_bf16 v[64:67], a[28:31], v[32:35], v[64:67]\n"
    "v_exp_f32 v40, v40\n"
    "v_fmac_f32_dpp v45, v6, v7 row_newbcast:5 row_mask:0xf bank_mask:0xf\n"
    "v_mfma_f32_16x16x32_bf16 v[68:71], a[32:35], v[32:35], v[68:71]\n"
    "s_waitcnt lgkmcnt(2)\n"
    "buffer_load_dwordx4 v[112:115], v0, %[srdw], s86 offen offset:2048 sc1\n"
    "v_fma_f32 v40, v40, v73, v73\n"
    "v_mfma_f32_16x16x32_bf16 v[60:63], a[36:39], v[36:39], v[60:63]\n"
    "s_add_u32 s90, s45, 11141120\n"
    "ds_read2st64_b64 v[32:35], v4 offset0:96 offset1:97\n"
    "v_rcp_f32 v40, v40\n"
    "v_mfma_f32_16x16x32_bf16 v[64:67], a[40:43], v[36:39], v[64:67]\n"
    "v_mul_f32 v40, v40, v44\n"
    "v_mul_f32 v45, v41, v45\n"
    "v_mul_f32 v41, v72, v41\n"
    "v_mfma_f32_16x16x32_bf16 v[68:71], a[44:47], v[36:39], v[68:71]\n"
    "s_waitcnt lgkmcnt(2)\n"
    "ds_read2st64_b64 v[36:39], v4 offset0:98 offset1:99\n"
    "v_exp_f32 v41, v41\n"
    "v_mfma_f32_16x16x32_bf16 v[60:63], a[48:51], v[24:27], v[60:63]\n"
    "buffer_load_dwordx4 v[116:119], v0, %[srdw], s88 offen offset:0 sc1\n"
    "v_fma_f32 v41, v41, v73, v73\n"
    "v_fmac_f32_dpp v42, v6, v7 row_newbcast:2 row_mask:0xf bank_mask:0xf\n"
    "v_mfma_f32_16x16x32_bf16 v[64:67], a[52:55], v[24:27], v[64:67]\n"
    "v_rcp_f32 v41, v41\n"
    "v_fmac_f32_dpp v46, v6, v7 row_newbcast:6 row_mask:0xf bank_mask:0xf\n"
    "v_mfma_f32_16x16x32_bf16 v[68:71], a[56:59], v[24:27], v[68:71]\n"
    "s_waitcnt lgkmcnt(2)\n"
    "ds_read2st64_b64 v[24:27], v4 offset0:100 offset1:101\n"
    "v_mul_f32 v41, v41, v45\n"
    "v_cvt_pk_bf16_f32 v40, v40, v41\n"
    "v_mfma_f32_16x16x32_bf16 v[60:63], a[60:63], v[28:31], v[60:63]\n"
    "s_add_u32 s91, s46, 11141120\n"
    "buffer_load_dwordx4 v[120:123], v0, %[srdw], s88 offen offset:1024 sc1\n"
    "v_min_u32 v40, -2, v40\n"
    "v_mfma_f32_16x16x32_bf16 v[64:67], a[64:67], v[28:31], v[64:67]\n"
    "v_mul_f32 v46, v42, v46\n"
    "v_mul_f32 v42, v72, v42\n"
    "v_fmac_f32_dpp v43, v6, v7 row_newbcast:3 row_mask:0xf bank_mask:0xf\n"
    "v_mfma_f32_16x16x32_bf16 v[68:71], a[68:71], v[28:31], v[68:71]\n"
    "s_waitcnt lgkmcnt(2)\n"
    "ds_read2st64_b64 v[28:31], v4 offset0:102 offset1:103\n"
    "v_exp_f32 v42, v42\n"
    "v_mfma_f32_16x16x32_bf16 v[60:63], a[72:75], v[32:35], v[60:63]\n"
    "buffer_load_dwordx4 v[124:127], v0, %[srdw], s88 offen offset:2048 sc1\n"
    "v_fma_f32 v42, v42, v73, v73\n"
    "v_fmac_f32_dpp v47, v6, v7 row_newbcast:7 row_mask:0xf bank_mask:0xf\n"
    "v_mfma_f32_16x16x32_bf16 v[64:67], a[76:79], v[32:35], v[64:67]\n"
    "v_rcp_f32 v42, v42\n"
    "v_mul_f32 v47, v43, v47\n"
    "v_mfma_f32_16x16x32_bf16 v[68:71], a[80:83], v[32:35], v[68:71]\n"
    "s_waitcnt lgkmcnt(2)\n"
    "ds_read2st64_b64 v[32:35], v4 offset0:104 offset1:105\n"
    "v_mul_f32 v42, v42, v46\n"
    "v_mul_f32 v43, v72, v43\n"
    "v_mfma_f32_16x16x32_bf16 v[60:63], a[84:87], v[36:39], v[60:63]\n"
    "s_add_u32 s92, s47, 11141120\n"
    "v_exp_f32 v43, v43\n"
    "v_mul_f32 v50, v48, v50\n"
    "v_mfma_f32_16x16x32_bf16 v[64:67], a[88:91], v[36:39], v[64:67]\n"
    "v_fma_f32 v43, v43, v73, v73\n"
    "v_rcp_f32 v43, v43\n"
    "v_mfma_f32_16x16x32_bf16 v[68:71], a[92:95], v[36:39], v[68:71]\n"
    "s_waitcnt lgkmcnt(2)\n"
    "ds_read2st64_b64 v[36:39], v4 offset0:106 offset1:107\n"
    "v_mul_f32 v43, v43, v47\n"
    "v_cvt_pk_bf16_f32 v41, v42, v43\n"
    "v_mfma_f32_16x16x32_bf16 v[60:63], a[96:99], v[24:27], v[60:63]\n"
    "v_min_u32 v41, -2, v41\n"
    "v_mul_f32 v48, v72, v48\n"
    "v_mul_f32 v51, v49, v51\n"
    "v_mfma_f32_16x16x32_bf16 v[64:67], a[100:103], v[24:27], v[64:67]\n"
    "v_exp_f32 v48, v48\n"
    "v_mul_f32 v49, v72, v49\n"
    "v_mfma_f32_16x16x32_bf16 v[68:71], a[104:107], v[24:27], v[68:71]\n"
    "s_waitcnt lgkmcnt(2)\n"
    "ds_read2st64_b64 v[24:27], v4 offset0:108 offset1:109\n"
    "v_fma_f32 v48, v48, v73, v73\n"
    "v_mfma_f32_16x16x32_bf16 v[60:63], a[108:111], v[28:31], v[60:63]\n"
    "v_rcp_f32 v48, v48\n"
    "v_mfma_f32_16x16x32_bf16 v[64:67], a[112:115], v[28:31], v[64:67]\n"
    "v_mul_f32 v48, v48, v50\n"
    "v_exp_f32 v49, v49\n"
    "v_mfma_f32_16x16x32_bf16 v[68:71], a[116:119], v[28:31], v[68:71]\n"
    "s_waitcnt lgkmcnt(2)\n"
    "ds_read2st64_b64 v[28:31], v4 offset0:110 offset1:111\n"
    "v_fma_f32 v49, v49, v73, v73\n"
    "v_mfma_f32_16x16x32_bf16 v[60:63], a[120:123], v[32:35], v[60:63]\n"
    "v_rcp_f32 v49, v49\n"
    "v_mfma_f32_16x16x32_bf16 v[64:67], a[124:127], v[32:35], v[64:67]\n"
    "v_mul_f32 v49, v49, v51\n"
    "v_cvt_pk_bf16_f32 v42, v48, v49\n"
    "v_min_u32 v42, -2, v42\n"
    "v_mfma_f32_16x16x32_bf16 v[68:71], a[128:131], v[32:35], v[68:71]\n"
    "s_waitcnt lgkmcnt(2)\n"
    "ds_read2st64_b64 v[32:35], v4 offset0:112 offset1:113\n"
    "v_permlane32_swap_b32 v41, v43\n"
    "v_mfma_f32_16x16x32_bf16 v[60:63], a[132:135], v[36:39], v[60:63]\n"
    "v_mfma_f32_16x16x32_bf16 v[64:67], a[136:139], v[36:39], v[64:67]\n"
    "v_permlane16_swap_b32 v42, v43\n"
    "v_mfma_f32_16x16x32_bf16 v[68:71], a[140:143], v[36:39], v[68:71]\n"
    "s_waitcnt lgkmcnt(2)\n"
    "ds_read2st64_b64 v[36:39], v4 offset0:114 offset1:115\n"
    "v_permlane16_swap_b32 v40, v41\n"
    "v_mfma_f32_16x16x32_bf16 v[60:63], a[144:147], v[24:27], v[60:63]\n"
    "buffer_store_dwordx4 v[40:43], v10, %[srdw], s87 offen sc1\n"
    "v_mfma_f32_16x16x32_bf16 v[64:67], a[148:151], v[24:27], v[64:67]\n"
    "v_mfma_f32_16x16x32_bf16 v[68:71], a[152:155], v[24:27], v[68:71]\n"
    "s_waitcnt lgkmcnt(2)\n"
    "ds_read2st64_b64 v[24:27], v4 offset0:116 offset1:117\n"
    "v_mfma_f32_16x16x32_bf16 v[60:63], a[156:159], v[28:31], v[60:63]\n"
    "v_mfma_f32_16x16x32_bf16 v[64:67], a[160:163], v[28:31], v[64:67]\n"
    "v_mfma_f32_16x16x32_bf16 v[68:71], a[164:167], v[28:31], v[68:71]\n"
    "s_waitcnt lgkmcnt(2)\n"
    "ds_read2st64_b64 v[28:31], v4 offset0:118 offset1:119\n"
    "v_mfma_f32_16x16x32_bf16 v[60:63], a[168:171], v[32:35], v[60:63]\n"
    "v_mfma_f32_16x16x32_bf16 v[64:67], a[172:175], v[32:35], v[64:67]\n"
    "ds_read_b128 v[52:55], v9 offset:43008\n"
    "ds_read_b128 v[56:59], v9 offset:43024\n"
    "v_mfma_f32_16x16x32_bf16 v[68:71], a[176:179], v[32:35], v[68:71]\n"
    "s_waitcnt lgkmcnt(4) vmcnt(12)\n"
    "v_cmp_ne_u32 vcc, %[tag], v3\n"
    "s_nop 0\n"
    "s_and_b32 vcc_lo, vcc_lo, 0xf\n"
    "s_cbranch_scc1 L57_%=\n"
    "L58_%=:\n"
    "v_mfma_f32_16x16x32_bf16 v[60:63], a[180:183], v[36:39], v[60:63]\n"
    "buffer_load_dwordx4 a[0:3], v0, %[srdw], s89 offen offset:0 sc1\n"
    "ds_read2st64_b64 v[32:35], v4 offset0:120 offset1:121\n"
    "v_mfma_f32_16x16x32_bf16 v[64:67], a[184:187], v[36:39], v[64:67]\n"
    "buffer_load_dwordx4 a[4:7], v0, %[srdw], s89 offen offset:1024 sc1\n"
    "v_mfma_f32_16x16x32_bf16 v[68:71], a[188:191], v[36:39], v[68:71]\n"
    "s_waitcnt lgkmcnt(4)\n"
    "ds_read2st64_b64 v[36:39], v4 offset0:122 offset1:123\n"
    "v_mfma_f32_16x16x32_bf16 v[60:63], a[192:195], v[24:27], v[60:63]\n"
    "s_waitcnt lgkmcnt(3)\n"
    "v_add_f32 v52, v52, v54\n"
    "v_add_f32 v53, v53, v55\n"
    "v_mfma_f32_16x16x32_bf16 v[64:67], a[196:199], v[24:27], v[64:67]\n"
    "s_waitcnt lgkmcnt(2)\n"
    "v_add_f32 v56, v56, v58\n"
    "v_add_f32 v57, v57, v59\n"
    "v_mfma_f32_16x16x32_bf16 v[68:71], a[200:203], v[24:27], v[68:71]\n"
    "buffer_load_dwordx4 a[8:11], v0, %[srdw], s89 offen offset:2048 sc1\n"
    "ds_read_b128 v[24:27], v11 offset:0\n"
    "v_mfma_f32_16x16x32_bf16 v[60:63], a[204:207], v[28:31], v[60:63]\n"
    "v_add_f32 v52, v52, v56\n"
    "v_add_f32 v53, v53, v57\n"
    "v_mfma_f32_16x16x32_bf16 v[64:67], a[208:211], v[28:31], v[64:67]\n"
    "s_add_u32 s56, s44, 18481152\n"
    "v_mul_f32 v3, 0xbacccccd, v52\n"
    "v_mul_f32 v53, 0x3acccccd, v53\n"
    "v_mfma_f32_16x16x32_bf16 v[68:71], a[212:215], v[28:31], v[68:71]\n"
    "s_waitcnt lgkmcnt(2)\n"
    "ds_read_b128 v[28:31], v11 offset:1024\n"
    "v_fma_f32 v53, -v3, v3, v53\n"
    "v_mfma_f32_16x16x32_bf16 v[60:63], a[216:219], v[32:35], v[60:63]\n"
    "buffer_load_dwordx4 a[12:15], v0, %[srdw], s90 offen offset:0 sc1\n"
    "v_max_f32 v53, 0, v53\n"
    "v_mfma_f32_16x16x32_bf16 v[64:67], a[220:223], v[32:35], v[64:67]\n"
    "v_add_f32 v5, 0x3727c5ac, v53\n"
    "v_mfma_f32_16x16x32_bf16 v[68:71], a[224:227], v[32:35], v[68:71]\n"
    "s_waitcnt lgkmcnt(2)\n"
    "ds_read_b128 v[32:35], v11 offset:2048\n"
    "v_mfma_f32_16x16x32_bf16 v[60:63], a[228:231], v[36:39], v[60:63]\n"
    "v_rsq_f32 v52, v5\n"
    "v_mfma_f32_16x16x32_bf16 v[64:67], a[232:235], v[36:39], v[64:67]\n"
    "s_add_u32 s58, s45, 18481152\n"
    "buffer_load_dwordx4 a[16:19], v0, %[srdw], s90 offen offset:1024 sc1\n"
    "v_mfma_f32_16x16x32_bf16 v[68:71], a[236:239], v[36:39], v[68:71]\n"
    "s_add_u32 s57, s43, 20840448\n"
    "ds_read_b128 v[36:39], v11 offset:3072\n"
    "v_mul_f32 v8, 0xbfb8aa3b, v52\n"
    "v_fmac_f32_dpp v60, v6, v3 row_newbcast:0 row_mask:0xf bank_mask:0xf\n"
    "v_fmac_f32_dpp v61, v6, v3 row_newbcast:1 row_mask:0xf bank_mask:0xf\n"
    "v_mfma_f32_16x16x32_bf16 v[40:43], a[240:243], v[80:83], 0\n"
    "s_waitcnt lgkmcnt(3)\n"
    "v_fmac_f32_dpp v64, v6, v3 row_newbcast:4 row_mask:0xf bank_mask:0xf\n"
    "v_fmac_f32_dpp v68, v6, v3 row_newbcast:8 row_mask:0xf bank_mask:0xf\n"
    "v_fmac_f32_dpp v69, v6, v3 row_newbcast:9 row_mask:0xf bank_mask:0xf\n"
    "v_fmac_f32_dpp v70, v6, v3 row_newbcast:10 row_mask:0xf bank_mask:0xf\n"
    "v_mfma_f32_16x16x32_bf16 v[44:47], v[24:27], v[80:83], 0\n"
    "buffer_load_dwordx4 a[20:23], v0, %[srdw], s90 offen offset:2048 sc1\n"
    "ds_read_b128 v[24:27], v11 offset:4096\n"
    "v_fmac_f32_dpp v71, v6, v3 row_newbcast:11 row_mask:0xf bank_mask:0xf\n"
    "v_mul_f32 v64, v60, v64\n"
    "v_mfma_f32_16x16x32_bf16 v[48:51], a[244:247], v[80:83], 0\n"
    "s_waitcnt lgkmcnt(3)\n"
    "v_permlane16_swap_b32 v68, v70\n"
    "v_permlane16_swap_b32 v69, v71\n"
    "v_mfma_f32_16x16x32_bf16 v[52:55], v[28:31], v[80:83], 0\n"
    "ds_read_b128 v[28:31], v11 offset:5120\n"
    "v_mul_f32 v60, v8, v60\n"
    "v_exp_f32 v60, v60\n"
    "v_mfma_f32_16x16x32_bf16 v[56:59], a[248:251], v[80:83], 0\n"
    "s_waitcnt vmcnt(17)\n"
    "buffer_load_dwordx4 a[24:27], v0, %[srdw], s91 offen offset:0 sc1\n"
    "v_fma_f32 v60, v60, v5, v5\n"
    "v_fmac_f32_dpp v65, v6, v3 row_newbcast:5 row_mask:0xf bank_mask:0xf\n"
    "v_mfma_f32_16x16x32_bf16 v[40:43], a[252:255], v[84:87], v[40:43]\n"
    "s_waitcnt lgkmcnt(3)\n"
    "v_rcp_f32 v60, v60\n"
    "v_mul_f32 v65, v61, v65\n"
    "v_mul_f32 v60, v60, v64\n"
    "v_mfma_f32_16x16x32_bf16 v[44:47], v[32:35], v[84:87], v[44:47]\n"
    "s_add_u32 s59, s46, 18481152\n"
    "buffer_load_dwordx4 a[28:31], v0, %[srdw], s91 offen offset:1024 sc1\n"
    "v_mul_f32 v61, v8, v61\n"
    "v_fmac_f32_dpp v62, v6, v3 row_newbcast:2 row_mask:0xf bank_mask:0xf\n"
    "v_mfma_f32_16x16x32_bf16 v[48:51], v[128:131], v[84:87], v[48:51]\n"
    "s_waitcnt lgkmcnt(2)\n"
    "ds_read_b128 v[32:35], v11 offset:6144\n"
    "v_exp_f32 v61, v61\n"
    "v_fmac_f32_dpp v66, v6, v3 row_newbcast:6 row_mask:0xf bank_mask:0xf\n"
    "v_mfma_f32_16x16x32_bf16 v[52:55], v[36:39], v[84:87], v[52:55]\n"
    "ds_read_b128 v[36:39], v11 offset:7168\n"
    "v_fma_f32 v61, v61, v5, v5\n"
    "v_rcp_f32 v61, v61\n"
    "v_mfma_f32_16x16x32_bf16 v[56:59], v[132:135], v[84:87], v[56:59]\n"
    "s_waitcnt vmcnt(18)\n"
    "buffer_load_dwordx4 a[32:35], v0, %[srdw], s91 offen offset:2048 sc1\n"
    "v_mul_f32 v61, v61, v65\n"
    "v_cvt_pk_bf16_f32 v60, v60, v61\n"
    "v_mfma_f32_16x16x32_bf16 v[40:43], v[136:139], v[88:91], v[40:43]\n"
    "s_waitcnt lgkmcnt(3)\n"
    "v_min_u32 v60, -2, v60\n"
    "v_mul_f32 v66, v62, v66\n"
    "v_mul_f32 v62, v8, v62\n"
    "v_fmac_f32_dpp v63, v6, v3 row_newbcast:3 row_mask:0xf bank_mask:0xf\n"
    "v_mfma_f32_16x16x32_bf16 v[44:47], v[24:27], v[88:91], v[44:47]\n"
    "s_add_u32 s60, s47, 18481152\n"
    "ds_read_b128 v[24:27], v11 offset:8192\n"
    "v_exp_f32 v62, v62\n"
    "v_fmac_f32_dpp v67, v6, v3 row_newbcast:7 row_mask:0xf bank_mask:0xf\n"
    "v_mfma_f32_16x16x32_bf16 v[48:51], v[140:143], v[88:91], v[48:51]\n"
    "s_waitcnt lgkmcnt(3)\n"
    "buffer_load_dwordx4 a[36:39], v0, %[srdw], s92 offen offset:0 sc1\n"
    "v_fma_f32 v62, v62, v5, v5\n"
    "v_mul_f32 v67, v63, v67\n"
    "v_mfma_f32_16x16x32_bf16 v[52:55], v[28:31], v[88:91], v[52:55]\n"
    "ds_read_b128 v[28:31], v11 offset:9216\n"
    "v_rcp_f32 v62, v62\n"
    "v_mul_f32 v63, v8, v63\n"
    "v_mfma_f32_16x16x32_bf16 v[56:59], v[144:147], v[88:91], v[56:59]\n"
    "s_waitcnt vmcnt(19)\n"
    "buffer_load_dwordx4 a[40:43], v0, %[srdw], s92 offen offset:1024 sc1\n"
    "v_mul_f32 v62, v62, v66\n"
    "v_mul_f32 v70, v68, v70\n"
    "v_mfma_f32_16x16x32_bf16 v[40:43], v[148:151], v[92:95], v[40:43]\n"
    "s_waitcnt lgkmcnt(3)\n"
    "v_exp_f32 v63, v63\n"
    "v_mul_f32 v68, v8, v68\n"
    "v_fma_f32 v63, v63, v5, v5\n"
    "v_mfma_f32_16x16x32_bf16 v[44:47], v[32:35], v[92:95], v[44:47]\n"
    "ds_read_b128 v[32:35], v11 offset:10240\n"
    "v_rcp_f32 v63, v63\n"
    "v_mul_f32 v71, v69, v71\n"
    "v_mfma_f32_16x16x32_bf16 v[48:51], v[152:155], v[92:95], v[48:51]\n"
    "s_waitcnt lgkmcnt(3)\n"
    "buffer_load_dwordx4 a[44:47], v0, %[srdw], s92 offen offset:2048 sc1\n"
    "v_mul_f32 v63, v63, v67\n"
    "v_cvt_pk_bf16_f32 v61, v62, v63\n"
    "v_mfma_f32_16x16x32_bf16 v[52:55], v[36:39], v[92:95], v[52:55]\n"
    "ds_read_b128 v[36:39], v11 offset:11264\n"
    "v_min_u32 v61, -2, v61\n"
    "v_exp_f32 v68, v68\n"
    "v_mfma_f32_16x16x32_bf16 v[56:59], v[156:159], v[92:95], v[56:59]\n"
    "s_waitcnt vmcnt(20)\n"
    "v_fma_f32 v68, v68, v5, v5\n"
    "v_rcp_f32 v68, v68\n"
    "v_mul_f32 v69, v8, v69\n"
    "v_mfma_f32_16x16x32_bf16 v[40:43], v[160:163], v[96:99], v[40:43]\n"
    "s_waitcnt lgkmcnt(3)\n"
    "buffer_load_dwordx4 a[48:51], v0, %[srdw], s56 offen offset:0 sc1\n"
    "v_mul_f32 v68, v68, v70\n"
    "v_mfma_f32_16x16x32_bf16 v[44:47], v[24:27], v[96:99], v[44:47]\n"
    "ds_read_b128 v[24:27], v11 offset:12288\n"
    "v_exp_f32 v69, v69\n"
    "v_mfma_f32_16x16x32_bf16 v[48:51], v[164:167], v[96:99], v[48:51]\n"
    "s_waitcnt lgkmcnt(3)\n"
    "v_fma_f32 v69, v69, v5, v5\n"
    "v_rcp_f32 v69, v69\n"
    "v_mfma_f32_16x16x32_bf16 v[52:55], v[28:31], v[96:99], v[52:55]\n"
    "buffer_load_dwordx4 a[52:55], v0, %[srdw], s56 offen offset:1024 sc1\n"
    "ds_read_b128 v[28:31], v11 offset:13312\n"
    "v_mul_f32 v69, v69, v71\n"
    "v_cvt_pk_bf16_f32 v62, v68, v69\n"
    "v_mfma_f32_16x16x32_bf16 v[56:59], v[168:171], v[96:99], v[56:59]\n"
    "s_waitcnt vmcnt(21)\n"
    "v_min_u32 v62, -2, v62\n"
    "v_permlane32_swap_b32 v61, v63\n"
    "v_mfma_f32_16x16x32_bf16 v[40:43], v[172:175], v[100:103], v[40:43]\n"
    "s_waitcnt lgkmcnt(3)\n"
    "v_permlane16_swap_b32 v62, v63\n"
    "v_permlane16_swap_b32 v60, v61\n"
    "v_mfma_f32_16x16x32_bf16 v[44:47], v[32:35], v[100:103], v[44:47]\n"
    "buffer_load_dwordx4 a[56:59], v0, %[srdw], s56 offen offset:2048 sc1\n"
    "ds_read_b128 v[32:35], v11 offset:14336\n"
    "v_mfma_f32_16x16x32_bf16 v[48:51], v[176:179], v[100:103], v[48:51]\n"
    "s_waitcnt lgkmcnt(3)\n"
    "buffer_store_dwordx4 v[60:63], v10, %[srdw], s57 offen sc1\n"
    "v_mfma_f32_16x16x32_bf16 v[52:55], v[36:39], v[100:103], v[52:55]\n"
    "ds_read_b128 v[36:39], v11 offset:15360\n"
    "v_mfma_f32_16x16x32_bf16 v[56:59], v[180:183], v[100:103], v[56:59]\n"
    "s_waitcnt vmcnt(22)\n"
    "v_mfma_f32_16x16x32_bf16 v[40:43], v[184:187], v[104:107], v[40:43]\n"
    "s_waitcnt lgkmcnt(3)\n"
    "v_mfma_f32_16x16x32_bf16 v[44:47], v[24:27], v[104:107], v[44:47]\n"
    "ds_read_b128 v[24:27], v11 offset:16384\n"
    "v_mfma_f32_16x16x32_bf16 v[48:51], v[188:191], v[104:107], v[48:51]\n"
    "s_waitcnt lgkmcnt(3)\n"
    "buffer_load_dwordx4 a[60:63], v0, %[srdw], s58 offen offset:0 sc1\n"
    "v_mfma_f32_16x16x32_bf16 v[52:55], v[28:31], v[104:107], v[52:55]\n"
    "ds_read_b128 v[28:31], v11 offset:18432\n"
    "v_mfma_f32_16x16x32_bf16 v[56:59], v[192:195], v[104:107], v[56:59]\n"
    "s_waitcnt vmcnt(22)\n"
    "v_mfma_f32_16x16x32_bf16 v[40:43], v[196:199], v[108:111], v[40:43]\n"
    "s_waitcnt lgkmcnt(3)\n"
    "v_bfe_u32 v3, v0, 9, 1\n"
    "v_lshlrev_b32 v19, 0, v3\n"
    "v_mfma_f32_16x16x32_bf16 v[44:47], v[32:35], v[108:111], v[44:47]\n"
    "buffer_load_dwordx4 a[64:67], v0, %[srdw], s58 offen offset:1024 sc1\n"
    "ds_read_b128 v[32:35], v11 offset:20480\n"
    "v_mfma_f32_16x16x32_bf16 v[48:51], v[200:203], v[108:111], v[48:51]\n"
    "s_waitcnt lgkmcnt(3)\n"
    "v_bfe_u32 v8, v0, 8, 1\n"
    "v_lshl_or_b32 v19, v8, 3, v19\n"
    "v_mfma_f32_16x16x32_bf16 v[52:55], v[36:39], v[108:111], v[52:55]\n"
    "ds_read_b128 v[36:39], v11 offset:22528\n"
    "v_mul_u32_u24 v19, 1280, v19\n"
    "v_mfma_f32_16x16x32_bf16 v[56:59], v[204:207], v[108:111], v[56:59]\n"
    "s_waitcnt vmcnt(22)\n"
    "v_add_u32 v19, %[vo], v19\n"
    "v_bfe_u32 v3, v0, 4, 4\n"
    "v_mfma_f32_16x16x32_bf16 v[40:43], v[208:211], v[112:115], v[40:43]\n"
    "s_waitcnt vmcnt(21)\n"
    "buffer_load_dwordx4 a[68:71], v0, %[srdw], s58 offen offset:2048 sc1\n"
    "v_mfma_f32_16x16x32_bf16 v[40:43], v[220:223], v[116:119], v[40:43]\n"
    "s_waitcnt vmcnt(21)\n"
    "v_lshl_add_u32 v18, v3, 3, v19\n"
    "v_lshl_add_u32 v19, v3, 1, v19\n"
    "v_mfma_f32_16x16x32_bf16 v[40:43], v[232:235], v[120:123], v[40:43]\n"
    "s_waitcnt vmcnt(20)\n"
    "v_add_u32 v19, 0x80, v19\n"
    "v_lshlrev_b32 v60, 16, v76\n"
    "v_mfma_f32_16x16x32_bf16 v[40:43], v[244:247], v[124:127], v[40:43]\n"
    "s_waitcnt lgkmcnt(3)\n"
    "s_mov_b32 exec_lo, 0xeeeeeeee\n"
    "s_mov_b32 exec_hi, 0xeeeeeeee\n"
    "v_mov_b32 v19, 0x7ffffff0\n"
    "s_mov_b64 exec, -1\n"
    "v_mfma_f32_16x16x32_bf16 v[44:47], v[24:27], v[112:115], v[44:47]\n"
    "s_waitcnt lgkmcnt(2)\n"
    "buffer_load_dwordx4 a[72:75], v0, %[srdw], s59 offen offset:0 sc1\n"
    "v_mfma_f32_16x16x32_bf16 v[44:47], v[28:31], v[116:119], v[44:47]\n"
    "s_waitcnt lgkmcnt(1)\n"
    "ds_read_b128 v[24:27], v11 offset:17408\n"
    "ds_read_b128 v[28:31], v11 offset:19456\n"
    "v_mfma_f32_16x16x32_bf16 v[44:47], v[32:35], v[120:123], v[44:47]\n"
    "s_waitcnt lgkmcnt(2)\n"
    "ds_read_b128 v[32:35], v11 offset:21504\n"
    "v_and_b32 v61, 0xffff0000, v76\n"
    "v_mfma_f32_16x16x32_bf16 v[44:47], v[36:39], v[124:127], v[44:47]\n"
    "s_add_u32 s61, s44, 20054016\n"
    "ds_read_b128 v[36:39], v11 offset:23552\n"
    "v_lshlrev_b32 v62, 16, v77\n"
    "v_mfma_f32_16x16x32_bf16 v[48:51], v[212:215], v[112:115], v[48:51]\n"
    "buffer_load_dwordx4 a[76:79], v0, %[srdw], s59 offen offset:1024 sc1\n"
    "v_and_b32 v63, 0xffff0000, v77\n"
    "v_mfma_f32_16x16x32_bf16 v[48:51], v[224:227], v[116:119], v[48:51]\n"
    "ds_write_b128 v12, v[40:43] offset:0\n"
    "v_mfma_f32_16x16x32_bf16 v[48:51], v[236:239], v[120:123], v[48:51]\n"
    "ds_write_b128 v12, v[44:47] offset:64\n"
    "v_mfma_f32_16x16x32_bf16 v[48:51], v[248:251], v[124:127], v[48:51]\n"
    "s_waitcnt lgkmcnt(5)\n"
    "buffer_load_dwordx4 a[80:83], v0, %[srdw], s59 offen offset:2048 sc1\n"
    "v_mfma_f32_16x16x32_bf16 v[52:55], v[24:27], v[112:115], v[52:55]\n"
    "s_waitcnt lgkmcnt(4)\n"
    "ds_read_b128 v[24:27], v11 offset:0\n"
    "v_mfma_f32_16x16x32_bf16 v[52:55], v[28:31], v[116:119], v[52:55]\n"
    "s_waitcnt lgkmcnt(4)\n"
    "ds_read_b128 v[28:31], v11 offset:1024\n"
    "s_add_u32 s62, s45, 20054016\n"
    "v_mfma_f32_16x16x32_bf16 v[52:55], v[32:35], v[120:123], v[52:55]\n"
    "s_waitcnt lgkmcnt(4)\n"
    "buffer_load_dwordx4 a[84:87], v0, %[srdw], s60 offen offset:0 sc1\n"
    "v_mfma_f32_16x16x32_bf16 v[52:55], v[36:39], v[124:127], v[52:55]\n"
    "ds_read_b128 v[32:35], v11 offset:2048\n"
    "ds_read_b128 v[36:39], v11 offset:3072\n"
    "v_mfma_f32_16x16x32_bf16 v[56:59], v[216:219], v[112:115], v[56:59]\n"
    "ds_write_b128 v12, v[48:51] offset:128\n"
    "v_mfma_f32_16x16x32_bf16 v[56:59], v[228:231], v[116:119], v[56:59]\n"
    "buffer_load_dwordx4 a[88:91], v0, %[srdw], s60 offen offset:1024 sc1\n"
    "v_mfma_f32_16x16x32_bf16 v[56:59], v[240:243], v[120:123], v[56:59]\n"
    "v_mfma_f32_16x16x32_bf16 v[56:59], v[252:255], v[124:127], v[56:59]\n"
    "s_waitcnt vmcnt(23)\n"
    "ds_write_b128 v12, v[52:55] offset:192\n"
    "v_mfma_f32_16x16x32_bf16 v[40:43], a[240:243], a[0:3], 0\n"
    "s_waitcnt lgkmcnt(5)\n"
    "s_add_u32 s63, %[xv], 0\n"
    "s_add_u32 s64, s46, 20054016\n"
    "v_mfma_f32_16x16x32_bf16 v[44:47], v[24:27], a[0:3], 0\n"
    "buffer_load_dwordx4 a[92:95], v0, %[srdw], s60 offen offset:2048 sc1\n"
    "ds_read_b128 v[24:27], v11 offset:4096\n"
    "v_mfma_f32_16x16x32_bf16 v[48:51], a[244:247], a[0:3], 0\n"
    "s_waitcnt lgkmcnt(5)\n"
    "ds_write_b128 v12, v[56:59] offset:256\n"
    "v_mfma_f32_16x16x32_bf16 v[52:55], v[28:31], a[0:3], 0\n"
    "ds_read_b128 v[28:31], v11 offset:5120\n"
    "v_mfma_f32_16x16x32_bf16 v[56:59], a[248:251], a[0:3], 0\n"
    "s_waitcnt vmcnt(23)\n"
    "buffer_load_dwordx4 v[80:83], v0, %[srdw], s61 offen offset:0 sc1\n"
    "v_mfma_f32_16x16x32_bf16 v[40:43], a[252:255], a[4:7], v[40:43]\n"
    "s_waitcnt lgkmcnt(6)\n"
    "s_add_u32 s65, s47, 20054016\n"
    "v_mfma_f32_16x16x32_bf16 v[44:47], v[32:35], a[4:7], v[44:47]\n"
    "ds_read_b128 v[32:35], v11 offset:6144\n"
    "v_mfma_f32_16x16x32_bf16 v[48:51], v[128:131], a[4:7], v[48:51]\n"
    "s_waitcnt lgkmcnt(6)\n"
    "v_mfma_f32_16x16x32_bf16 v[52:55], v[36:39], a[4:7], v[52:55]\n"
    "buffer_load_dwordx4 v[84:87], v0, %[srdw], s61 offen offset:1024 sc1\n"
    "ds_read_b128 v[36:39], v11 offset:7168\n"
    "v_mfma_f32_16x16x32_bf16 v[56:59], v[132:135], a[4:7], v[56:59]\n"
    "s_waitcnt vmcnt(24)\n"
    "s_waitcnt lgkmcnt(3)\n"
    "s_barrier\n"
    "v_mfma_f32_16x16x32_bf16 v[40:43], v[136:139], a[8:11], v[40:43]\n"
    "ds_read_b128 v[4:7], v13 offset:0\n"
    "v_mfma_f32_16x16x32_bf16 v[44:47], v[24:27], a[8:11], v[44:47]\n"
    "ds_read_b128 v[24:27], v11 offset:8192\n"
    "v_mfma_f32_16x16x32_bf16 v[48:51], v[140:143], a[8:11], v[48:51]\n"
    "s_waitcnt lgkmcnt(4)\n"
    "buffer_load_dwordx4 v[88:91], v0, %[srdw], s61 offen offset:2048 sc1\n"
    "v_mfma_f32_16x16x32_bf16 v[52:55], v[28:31], a[8:11], v[52:55]\n"
    "ds_read_b128 v[28:31], v11 offset:9216\n"
    "v_mfma_f32_16x16x32_bf16 v[56:59], v[144:147], a[8:11], v[56:59]\n"
    "s_waitcnt vmcnt(24)\n"
    "ds_read_b32 v3, v14 offset:0\n"
    "ds_read_b128 v[64:67], v13 offset:5376\n"
    "v_mfma_f32_16x16x32_bf16 v[40:43], v[148:151], a[12:15], v[40:43]\n"
    "s_waitcnt lgkmcnt(6)\n"
    "ds_read_b32 v8, v14 offset:5376\n"
    "v_mfma_f32_16x16x32_bf16 v[44:47], v[32:35], a[12:15], v[44:47]\n"
    "buffer_load_dwordx4 v[92:95], v0, %[srdw], s62 offen offset:0 sc1\n"
    "ds_read_b128 v[32:35], v11 offset:10240\n"
    "v_mfma_f32_16x16x32_bf16 v[48:51], v[152:155], a[12:15], v[48:51]\n"
    "s_waitcnt lgkmcnt(7)\n"
    "ds_read_b128 v[68:71], v13 offset:10752\n"
    "v_mfma_f32_16x16x32_bf16 v[52:55], v[36:39], a[12:15], v[52:55]\n"
    "ds_read_b128 v[36:39], v11 offset:11264\n"
    "v_mfma_f32_16x16x32_bf16 v[56:59], v[156:159], a[12:15], v[56:59]\n"
    "s_waitcnt vmcnt(24) lgkmcnt(8)\n"
    "ds_read_b32 v9, v14 offset:10752\n"
    "v_add_f32 v60, v60, v4\n"
    "v_mfma_f32_16x16x32_bf16 v[40:43], v[160:163], a[16:19], v[40:43]\n"
    "s_waitcnt lgkmcnt(8)\n"
    "buffer_load_dwordx4 v[96:99], v0, %[srdw], s62 offen offset:1024 sc1\n"
    "v_mfma_f32_16x16x32_bf16 v[44:47], v[24:27], a[16:19], v[44:47]\n"
    "ds_read_b128 v[24:27], v11 offset:12288\n"
    "v_add_f32 v61, v61, v5\n"
    "v_mfma_f32_16x16x32_bf16 v[48:51], v[164:167], a[16:19], v[48:51]\n"
    "s_waitcnt lgkmcnt(8)\n"
    "v_add_f32 v62, v62, v6\n"
    "v_add_f32 v63, v63, v7\n"
    "v_mfma_f32_16x16x32_bf16 v[52:55], v[28:31], a[16:19], v[52:55]\n"
    "s_waitcnt lgkmcnt(7)\n"
    "ds_read_b128 v[28:31], v11 offset:13312\n"
    "v_add_f32 v75, v75, v3\n"
    "v_mfma_f32_16x16x32_bf16 v[56:59], v[168:171], a[16:19], v[56:59]\n"
    "s_waitcnt vmcnt(24)\n"
    "buffer_load_dwordx4 v[100:103], v0, %[srdw], s62 offen offset:2048 sc1\n"
    "v_mfma_f32_16x16x32_bf16 v[40:43], v[172:175], a[20:23], v[40:43]\n"
    "s_waitcnt lgkmcnt(5) vmcnt(18)\n"
    "ds_read_b128 v[4:7], v13 offset:16128\n"
    "v_accvgpr_read_b32 v72, a48\n"
    "v_mfma_f32_16x16x32_bf16 v[44:47], v[32:35], a[20:23], v[44:47]\n"
    "ds_read_b128 v[32:35], v11 offset:14336\n"
    "v_add_f32 v60, v60, v64\n"
    "v_mfma_f32_16x16x32_bf16 v[48:51], v[176:179], a[20:23], v[48:51]\n"
    "s_waitcnt lgkmcnt(5)\n"
    "ds_read_b32 v3, v14 offset:16128\n"
    "v_add_f32 v61, v61, v65\n"
    "v_mfma_f32_16x16x32_bf16 v[52:55], v[36:39], a[20:23], v[52:55]\n"
    "buffer_load_dwordx4 v[104:107], v0, %[srdw], s64 offen offset:0 sc1\n"
    "ds_read_b128 v[36:39], v11 offset:15360\n"
    "v_mfma_f32_16x16x32_bf16 v[56:59], v[180:183], a[20:23], v[56:59]\n"
    "s_waitcnt vmcnt(18)\n"
    "v_accvgpr_read_b32 v73, a52\n"
    "v_max_u32 v78, v72, v73\n"
    "v_mfma_f32_16x16x32_bf16 v[40:43], v[184:187], a[24:27], v[40:43]\n"
    "s_waitcnt lgkmcnt(5)\n"
    "v_add_f32 v62, v62, v66\n"
    "v_add_f32 v63, v63, v67\n"
    "v_mfma_f32_16x16x32_bf16 v[44:47], v[24:27], a[24:27], v[44:47]\n"
    "ds_read_b128 v[24:27], v11 offset:16384\n"
    "v_add_f32 v75, v75, v8\n"
    "v_mfma_f32_16x16x32_bf16 v[48:51], v[188:191], a[24:27], v[48:51]\n"
    "s_waitcnt lgkmcnt(5)\n"
    "buffer_load_dwordx4 v[108:111], v0, %[srdw], s64 offen offset:1024 sc1\n"
    "v_mfma_f32_16x16x32_bf16 v[52:55], v[28:31], a[24:27], v[52:55]\n"
    "s_waitcnt vmcnt(18)\n"
    "ds_read_b128 v[28:31], v11 offset:18432\n"
    "v_accvgpr_read_b32 v72, a56\n"
    "v_mfma_f32_16x16x32_bf16 v[56:59], v[192:195], a[24:27], v[56:59]\n"
    "v_add_f32 v60, v60, v68\n"
    "v_add_f32 v61, v61, v69\n"
    "v_mfma_f32_16x16x32_bf16 v[40:43], v[196:199], a[28:31], v[40:43]\n"
    "s_waitcnt lgkmcnt(4)\n"
    "v_add_f32 v62, v62, v70\n"
    "v_add_f32 v63, v63, v71\n"
    "v_mfma_f32_16x16x32_bf16 v[44:47], v[32:35], a[28:31], v[44:47]\n"
    "buffer_load_dwordx4 v[112:115], v0, %[srdw], s64 offen offset:2048 sc1\n"
    "ds_read_b128 v[32:35], v11 offset:20480\n"
    "v_mfma_f32_16x16x32_bf16 v[48:51], v[200:203], a[28:31], v[48:51]\n"
    "s_waitcnt lgkmcnt(3)\n"
    "v_add_f32 v75, v75, v9\n"
    "v_add_f32 v60, v60, v4\n"
    "v_mfma_f32_16x16x32_bf16 v[52:55], v[36:39], a[28:31], v[52:55]\n"
    "ds_read_b128 v[36:39], v11 offset:22528\n"
    "v_add_f32 v61, v61, v5\n"
    "v_mfma_f32_16x16x32_bf16 v[56:59], v[204:207], a[28:31], v[56:59]\n"
    "kx_a10:\n"
    "s_cbranch_execz kx_ret\n"
    "v_add_f32 v62, v62, v6\n"
    "v_add_f32 v63, v63, v7\n"
    "v_mfma_f32_16x16x32_bf16 v[40:43], v[208:211], a[32:35], v[40:43]\n"
    "buffer_load_dwordx4 v[116:119], v0, %[srdw], s65 offen offset:0 sc1\n"
    "v_add_f32 v75, v75, v3\n"
    "v_mfma_f32_16x16x32_bf16 v[40:43], v[220:223], a[36:39], v[40:43]\n"
    "v_cvt_pk_bf16_f32 v4, v60, v61\n"
    "v_cvt_pk_bf16_f32 v5, v62, v63\n"
    "v_mfma_f32_16x16x32_bf16 v[40:43], v[232:235], a[40:43], v[40:43]\n"
    "v_mov_b32_dpp v7, v75 quad_perm:[1,0,3,2] row_mask:0xf bank_mask:0xf\n"
    "v_cvt_pk_bf16_f32 v6, v75, v7\n"
    "v_mfma_f32_16x16x32_bf16 v[40:43], v[244:247], a[44:47], v[40:43]\n"
    "s_waitcnt lgkmcnt(3)\n"
    "v_mov_b32_dpp v7, v6 quad_perm:[2,2,2,2] row_mask:0xf bank_mask:0xf\n"
    "v_lshlrev_b32 v60, 16, v20\n"
    "v_mfma_f32_16x16x32_bf16 v[44:47], v[24:27], a[32:35], v[44:47]\n"
    "s_waitcnt lgkmcnt(2)\n"
    "buffer_load_dwordx4 v[120:123], v0, %[srdw], s65 offen offset:1024 sc1\n"
    "v_mfma_f32_16x16x32_bf16 v[44:47], v[28:31], a[36:39], v[44:47]\n"
    "s_waitcnt lgkmcnt(1)\n"
    "ds_read_b128 v[24:27], v11 offset:17408\n"
    "ds_read_b128 v[28:31], v11 offset:19456\n"
    "v_mfma_f32_16x16x32_bf16 v[44:47], v[32:35], a[40:43], v[44:47]\n"
    "s_waitcnt lgkmcnt(2)\n"
    "ds_read_b128 v[32:35], v11 offset:21504\n"
    "v_and_b32 v61, 0xffff0000, v20\n"
    "v_mfma_f32_16x16x32_bf16 v[44:47], v[36:39], a[44:47], v[44:47]\n"
    "ds_read_b128 v[36:39], v11 offset:23552\n"
    "v_lshlrev_b32 v62, 16, v21\n"
    "v_mfma_f32_16x16x32_bf16 v[48:51], v[212:215], a[32:35], v[48:51]\n"
    "buffer_load_dwordx4 v[124:127], v0, %[srdw], s65 offen offset:2048 sc1\n"
    "v_and_b32 v63, 0xffff0000, v21\n"
    "v_mfma_f32_16x16x32_bf16 v[48:51], v[224:227], a[36:39], v[48:51]\n"
    "s_waitcnt vmcnt(20)\n"
    "v_accvgpr_read_b32 v73, a60\n"
    "v_max3_u32 v78, v72, v73, v78\n"
    "v_mfma_f32_16x16x32_bf16 v[48:51], v[236:239], a[40:43], v[48:51]\n"
    "s_waitcnt vmcnt(18)\n"
    "v_accvgpr_read_b32 v72, a64\n"
    "v_accvgpr_read_b32 v73, a68\n"
    "v_mfma_f32_16x16x32_bf16 v[48:51], v[248:251], a[44:47], v[48:51]\n"
    "s_waitcnt lgkmcnt(3)\n"
    "v_max3_u32 v78, v72, v73, v78\n"
    "v_mfma_f32_16x16x32_bf16 v[52:55], v[24:27], a[32:35], v[52:55]\n"
    "s_waitcnt lgkmcnt(2) vmcnt(17)\n"
    "ds_read_b128 v[24:27], v11 offset:0\n"
    "v_accvgpr_read_b32 v72, a72\n"
    "v_mfma_f32_16x16x32_bf16 v[52:55], v[28:31], a[36:39], v[52:55]\n"
    "s_waitcnt lgkmcnt(2)\n"
    "ds_read_b128 v[28:31], v11 offset:1024\n"
    "v_mfma_f32_16x16x32_bf16 v[52:55], v[32:35], a[40:43], v[52:55]\n"
    "s_waitcnt lgkmcnt(2)\n"
    "ds_read_b128 v[32:35], v11 offset:2048\n"
    "v_mfma_f32_16x16x32_bf16 v[52:55], v[36:39], a[44:47], v[52:55]\n"
    "ds_read_b128 v[36:39], v11 offset:3072\n"
    "v_mfma_f32_16x16x32_bf16 v[56:59], v[216:219], a[32:35], v[56:59]\n"
    "s_waitcnt vmcnt(16)\n"
    "v_accvgpr_read_b32 v73, a76\n"
    "v_max3_u32 v78, v72, v73, v78\n"
    "v_mfma_f32_16x16x32_bf16 v[56:59], v[228:231], a[36:39], v[56:59]\n"
    "ds_write_b128 v12, v[40:43] offset:21504\n"
    "v_mfma_f32_16x16x32_bf16 v[56:59], v[240:243], a[40:43], v[56:59]\n"
    "s_waitcnt vmcnt(15)\n"
    "ds_write_b128 v12, v[52:55] offset:21696\n"
    "v_mfma_f32_16x16x32_bf16 v[56:59], v[252:255], a[44:47], v[56:59]\n"
    "s_add_u32 s66, %[xv], 655360\n"
    "v_accvgpr_read_b32 v72, a80\n"
    "s_waitcnt vmcnt(14)\n"
    "v_accvgpr_read_b32 v73, a84\n"
    "v_max3_u32 v78, v72, v73, v78\n"
    "ds_write_b128 v12, v[44:47] offset:21568\n"
    "ds_write_b128 v12, v[48:51] offset:21632\n"
    "s_waitcnt vmcnt(13)\n"
    "v_accvgpr_read_b32 v72, a88\n"
    "ds_write_b128 v12, v[56:59] offset:21760\n"
    "buffer_store_dwordx2 v[4:5], v18, %[srdo], s63 offen\n"
    "buffer_store_dwordx2 v[6:7], v19, %[srdo], s63 offen\n"
    "s_waitcnt vmcnt(14)\n"
    "v_accvgpr_read_b32 v73, a92\n"
    "v_max3_u32 v78, v72, v73, v78\n"
    "v_cmp_eq_u32 vcc, -1, v78\n"
    "s_nop 0\n"
    "s_cmp_lg_u64 vcc, 0\n"
    "s_cbranch_scc0 L60_%=\n"
    " L59_%=_l:\n"
    " s_sub_u32 s54, s54, 1\n"
    " s_cselect_b32 s54, 0, s54\n"
    " s_cbranch_scc1 L59_%=_x\n"
    " s_add_u32 s98, s44, 18481152\n"
    " buffer_load_dwordx4 a[48:51], v0, %[srdw], s98 offen offset:0 sc1\n"
    " buffer_load_dwordx4 a[52:55], v0, %[srdw], s98 offen offset:1024 sc1\n"
    " buffer_load_dwordx4 a[56:59], v0, %[srdw], s98 offen offset:2048 sc1\n"
    " s_add_u32 s98, s45, 18481152\n"
    " buffer_load_dwordx4 a[60:63], v0, %[srdw], s98 offen offset:0 sc1\n"
    " buffer_load_dwordx4 a[64:67], v0, %[srdw], s98 offen offset:1024 sc1\n"
    " buffer_load_dwordx4 a[68:71], v0, %[srdw], s98 offen offset:2048 sc1\n"
    " s_add_u32 s98, s46, 18481152\n"
    " buffer_load_dwordx4 a[72:75], v0, %[srdw], s98 offen offset:0 sc1\n"
    " buffer_load_dwordx4 a[76:79], v0, %[srdw], s98 offen offset:1024 sc1\n"
    " buffer_load_dwordx4 a[80:83], v0, %[srdw], s98 offen offset:2048 sc1\n"
    " s_add_u32 s98, s47, 18481152\n"
    " buffer_load_dwordx4 a[84:87], v0, %[srdw], s98 offen offset:0 sc1\n"
    " buffer_load_dwordx4 a[88:91], v0, %[srdw], s98 offen offset:1024 sc1\n"
    " buffer_load_dwordx4 a[92:95], v0, %[srdw], s98 offen offset:2048 sc1\n"
    " s_waitcnt vmcnt(0)\n"
    " v_accvgpr_read_b32 v72, a48\n"
    " s_nop 1\n"
    " v_accvgpr_read_b32 v73, a52\n"
    " s_nop 1\n"
    " v_max_u32 v78, v72, v73\n"
    " s_nop 1\n"
    " v_accvgpr_read_b32 v72, a56\n"
    " s_nop 1\n"
    " v_accvgpr_read_b32 v73, a60\n"
    " s_nop 1\n"
    " v_max3_u32 v78, v72, v73, v78\n"
    " s_nop 1\n"
    " v_accvgpr_read_b32 v72, a64\n"
    " s_nop 1\n"
    " v_accvgpr_read_b32 v73, a68\n"
    " s_nop 1\n"
    " v_max3_u32 v78, v72, v73, v78\n"
    " s_nop 1\n"
    " v_accvgpr_read_b32 v72, a72\n"
    " s_nop 1\n"
    " v_accvgpr_read_b32 v73, a76\n"
    " s_nop 1\n"
    " v_max3_u32 v78, v72, v73, v78\n"
    " s_nop 1\n"
    " v_accvgpr_read_b32 v72, a80\n"
    " s_nop 1\n"
    " v_accvgpr_read_b32 v73, a84\n"
    " s_nop 1\n"
    " v_max3_u32 v78, v72, v73, v78\n"
    " s_nop 1\n"
    " v_accvgpr_read_b32 v72, a88\n"
    " s_nop 1\n"
    " v_accvgpr_read_b32 v73, a92\n"
    " s_nop 1\n"
    " v_max3_u32 v78, v72, v73, v78\n"
    " s_nop 1\n"
    " v_cmp_eq_u32 vcc, -1, v78\n"
    " s_nop 1\n"
    " s_cmp_lg_u64 vcc, 0\n"
    " s_cbranch_scc1 L59_%=_l\n"
    " L59_%=_x:\n"
    "L60_%=:\n"
    "v_mfma_f32_16x16x32_bf16 v[40:43], a[240:243], a[48:51], 0\n"
    "s_waitcnt lgkmcnt(8)\n"
    "s_waitcnt lgkmcnt(0)\n"
    "s_barrier\n"
    "v_mfma_f32_16x16x32_bf16 v[44:47], v[24:27], a[48:51], 0\n"
    "ds_read_b128 v[24:27], v11 offset:4096\n"
    "v_mfma_f32_16x16x32_bf16 v[48:51], a[244:247], a[48:51], 0\n"
    "ds_read_b128 v[4:7], v13 offset:21504\n"
    "v_mfma_f32_16x16x32_bf16 v[52:55], v[28:31], a[48:51], 0\n"
    "ds_read_b128 v[28:31], v11 offset:5120\n"
    "v_mfma_f32_16x16x32_bf16 v[56:59], a[248:251], a[48:51], 0\n"
    "ds_read_b32 v3, v14 offset:21504\n"
    "ds_read_b128 v[64:67], v13 offset:26880\n"
    "v_mfma_f32_16x16x32_bf16 v[40:43], a[252:255], a[52:55], v[40:43]\n"
    "s_waitcnt vmcnt(12)\n"
    "ds_read_b32 v8, v14 offset:26880\n"
    "v_max_u32 v78, v80, v84\n"
    "v_mfma_f32_16x16x32_bf16 v[44:47], v[32:35], a[52:55], v[44:47]\n"
    "ds_read_b128 v[32:35], v11 offset:6144\n"
    "v_mfma_f32_16x16x32_bf16 v[48:51], v[128:131], a[52:55], v[48:51]\n"
    "ds_read_b128 v[68:71], v13 offset:32256\n"
    "v_mfma_f32_16x16x32_bf16 v[52:55], v[36:39], a[52:55], v[52:55]\n"
    "ds_read_b128 v[36:39], v11 offset:7168\n"
    "v_mfma_f32_16x16x32_bf16 v[56:59], v[132:135], a[52:55], v[56:59]\n"
    "s_waitcnt lgkmcnt(7)\n"
    "ds_read_b32 v9, v14 offset:32256\n"
    "v_add_f32 v60, v60, v4\n"
    "v_mfma_f32_16x16x32_bf16 v[40:43], v[136:139], a[56:59], v[40:43]\n"
    "v_add_f32 v61, v61, v5\n"
    "v_add_f32 v62, v62, v6\n"
    "v_mfma_f32_16x16x32_bf16 v[44:47], v[24:27], a[56:59], v[44:47]\n"
    "ds_read_b128 v[24:27], v11 offset:8192\n"
    "v_add_f32 v63, v63, v7\n"
    "v_mfma_f32_16x16x32_bf16 v[48:51], v[140:143], a[56:59], v[48:51]\n"
    "s_waitcnt lgkmcnt(7)\n"
    "ds_read_b128 v[4:7], v13 offset:37632\n"
    "v_add_f32 v79, v79, v3\n"
    "v_mfma_f32_16x16x32_bf16 v[52:55], v[28:31], a[56:59], v[52:55]\n"
    "s_waitcnt vmcnt(10)\n"
    "ds_read_b128 v[28:31], v11 offset:9216\n"
    "v_max3_u32 v78, v88, v92, v78\n"
    "v_mfma_f32_16x16x32_bf16 v[56:59], v[144:147], a[56:59], v[56:59]\n"
    "s_waitcnt lgkmcnt(8)\n"
    "ds_read_b32 v3, v14 offset:37632\n"
    "v_add_f32 v60, v60, v64\n"
    "v_mfma_f32_16x16x32_bf16 v[40:43], v[148:151], a[60:63], v[40:43]\n"
    "s_waitcnt lgkmcnt(7)\n"
    "v_add_f32 v61, v61, v65\n"
    "v_add_f32 v62, v62, v66\n"
    "v_mfma_f32_16x16x32_bf16 v[44:47], v[32:35], a[60:63], v[44:47]\n"
    "ds_read_b128 v[32:35], v11 offset:10240\n"
    "v_add_f32 v63, v63, v67\n"
    "v_mfma_f32_16x16x32_bf16 v[48:51], v[152:155], a[60:63], v[48:51]\n"
    "s_waitcnt lgkmcnt(6)\n"
    "v_add_f32 v79, v79, v8\n"
    "v_add_f32 v60, v60, v68\n"
    "v_mfma_f32_16x16x32_bf16 v[52:55], v[36:39], a[60:63], v[52:55]\n"
    "ds_read_b128 v[36:39], v11 offset:11264\n"
    "v_add_f32 v61, v61, v69\n"
    "v_mfma_f32_16x16x32_bf16 v[56:59], v[156:159], a[60:63], v[56:59]\n"
    "v_add_f32 v62, v62, v70\n"
    "v_add_f32 v63, v63, v71\n"
    "v_mfma_f32_16x16x32_bf16 v[40:43], v[160:163], a[64:67], v[40:43]\n"
    "s_waitcnt lgkmcnt(4)\n"
    "v_add_f32 v79, v79, v9\n"
    "v_add_f32 v60, v60, v4\n"
    "v_mfma_f32_16x16x32_bf16 v[44:47], v[24:27], a[64:67], v[44:47]\n"
    "s_waitcnt vmcnt(8)\n"
    "ds_read_b128 v[24:27], v11 offset:12288\n"
    "v_max3_u32 v78, v96, v100, v78\n"
    "v_mfma_f32_16x16x32_bf16 v[48:51], v[164:167], a[64:67], v[48:51]\n"
    "s_waitcnt lgkmcnt(4)\n"
    "v_add_f32 v61, v61, v5\n"
    "v_add_f32 v62, v62, v6\n"
    "v_mfma_f32_16x16x32_bf16 v[52:55], v[28:31], a[64:67], v[52:55]\n"
    "ds_read_b128 v[28:31], v11 offset:13312\n"
    "v_add_f32 v63, v63, v7\n"
    "v_mfma_f32_16x16x32_bf16 v[56:59], v[168:171], a[64:67], v[56:59]\n"
    "s_waitcnt lgkmcnt(4)\n"
    "v_add_f32 v79, v79, v3\n"
    "v_cvt_pk_bf16_f32 v4, v60, v61\n"
    "v_mfma_f32_16x16x32_bf16 v[40:43], v[172:175], a[68:71], v[40:43]\n"
    "s_waitcnt lgkmcnt(3)\n"
    "v_cvt_pk_bf16_f32 v5, v62, v63\n"
    "v_mov_b32_dpp v7, v79 quad_perm:[1,0,3,2] row_mask:0xf bank_mask:0xf\n"
    "v_mfma_f32_16x16x32_bf16 v[44:47], v[32:35], a[68:71], v[44:47]\n"
    "ds_read_b128 v[32:35], v11 offset:14336\n"
    "v_cvt_pk_bf16_f32 v6, v79, v7\n"
    "v_mfma_f32_16x16x32_bf16 v[48:51], v[176:179], a[68:71], v[48:51]\n"
    "s_waitcnt lgkmcnt(3)\n"
    "v_mov_b32_dpp v7, v6 quad_perm:[2,2,2,2] row_mask:0xf bank_mask:0xf\n"
    "v_mfma_f32_16x16x32_bf16 v[52:55], v[36:39], a[68:71], v[52:55]\n"
    "ds_read_b128 v[36:39], v11 offset:15360\n"
    "v_mfma_f32_16x16x32_bf16 v[56:59], v[180:183], a[68:71], v[56:59]\n"
    "s_waitcnt vmcnt(6)\n"
    "v_max3_u32 v78, v104, v108, v78\n"
    "v_mfma_f32_16x16x32_bf16 v[40:43], v[184:187], a[72:75], v[40:43]\n"
    "s_waitcnt lgkmcnt(3)\n"
    "buffer_store_dwordx2 v[4:5], v18, %[srdo], s66 offen\n"
    "v_mfma_f32_16x16x32_bf16 v[44:47], v[24:27], a[72:75], v[44:47]\n"
    "ds_read_b128 v[24:27], v11 offset:16384\n"
    "v_mfma_f32_16x16x32_bf16 v[48:51], v[188:191], a[72:75], v[48:51]\n"
    "s_waitcnt lgkmcnt(3)\n"
    "buffer_store_dwordx2 v[6:7], v19, %[srdo], s66 offen\n"
    "v_mfma_f32_16x16x32_bf16 v[52:55], v[28:31], a[72:75], v[52:55]\n"
    "ds_read_b128 v[28:31], v11 offset:18432\n"
    "v_mfma_f32_16x16x32_bf16 v[56:59], v[192:195], a[72:75], v[56:59]\n"
    "v_mfma_f32_16x16x32_bf16 v[40:43], v[196:199], a[76:79], v[40:43]\n"
    "s_waitcnt lgkmcnt(3)\n"
    "v_mfma_f32_16x16x32_bf16 v[44:47], v[32:35], a[76:79], v[44:47]\n"
    "ds_read_b128 v[32:35], v11 offset:20480\n"
    "v_mfma_f32_16x16x32_bf16 v[48:51], v[200:203], a[76:79], v[48:51]\n"
    "s_waitcnt lgkmcnt(3) vmcnt(6)\n"
    "v_max3_u32 v78, v112, v116, v78\n"
    "v_mfma_f32_16x16x32_bf16 v[52:55], v[36:39], a[76:79], v[52:55]\n"
    "ds_read_b128 v[36:39], v11 offset:22528\n"
    "v_mfma_f32_16x16x32_bf16 v[56:59], v[204:207], a[76:79], v[56:59]\n"
    "v_lshlrev_b32 v60, 16, v22\n"
    "v_and_b32 v61, 0xffff0000, v22\n"
    "v_mfma_f32_16x16x32_bf16 v[40:43], v[208:211], a[80:83], v[40:43]\n"
    "v_lshlrev_b32 v62, 16, v23\n"
    "v_and_b32 v63, 0xffff0000, v23\n"
    "v_mfma_f32_16x16x32_bf16 v[40:43], v[220:223], a[84:87], v[40:43]\n"
    "v_mfma_f32_16x16x32_bf16 v[40:43], v[232:235], a[88:91], v[40:43]\n"
    "v_mfma_f32_16x16x32_bf16 v[40:43], v[244:247], a[92:95], v[40:43]\n"
    "s_waitcnt lgkmcnt(3)\n"
    "v_mfma_f32_16x16x32_bf16 v[44:47], v[24:27], a[80:83], v[44:47]\n"
    "s_waitcnt lgkmcnt(2)\n"
    "ds_read_b128 v[24:27], v11 offset:17408\n"
    "v_mfma_f32_16x16x32_bf16 v[44:47], v[28:31], a[84:87], v[44:47]\n"
    "s_waitcnt lgkmcnt(2) vmcnt(4)\n"
    "ds_read_b128 v[28:31], v11 offset:19456\n"
    "v_max3_u32 v78, v120, v124, v78\n"
    "v_mfma_f32_16x16x32_bf16 v[44:47], v[32:35], a[88:91], v[44:47]\n"
    "s_waitcnt lgkmcnt(2)\n"
    "ds_read_b128 v[32:35], v11 offset:21504\n"
    "v_mfma_f32_16x16x32_bf16 v[44:47], v[36:39], a[92:95], v[44:47]\n"
    "v_cmp_u_f32 vcc, v40, v40\n"
    "s_nop 0\n"
    "s_cmp_lg_u64 vcc, 0\n"
    "s_cbranch_scc1 L63_%=\n"
    "L64_%=:\n"
    "v_mfma_f32_16x16x32_bf16 v[48:51], v[212:215], a[80:83], v[48:51]\n"
    "ds_read_b128 v[36:39], v11 offset:23552\n"
    "v_mfma_f32_16x16x32_bf16 v[48:51], v[224:227], a[84:87], v[48:51]\n"
    "v_cmp_eq_u32 vcc, -1, v78\n"
    "s_nop 0\n"
    "s_cmp_lg_u64 vcc, 0\n"
    "s_cbranch_scc0 L62_%=\n"
    " L61_%=_l:\n"
    " s_sub_u32 s54, s54, 1\n"
    " s_cselect_b32 s54, 0, s54\n"
    " s_cbranch_scc1 L61_%=_x\n"
    " s_add_u32 s98, s44, 20054016\n"
    " buffer_load_dwordx4 v[80:83], v0, %[srdw], s98 offen offset:0 sc1\n"
    " buffer_load_dwordx4 v[84:87], v0, %[srdw], s98 offen offset:1024 sc1\n"
    " buffer_load_dwordx4 v[88:91], v0, %[srdw], s98 offen offset:2048 sc1\n"
    " s_add_u32 s98, s45, 20054016\n"
    " buffer_load_dwordx4 v[92:95], v0, %[srdw], s98 offen offset:0 sc1\n"
    " buffer_load_dwordx4 v[96:99], v0, %[srdw], s98 offen offset:1024 sc1\n"
    " buffer_load_dwordx4 v[100:103], v0, %[srdw], s98 offen offset:2048 sc1\n"
    " s_add_u32 s98, s46, 20054016\n"
    " buffer_load_dwordx4 v[104:107], v0, %[srdw], s98 offen offset:0 sc1\n"
    " buffer_load_dwordx4 v[108:111], v0, %[srdw], s98 offen offset:1024 sc1\n"
    " buffer_load_dwordx4 v[112:115], v0, %[srdw], s98 offen offset:2048 sc1\n"
    " s_add_u32 s98, s47, 20054016\n"
    " buffer_load_dwordx4 v[116:119], v0, %[srdw], s98 offen offset:0 sc1\n"
    " buffer_load_dwordx4 v[120:123], v0, %[srdw], s98 offen offset:1024 sc1\n"
    " buffer_load_dwordx4 v[124:127], v0, %[srdw], s98 offen offset:2048 sc1\n"
    " s_waitcnt vmcnt(0)\n"
    " v_max_u32 v78, v80, v84\n"
    " s_nop 1\n"
    " v_max3_u32 v78, v88, v92, v78\n"
    " s_nop 1\n"
    " v_max3_u32 v78, v96, v100, v78\n"
    " s_nop 1\n"
    " v_max3_u32 v78, v104, v108, v78\n"
    " s_nop 1\n"
    " v_max3_u32 v78, v112, v116, v78\n"
    " s_nop 1\n"
    " v_max3_u32 v78, v120, v124, v78\n"
    " s_nop 1\n"
    " v_cmp_eq_u32 vcc, -1, v78\n"
    " s_nop 1\n"
    " s_cmp_lg_u64 vcc, 0\n"
    " s_cbranch_scc1 L61_%=_l\n"
    " L61_%=_x:\n"
    "L62_%=:\n"
    "v_mfma_f32_16x16x32_bf16 v[48:51], v[236:239], a[88:91], v[48:51]\n"
    "ds_write_b128 v12, v[40:43] offset:43008\n"
    "v_mfma_f32_16x16x32_bf16 v[48:51], v[248:251], a[92:95], v[48:51]\n"
    "s_waitcnt lgkmcnt(4)\n"
    "ds_write_b128 v12, v[44:47] offset:43072\n"
    "v_mfma_f32_16x16x32_bf16 v[52:55], v[24:27], a[80:83], v[52:55]\n"
    "s_waitcnt lgkmcnt(4)\n"
    "ds_read_b128 v[24:27], v11 offset:0\n"
    "v_mfma_f32_16x16x32_bf16 v[52:55], v[28:31], a[84:87], v[52:55]\n"
    "s_waitcnt lgkmcnt(4)\n"
    "ds_read_b128 v[28:31], v11 offset:1024\n"
    "v_mfma_f32_16x16x32_bf16 v[52:55], v[32:35], a[88:91], v[52:55]\n"
    "s_waitcnt lgkmcnt(4)\n"
    "ds_read_b128 v[32:35], v11 offset:2048\n"
    "v_mfma_f32_16x16x32_bf16 v[52:55], v[36:39], a[92:95], v[52:55]\n"
    "ds_read_b128 v[36:39], v11 offset:3072\n"
    "v_mfma_f32_16x16x32_bf16 v[56:59], v[216:219], a[80:83], v[56:59]\n"
    "ds_write_b128 v12, v[48:51] offset:43136\n"
    "v_mfma_f32_16x16x32_bf16 v[56:59], v[228:231], a[84:87], v[56:59]\n"
    "v_mfma_f32_16x16x32_bf16 v[56:59], v[240:243], a[88:91], v[56:59]\n"
    "v_mfma_f32_16x16x32_bf16 v[56:59], v[252:255], a[92:95], v[56:59]\n"
    "s_add_u32 s67, %[xv], 1310720\n"
    "v_mfma_f32_16x16x32_bf16 v[40:43], a[240:243], v[80:83], 0\n"
    "s_waitcnt lgkmcnt(4)\n"
    "ds_write_b128 v12, v[52:55] offset:43200\n"
    "v_mfma_f32_16x16x32_bf16 v[44:47], v[24:27], v[80:83], 0\n"
    "ds_read_b128 v[24:27], v11 offset:4096\n"
    "v_mfma_f32_16x16x32_bf16 v[48:51], a[244:247], v[80:83], 0\n"
    "s_waitcnt lgkmcnt(5)\n"
    "ds_write_b128 v12, v[56:59] offset:43264\n"
    "v_mfma_f32_16x16x32_bf16 v[52:55], v[28:31], v[80:83], 0\n"
    "ds_read_b128 v[28:31], v11 offset:5120\n"
    "v_mfma_f32_16x16x32_bf16 v[56:59], a[248:251], v[80:83], 0\n"
    "v_mfma_f32_16x16x32_bf16 v[40:43], a[252:255], v[84:87], v[40:43]\n"
    "s_waitcnt lgkmcnt(6)\n"
    "v_mfma_f32_16x16x32_bf16 v[44:47], v[32:35], v[84:87], v[44:47]\n"
    "ds_read_b128 v[32:35], v11 offset:6144\n"
    "v_mfma_f32_16x16x32_bf16 v[48:51], v[128:131], v[84:87], v[48:51]\n"
    "s_waitcnt lgkmcnt(6)\n"
    "v_mfma_f32_16x16x32_bf16 v[52:55], v[36:39], v[84:87], v[52:55]\n"
    "ds_read_b128 v[36:39], v11 offset:7168\n"
    "v_mfma_f32_16x16x32_bf16 v[56:59], v[132:135], v[84:87], v[56:59]\n"
    "s_waitcnt lgkmcnt(3)\n"
    "s_barrier\n"
    "v_mfma_f32_16x16x32_bf16 v[40:43], v[136:139], v[88:91], v[40:43]\n"
    "ds_read_b128 v[4:7], v13 offset:43008\n"
    "v_mfma_f32_16x16x32_bf16 v[44:47], v[24:27], v[88:91], v[44:47]\n"
    "ds_read_b128 v[24:27], v11 offset:8192\n"
    "v_mfma_f32_16x16x32_bf16 v[48:51], v[140:143], v[88:91], v[48:51]\n"
    "s_waitcnt lgkmcnt(4)\n"
    "ds_read_b32 v3, v14 offset:43008\n"
    "ds_read_b128 v[64:67], v13 offset:48384\n"
    "v_mfma_f32_16x16x32_bf16 v[52:55], v[28:31], v[88:91], v[52:55]\n"
    "ds_read_b128 v[28:31], v11 offset:9216\n"
    "v_mfma_f32_16x16x32_bf16 v[56:59], v[144:147], v[88:91], v[56:59]\n"
    "ds_read_b32 v8, v14 offset:48384\n"
    "v_mfma_f32_16x16x32_bf16 v[40:43], v[148:151], v[92:95], v[40:43]\n"
    "s_waitcnt lgkmcnt(7)\n"
    "ds_read_b128 v[68:71], v13 offset:53760\n"
    "v_mfma_f32_16x16x32_bf16 v[44:47], v[32:35], v[92:95], v[44:47]\n"
    "ds_read_b128 v[32:35], v11 offset:10240\n"
    "v_mfma_f32_16x16x32_bf16 v[48:51], v[152:155], v[92:95], v[48:51]\n"
    "s_waitcnt lgkmcnt(7)\n"
    "ds_read_b32 v9, v14 offset:53760\n"
    "v_add_f32 v60, v60, v4\n"
    "v_mfma_f32_16x16x32_bf16 v[52:55], v[36:39], v[92:95], v[52:55]\n"
    "ds_read_b128 v[36:39], v11 offset:11264\n"
    "v_add_f32 v61, v61, v5\n"
    "v_mfma_f32_16x16x32_bf16 v[56:59], v[156:159], v[92:95], v[56:59]\n"
    "v_add_f32 v62, v62, v6\n"
    "v_add_f32 v63, v63, v7\n"
    "v_mfma_f32_16x16x32_bf16 v[40:43], v[160:163], v[96:99], v[40:43]\n"
    "s_waitcnt lgkmcnt(7)\n"
    "ds_read_b128 v[4:7], v13 offset:59136\n"
    "v_add_f32 v74, v74, v3\n"
    "v_mfma_f32_16x16x32_bf16 v[44:47], v[24:27], v[96:99], v[44:47]\n"
    "ds_read_b128 v[24:27], v11 offset:12288\n"
    "v_mfma_f32_16x16x32_bf16 v[48:51], v[164:167], v[96:99], v[48:51]\n"
    "s_waitcnt lgkmcnt(7)\n"
    "ds_read_b32 v3, v14 offset:59136\n"
    "v_add_f32 v60, v60, v64\n"
    "v_mfma_f32_16x16x32_bf16 v[52:55], v[28:31], v[96:99], v[52:55]\n"
    "ds_read_b128 v[28:31], v11 offset:13312\n"
    "v_add_f32 v61, v61, v65\n"
    "v_mfma_f32_16x16x32_bf16 v[56:59], v[168:171], v[96:99], v[56:59]\n"
    "v_add_f32 v62, v62, v66\n"
    "v_add_f32 v63, v63, v67\n"
    "v_mfma_f32_16x16x32_bf16 v[40:43], v[172:175], v[100:103], v[40:43]\n"
    "s_waitcnt lgkmcnt(6)\n"
    "v_add_f32 v74, v74, v8\n"
    "v_add_f32 v60, v60, v68\n"
    "v_mfma_f32_16x16x32_bf16 v[44:47], v[32:35], v[100:103], v[44:47]\n"
    "ds_read_b128 v[32:35], v11 offset:14336\n"
    "v_add_f32 v61, v61, v69\n"
    "v_mfma_f32_16x16x32_bf16 v[48:51], v[176:179], v[100:103], v[48:51]\n"
    "s_waitcnt lgkmcnt(5)\n"
    "v_add_f32 v62, v62, v70\n"
    "v_add_f32 v63, v63, v71\n"
    "v_mfma_f32_16x16x32_bf16 v[52:55], v[36:39], v[100:103], v[52:55]\n"
    "ds_read_b128 v[36:39], v11 offset:15360\n"
    "v_add_f32 v74, v74, v9\n"
    "v_mfma_f32_16x16x32_bf16 v[56:59], v[180:183], v[100:103], v[56:59]\n"
    "s_waitcnt lgkmcnt(5)\n"
    "v_add_f32 v60, v60, v4\n"
    "v_add_f32 v61, v61, v5\n"
    "v_mfma_f32_16x16x32_bf16 v[40:43], v[184:187], v[104:107], v[40:43]\n"
    "s_waitcnt lgkmcnt(4)\n"
    "v_add_f32 v62, v62, v6\n"
    "v_add_f32 v63, v63, v7\n"
    "v_mfma_f32_16x16x32_bf16 v[44:47], v[24:27], v[104:107], v[44:47]\n"
    "s_waitcnt lgkmcnt(3)\n"
    "ds_read_b128 v[24:27], v11 offset:16384\n"
    "v_add_f32 v74, v74, v3\n"
    "v_mfma_f32_16x16x32_bf16 v[48:51], v[188:191], v[104:107], v[48:51]\n"
    "s_waitcnt lgkmcnt(3)\n"
    "v_cvt_pk_bf16_f32 v4, v60, v61\n"
    "v_cvt_pk_bf16_f32 v5, v62, v63\n"
    "v_mfma_f32_16x16x32_bf16 v[52:55], v[28:31], v[104:107], v[52:55]\n"
    "ds_read_b128 v[28:31], v11 offset:18432\n"
    "v_mov_b32_dpp v7, v74 quad_perm:[1,0,3,2] row_mask:0xf bank_mask:0xf\n"
    "v_mfma_f32_16x16x32_bf16 v[56:59], v[192:195], v[104:107], v[56:59]\n"
    "v_cvt_pk_bf16_f32 v6, v74, v7\n"
    "v_mfma_f32_16x16x32_bf16 v[40:43], v[196:199], v[108:111], v[40:43]\n"
    "s_waitcnt lgkmcnt(3)\n"
    "v_mov_b32_dpp v7, v6 quad_perm:[2,2,2,2] row_mask:0xf bank_mask:0xf\n"
    "v_mfma_f32_16x16x32_bf16 v[44:47], v[32:35], v[108:111], v[44:47]\n"
    "ds_read_b128 v[32:35], v11 offset:20480\n"
    "v_mfma_f32_16x16x32_bf16 v[48:51], v[200:203], v[108:111], v[48:51]\n"
    "s_waitcnt lgkmcnt(3)\n"
    "buffer_store_dwordx2 v[4:5], v18, %[srdo], s67 offen\n"
    "v_mfma_f32_16x16x32_bf16 v[52:55], v[36:39], v[108:111], v[52:55]\n"
    "ds_read_b128 v[36:39], v11 offset:22528\n"
    "v_mfma_f32_16x16x32_bf16 v[56:59], v[204:207], v[108:111], v[56:59]\n"
    "buffer_store_dwordx2 v[6:7], v19, %[srdo], s67 offen\n"
    "v_mfma_f32_16x16x32_bf16 v[40:43], v[208:211], v[112:115], v[40:43]\n"
    "v_lshlrev_b32 v60, 16, v16\n"
    "v_and_b32 v61, 0xffff0000, v16\n"
    "v_mfma_f32_16x16x32_bf16 v[40:43], v[220:223], v[116:119], v[40:43]\n"
    "v_lshlrev_b32 v62, 16, v17\n"
    "v_and_b32 v63, 0xffff0000, v17\n"
    "v_mfma_f32_16x16x32_bf16 v[40:43], v[232:235], v[120:123], v[40:43]\n"
    "v_mfma_f32_16x16x32_bf16 v[40:43], v[244:247], v[124:127], v[40:43]\n"
    "s_waitcnt lgkmcnt(3)\n"
    "v_mfma_f32_16x16x32_bf16 v[44:47], v[24:27], v[112:115], v[44:47]\n"
    "s_waitcnt lgkmcnt(2)\n"
    "ds_read_b128 v[24:27], v11 offset:17408\n"
    "v_mfma_f32_16x16x32_bf16 v[44:47], v[28:31], v[116:119], v[44:47]\n"
    "s_waitcnt lgkmcnt(2)\n"
    "ds_read_b128 v[28:31], v11 offset:19456\n"
    "v_mfma_f32_16x16x32_bf16 v[44:47], v[32:35], v[120:123], v[44:47]\n"
    "s_waitcnt lgkmcnt(2)\n"
    "ds_read_b128 v[32:35], v11 offset:21504\n"
    "v_mfma_f32_16x16x32_bf16 v[44:47], v[36:39], v[124:127], v[44:47]\n"
    "v_cmp_u_f32 vcc, v40, v40\n"
    "s_nop 0\n"
    "s_cmp_lg_u64 vcc, 0\n"
    "s_cbranch_scc1 L65_%=\n"
    "L66_%=:\n"
    "v_mfma_f32_16x16x32_bf16 v[48:51], v[212:215], v[112:115], v[48:51]\n"
    "ds_read_b128 v[36:39], v11 offset:23552\n"
    "v_mfma_f32_16x16x32_bf16 v[48:51], v[224:227], v[116:119], v[48:51]\n"
    "ds_write_b128 v12, v[40:43] offset:0\n"
    "v_mfma_f32_16x16x32_bf16 v[48:51], v[236:239], v[120:123], v[48:51]\n"
    "ds_write_b128 v12, v[44:47] offset:64\n"
    "v_mfma_f32_16x16x32_bf16 v[48:51], v[248:251], v[124:127], v[48:51]\n"
    "s_waitcnt lgkmcnt(5)\n"
    "v_mfma_f32_16x16x32_bf16 v[52:55], v[24:27], v[112:115], v[52:55]\n"
    "s_waitcnt lgkmcnt(4)\n"
    "v_mfma_f32_16x16x32_bf16 v[52:55], v[28:31], v[116:119], v[52:55]\n"
    "s_waitcnt lgkmcnt(3)\n"
    "v_mfma_f32_16x16x32_bf16 v[52:55], v[32:35], v[120:123], v[52:55]\n"
    "s_waitcnt lgkmcnt(2)\n"
    "v_mfma_f32_16x16x32_bf16 v[52:55], v[36:39], v[124:127], v[52:55]\n"
    "ds_write_b128 v12, v[48:51] offset:128\n"
    "v_mfma_f32_16x16x32_bf16 v[56:59], v[216:219], v[112:115], v[56:59]\n"
    "v_mfma_f32_16x16x32_bf16 v[56:59], v[228:231], v[116:119], v[56:59]\n"
    "v_mfma_f32_16x16x32_bf16 v[56:59], v[240:243], v[120:123], v[56:59]\n"
    "v_mfma_f32_16x16x32_bf16 v[56:59], v[252:255], v[124:127], v[56:59]\n"
    "s_add_u32 s78, %[xv], 1966080\n"
    "s_nop 1\n"
    "ds_write_b128 v12, v[52:55] offset:192\n"
    "s_nop 3\n"
    "ds_write_b128 v12, v[56:59] offset:256\n"
    "s_waitcnt lgkmcnt(0)\n"
    "s_barrier\n"
    "ds_read_b128 v[88:91], v13 offset:0\n"
    "ds_read_b32 v104, v14 offset:0\n"
    "ds_read_b128 v[92:95], v13 offset:5376\n"
    "ds_read_b32 v105, v14 offset:5376\n"
    "ds_read_b128 v[96:99], v13 offset:10752\n"
    "ds_read_b32 v106, v14 offset:10752\n"
    "ds_read_b128 v[100:103], v13 offset:16128\n"
    "ds_read_b32 v107, v14 offset:16128\n"
    "s_waitcnt lgkmcnt(7)\n"
    "v_add_f32 v60, v60, v88\n"
    "v_add_f32 v61, v61, v89\n"
    "v_add_f32 v62, v62, v90\n"
    "v_add_f32 v63, v63, v91\n"
    "s_waitcnt lgkmcnt(6)\n"
    "v_add_f32 v15, v15, v104\n"
    "s_waitcnt lgkmcnt(5)\n"
    "v_add_f32 v60, v60, v92\n"
    "v_add_f32 v61, v61, v93\n"
    "v_add_f32 v62, v62, v94\n"
    "v_add_f32 v63, v63, v95\n"
    "s_waitcnt lgkmcnt(4)\n"
    "v_add_f32 v15, v15, v105\n"
    "s_waitcnt lgkmcnt(3)\n"
    "v_add_f32 v60, v60, v96\n"
    "v_add_f32 v61, v61, v97\n"
    "v_add_f32 v62, v62, v98\n"
    "v_add_f32 v63, v63, v99\n"
    "s_waitcnt lgkmcnt(2)\n"
    "v_add_f32 v15, v15, v106\n"
    "s_waitcnt lgkmcnt(1)\n"
    "v_add_f32 v60, v60, v100\n"
    "v_add_f32 v61, v61, v101\n"
    "v_add_f32 v62, v62, v102\n"
    "v_add_f32 v63, v63, v103\n"
    "s_waitcnt lgkmcnt(0)\n"
    "v_add_f32 v15, v15, v107\n"
    "v_cvt_pk_bf16_f32 v88, v60, v61\n"
    "v_cvt_pk_bf16_f32 v89, v62, v63\n"
    "v_mov_b32_dpp v91, v15 quad_perm:[1,0,3,2] row_mask:0xf bank_mask:0xf\n"
    "v_cvt_pk_bf16_f32 v90, v15, v91\n"
    "buffer_store_dwordx2 v[88:89], v18, %[srdo], s78 offen\n"
    "s_nop 0\n"
    "v_mov_b32_dpp v91, v90 quad_perm:[2,2,2,2] row_mask:0xf bank_mask:0xf\n"
    "buffer_store_dwordx2 v[90:91], v19, %[srdo], s78 offen\n"
    "s_endpgm\n"
    "LNC_%=:\n"
    "s_cmp_ge_u32 %[dbg], 229376\n"
    "s_cbranch_scc1 LND_%=\n"
    "s_cmp_ge_u32 %[dbg], 196608\n"
    "s_cselect_b32 s77, 0x40000000, 0\n"
    "s_add_u32 s76, %[q], 152605065\n"
    "s_mov_b32 s79, 1400832\n"
    "buffer_load_dword v120, v246, %[srdm], s79 offen\n"
    "s_waitcnt vmcnt(6)\n"
    "ds_write_b128 v250, v[128:131] offset:0\n"
    "s_waitcnt vmcnt(5)\n"
    "ds_write_b128 v250, v[132:135] offset:256\n"
    "s_waitcnt vmcnt(4)\n"
    "ds_write_b128 v250, v[136:139] offset:512\n"
    "s_waitcnt vmcnt(3)\n"
    "ds_write_b128 v250, v[140:143] offset:768\n"
    "s_waitcnt vmcnt(2)\n"
    "ds_write_b128 v250, v[144:147] offset:1024\n"
    "s_mov_b32 s80, 4980736\n"
    "s_mov_b32 s81, 4984832\n"
    "s_mov_b32 s82, 4988928\n"
    "s_mov_b32 s83, 4993024\n"
    "s_mov_b32 s84, 4997120\n"
    "v_mov_b32 v208, 0\n"
    "v_mov_b32 v209, 0\n"
    "v_mov_b32 v210, 0\n"
    "v_mov_b32 v211, 0\n"
    "v_dot2c_f32_bf16 v209, v20, v128\n"
    "s_waitcnt lgkmcnt(0)\n"
    "s_barrier\n"
    "ds_read_b64 v[168:169], v251 offset:0\n"
    "ds_read_b64 v[170:171], v251 offset:10368\n"
    "ds_read_b64 v[172:173], v251 offset:256\n"
    "ds_read_b64 v[174:175], v251 offset:10624\n"
    "ds_read_b64 v[176:177], v251 offset:512\n"
    "ds_read_b64 v[178:179], v251 offset:10880\n"
    "ds_read_b64 v[180:181], v251 offset:768\n"
    "ds_read_b64 v[182:183], v251 offset:11136\n"
    "ds_read_b64 v[184:185], v251 offset:1024\n"
    "ds_read_b64 v[186:187], v251 offset:11392\n"
    "v_dot2c_f32_bf16 v208, v128, v128\n"
    "v_dot2c_f32_bf16 v211, v20, v129\n"
    "v_dot2c_f32_bf16 v210, v129, v129\n"
    "v_dot2c_f32_bf16 v209, v20, v130\n"
    "v_dot2c_f32_bf16 v208, v130, v130\n"
    "v_dot2c_f32_bf16 v211, v20, v131\n"
    "v_dot2c_f32_bf16 v210, v131, v131\n"
    "v_dot2c_f32_bf16 v209, v20, v132\n"
    "v_dot2c_f32_bf16 v208, v132, v132\n"
    "v_dot2c_f32_bf16 v211, v20, v133\n"
    "v_dot2c_f32_bf16 v210, v133, v133\n"
    "v_dot2c_f32_bf16 v209, v20, v134\n"
    "v_dot2c_f32_bf16 v208, v134, v134\n"
    "v_dot2c_f32_bf16 v211, v20, v135\n"
    "v_dot2c_f32_bf16 v210, v135, v135\n"
    "v_dot2c_f32_bf16 v209, v20, v136\n"
    "v_dot2c_f32_bf16 v208, v136, v136\n"
    "v_dot2c_f32_bf16 v211, v20, v137\n"
    "v_dot2c_f32_bf16 v210, v137, v137\n"
    "v_dot2c_f32_bf16 v209, v20, v138\n"
    "v_dot2c_f32_bf16 v208, v138, v138\n"
    "v_dot2c_f32_bf16 v211, v20, v139\n"
    "v_dot2c_f32_bf16 v210, v139, v139\n"
    "s_waitcnt lgkmcnt(8)\n"
    "buffer_store_dwordx4 v[168:171], v255, %[srdw], s80 offen sc1\n"
    "s_waitcnt lgkmcnt(6)\n"
    "buffer_store_dwordx4 v[172:175], v255, %[srdw], s81 offen sc1\n"
    "s_waitcnt lgkmcnt(4)\n"
    "buffer_store_dwordx4 v[176:179], v255, %[srdw], s82 offen sc1\n"
    "s_waitcnt lgkmcnt(2)\n"
    "buffer_store_dwordx4 v[180:183], v255, %[srdw], s83 offen sc1\n"
    "s_waitcnt lgkmcnt(0)\n"
    "buffer_store_dwordx4 v[184:187], v255, %[srdw], s84 offen sc1\n"
    "v_dot2c_f32_bf16 v209, v20, v140\n"
    "v_dot2c_f32_bf16 v208, v140, v140\n"
    "v_dot2c_f32_bf16 v211, v20, v141\n"
    "v_dot2c_f32_bf16 v210, v141, v141\n"
    "v_dot2c_f32_bf16 v209, v20, v142\n"
    "v_dot2c_f32_bf16 v208, v142, v142\n"
    "v_dot2c_f32_bf16 v211, v20, v143\n"
    "v_dot2c_f32_bf16 v210, v143, v143\n"
    "v_dot2c_f32_bf16 v209, v20, v144\n"
    "v_dot2c_f32_bf16 v208, v144, v144\n"
    "v_dot2c_f32_bf16 v211, v20, v145\n"
    "v_dot2c_f32_bf16 v210, v145, v145\n"
    "v_dot2c_f32_bf16 v209, v20, v146\n"
    "v_dot2c_f32_bf16 v208, v146, v146\n"
    "v_dot2c_f32_bf16 v211, v20, v147\n"
    "v_dot2c_f32_bf16 v210, v147, v147\n"
    "s_nop 2\n"
    "v_add_f32 v208, v208, v210\n"
    "v_add_f32 v209, v209, v211\n"
    "s_nop 0\n"
    "v_add_f32_dpp v208, v208, v208 row_ror:8 row_mask:0xf bank_mask:0xf\n"
    "v_add_f32_dpp v209, v209, v209 row_ror:8 row_mask:0xf bank_mask:0xf\n"
    "s_nop 0\n"
    "v_add_f32_dpp v208, v208, v208 row_ror:4 row_mask:0xf bank_mask:0xf\n"
    "v_add_f32_dpp v209, v209, v209 row_ror:4 row_mask:0xf bank_mask:0xf\n"
    "s_nop 0\n"
    "v_add_f32_dpp v208, v208, v208 row_ror:2 row_mask:0xf bank_mask:0xf\n"
    "v_add_f32_dpp v209, v209, v209 row_ror:2 row_mask:0xf bank_mask:0xf\n"
    "s_nop 0\n"
    "v_add_f32_dpp v208, v208, v208 row_ror:1 row_mask:0xf bank_mask:0xf\n"
    "v_add_f32_dpp v209, v209, v209 row_ror:1 row_mask:0xf bank_mask:0xf\n"
    "v_mul_f32 v209, 0xbacccccd, v209\n"
    "v_mul_f32 v208, 0x3acccccd, v208\n"
    "v_fma_f32 v208, -v209, v209, v208\n"
    "v_max_f32 v208, 0, v208\n"
    "v_add_f32 v208, 0x3727c5ac, v208\n"
    "v_rsq_f32 v208, v208\n"
    "s_nop 0\n"
    "v_mul_f32 v209, v209, v208\n"
    "buffer_store_dwordx2 v[208:209], v78, %[srdw], 0 offen offset:0 sc1\n"
    "s_add_u32 m0, s93, 0\n"
    "s_mov_b32 s68, 0\n"
    "buffer_load_dwordx4 v76, %[srdm], s68 offen lds\n"
    "buffer_load_dwordx4 v77, %[srdm], s68 offen offset:1024 lds\n"
    "buffer_load_dwordx4 v247, %[srdm], s68 offen offset:2048 lds\n"
    "s_add_u32 m0, s93, 12288\n"
    "s_mov_b32 s68, 98304\n"
    "buffer_load_dwordx4 v76, %[srdm], s68 offen lds\n"
    "buffer_load_dwordx4 v77, %[srdm], s68 offen offset:1024 lds\n"
    "buffer_load_dwordx4 v247, %[srdm], s68 offen offset:2048 lds\n"
    "s_add_u32 m0, s93, 24576\n"
    "s_mov_b32 s68, 196608\n"
    "buffer_load_dwordx4 v76, %[srdm], s68 offen lds\n"
    "buffer_load_dwordx4 v77, %[srdm], s68 offen offset:1024 lds\n"
    "buffer_load_dwordx4 v247, %[srdm], s68 offen offset:2048 lds\n"
    "v_mov_b32 v216, %[tag]\n"
    "s_andn2_b32 s85, %[p1], 3\n"
    "s_waitcnt vmcnt(9)\n"
    "s_barrier\n"
    "s_and_b32 exec_lo, %[p1], 3\n"
    "s_mov_b32 exec_hi, 0\n"
    "buffer_store_dword v216, v1, %[srdw], s85 offen sc1\n"
    "s_mov_b64 exec, -1\n"
    "s_add_u32 m0, s93, 36864\n"
    "s_mov_b32 s68, 294912\n"
    "buffer_load_dwordx4 v76, %[srdm], s68 offen lds\n"
    "buffer_load_dwordx4 v77, %[srdm], s68 offen offset:1024 lds\n"
    "buffer_load_dwordx4 v247, %[srdm], s68 offen offset:2048 lds\n"
    "s_add_u32 m0, s93, 49152\n"
    "s_mov_b32 s68, 393216\n"
    "buffer_load_dwordx4 v76, %[srdm], s68 offen lds\n"
    "buffer_load_dwordx4 v77, %[srdm], s68 offen offset:1024 lds\n"
    "buffer_load_dwordx4 v247, %[srdm], s68 offen offset:2048 lds\n"
    "buffer_load_dwordx4 v[80:83], v240, s[44:47], 0 offen offset:0 nt\n"
    "buffer_load_dwordx4 v[84:87], v240, s[44:47], 0 offen offset:128 nt\n"
    "buffer_load_dwordx4 v[88:91], v240, s[44:47], 0 offen offset:256 nt\n"
    "buffer_load_dwordx4 v[92:95], v240, s[44:47], 0 offen offset:384 nt\n"
    "buffer_load_dwordx4 v[96:99], v240, s[44:47], 0 offen offset:512 nt\n"
    "s_add_u32 s86, s77, 122880\n"
    "buffer_load_dwordx4 v[148:151], v240, s[44:47], s86 offen offset:0 nt\n"
    "buffer_load_dwordx4 v[152:155], v240, s[44:47], s86 offen offset:128 nt\n"
    "buffer_load_dwordx4 v[156:159], v240, s[44:47], s86 offen offset:256 nt\n"
    "buffer_load_dwordx4 v[160:163], v240, s[44:47], s86 offen offset:384 nt\n"
    "buffer_load_dwordx4 v[164:167], v240, s[44:47], s86 offen offset:512 nt\n"
    "s_mov_b64 exec, 0\n"
    "s_getpc_b64 s[98:99]\n"
    "kx_c1:\n"
    "s_add_u32 s98, s98, kx_r1-kx_c1\n"
    "s_addc_u32 s99, s99, 0\n"
    "s_cmp_eq_u32 %[w1k], 0\n"
    "s_cbranch_scc1 kx_a0\n"
    "s_cmp_eq_u32 %[w1k], 1024\n"
    "s_cbranch_scc1 kx_a1\n"
    "s_cmp_eq_u32 %[w1k], 2048\n"
    "s_cbranch_scc1 kx_a2\n"
    "s_branch kx_a3\n"
    "kx_r1:\n"
    "s_mov_b64 exec, -1\n"
    "s_waitcnt vmcnt(23)\n"
    "s_barrier\n"
    "ds_read_b64_tr_b16 a[0:1], v6 offset:0\n"
    "ds_read_b64_tr_b16 a[2:3], v6 offset:768\n"
    "ds_read_b64_tr_b16 a[4:5], v6 offset:6144\n"
    "ds_read_b64_tr_b16 a[6:7], v6 offset:6912\n"
    "ds_read_b64_tr_b16 a[8:9], v7 offset:0\n"
    "ds_read_b64_tr_b16 a[10:11], v7 offset:768\n"
    "s_waitcnt lgkmcnt(4)\n"
    "v_mfma_f32_16x16x32_bf16 v[24:27], a[0:3], v[20:23], 0\n"
    "s_waitcnt lgkmcnt(2)\n"
    "v_mfma_f32_16x16x32_bf16 v[28:31], a[4:7], v[20:23], 0\n"
    "s_waitcnt lgkmcnt(0)\n"
    "v_mfma_f32_16x16x32_bf16 v[32:35], a[8:11], v[20:23], 0\n"
    "s_waitcnt vmcnt(20)\n"
    "s_barrier\n"
    "ds_read_b64_tr_b16 a[12:13], v6 offset:12288\n"
    "ds_read_b64_tr_b16 a[14:15], v6 offset:13056\n"
    "ds_read_b64_tr_b16 a[16:17], v6 offset:18432\n"
    "ds_read_b64_tr_b16 a[18:19], v6 offset:19200\n"
    "ds_read_b64_tr_b16 a[20:21], v7 offset:12288\n"
    "ds_read_b64_tr_b16 a[22:23], v7 offset:13056\n"
    "s_add_u32 m0, s93, 0\n"
    "s_mov_b32 s68, 491520\n"
    "buffer_load_dwordx4 v76, %[srdm], s68 offen lds\n"
    "buffer_load_dwordx4 v77, %[srdm], s68 offen offset:1024 lds\n"
    "buffer_load_dwordx4 v247, %[srdm], s68 offen offset:2048 lds\n"
    "s_waitcnt lgkmcnt(4)\n"
    "v_mfma_f32_16x16x32_bf16 v[24:27], a[12:15], v[20:23], v[24:27]\n"
    "s_waitcnt lgkmcnt(2)\n"
    "v_mfma_f32_16x16x32_bf16 v[28:31], a[16:19], v[20:23], v[28:31]\n"
    "s_waitcnt lgkmcnt(0)\n"
    "v_mfma_f32_16x16x32_bf16 v[32:35], a[20:23], v[20:23], v[32:35]\n"
    "s_waitcnt vmcnt(20)\n"
    "s_barrier\n"
    "ds_read_b64_tr_b16 a[24:25], v6 offset:24576\n"
    "ds_read_b64_tr_b16 a[26:27], v6 offset:25344\n"
    "ds_read_b64_tr_b16 a[28:29], v6 offset:30720\n"
    "ds_read_b64_tr_b16 a[30:31], v6 offset:31488\n"
    "ds_read_b64_tr_b16 a[32:33], v7 offset:24576\n"
    "ds_read_b64_tr_b16 a[34:35], v7 offset:25344\n"
    "s_add_u32 m0, s93, 12288\n"
    "s_mov_b32 s68, 589824\n"
    "buffer_load_dwordx4 v76, %[srdm], s68 offen lds\n"
    "buffer_load_dwordx4 v77, %[srdm], s68 offen offset:1024 lds\n"
    "buffer_load_dwordx4 v247, %[srdm], s68 offen offset:2048 lds\n"
    "s_waitcnt lgkmcnt(4)\n"
    "v_mfma_f32_16x16x32_bf16 v[24:27], a[24:27], v[20:23], v[24:27]\n"
    "s_waitcnt lgkmcnt(2)\n"
    "v_mfma_f32_16x16x32_bf16 v[28:31], a[28:31], v[20:23], v[28:31]\n"
    "s_waitcnt lgkmcnt(0)\n"
    "v_mfma_f32_16x16x32_bf16 v[32:35], a[32:35], v[20:23], v[32:35]\n"
    "s_waitcnt vmcnt(19)\n"
    "s_barrier\n"
    "ds_read_b64_tr_b16 a[36:37], v6 offset:36864\n"
    "ds_read_b64_tr_b16 a[38:39], v6 offset:37632\n"
    "ds_read_b64_tr_b16 a[40:41], v6 offset:43008\n"
    "ds_read_b64_tr_b16 a[42:43], v6 offset:43776\n"
    "ds_read_b64_tr_b16 a[44:45], v7 offset:36864\n"
    "ds_read_b64_tr_b16 a[46:47], v7 offset:37632\n"
    "s_add_u32 m0, s93, 24576\n"
    "s_mov_b32 s68, 688128\n"
    "buffer_load_dwordx4 v76, %[srdm], s68 offen lds\n"
    "buffer_load_dwordx4 v77, %[srdm], s68 offen offset:1024 lds\n"
    "buffer_load_dwordx4 v247, %[srdm], s68 offen offset:2048 lds\n"
    "s_waitcnt lgkmcnt(4)\n"
    "v_mfma_f32_16x16x32_bf16 v[24:27], a[36:39], v[20:23], v[24:27]\n"
    "s_waitcnt lgkmcnt(2)\n"
    "v_mfma_f32_16x16x32_bf16 v[28:31], a[40:43], v[20:23], v[28:31]\n"
    "s_waitcnt lgkmcnt(0)\n"
    "v_mfma_f32_16x16x32_bf16 v[32:35], a[44:47], v[20:23], v[32:35]\n"
    "s_waitcnt vmcnt(19)\n"
    "s_barrier\n"
    "ds_read_b64_tr_b16 a[48:49], v6 offset:49152\n"
    "ds_read_b64_tr_b16 a[50:51], v6 offset:49920\n"
    "ds_read_b64_tr_b16 a[52:53], v6 offset:55296\n"
    "ds_read_b64_tr_b16 a[54:55], v6 offset:56064\n"
    "ds_read_b64_tr_b16 a[56:57], v7 offset:49152\n"
    "ds_read_b64_tr_b16 a[58:59], v7 offset:49920\n"
    "s_add_u32 m0, s93, 36864\n"
    "s_mov_b32 s68, 786432\n"
    "buffer_load_dwordx4 v76, %[srdm], s68 offen lds\n"
    "buffer_load_dwordx4 v77, %[srdm], s68 offen offset:1024 lds\n"
    "s_waitcnt lgkmcnt(4)\n"
    "v_mfma_f32_16x16x32_bf16 v[24:27], a[48:51], v[20:23], v[24:27]\n"
    "s_waitcnt lgkmcnt(2)\n"
    "v_mfma_f32_16x16x32_bf16 v[28:31], a[52:55], v[20:23], v[28:31]\n"
    "s_waitcnt lgkmcnt(0)\n"
    "v_mfma_f32_16x16x32_bf16 v[32:35], a[56:59], v[20:23], v[32:35]\n"
    "buffer_load_dwordx4 v247, %[srdm], s68 offen offset:2048 lds\n"
    "s_waitcnt vmcnt(9)\n"
    "s_barrier\n"
    "ds_read_b64_tr_b16 a[60:61], v6 offset:0\n"
    "ds_read_b64_tr_b16 a[62:63], v6 offset:768\n"
    "ds_read_b64_tr_b16 a[64:65], v6 offset:6144\n"
    "ds_read_b64_tr_b16 a[66:67], v6 offset:6912\n"
    "ds_read_b64_tr_b16 a[68:69], v7 offset:0\n"
    "ds_read_b64_tr_b16 a[70:71], v7 offset:768\n"
    "s_add_u32 m0, s93, 49152\n"
    "s_mov_b32 s68, 884736\n"
    "buffer_load_dwordx4 v76, %[srdm], s68 offen lds\n"
    "buffer_load_dwordx4 v77, %[srdm], s68 offen offset:1024 lds\n"
    "buffer_load_dwordx4 v247, %[srdm], s68 offen offset:2048 lds\n"
    "s_waitcnt lgkmcnt(4)\n"
    "v_mfma_f32_16x16x32_bf16 v[24:27], a[60:63], v[20:23], v[24:27]\n"
    "s_waitcnt lgkmcnt(2)\n"
    "v_mfma_f32_16x16x32_bf16 v[28:31], a[64:67], v[20:23], v[28:31]\n"
    "s_waitcnt lgkmcnt(0)\n"
    "v_mfma_f32_16x16x32_bf16 v[32:35], a[68:71], v[20:23], v[32:35]\n"
    "s_waitcnt vmcnt(9)\n"
    "s_barrier\n"
    "ds_read_b64_tr_b16 a[72:73], v6 offset:12288\n"
    "ds_read_b64_tr_b16 a[74:75], v6 offset:13056\n"
    "ds_read_b64_tr_b16 a[76:77], v6 offset:18432\n"
    "ds_read_b64_tr_b16 a[78:79], v6 offset:19200\n"
    "ds_read_b64_tr_b16 a[80:81], v7 offset:12288\n"
    "ds_read_b64_tr_b16 a[82:83], v7 offset:13056\n"
    "s_add_u32 m0, s93, 0\n"
    "s_mov_b32 s68, 983040\n"
    "buffer_load_dwordx4 v76, %[srdm], s68 offen lds\n"
    "buffer_load_dwordx4 v77, %[srdm], s68 offen offset:1024 lds\n"
    "buffer_load_dwordx4 v247, %[srdm], s68 offen offset:2048 lds\n"
    "s_waitcnt lgkmcnt(4)\n"
    "v_mfma_f32_16x16x32_bf16 v[24:27], a[72:75], v[20:23], v[24:27]\n"
    "s_waitcnt lgkmcnt(2)\n"
    "v_mfma_f32_16x16x32_bf16 v[28:31], a[76:79], v[20:23], v[28:31]\n"
    "s_waitcnt lgkmcnt(0)\n"
    "v_mfma_f32_16x16x32_bf16 v[32:35], a[80:83], v[20:23], v[32:35]\n"
    "s_waitcnt vmcnt(9)\n"
    "s_barrier\n"
    "ds_read_b64_tr_b16 a[84:85], v6 offset:24576\n"
    "ds_read_b64_tr_b16 a[86:87], v6 offset:25344\n"
    "ds_read_b64_tr_b16 a[88:89], v6 offset:30720\n"
    "ds_read_b64_tr_b16 a[90:91], v6 offset:31488\n"
    "ds_read_b64_tr_b16 a[92:93], v7 offset:24576\n"
    "ds_read_b64_tr_b16 a[94:95], v7 offset:25344\n"
    "s_add_u32 m0, s93, 12288\n"
    "s_mov_b32 s68, 1081344\n"
    "buffer_load_dwordx4 v76, %[srdm], s68 offen lds\n"
    "buffer_load_dwordx4 v77, %[srdm], s68 offen offset:1024 lds\n"
    "buffer_load_dwordx4 v247, %[srdm], s68 offen offset:2048 lds\n"
    "s_waitcnt lgkmcnt(4)\n"
    "v_mfma_f32_16x16x32_bf16 v[24:27], a[84:87], v[20:23], v[24:27]\n"
    "s_waitcnt lgkmcnt(2)\n"
    "v_mfma_f32_16x16x32_bf16 v[28:31], a[88:91], v[20:23], v[28:31]\n"
    "s_waitcnt lgkmcnt(0)\n"
    "v_mfma_f32_16x16x32_bf16 v[32:35], a[92:95], v[20:23], v[32:35]\n"
    "s_waitcnt vmcnt(9)\n"
    "s_barrier\n"
    "ds_read_b64_tr_b16 a[96:97], v6 offset:36864\n"
    "ds_read_b64_tr_b16 a[98:99], v6 offset:37632\n"
    "ds_read_b64_tr_b16 a[100:101], v6 offset:43008\n"
    "ds_read_b64_tr_b16 a[102:103], v6 offset:43776\n"
    "ds_read_b64_tr_b16 a[104:105], v7 offset:36864\n"
    "ds_read_b64_tr_b16 a[106:107], v7 offset:37632\n"
    "s_add_u32 m0, s93, 24576\n"
    "s_mov_b32 s68, 1179648\n"
    "buffer_load_dwordx4 v76, %[srdm], s68 offen lds\n"
    "buffer_load_dwordx4 v77, %[srdm], s68 offen offset:1024 lds\n"
    "buffer_load_dwordx4 v247, %[srdm], s68 offen offset:2048 lds\n"
    "s_waitcnt lgkmcnt(4)\n"
    "v_mfma_f32_16x16x32_bf16 v[24:27], a[96:99], v[20:23], v[24:27]\n"
    "s_waitcnt lgkmcnt(2)\n"
    "v_mfma_f32_16x16x32_bf16 v[28:31], a[100:103], v[20:23], v[28:31]\n"
    "s_waitcnt lgkmcnt(0)\n"
    "v_mfma_f32_16x16x32_bf16 v[32:35], a[104:107], v[20:23], v[32:35]\n"
    "s_waitcnt vmcnt(9)\n"
    "s_barrier\n"
    "ds_read_b64_tr_b16 a[108:109], v6 offset:49152\n"
    "ds_read_b64_tr_b16 a[110:111], v6 offset:49920\n"
    "ds_read_b64_tr_b16 a[112:113], v6 offset:55296\n"
    "ds_read_b64_tr_b16 a[114:115], v6 offset:56064\n"
    "ds_read_b64_tr_b16 a[116:117], v7 offset:49152\n"
    "ds_read_b64_tr_b16 a[118:119], v7 offset:49920\n"
    "s_add_u32 m0, s93, 36864\n"
    "s_mov_b32 s68, 1277952\n"
    "buffer_load_dwordx4 v76, %[srdm], s68 offen lds\n"
    "buffer_load_dwordx4 v77, %[srdm], s68 offen offset:1024 lds\n"
    "s_waitcnt lgkmcnt(4)\n"
    "v_mfma_f32_16x16x32_bf16 v[24:27], a[108:111], v[20:23], v[24:27]\n"
    "s_waitcnt lgkmcnt(2)\n"
    "v_mfma_f32_16x16x32_bf16 v[28:31], a[112:115], v[20:23], v[28:31]\n"
    "s_waitcnt lgkmcnt(0)\n"
    "v_mfma_f32_16x16x32_bf16 v[32:35], a[116:119], v[20:23], v[32:35]\n"
    "buffer_load_dwordx4 v247, %[srdm], s68 offen offset:2048 lds\n"
    "s_waitcnt vmcnt(9)\n"
    "s_barrier\n"
    "ds_read_b64_tr_b16 a[120:121], v6 offset:0\n"
    "ds_read_b64_tr_b16 a[122:123], v6 offset:768\n"
    "ds_read_b64_tr_b16 a[124:125], v6 offset:6144\n"
    "ds_read_b64_tr_b16 a[126:127], v6 offset:6912\n"
    "ds_read_b64_tr_b16 a[128:129], v7 offset:0\n"
    "ds_read_b64_tr_b16 a[130:131], v7 offset:768\n"
    "s_add_u32 m0, s93, 49152\n"
    "s_mov_b32 s68, 1376256\n"
    "buffer_load_dwordx4 v76, %[srdm], s68 offen lds\n"
    "buffer_load_dwordx4 v77, %[srdm], s68 offen offset:1024 lds\n"
    "s_waitcnt lgkmcnt(4)\n"
    "v_mfma_f32_16x16x32_bf16 v[24:27], a[120:123], v[20:23], v[24:27]\n"
    "s_waitcnt lgkmcnt(2)\n"
    "v_mfma_f32_16x16x32_bf16 v[28:31], a[124:127], v[20:23], v[28:31]\n"
    "s_waitcnt lgkmcnt(0)\n"
    "v_mfma_f32_16x16x32_bf16 v[32:35], a[128:131], v[20:23], v[32:35]\n"
    "buffer_load_dwordx4 v247, %[srdm], s68 offen offset:2048 lds\n"
    "s_waitcnt vmcnt(9)\n"
    "s_barrier\n"
    "ds_read_b64_tr_b16 a[132:133], v6 offset:12288\n"
    "ds_read_b64_tr_b16 a[134:135], v6 offset:13056\n"
    "ds_read_b64_tr_b16 a[136:137], v6 offset:18432\n"
    "ds_read_b64_tr_b16 a[138:139], v6 offset:19200\n"
    "ds_read_b64_tr_b16 a[140:141], v7 offset:12288\n"
    "ds_read_b64_tr_b16 a[142:143], v7 offset:13056\n"
    "s_add_u32 m0, s93, 0\n"
    "s_mov_b32 s68, 1474560\n"
    "buffer_load_dwordx4 v76, %[srdm], s68 offen lds\n"
    "s_waitcnt lgkmcnt(4)\n"
    "v_mfma_f32_16x16x32_bf16 v[24:27], a[132:135], v[20:23], v[24:27]\n"
    "s_waitcnt lgkmcnt(2)\n"
    "v_mfma_f32_16x16x32_bf16 v[28:31], a[136:139], v[20:23], v[28:31]\n"
    "s_waitcnt lgkmcnt(0)\n"
    "v_mfma_f32_16x16x32_bf16 v[32:35], a[140:143], v[20:23], v[32:35]\n"
    "buffer_load_dwordx4 v77, %[srdm], s68 offen offset:1024 lds\n"
    "buffer_load_dwordx4 v247, %[srdm], s68 offen offset:2048 lds\n"
    "s_waitcnt vmcnt(9)\n"
    "s_barrier\n"
    "ds_read_b64_tr_b16 a[144:145], v6 offset:24576\n"
    "ds_read_b64_tr_b16 a[146:147], v6 offset:25344\n"
    "ds_read_b64_tr_b16 a[148:149], v6 offset:30720\n"
    "ds_read_b64_tr_b16 a[150:151], v6 offset:31488\n"
    "ds_read_b64_tr_b16 a[152:153], v7 offset:24576\n"
    "ds_read_b64_tr_b16 a[154:155], v7 offset:25344\n"
    "s_add_u32 m0, s93, 12288\n"
    "s_mov_b32 s68, 1572864\n"
    "buffer_load_dwordx4 v76, %[srdm], s68 offen lds\n"
    "s_waitcnt lgkmcnt(4)\n"
    "v_mfma_f32_16x16x32_bf16 v[24:27], a[144:147], v[20:23], v[24:27]\n"
    "s_waitcnt lgkmcnt(2)\n"
    "v_mfma_f32_16x16x32_bf16 v[28:31], a[148:151], v[20:23], v[28:31]\n"
    "s_waitcnt lgkmcnt(0)\n"
    "v_mfma_f32_16x16x32_bf16 v[32:35], a[152:155], v[20:23], v[32:35]\n"
    "buffer_load_dwordx4 v77, %[srdm], s68 offen offset:1024 lds\n"
    "buffer_load_dwordx4 v247, %[srdm], s68 offen offset:2048 lds\n"
    "s_waitcnt vmcnt(9)\n"
    "s_barrier\n"
    "ds_read_b64_tr_b16 a[156:157], v6 offset:36864\n"
    "ds_read_b64_tr_b16 a[158:159], v6 offset:37632\n"
    "ds_read_b64_tr_b16 a[160:161], v6 offset:43008\n"
    "ds_read_b64_tr_b16 a[162:163], v6 offset:43776\n"
    "ds_read_b64_tr_b16 a[164:165], v7 offset:36864\n"
    "ds_read_b64_tr_b16 a[166:167], v7 offset:37632\n"
    "s_add_u32 m0, s93, 24576\n"
    "s_mov_b32 s68, 1671168\n"
    "buffer_load_dwordx4 v76, %[srdm], s68 offen lds\n"
    "s_waitcnt lgkmcnt(4)\n"
    "v_mfma_f32_16x16x32_bf16 v[24:27], a[156:159], v[20:23], v[24:27]\n"
    "s_waitcnt lgkmcnt(2)\n"
    "v_mfma_f32_16x16x32_bf16 v[28:31], a[160:163], v[20:23], v[28:31]\n"
    "s_waitcnt lgkmcnt(0)\n"
    "v_mfma_f32_16x16x32_bf16 v[32:35], a[164:167], v[20:23], v[32:35]\n"
    "buffer_load_dwordx4 v77, %[srdm], s68 offen offset:1024 lds\n"
    "buffer_load_dwordx4 v247, %[srdm], s68 offen offset:2048 lds\n"
    "s_waitcnt vmcnt(9)\n"
    "s_barrier\n"
    "ds_read_b64_tr_b16 a[168:169], v6 offset:49152\n"
    "ds_read_b64_tr_b16 a[170:171], v6 offset:49920\n"
    "ds_read_b64_tr_b16 a[172:173], v6 offset:55296\n"
    "ds_read_b64_tr_b16 a[174:175], v6 offset:56064\n"
    "ds_read_b64_tr_b16 a[176:177], v7 offset:49152\n"
    "ds_read_b64_tr_b16 a[178:179], v7 offset:49920\n"
    "s_add_u32 m0, s93, 36864\n"
    "s_mov_b32 s68, 1769472\n"
    "buffer_load_dwordx4 v76, %[srdm], s68 offen lds\n"
    "s_waitcnt lgkmcnt(4)\n"
    "v_mfma_f32_16x16x32_bf16 v[24:27], a[168:171], v[20:23], v[24:27]\n"
    "s_waitcnt lgkmcnt(2)\n"
    "v_mfma_f32_16x16x32_bf16 v[28:31], a[172:175], v[20:23], v[28:31]\n"
    "s_waitcnt lgkmcnt(0)\n"
    "v_mfma_f32_16x16x32_bf16 v[32:35], a[176:179], v[20:23], v[32:35]\n"
    "buffer_load_dwordx4 v77, %[srdm], s68 offen offset:1024 lds\n"
    "buffer_load_dwordx4 v247, %[srdm], s68 offen offset:2048 lds\n"
    "ds_write_b128 v241, v[80:83] offset:0\n"
    "ds_write_b128 v241, v[84:87] offset:128\n"
    "ds_write_b128 v241, v[88:91] offset:256\n"
    "ds_write_b128 v241, v[92:95] offset:384\n"
    "ds_write_b128 v241, v[96:99] offset:512\n"
    "ds_write_b128 v241, v[148:151] offset:25600\n"
    "ds_write_b128 v241, v[152:155] offset:25728\n"
    "ds_write_b128 v241, v[156:159] offset:25856\n"
    "ds_write_b128 v241, v[160:163] offset:25984\n"
    "ds_write_b128 v241, v[164:167] offset:26112\n"
    "s_waitcnt lgkmcnt(0) vmcnt(9)\n"
    "s_barrier\n"
    "ds_read_b64_tr_b16 a[180:181], v6 offset:0\n"
    "ds_read_b64_tr_b16 a[182:183], v6 offset:768\n"
    "ds_read_b64_tr_b16 a[184:185], v6 offset:6144\n"
    "ds_read_b64_tr_b16 a[186:187], v6 offset:6912\n"
    "ds_read_b64_tr_b16 a[188:189], v7 offset:0\n"
    "ds_read_b64_tr_b16 a[190:191], v7 offset:768\n"
    "s_add_u32 m0, s93, 49152\n"
    "s_mov_b32 s68, 1867776\n"
    "buffer_load_dwordx4 v76, %[srdm], s68 offen lds\n"
    "buffer_load_dwordx4 v77, %[srdm], s68 offen offset:1024 lds\n"
    "buffer_load_dwordx4 v247, %[srdm], s68 offen offset:2048 lds\n"
    "s_waitcnt lgkmcnt(4)\n"
    "v_mfma_f32_16x16x32_bf16 v[24:27], a[180:183], v[20:23], v[24:27]\n"
    "s_waitcnt lgkmcnt(2)\n"
    "v_mfma_f32_16x16x32_bf16 v[28:31], a[184:187], v[20:23], v[28:31]\n"
    "s_waitcnt lgkmcnt(0)\n"
    "v_mfma_f32_16x16x32_bf16 v[32:35], a[188:191], v[20:23], v[32:35]\n"
    "ds_read_b64_tr_b16 v[100:101], v242 offset:0\n"
    "ds_read_b64_tr_b16 v[102:103], v242 offset:3200\n"
    "ds_read_b64_tr_b16 v[104:105], v242 offset:32\n"
    "ds_read_b64_tr_b16 v[106:107], v242 offset:3232\n"
    "ds_read_b64_tr_b16 v[108:109], v242 offset:64\n"
    "ds_read_b64_tr_b16 v[110:111], v242 offset:3264\n"
    "ds_read_b64_tr_b16 v[112:113], v242 offset:96\n"
    "ds_read_b64_tr_b16 v[114:115], v242 offset:3296\n"
    "ds_read_b64_tr_b16 v[116:117], v242 offset:128\n"
    "ds_read_b64_tr_b16 v[118:119], v242 offset:3328\n"
    "s_bfe_u32 s87, %[q], 0x60000\n"
    "s_lshl_b32 s87, s87, 10\n"
    "s_add_u32 s87, s87, %[yst]\n"
    "s_bfe_u32 s88, %[q], 0x60006\n"
    "s_lshl_b32 s88, s88, 10\n"
    "s_add_u32 s88, s88, %[yst]\n"
    "s_bfe_u32 s89, %[q], 0x6000c\n"
    "s_lshl_b32 s89, s89, 10\n"
    "s_add_u32 s89, s89, %[yst]\n"
    "s_bfe_u32 s90, %[q], 0x60012\n"
    "s_lshl_b32 s90, s90, 10\n"
    "s_add_u32 s90, s90, %[yst]\n"
    "s_bfe_u32 s91, %[q], 0x60018\n"
    "s_lshl_b32 s91, s91, 10\n"
    "s_add_u32 s91, s91, %[yst]\n"
    "s_waitcnt lgkmcnt(8)\n"
    "buffer_store_dwordx4 v[100:103], v0, %[srdw], s87 offen sc1\n"
    "s_waitcnt lgkmcnt(6)\n"
    "buffer_store_dwordx4 v[104:107], v0, %[srdw], s88 offen sc1\n"
    "s_waitcnt lgkmcnt(4)\n"
    "buffer_store_dwordx4 v[108:111], v0, %[srdw], s89 offen sc1\n"
    "s_waitcnt lgkmcnt(2)\n"
    "buffer_store_dwordx4 v[112:115], v0, %[srdw], s90 offen sc1\n"
    "s_waitcnt lgkmcnt(0)\n"
    "buffer_store_dwordx4 v[116:119], v0, %[srdw], s91 offen sc1\n"
    "ds_read_b64_tr_b16 v[168:169], v242 offset:25600\n"
    "ds_read_b64_tr_b16 v[170:171], v242 offset:28800\n"
    "ds_read_b64_tr_b16 v[172:173], v242 offset:25632\n"
    "ds_read_b64_tr_b16 v[174:175], v242 offset:28832\n"
    "ds_read_b64_tr_b16 v[176:177], v242 offset:25664\n"
    "ds_read_b64_tr_b16 v[178:179], v242 offset:28864\n"
    "ds_read_b64_tr_b16 v[180:181], v242 offset:25696\n"
    "ds_read_b64_tr_b16 v[182:183], v242 offset:28896\n"
    "ds_read_b64_tr_b16 v[184:185], v242 offset:25728\n"
    "ds_read_b64_tr_b16 v[186:187], v242 offset:28928\n"
    "s_add_u32 s92, %[yst], s77\n"
    "s_bfe_u32 s56, s76, 0x60000\n"
    "s_lshl_b32 s56, s56, 10\n"
    "s_add_u32 s56, s56, s92\n"
    "s_bfe_u32 s57, s76, 0x60006\n"
    "s_lshl_b32 s57, s57, 10\n"
    "s_add_u32 s57, s57, s92\n"
    "s_bfe_u32 s58, s76, 0x6000c\n"
    "s_lshl_b32 s58, s58, 10\n"
    "s_add_u32 s58, s58, s92\n"
    "s_bfe_u32 s59, s76, 0x60012\n"
    "s_lshl_b32 s59, s59, 10\n"
    "s_add_u32 s59, s59, s92\n"
    "s_bfe_u32 s60, s76, 0x60018\n"
    "s_lshl_b32 s60, s60, 10\n"
    "s_add_u32 s60, s60, s92\n"
    "s_waitcnt lgkmcnt(8)\n"
    "buffer_store_dwordx4 v[168:171], v0, %[srdw], s56 offen sc1\n"
    "s_waitcnt lgkmcnt(6)\n"
    "buffer_store_dwordx4 v[172:175], v0, %[srdw], s57 offen sc1\n"
    "s_waitcnt lgkmcnt(4)\n"
    "buffer_store_dwordx4 v[176:179], v0, %[srdw], s58 offen sc1\n"
    "s_waitcnt lgkmcnt(2)\n"
    "buffer_store_dwordx4 v[180:183], v0, %[srdw], s59 offen sc1\n"
    "s_waitcnt lgkmcnt(0)\n"
    "buffer_store_dwordx4 v[184:187], v0, %[srdw], s60 offen sc1\n"
    "s_waitcnt vmcnt(19)\n"
    "s_barrier\n"
    "ds_read_b64_tr_b16 a[192:193], v6 offset:12288\n"
    "ds_read_b64_tr_b16 a[194:195], v6 offset:13056\n"
    "ds_read_b64_tr_b16 a[196:197], v6 offset:18432\n"
    "ds_read_b64_tr_b16 a[198:199], v6 offset:19200\n"
    "ds_read_b64_tr_b16 a[200:201], v7 offset:12288\n"
    "ds_read_b64_tr_b16 a[202:203], v7 offset:13056\n"
    "s_waitcnt lgkmcnt(4)\n"
    "v_mfma_f32_16x16x32_bf16 v[24:27], a[192:195], v[20:23], v[24:27]\n"
    "s_waitcnt lgkmcnt(2)\n"
    "v_mfma_f32_16x16x32_bf16 v[28:31], a[196:199], v[20:23], v[28:31]\n"
    "s_waitcnt lgkmcnt(0)\n"
    "v_mfma_f32_16x16x32_bf16 v[32:35], a[200:203], v[20:23], v[32:35]\n"
    "s_waitcnt vmcnt(16)\n"
    "s_barrier\n"
    "ds_read_b64_tr_b16 a[204:205], v6 offset:24576\n"
    "ds_read_b64_tr_b16 a[206:207], v6 offset:25344\n"
    "ds_read_b64_tr_b16 a[208:209], v6 offset:30720\n"
    "ds_read_b64_tr_b16 a[210:211], v6 offset:31488\n"
    "ds_read_b64_tr_b16 a[212:213], v7 offset:24576\n"
    "ds_read_b64_tr_b16 a[214:215], v7 offset:25344\n"
    "s_waitcnt lgkmcnt(4)\n"
    "v_mfma_f32_16x16x32_bf16 v[24:27], a[204:207], v[20:23], v[24:27]\n"
    "s_waitcnt lgkmcnt(2)\n"
    "v_mfma_f32_16x16x32_bf16 v[28:31], a[208:211], v[20:23], v[28:31]\n"
    "s_waitcnt lgkmcnt(0)\n"
    "v_mfma_f32_16x16x32_bf16 v[32:35], a[212:215], v[20:23], v[32:35]\n"
    "s_waitcnt vmcnt(13)\n"
    "s_barrier\n"
    "ds_read_b64_tr_b16 a[216:217], v6 offset:36864\n"
    "ds_read_b64_tr_b16 a[218:219], v6 offset:37632\n"
    "ds_read_b64_tr_b16 a[220:221], v6 offset:43008\n"
    "ds_read_b64_tr_b16 a[222:223], v6 offset:43776\n"
    "ds_read_b64_tr_b16 a[224:225], v7 offset:36864\n"
    "ds_read_b64_tr_b16 a[226:227], v7 offset:37632\n"
    "s_waitcnt lgkmcnt(4)\n"
    "v_mfma_f32_16x16x32_bf16 v[24:27], a[216:219], v[20:23], v[24:27]\n"
    "s_waitcnt lgkmcnt(2)\n"
    "v_mfma_f32_16x16x32_bf16 v[28:31], a[220:223], v[20:23], v[28:31]\n"
    "s_waitcnt lgkmcnt(0)\n"
    "v_mfma_f32_16x16x32_bf16 v[32:35], a[224:227], v[20:23], v[32:35]\n"
    "s_waitcnt vmcnt(10)\n"
    "s_barrier\n"
    "ds_read_b64_tr_b16 a[228:229], v6 offset:49152\n"
    "ds_read_b64_tr_b16 a[230:231], v6 offset:49920\n"
    "ds_read_b64_tr_b16 a[232:233], v6 offset:55296\n"
    "ds_read_b64_tr_b16 a[234:235], v6 offset:56064\n"
    "ds_read_b64_tr_b16 a[236:237], v7 offset:49152\n"
    "ds_read_b64_tr_b16 a[238:239], v7 offset:49920\n"
    "s_waitcnt lgkmcnt(4)\n"
    "v_mfma_f32_16x16x32_bf16 v[24:27], a[228:231], v[20:23], v[24:27]\n"
    "s_waitcnt lgkmcnt(2)\n"
    "v_mfma_f32_16x16x32_bf16 v[28:31], a[232:235], v[20:23], v[28:31]\n"
    "s_waitcnt lgkmcnt(0)\n"
    "v_mfma_f32_16x16x32_bf16 v[32:35], a[236:239], v[20:23], v[32:35]\n"
    "s_nop 3\n"
    "v_cndmask_b32_e64 v24, v24, v25, s[48:49]\n"
    "v_cndmask_b32_e64 v24, v24, v26, s[50:51]\n"
    "v_cndmask_b32_e64 v24, v24, v27, s[52:53]\n"
    "v_cndmask_b32_e64 v28, v28, v29, s[48:49]\n"
    "v_cndmask_b32_e64 v28, v28, v30, s[50:51]\n"
    "v_cndmask_b32_e64 v28, v28, v31, s[52:53]\n"
    "v_cndmask_b32_e64 v32, v32, v33, s[48:49]\n"
    "v_cndmask_b32_e64 v32, v32, v34, s[50:51]\n"
    "v_mov_b32_dpp v24, v28 quad_perm:[0,1,2,3] row_mask:0xf bank_mask:0x2\n"
    "v_cndmask_b32_e64 v32, v32, v35, s[52:53]\n"
    "v_mov_b32 v25, %[tag]\n"
    "s_nop 0\n"
    "v_mov_b32_dpp v24, v32 quad_perm:[0,1,2,3] row_mask:0xf bank_mask:0x4\n"
    "buffer_store_dwordx2 v[24:25], v249, %[srdw], %[cs] offen sc1\n"
    "s_lshl_b32 s61, %[f3p], 3\n"
    "s_add_u32 s62, s61, 53248\n"
    "s_mov_b64 exec, 0x1\n"
    "buffer_load_dword v246, v1, %[srdw], s62 offen sc1\n"
    "s_mov_b64 exec, -1\n"
    "s_add_u32 s63, %[fwp], 0\n"
    "s_add_u32 s64, %[fwp], s77\n"
    "s_add_u32 s65, s64, 384\n"
    "s_waitcnt vmcnt(2)\n"
    "s_barrier\n"
    "s_mov_b64 exec, s[94:95]\n"
    "buffer_store_dword v2, off, %[srdw], s63 sc1\n"
    "s_mov_b64 exec, -1\n"
    "s_mov_b64 exec, s[94:95]\n"
    "buffer_store_dword v2, off, %[srdw], s65 sc1\n"
    "s_mov_b64 exec, -1\n"
    "s_waitcnt vmcnt(2)\n"
    "v_cmp_ne_u32 vcc, %[tag], v246\n"
    "s_nop 0\n"
    "s_and_b32 vcc_lo, vcc_lo, 0x1\n"
    "s_cbranch_scc1 L67_%=\n"
    "L68_%=:\n"
    "s_add_u32 s66, %[ldsx], 73728\n"
    "s_mov_b32 exec_hi, 0xff\n"
    "buffer_load_dword v246, v1, %[srdw], s66 offen sc1\n"
    "s_mov_b32 exec_hi, -1\n"
    "s_waitcnt vmcnt(0)\n"
    "v_cmp_ne_u32 vcc, %[tag], v246\n"
    "s_nop 0\n"
    "s_and_b32 vcc_hi, vcc_hi, 0xff\n"
    "s_cmp_lg_u64 vcc, 0\n"
    "s_cbranch_scc1 L69_%=\n"
    "L70_%=:\n"
    "s_add_u32 m0, %[ldsx], 0\n"
    "s_add_u32 s68, %[x], 4980736\n"
    "buffer_load_dwordx4 v3, %[srdw], s68 offen sc1 lds\n"
    "buffer_load_dwordx4 v3, %[srdw], s68 offen offset:1024 sc1 lds\n"
    "buffer_load_dwordx4 v3, %[srdw], s68 offen offset:2048 sc1 lds\n"
    "buffer_load_dwordx4 v3, %[srdw], s68 offen offset:3072 sc1 lds\n"
    "s_add_u32 m0, %[ldsx], 4096\n"
    "s_add_u32 s68, %[x], 4984832\n"
    "buffer_load_dwordx4 v3, %[srdw], s68 offen sc1 lds\n"
    "s_add_u32 s84, %[fwd], 0\n"
    "s_mov_b64 exec, 0x1c7\n"
    "buffer_load_dword v79, v1, %[srdw], s84 offen sc1\n"
    "s_mov_b64 exec, -1\n"
    "s_bfe_u32 s85, %[dbg], 0xd\n"
    "s_mul_i32 s42, s85, -655360\n"
    "s_add_u32 s42, s42, %[x]\n"
    "s_mul_i32 s43, s85, -1572864\n"
    "s_add_u32 s43, s43, %[hst]\n"
    "s_mul_i32 s93, s85, -262144\n"
    "s_add_u32 s93, s93, %[f4p]\n"
    "s_lshr_b32 s86, %[dbg], 3\n"
    "s_add_u32 s86, s86, %[dbg]\n"
    "s_mov_b32 s44, 0\n"
    "s_bfe_u32 s44, s44, 0x2000d\n"
    "s_mul_i32 s44, s44, 3072\n"
    "s_mov_b32 s45, 8192\n"
    "s_bfe_u32 s45, s45, 0x2000d\n"
    "s_mul_i32 s45, s45, 3072\n"
    "s_mov_b32 s46, 16384\n"
    "s_bfe_u32 s46, s46, 0x2000d\n"
    "s_mul_i32 s46, s46, 3072\n"
    "s_mov_b32 s47, 24576\n"
    "s_bfe_u32 s47, s47, 0x2000d\n"
    "s_mul_i32 s47, s47, 3072\n"
    "s_add_u32 m0, %[ldsx], 21504\n"
    "s_add_u32 s68, s42, 5308416\n"
    "buffer_load_dwordx4 v3, %[srdw], s68 offen sc1 lds\n"
    "buffer_load_dwordx4 v3, %[srdw], s68 offen offset:1024 sc1 lds\n"
    "buffer_load_dwordx4 v3, %[srdw], s68 offen offset:2048 sc1 lds\n"
    "buffer_load_dwordx4 v3, %[srdw], s68 offen offset:3072 sc1 lds\n"
    "s_add_u32 m0, %[ldsx], 25600\n"
    "s_add_u32 s68, s42, 5312512\n"
    "buffer_load_dwordx4 v3, %[srdw], s68 offen sc1 lds\n"
    "s_waitcnt vmcnt(0) lgkmcnt(0)\n"
    "s_branch LJOIN_%=\n"
    "LND_%=:\n"
    "global_load_dwordx4 v[128:131], v248, s[42:43] offset:0 nt\n"
    "global_load_dwordx4 v[132:135], v248, s[42:43] offset:256 nt\n"
    "global_load_dwordx4 v[136:139], v248, s[42:43] offset:512 nt\n"
    "global_load_dwordx4 v[140:143], v248, s[42:43] offset:768 nt\n"
    "global_load_dwordx4 v[144:147], v248, s[42:43] offset:1024 nt\n"
    "v_add_u32 v248, 20480, v248\n"
    "global_load_dwordx4 v[148:151], v248, s[42:43] offset:0 nt\n"
    "global_load_dwordx4 v[152:155], v248, s[42:43] offset:256 nt\n"
    "global_load_dwordx4 v[156:159], v248, s[42:43] offset:512 nt\n"
    "global_load_dwordx4 v[160:163], v248, s[42:43] offset:768 nt\n"
    "global_load_dwordx4 v[164:167], v248, s[42:43] offset:1024 nt\n"
    "s_mov_b64 exec, 0\n"
    "s_getpc_b64 s[98:99]\n"
    "kx_c2:\n"
    "s_add_u32 s98, s98, kx_r2-kx_c2\n"
    "s_addc_u32 s99, s99, 0\n"
    "s_cmp_eq_u32 %[w1k], 0\n"
    "s_cbranch_scc1 kx_a0\n"
    "s_cmp_eq_u32 %[w1k], 1024\n"
    "s_cbranch_scc1 kx_a1\n"
    "s_cmp_eq_u32 %[w1k], 2048\n"
    "s_cbranch_scc1 kx_a2\n"
    "s_branch kx_a3\n"
    "kx_r2:\n"
    "s_mov_b64 exec, -1\n"
    "s_mov_b32 s87, 4980736\n"
    "s_mov_b32 s88, 4984832\n"
    "s_mov_b32 s89, 4988928\n"
    "s_mov_b32 s90, 4993024\n"
    "s_mov_b32 s91, 4997120\n"
    "s_mov_b32 s92, 5001216\n"
    "s_mov_b32 s56, 5005312\n"
    "s_mov_b32 s57, 5009408\n"
    "s_mov_b32 s58, 5013504\n"
    "s_mov_b32 s59, 5017600\n"
    "v_mov_b32 v208, 0\n"
    "v_mov_b32 v209, 0\n"
    "v_mov_b32 v210, 0\n"
    "v_mov_b32 v211, 0\n"
    "v_mov_b32 v212, 0\n"
    "v_mov_b32 v213, 0\n"
    "v_mov_b32 v214, 0\n"
    "v_mov_b32 v215, 0\n"
    "s_waitcnt vmcnt(9)\n"
    "ds_write_b128 v250, v[128:131] offset:0\n"
    "v_dot2c_f32_bf16 v209, v20, v128\n"
    "v_dot2c_f32_bf16 v208, v128, v128\n"
    "v_dot2c_f32_bf16 v211, v20, v129\n"
    "v_dot2c_f32_bf16 v210, v129, v129\n"
    "v_dot2c_f32_bf16 v209, v20, v130\n"
    "v_dot2c_f32_bf16 v208, v130, v130\n"
    "v_dot2c_f32_bf16 v211, v20, v131\n"
    "v_dot2c_f32_bf16 v210, v131, v131\n"
    "s_waitcnt vmcnt(8)\n"
    "ds_write_b128 v250, v[132:135] offset:256\n"
    "v_dot2c_f32_bf16 v209, v20, v132\n"
    "v_dot2c_f32_bf16 v208, v132, v132\n"
    "v_dot2c_f32_bf16 v211, v20, v133\n"
    "v_dot2c_f32_bf16 v210, v133, v133\n"
    "v_dot2c_f32_bf16 v209, v20, v134\n"
    "v_dot2c_f32_bf16 v208, v134, v134\n"
    "v_dot2c_f32_bf16 v211, v20, v135\n"
    "v_dot2c_f32_bf16 v210, v135, v135\n"
    "s_waitcnt vmcnt(7)\n"
    "ds_write_b128 v250, v[136:139] offset:512\n"
    "v_dot2c_f32_bf16 v209, v20, v136\n"
    "v_dot2c_f32_bf16 v208, v136, v136\n"
    "v_dot2c_f32_bf16 v211, v20, v137\n"
    "v_dot2c_f32_bf16 v210, v137, v137\n"
    "v_dot2c_f32_bf16 v209, v20, v138\n"
    "v_dot2c_f32_bf16 v208, v138, v138\n"
    "v_dot2c_f32_bf16 v211, v20, v139\n"
    "v_dot2c_f32_bf16 v210, v139, v139\n"
    "s_waitcnt vmcnt(6)\n"
    "ds_write_b128 v250, v[140:143] offset:768\n"
    "v_dot2c_f32_bf16 v209, v20, v140\n"
    "v_dot2c_f32_bf16 v208, v140, v140\n"
    "v_dot2c_f32_bf16 v211, v20, v141\n"
    "v_dot2c_f32_bf16 v210, v141, v141\n"
    "v_dot2c_f32_bf16 v209, v20, v142\n"
    "v_dot2c_f32_bf16 v208, v142, v142\n"
    "v_dot2c_f32_bf16 v211, v20, v143\n"
    "v_dot2c_f32_bf16 v210, v143, v143\n"
    "s_waitcnt vmcnt(5)\n"
    "ds_write_b128 v250, v[144:147] offset:1024\n"
    "v_dot2c_f32_bf16 v209, v20, v144\n"
    "v_dot2c_f32_bf16 v208, v144, v144\n"
    "v_dot2c_f32_bf16 v211, v20, v145\n"
    "v_dot2c_f32_bf16 v210, v145, v145\n"
    "v_dot2c_f32_bf16 v209, v20, v146\n"
    "v_dot2c_f32_bf16 v208, v146, v146\n"
    "v_dot2c_f32_bf16 v211, v20, v147\n"
    "v_dot2c_f32_bf16 v210, v147, v147\n"
    "s_nop 2\n"
    "v_add_f32 v208, v208, v210\n"
    "v_add_f32 v209, v209, v211\n"
    "s_nop 0\n"
    "v_add_f32_dpp v208, v208, v208 row_ror:8 row_mask:0xf bank_mask:0xf\n"
    "v_add_f32_dpp v209, v209, v209 row_ror:8 row_mask:0xf bank_mask:0xf\n"
    "s_waitcnt vmcnt(4)\n"
    "ds_write_b128 v250, v[148:151] offset:20736\n"
    "v_add_f32_dpp v208, v208, v208 row_ror:4 row_mask:0xf bank_mask:0xf\n"
    "v_add_f32_dpp v209, v209, v209 row_ror:4 row_mask:0xf bank_mask:0xf\n"
    "v_dot2c_f32_bf16 v213, v20, v148\n"
    "v_add_f32_dpp v208, v208, v208 row_ror:2 row_mask:0xf bank_mask:0xf\n"
    "v_add_f32_dpp v209, v209, v209 row_ror:2 row_mask:0xf bank_mask:0xf\n"
    "v_dot2c_f32_bf16 v212, v148, v148\n"
    "v_add_f32_dpp v208, v208, v208 row_ror:1 row_mask:0xf bank_mask:0xf\n"
    "v_add_f32_dpp v209, v209, v209 row_ror:1 row_mask:0xf bank_mask:0xf\n"
    "v_mul_f32 v209, 0xbacccccd, v209\n"
    "v_mul_f32 v208, 0x3acccccd, v208\n"
    "v_fma_f32 v208, -v209, v209, v208\n"
    "v_max_f32 v208, 0, v208\n"
    "v_add_f32 v208, 0x3727c5ac, v208\n"
    "v_rsq_f32 v208, v208\n"
    "s_waitcnt vmcnt(3)\n"
    "ds_write_b128 v250, v[152:155] offset:20992\n"
    "v_mul_f32 v209, v209, v208\n"
    "buffer_store_dwordx2 v[208:209], v78, %[srdw], 0 offen offset:0 sc1\n"
    "v_dot2c_f32_bf16 v215, v20, v149\n"
    "v_dot2c_f32_bf16 v214, v149, v149\n"
    "v_dot2c_f32_bf16 v213, v20, v150\n"
    "v_dot2c_f32_bf16 v212, v150, v150\n"
    "v_dot2c_f32_bf16 v215, v20, v151\n"
    "v_dot2c_f32_bf16 v214, v151, v151\n"
    "v_dot2c_f32_bf16 v213, v20, v152\n"
    "v_dot2c_f32_bf16 v212, v152, v152\n"
    "v_dot2c_f32_bf16 v215, v20, v153\n"
    "s_waitcnt vmcnt(3)\n"
    "ds_write_b128 v250, v[156:159] offset:21248\n"
    "v_dot2c_f32_bf16 v214, v153, v153\n"
    "v_dot2c_f32_bf16 v213, v20, v154\n"
    "v_dot2c_f32_bf16 v212, v154, v154\n"
    "v_dot2c_f32_bf16 v215, v20, v155\n"
    "v_dot2c_f32_bf16 v214, v155, v155\n"
    "v_dot2c_f32_bf16 v213, v20, v156\n"
    "v_dot2c_f32_bf16 v212, v156, v156\n"
    "v_dot2c_f32_bf16 v215, v20, v157\n"
    "v_dot2c_f32_bf16 v214, v157, v157\n"
    "v_dot2c_f32_bf16 v213, v20, v158\n"
    "v_dot2c_f32_bf16 v212, v158, v158\n"
    "v_dot2c_f32_bf16 v215, v20, v159\n"
    "v_dot2c_f32_bf16 v214, v159, v159\n"
    "s_waitcnt vmcnt(2)\n"
    "ds_write_b128 v250, v[160:163] offset:21504\n"
    "v_dot2c_f32_bf16 v213, v20, v160\n"
    "v_dot2c_f32_bf16 v212, v160, v160\n"
    "v_dot2c_f32_bf16 v215, v20, v161\n"
    "v_dot2c_f32_bf16 v214, v161, v161\n"
    "v_dot2c_f32_bf16 v213, v20, v162\n"
    "v_dot2c_f32_bf16 v212, v162, v162\n"
    "v_dot2c_f32_bf16 v215, v20, v163\n"
    "v_dot2c_f32_bf16 v214, v163, v163\n"
    "s_waitcnt vmcnt(1)\n"
    "ds_write_b128 v250, v[164:167] offset:21760\n"
    "v_dot2c_f32_bf16 v213, v20, v164\n"
    "v_dot2c_f32_bf16 v212, v164, v164\n"
    "v_dot2c_f32_bf16 v215, v20, v165\n"
    "v_dot2c_f32_bf16 v214, v165, v165\n"
    "v_dot2c_f32_bf16 v213, v20, v166\n"
    "v_dot2c_f32_bf16 v212, v166, v166\n"
    "v_dot2c_f32_bf16 v215, v20, v167\n"
    "v_dot2c_f32_bf16 v214, v167, v167\n"
    "s_nop 2\n"
    "v_add_f32 v212, v212, v214\n"
    "s_waitcnt lgkmcnt(0)\n"
    "s_barrier\n"
    "ds_read_b64 v[168:169], v251 offset:0\n"
    "ds_read_b64 v[170:171], v251 offset:10368\n"
    "ds_read_b64 v[172:173], v251 offset:256\n"
    "ds_read_b64 v[174:175], v251 offset:10624\n"
    "ds_read_b64 v[176:177], v251 offset:512\n"
    "ds_read_b64 v[178:179], v251 offset:10880\n"
    "ds_read_b64 v[180:181], v251 offset:768\n"
    "ds_read_b64 v[182:183], v251 offset:11136\n"
    "ds_read_b64 v[184:185], v251 offset:1024\n"
    "ds_read_b64 v[186:187], v251 offset:11392\n"
    "ds_read_b64 v[188:189], v251 offset:20736\n"
    "ds_read_b64 v[190:191], v251 offset:31104\n"
    "ds_read_b64 v[192:193], v251 offset:20992\n"
    "ds_read_b64 v[194:195], v251 offset:31360\n"
    "ds_read_b64 v[196:197], v251 offset:21248\n"
    "ds_read_b64 v[198:199], v251 offset:31616\n"
    "ds_read_b64 v[200:201], v251 offset:21504\n"
    "ds_read_b64 v[202:203], v251 offset:31872\n"
    "ds_read_b64 v[204:205], v251 offset:21760\n"
    "ds_read_b64 v[206:207], v251 offset:32128\n"
    "v_add_f32 v213, v213, v215\n"
    "v_add_f32_dpp v212, v212, v212 row_ror:8 row_mask:0xf bank_mask:0xf\n"
    "s_nop 0\n"
    "v_add_f32_dpp v213, v213, v213 row_ror:8 row_mask:0xf bank_mask:0xf\n"
    "v_add_f32_dpp v212, v212, v212 row_ror:4 row_mask:0xf bank_mask:0xf\n"
    "s_nop 0\n"
    "v_add_f32_dpp v213, v213, v213 row_ror:4 row_mask:0xf bank_mask:0xf\n"
    "v_add_f32_dpp v212, v212, v212 row_ror:2 row_mask:0xf bank_mask:0xf\n"
    "s_nop 0\n"
    "v_add_f32_dpp v213, v213, v213 row_ror:2 row_mask:0xf bank_mask:0xf\n"
    "v_add_f32_dpp v212, v212, v212 row_ror:1 row_mask:0xf bank_mask:0xf\n"
    "v_mul_f32 v212, 0x3acccccd, v212\n"
    "v_add_f32_dpp v213, v213, v213 row_ror:1 row_mask:0xf bank_mask:0xf\n"
    "s_waitcnt lgkmcnt(15)\n"
    "buffer_store_dwordx4 v[168:171], v255, %[srdw], s87 offen sc1\n"
    "buffer_store_dwordx4 v[172:175], v255, %[srdw], s88 offen sc1\n"
    "s_waitcnt lgkmcnt(14)\n"
    "buffer_store_dwordx4 v[176:179], v255, %[srdw], s89 offen sc1\n"
    "s_waitcnt lgkmcnt(12)\n"
    "buffer_store_dwordx4 v[180:183], v255, %[srdw], s90 offen sc1\n"
    "s_waitcnt lgkmcnt(10)\n"
    "buffer_store_dwordx4 v[184:187], v255, %[srdw], s91 offen sc1\n"
    "s_waitcnt lgkmcnt(8)\n"
    "buffer_store_dwordx4 v[188:191], v255, %[srdw], s92 offen sc1\n"
    "s_waitcnt lgkmcnt(6)\n"
    "buffer_store_dwordx4 v[192:195], v255, %[srdw], s56 offen sc1\n"
    "s_waitcnt lgkmcnt(4)\n"
    "buffer_store_dwordx4 v[196:199], v255, %[srdw], s57 offen sc1\n"
    "s_waitcnt lgkmcnt(2)\n"
    "buffer_store_dwordx4 v[200:203], v255, %[srdw], s58 offen sc1\n"
    "s_waitcnt lgkmcnt(0)\n"
    "buffer_store_dwordx4 v[204:207], v255, %[srdw], s59 offen sc1\n"
    "v_mul_f32 v213, 0xbacccccd, v213\n"
    "v_fma_f32 v212, -v213, v213, v212\n"
    "v_max_f32 v212, 0, v212\n"
    "v_add_f32 v212, 0x3727c5ac, v212\n"
    "v_rsq_f32 v212, v212\n"
    "s_nop 0\n"
    "v_mul_f32 v213, v213, v212\n"
    "buffer_store_dwordx2 v[212:213], v78, %[srdw], 0 offen offset:128 sc1\n"
    "s_add_u32 m0, s93, 0\n"
    "s_mov_b32 s68, 0\n"
    "buffer_load_dwordx4 v76, %[srdm], s68 offen lds\n"
    "buffer_load_dwordx4 v77, %[srdm], s68 offen offset:1024 lds\n"
    "buffer_load_dwordx4 v247, %[srdm], s68 offen offset:2048 lds\n"
    "s_add_u32 m0, s93, 12288\n"
    "s_mov_b32 s68, 98304\n"
    "buffer_load_dwordx4 v76, %[srdm], s68 offen lds\n"
    "buffer_load_dwordx4 v77, %[srdm], s68 offen offset:1024 lds\n"
    "buffer_load_dwordx4 v247, %[srdm], s68 offen offset:2048 lds\n"
    "s_add_u32 m0, s93, 24576\n"
    "s_mov_b32 s68, 196608\n"
    "buffer_load_dwordx4 v76, %[srdm], s68 offen lds\n"
    "buffer_load_dwordx4 v77, %[srdm], s68 offen offset:1024 lds\n"
    "buffer_load_dwordx4 v247, %[srdm], s68 offen offset:2048 lds\n"
    "s_add_u32 m0, s93, 36864\n"
    "s_mov_b32 s68, 294912\n"
    "buffer_load_dwordx4 v76, %[srdm], s68 offen lds\n"
    "buffer_load_dwordx4 v77, %[srdm], s68 offen offset:1024 lds\n"
    "buffer_load_dwordx4 v247, %[srdm], s68 offen offset:2048 lds\n"
    "s_add_u32 m0, s93, 49152\n"
    "s_mov_b32 s68, 393216\n"
    "buffer_load_dwordx4 v76, %[srdm], s68 offen lds\n"
    "buffer_load_dwordx4 v77, %[srdm], s68 offen offset:1024 lds\n"
    "buffer_load_dwordx4 v247, %[srdm], s68 offen offset:2048 lds\n"
    "s_waitcnt vmcnt(12)\n"
    "s_barrier\n"
    "ds_read_b64_tr_b16 a[0:1], v6 offset:0\n"
    "ds_read_b64_tr_b16 a[2:3], v6 offset:768\n"
    "ds_read_b64_tr_b16 a[4:5], v6 offset:6144\n"
    "ds_read_b64_tr_b16 a[6:7], v6 offset:6912\n"
    "ds_read_b64_tr_b16 a[8:9], v7 offset:0\n"
    "ds_read_b64_tr_b16 a[10:11], v7 offset:768\n"
    "s_waitcnt vmcnt(9) lgkmcnt(0)\n"
    "s_barrier\n"
    "ds_read_b64_tr_b16 a[12:13], v6 offset:12288\n"
    "ds_read_b64_tr_b16 a[14:15], v6 offset:13056\n"
    "ds_read_b64_tr_b16 a[16:17], v6 offset:18432\n"
    "ds_read_b64_tr_b16 a[18:19], v6 offset:19200\n"
    "ds_read_b64_tr_b16 a[20:21], v7 offset:12288\n"
    "ds_read_b64_tr_b16 a[22:23], v7 offset:13056\n"
    "s_add_u32 m0, s93, 0\n"
    "s_mov_b32 s68, 491520\n"
    "buffer_load_dwordx4 v76, %[srdm], s68 offen lds\n"
    "buffer_load_dwordx4 v77, %[srdm], s68 offen offset:1024 lds\n"
    "buffer_load_dwordx4 v247, %[srdm], s68 offen offset:2048 lds\n"
    "s_waitcnt lgkmcnt(0) vmcnt(9)\n"
    "s_barrier\n"
    "ds_read_b64_tr_b16 a[24:25], v6 offset:24576\n"
    "ds_read_b64_tr_b16 a[26:27], v6 offset:25344\n"
    "ds_read_b64_tr_b16 a[28:29], v6 offset:30720\n"
    "ds_read_b64_tr_b16 a[30:31], v6 offset:31488\n"
    "ds_read_b64_tr_b16 a[32:33], v7 offset:24576\n"
    "ds_read_b64_tr_b16 a[34:35], v7 offset:25344\n"
    "s_add_u32 m0, s93, 12288\n"
    "s_mov_b32 s68, 589824\n"
    "buffer_load_dwordx4 v76, %[srdm], s68 offen lds\n"
    "buffer_load_dwordx4 v77, %[srdm], s68 offen offset:1024 lds\n"
    "buffer_load_dwordx4 v247, %[srdm], s68 offen offset:2048 lds\n"
    "s_waitcnt lgkmcnt(0) vmcnt(9)\n"
    "s_barrier\n"
    "ds_read_b64_tr_b16 a[36:37], v6 offset:36864\n"
    "ds_read_b64_tr_b16 a[38:39], v6 offset:37632\n"
    "ds_read_b64_tr_b16 a[40:41], v6 offset:43008\n"
    "ds_read_b64_tr_b16 a[42:43], v6 offset:43776\n"
    "ds_read_b64_tr_b16 a[44:45], v7 offset:36864\n"
    "ds_read_b64_tr_b16 a[46:47], v7 offset:37632\n"
    "s_add_u32 m0, s93, 24576\n"
    "s_mov_b32 s68, 688128\n"
    "buffer_load_dwordx4 v76, %[srdm], s68 offen lds\n"
    "buffer_load_dwordx4 v77, %[srdm], s68 offen offset:1024 lds\n"
    "buffer_load_dwordx4 v247, %[srdm], s68 offen offset:2048 lds\n"
    "v_mov_b32 v216, %[tag]\n"
    "s_andn2_b32 s60, %[p1], 3\n"
    "s_waitcnt vmcnt(9) lgkmcnt(0)\n"
    "s_barrier\n"
    "s_and_b32 exec_lo, %[p1], 3\n"
    "s_mov_b32 exec_hi, 0\n"
    "buffer_store_dword v216, v1, %[srdw], s60 offen sc1\n"
    "s_mov_b64 exec, -1\n"
    "ds_read_b64_tr_b16 a[48:49], v6 offset:49152\n"
    "ds_read_b64_tr_b16 a[50:51], v6 offset:49920\n"
    "ds_read_b64_tr_b16 a[52:53], v6 offset:55296\n"
    "ds_read_b64_tr_b16 a[54:55], v6 offset:56064\n"
    "ds_read_b64_tr_b16 a[56:57], v7 offset:49152\n"
    "ds_read_b64_tr_b16 a[58:59], v7 offset:49920\n"
    "s_add_u32 m0, s93, 36864\n"
    "s_mov_b32 s68, 786432\n"
    "buffer_load_dwordx4 v76, %[srdm], s68 offen lds\n"
    "buffer_load_dwordx4 v77, %[srdm], s68 offen offset:1024 lds\n"
    "buffer_load_dwordx4 v247, %[srdm], s68 offen offset:2048 lds\n"
    "s_waitcnt lgkmcnt(0) vmcnt(10)\n"
    "s_barrier\n"
    "ds_read_b64_tr_b16 a[60:61], v6 offset:0\n"
    "ds_read_b64_tr_b16 a[62:63], v6 offset:768\n"
    "ds_read_b64_tr_b16 a[64:65], v6 offset:6144\n"
    "ds_read_b64_tr_b16 a[66:67], v6 offset:6912\n"
    "ds_read_b64_tr_b16 a[68:69], v7 offset:0\n"
    "ds_read_b64_tr_b16 a[70:71], v7 offset:768\n"
    "s_add_u32 m0, s93, 49152\n"
    "s_mov_b32 s68, 884736\n"
    "buffer_load_dwordx4 v76, %[srdm], s68 offen lds\n"
    "buffer_load_dwordx4 v77, %[srdm], s68 offen offset:1024 lds\n"
    "buffer_load_dwordx4 v247, %[srdm], s68 offen offset:2048 lds\n"
    "s_waitcnt lgkmcnt(0) vmcnt(10)\n"
    "s_barrier\n"
    "ds_read_b64_tr_b16 a[72:73], v6 offset:12288\n"
    "ds_read_b64_tr_b16 a[74:75], v6 offset:13056\n"
    "ds_read_b64_tr_b16 a[76:77], v6 offset:18432\n"
    "ds_read_b64_tr_b16 a[78:79], v6 offset:19200\n"
    "ds_read_b64_tr_b16 a[80:81], v7 offset:12288\n"
    "ds_read_b64_tr_b16 a[82:83], v7 offset:13056\n"
    "s_add_u32 m0, s93, 0\n"
    "s_mov_b32 s68, 983040\n"
    "buffer_load_dwordx4 v76, %[srdm], s68 offen lds\n"
    "buffer_load_dwordx4 v77, %[srdm], s68 offen offset:1024 lds\n"
    "buffer_load_dwordx4 v247, %[srdm], s68 offen offset:2048 lds\n"
    "s_waitcnt lgkmcnt(0) vmcnt(10)\n"
    "s_barrier\n"
    "ds_read_b64_tr_b16 a[84:85], v6 offset:24576\n"
    "ds_read_b64_tr_b16 a[86:87], v6 offset:25344\n"
    "ds_read_b64_tr_b16 a[88:89], v6 offset:30720\n"
    "ds_read_b64_tr_b16 a[90:91], v6 offset:31488\n"
    "ds_read_b64_tr_b16 a[92:93], v7 offset:24576\n"
    "ds_read_b64_tr_b16 a[94:95], v7 offset:25344\n"
    "s_add_u32 m0, s93, 12288\n"
    "s_mov_b32 s68, 1081344\n"
    "buffer_load_dwordx4 v76, %[srdm], s68 offen lds\n"
    "buffer_load_dwordx4 v77, %[srdm], s68 offen offset:1024 lds\n"
    "buffer_load_dwordx4 v247, %[srdm], s68 offen offset:2048 lds\n"
    "s_waitcnt lgkmcnt(0) vmcnt(9)\n"
    "s_barrier\n"
    "ds_read_b64_tr_b16 a[96:97], v6 offset:36864\n"
    "ds_read_b64_tr_b16 a[98:99], v6 offset:37632\n"
    "ds_read_b64_tr_b16 a[100:101], v6 offset:43008\n"
    "ds_read_b64_tr_b16 a[102:103], v6 offset:43776\n"
    "ds_read_b64_tr_b16 a[104:105], v7 offset:36864\n"
    "ds_read_b64_tr_b16 a[106:107], v7 offset:37632\n"
    "s_add_u32 m0, s93, 24576\n"
    "s_mov_b32 s68, 1179648\n"
    "buffer_load_dwordx4 v76, %[srdm], s68 offen lds\n"
    "buffer_load_dwordx4 v77, %[srdm], s68 offen offset:1024 lds\n"
    "buffer_load_dwordx4 v247, %[srdm], s68 offen offset:2048 lds\n"
    "s_waitcnt lgkmcnt(0) vmcnt(9)\n"
    "s_barrier\n"
    "ds_read_b64_tr_b16 a[108:109], v6 offset:49152\n"
    "ds_read_b64_tr_b16 a[110:111], v6 offset:49920\n"
    "ds_read_b64_tr_b16 a[112:113], v6 offset:55296\n"
    "ds_read_b64_tr_b16 a[114:115], v6 offset:56064\n"
    "ds_read_b64_tr_b16 a[116:117], v7 offset:49152\n"
    "ds_read_b64_tr_b16 a[118:119], v7 offset:49920\n"
    "s_add_u32 m0, s93, 36864\n"
    "s_mov_b32 s68, 1277952\n"
    "buffer_load_dwordx4 v76, %[srdm], s68 offen lds\n"
    "buffer_load_dwordx4 v77, %[srdm], s68 offen offset:1024 lds\n"
    "buffer_load_dwordx4 v247, %[srdm], s68 offen offset:2048 lds\n"
    "s_waitcnt vmcnt(9) lgkmcnt(0)\n"
    "s_barrier\n"
    "ds_read_b64_tr_b16 a[120:121], v6 offset:0\n"
    "ds_read_b64_tr_b16 a[122:123], v6 offset:768\n"
    "ds_read_b64_tr_b16 a[124:125], v6 offset:6144\n"
    "ds_read_b64_tr_b16 a[126:127], v6 offset:6912\n"
    "ds_read_b64_tr_b16 a[128:129], v7 offset:0\n"
    "ds_read_b64_tr_b16 a[130:131], v7 offset:768\n"
    "s_add_u32 m0, s93, 49152\n"
    "s_mov_b32 s68, 1376256\n"
    "buffer_load_dwordx4 v76, %[srdm], s68 offen lds\n"
    "buffer_load_dwordx4 v77, %[srdm], s68 offen offset:1024 lds\n"
    "buffer_load_dwordx4 v247, %[srdm], s68 offen offset:2048 lds\n"
    "s_waitcnt vmcnt(9) lgkmcnt(0)\n"
    "s_barrier\n"
    "ds_read_b64_tr_b16 a[132:133], v6 offset:12288\n"
    "ds_read_b64_tr_b16 a[134:135], v6 offset:13056\n"
    "ds_read_b64_tr_b16 a[136:137], v6 offset:18432\n"
    "ds_read_b64_tr_b16 a[138:139], v6 offset:19200\n"
    "ds_read_b64_tr_b16 a[140:141], v7 offset:12288\n"
    "ds_read_b64_tr_b16 a[142:143], v7 offset:13056\n"
    "s_add_u32 m0, s93, 0\n"
    "s_mov_b32 s68, 1474560\n"
    "buffer_load_dwordx4 v76, %[srdm], s68 offen lds\n"
    "buffer_load_dwordx4 v77, %[srdm], s68 offen offset:1024 lds\n"
    "buffer_load_dwordx4 v247, %[srdm], s68 offen offset:2048 lds\n"
    "s_waitcnt vmcnt(9) lgkmcnt(0)\n"
    "s_barrier\n"
    "ds_read_b64_tr_b16 a[144:145], v6 offset:24576\n"
    "ds_read_b64_tr_b16 a[146:147], v6 offset:25344\n"
    "ds_read_b64_tr_b16 a[148:149], v6 offset:30720\n"
    "ds_read_b64_tr_b16 a[150:151], v6 offset:31488\n"
    "ds_read_b64_tr_b16 a[152:153], v7 offset:24576\n"
    "ds_read_b64_tr_b16 a[154:155], v7 offset:25344\n"
    "s_add_u32 m0, s93, 12288\n"
    "s_mov_b32 s68, 1572864\n"
    "buffer_load_dwordx4 v76, %[srdm], s68 offen lds\n"
    "buffer_load_dwordx4 v77, %[srdm], s68 offen offset:1024 lds\n"
    "buffer_load_dwordx4 v247, %[srdm], s68 offen offset:2048 lds\n"
    "s_waitcnt lgkmcnt(0) vmcnt(9)\n"
    "s_barrier\n"
    "ds_read_b64_tr_b16 a[156:157], v6 offset:36864\n"
    "ds_read_b64_tr_b16 a[158:159], v6 offset:37632\n"
    "ds_read_b64_tr_b16 a[160:161], v6 offset:43008\n"
    "ds_read_b64_tr_b16 a[162:163], v6 offset:43776\n"
    "ds_read_b64_tr_b16 a[164:165], v7 offset:36864\n"
    "ds_read_b64_tr_b16 a[166:167], v7 offset:37632\n"
    "s_add_u32 m0, s93, 24576\n"
    "s_mov_b32 s68, 1671168\n"
    "buffer_load_dwordx4 v76, %[srdm], s68 offen lds\n"
    "buffer_load_dwordx4 v77, %[srdm], s68 offen offset:1024 lds\n"
    "buffer_load_dwordx4 v247, %[srdm], s68 offen offset:2048 lds\n"
    "s_waitcnt vmcnt(9) lgkmcnt(0)\n"
    "s_barrier\n"
    "ds_read_b64_tr_b16 a[168:169], v6 offset:49152\n"
    "ds_read_b64_tr_b16 a[170:171], v6 offset:49920\n"
    "ds_read_b64_tr_b16 a[172:173], v6 offset:55296\n"
    "ds_read_b64_tr_b16 a[174:175], v6 offset:56064\n"
    "ds_read_b64_tr_b16 a[176:177], v7 offset:49152\n"
    "ds_read_b64_tr_b16 a[178:179], v7 offset:49920\n"
    "s_add_u32 m0, s93, 36864\n"
    "s_mov_b32 s68, 1769472\n"
    "buffer_load_dwordx4 v76, %[srdm], s68 offen lds\n"
    "buffer_load_dwordx4 v77, %[srdm], s68 offen offset:1024 lds\n"
    "buffer_load_dwordx4 v247, %[srdm], s68 offen offset:2048 lds\n"
    "s_waitcnt lgkmcnt(0) vmcnt(9)\n"
    "s_barrier\n"
    "ds_read_b64_tr_b16 a[180:181], v6 offset:0\n"
    "ds_read_b64_tr_b16 a[182:183], v6 offset:768\n"
    "ds_read_b64_tr_b16 a[184:185], v6 offset:6144\n"
    "ds_read_b64_tr_b16 a[186:187], v6 offset:6912\n"
    "ds_read_b64_tr_b16 a[188:189], v7 offset:0\n"
    "ds_read_b64_tr_b16 a[190:191], v7 offset:768\n"
    "s_add_u32 m0, s93, 49152\n"
    "s_mov_b32 s68, 1867776\n"
    "buffer_load_dwordx4 v76, %[srdm], s68 offen lds\n"
    "buffer_load_dwordx4 v77, %[srdm], s68 offen offset:1024 lds\n"
    "buffer_load_dwordx4 v247, %[srdm], s68 offen offset:2048 lds\n"
    "s_waitcnt vmcnt(9) lgkmcnt(0)\n"
    "s_barrier\n"
    "ds_read_b64_tr_b16 a[192:193], v6 offset:12288\n"
    "ds_read_b64_tr_b16 a[194:195], v6 offset:13056\n"
    "ds_read_b64_tr_b16 a[196:197], v6 offset:18432\n"
    "ds_read_b64_tr_b16 a[198:199], v6 offset:19200\n"
    "ds_read_b64_tr_b16 a[200:201], v7 offset:12288\n"
    "ds_read_b64_tr_b16 a[202:203], v7 offset:13056\n"
    "s_waitcnt vmcnt(6) lgkmcnt(0)\n"
    "s_barrier\n"
    "ds_read_b64_tr_b16 a[204:205], v6 offset:24576\n"
    "ds_read_b64_tr_b16 a[206:207], v6 offset:25344\n"
    "ds_read_b64_tr_b16 a[208:209], v6 offset:30720\n"
    "ds_read_b64_tr_b16 a[210:211], v6 offset:31488\n"
    "ds_read_b64_tr_b16 a[212:213], v7 offset:24576\n"
    "ds_read_b64_tr_b16 a[214:215], v7 offset:25344\n"
    "s_waitcnt vmcnt(3) lgkmcnt(0)\n"
    "s_barrier\n"
    "ds_read_b64_tr_b16 a[216:217], v6 offset:36864\n"
    "ds_read_b64_tr_b16 a[218:219], v6 offset:37632\n"
    "ds_read_b64_tr_b16 a[220:221], v6 offset:43008\n"
    "ds_read_b64_tr_b16 a[222:223], v6 offset:43776\n"
    "ds_read_b64_tr_b16 a[224:225], v7 offset:36864\n"
    "ds_read_b64_tr_b16 a[226:227], v7 offset:37632\n"
    "s_waitcnt vmcnt(0) lgkmcnt(0)\n"
    "s_barrier\n"
    "ds_read_b64_tr_b16 a[228:229], v6 offset:49152\n"
    "ds_read_b64_tr_b16 a[230:231], v6 offset:49920\n"
    "ds_read_b64_tr_b16 a[232:233], v6 offset:55296\n"
    "ds_read_b64_tr_b16 a[234:235], v6 offset:56064\n"
    "ds_read_b64_tr_b16 a[236:237], v7 offset:49152\n"
    "ds_read_b64_tr_b16 a[238:239], v7 offset:49920\n"
    "s_lshl_b32 s61, %[f3p], 3\n"
    "s_add_u32 s62, s61, 53248\n"
    "s_mov_b64 exec, 0x1\n"
    "buffer_load_dword v246, v1, %[srdw], s62 offen sc1\n"
    "s_mov_b64 exec, -1\n"
    "s_waitcnt lgkmcnt(0)\n"
    "s_barrier\n"
    "s_waitcnt vmcnt(0)\n"
    "v_cmp_ne_u32 vcc, %[tag], v246\n"
    "s_nop 0\n"
    "s_and_b32 vcc_lo, vcc_lo, 0x1\n"
    "s_cbranch_scc1 L71_%=\n"
    "L72_%=:\n"
    "s_add_u32 s63, %[ldsx], 73728\n"
    "s_mov_b32 exec_hi, 0xff\n"
    "buffer_load_dword v246, v1, %[srdw], s63 offen sc1\n"
    "s_mov_b32 exec_hi, -1\n"
    "s_waitcnt vmcnt(0)\n"
    "v_cmp_ne_u32 vcc, %[tag], v246\n"
    "s_nop 0\n"
    "s_and_b32 vcc_hi, vcc_hi, 0xff\n"
    "s_cmp_lg_u64 vcc, 0\n"
    "s_cbranch_scc1 L73_%=\n"
    "L74_%=:\n"
    "s_add_u32 m0, %[ldsx], 0\n"
    "s_add_u32 s68, %[x], 4980736\n"
    "buffer_load_dwordx4 v3, %[srdw], s68 offen sc1 lds\n"
    "buffer_load_dwordx4 v3, %[srdw], s68 offen offset:1024 sc1 lds\n"
    "buffer_load_dwordx4 v3, %[srdw], s68 offen offset:2048 sc1 lds\n"
    "buffer_load_dwordx4 v3, %[srdw], s68 offen offset:3072 sc1 lds\n"
    "s_add_u32 m0, %[ldsx], 4096\n"
    "s_add_u32 s68, %[x], 4984832\n"
    "buffer_load_dwordx4 v3, %[srdw], s68 offen sc1 lds\n"
    "s_add_u32 s84, %[fwd], 0\n"
    "s_mov_b64 exec, 0x1c7\n"
    "buffer_load_dword v79, v1, %[srdw], s84 offen sc1\n"
    "s_mov_b64 exec, -1\n"
    "s_bfe_u32 s85, %[dbg], 0xd\n"
    "s_mul_i32 s42, s85, -655360\n"
    "s_add_u32 s42, s42, %[x]\n"
    "s_mul_i32 s43, s85, -1572864\n"
    "s_add_u32 s43, s43, %[hst]\n"
    "s_mul_i32 s93, s85, -262144\n"
    "s_add_u32 s93, s93, %[f4p]\n"
    "s_lshr_b32 s86, %[dbg], 3\n"
    "s_add_u32 s86, s86, %[dbg]\n"
    "s_mov_b32 s44, 0\n"
    "s_bfe_u32 s44, s44, 0x2000d\n"
    "s_mul_i32 s44, s44, 3072\n"
    "s_mov_b32 s45, 8192\n"
    "s_bfe_u32 s45, s45, 0x2000d\n"
    "s_mul_i32 s45, s45, 3072\n"
    "s_mov_b32 s46, 16384\n"
    "s_bfe_u32 s46, s46, 0x2000d\n"
    "s_mul_i32 s46, s46, 3072\n"
    "s_mov_b32 s47, 24576\n"
    "s_bfe_u32 s47, s47, 0x2000d\n"
    "s_mul_i32 s47, s47, 3072\n"
    "s_add_u32 m0, %[ldsx], 21504\n"
    "s_add_u32 s68, s42, 5308416\n"
    "buffer_load_dwordx4 v3, %[srdw], s68 offen sc1 lds\n"
    "buffer_load_dwordx4 v3, %[srdw], s68 offen offset:1024 sc1 lds\n"
    "buffer_load_dwordx4 v3, %[srdw], s68 offen offset:2048 sc1 lds\n"
    "buffer_load_dwordx4 v3, %[srdw], s68 offen offset:3072 sc1 lds\n"
    "s_add_u32 m0, %[ldsx], 25600\n"
    "s_add_u32 s68, s42, 5312512\n"
    "buffer_load_dwordx4 v3, %[srdw], s68 offen sc1 lds\n"
    "s_waitcnt vmcnt(0) lgkmcnt(0)\n"
    "s_branch LJOIN_%=\n"
    "L1_%=:\n"
    "s_mov_b32 s98, m0\n"
    "L1_%=_l:\n"
    "s_sub_u32 s54, s54, 1\n"
    "s_cselect_b32 s54, 0, s54\n"
    "s_cbranch_scc1 L1_%=_x\n"
    "s_sleep 1\n"
    "s_add_u32 m0, %[w1k], 82176\n"
    "s_add_u32 s99, %[yst], 2359296\n"
    "buffer_load_dwordx4 v0, %[srdw], s99 offen sc1 lds\n"
    "s_add_u32 m0, %[w1k], 103424\n"
    "s_add_u32 s99, %[yst], 2363392\n"
    "buffer_load_dwordx4 v0, %[srdw], s99 offen sc1 lds\n"
    "s_add_u32 m0, %[w1k], 124672\n"
    "s_add_u32 s99, %[yst], 2367488\n"
    "buffer_load_dwordx4 v0, %[srdw], s99 offen sc1 lds\n"
    "s_add_u32 m0, %[w1k], 145920\n"
    "s_add_u32 s99, %[yst], 2371584\n"
    "buffer_load_dwordx4 v0, %[srdw], s99 offen sc1 lds\n"
    "buffer_load_dwordx4 v[52:55], v0, %[srdw], s45 offen sc1\n"
    "s_waitcnt vmcnt(0)\n"
    "ds_read_b128 v[24:27], v40 offset:0\n"
    "ds_read_b128 v[28:31], v40 offset:21248\n"
    "ds_read_b128 v[32:35], v40 offset:42496\n"
    "ds_read_b128 v[36:39], v40 offset:63744\n"
    "s_waitcnt lgkmcnt(0)\n"
    "v_add3_u32 v24, v24, v25, v26\n"
    "v_add3_u32 v28, v28, v29, v30\n"
    "v_add3_u32 v32, v32, v33, v34\n"
    "v_add3_u32 v36, v36, v37, v38\n"
    "v_add3_u32 v24, v24, v27, v52\n"
    "v_add3_u32 v28, v28, v31, v53\n"
    "v_add3_u32 v32, v32, v35, v54\n"
    "v_add3_u32 v36, v36, v39, v55\n"
    "v_bitop3_b32 v24, v24, v28, %[tag] bitop3:0x7e\n"
    "v_bitop3_b32 v32, v32, v36, %[tag] bitop3:0x7e\n"
    "v_or_b32 v24, v24, v32\n"
    "v_cmp_ne_u32 vcc, 0, v24\n"
    "s_nop 1\n"
    "s_cmp_lg_u64 vcc, 0\n"
    "s_cbranch_scc1 L1_%=_l\n"
    "L1_%=_x:\n"
    "s_mov_b32 m0, s98\n"
    "s_branch L2_%=\n"
    "L3_%=:\n"
    "L3_%=_l:\n"
    "s_sub_u32 s54, s54, 1\n"
    "s_cselect_b32 s54, 0, s54\n"
    "s_cbranch_scc1 L3_%=_x\n"
    "s_sleep 1\n"
    "s_mov_b64 exec, 0xffff\n"
    "buffer_load_dword v79, v1, %[srdw], s59 offen sc1\n"
    "s_mov_b64 exec, -1\n"
    "s_waitcnt vmcnt(0)\n"
    "v_cmp_ne_u32 vcc, %[tag], v79\n"
    "s_nop 1\n"
    "s_and_b32 vcc_lo, vcc_lo, 0xffff\n"
    "s_cbranch_scc1 L3_%=_l\n"
    "L3_%=_x:\n"
    "s_branch L4_%=\n"
    "L5_%=:\n"
    "L5_%=_l:\n"
    "s_sub_u32 s54, s54, 1\n"
    "s_cselect_b32 s54, 0, s54\n"
    "s_cbranch_scc1 L5_%=_x\n"
    "s_sleep 1\n"
    "s_mov_b32 exec_hi, 0xff\n"
    "buffer_load_dword v246, v1, %[srdw], s78 offen sc1\n"
    "s_mov_b32 exec_hi, -1\n"
    "s_waitcnt vmcnt(0)\n"
    "v_cmp_ne_u32 vcc, %[tag], v246\n"
    "s_nop 1\n"
    "s_and_b32 vcc_hi, vcc_hi, 0xff\n"
    "s_cmp_lg_u64 vcc, 0\n"
    "s_cbranch_scc1 L5_%=_l\n"
    "L5_%=_x:\n"
    "s_branch L6_%=\n"
    "L7_%=:\n"
    "L7_%=_l:\n"
    "s_sub_u32 s54, s54, 1\n"
    "s_cselect_b32 s54, 0, s54\n"
    "s_cbranch_scc1 L7_%=_x\n"
    "s_sleep 1\n"
    "s_mov_b64 exec, 0xffff\n"
    "buffer_load_dword v79, v1, %[srdw], s79 offen sc1\n"
    "s_mov_b64 exec, -1\n"
    "s_waitcnt vmcnt(0)\n"
    "v_cmp_ne_u32 vcc, %[tag], v79\n"
    "s_nop 1\n"
    "s_and_b32 vcc_lo, vcc_lo, 0xffff\n"
    "s_cbranch_scc1 L7_%=_l\n"
    "L7_%=_x:\n"
    "s_branch L8_%=\n"
    "L9_%=:\n"
    "L9_%=_l:\n"
    "s_sub_u32 s54, s54, 1\n"
    "s_cselect_b32 s54, 0, s54\n"
    "s_cbranch_scc1 L9_%=_x\n"
    "s_sleep 1\n"
    "s_mov_b64 exec, 0x1c7\n"
    "buffer_load_dword v79, v1, %[srdw], s84 offen sc1\n"
    "s_mov_b64 exec, -1\n"
    "s_waitcnt vmcnt(0)\n"
    "v_cmp_ne_u32 vcc, %[tag], v79\n"
    "s_nop 1\n"
    "s_and_b32 vcc_lo, vcc_lo, 0x1c7\n"
    "s_cbranch_scc1 L9_%=_l\n"
    "L9_%=_x:\n"
    "s_branch L10_%=\n"
    "L11_%=:\n"
    "L11_%=_l:\n"
    "s_sub_u32 s54, s54, 1\n"
    "s_cselect_b32 s54, 0, s54\n"
    "s_cbranch_scc1 L11_%=_x\n"
    "s_sleep 1\n"
    "buffer_load_dwordx2 v[88:89], v90, %[srdw], %[cs] offen sc1\n"
    "s_waitcnt vmcnt(0)\n"
    "v_cmp_ne_u32 vcc, %[tag], v89\n"
    "s_nop 1\n"
    "s_cmp_lg_u64 vcc, 0\n"
    "s_cbranch_scc1 L11_%=_l\n"
    "L11_%=_x:\n"
    "s_branch L12_%=\n"
    "L13_%=:\n"
    "L13_%=_l:\n"
    "s_sub_u32 s54, s54, 1\n"
    "s_cselect_b32 s54, 0, s54\n"
    "s_cbranch_scc1 L13_%=_x\n"
    "s_sleep 1\n"
    "s_mov_b64 exec, 0xfff\n"
    "buffer_load_dword v78, v1, %[srdw], s56 offen sc1\n"
    "s_mov_b64 exec, -1\n"
    "s_waitcnt vmcnt(0)\n"
    "v_cmp_ne_u32 vcc, %[tag], v78\n"
    "s_nop 1\n"
    "s_and_b32 vcc_lo, vcc_lo, 0xfff\n"
    "s_cbranch_scc1 L13_%=_l\n"
    "L13_%=_x:\n"
    "s_branch L14_%=\n"
    "L15_%=:\n"
    "L15_%=_l:\n"
    "s_sub_u32 s54, s54, 1\n"
    "s_cselect_b32 s54, 0, s54\n"
    "s_cbranch_scc1 L15_%=_x\n"
    "s_sleep 1\n"
    "s_mov_b32 exec_hi, 0xff\n"
    "buffer_load_dword v80, v0, %[srdw], s80 offen sc1\n"
    "s_mov_b32 exec_hi, -1\n"
    "s_waitcnt vmcnt(0)\n"
    "v_cmp_ne_u32 vcc, %[tag], v80\n"
    "s_nop 1\n"
    "s_and_b32 vcc_hi, vcc_hi, 0xff\n"
    "s_cmp_lg_u64 vcc, 0\n"
    "s_cbranch_scc1 L15_%=_l\n"
    "L15_%=_x:\n"
    "s_branch L16_%=\n"
    "L17_%=:\n"
    "L17_%=_l:\n"
    "s_sub_u32 s54, s54, 1\n"
    "s_cselect_b32 s54, 0, s54\n"
    "s_cbranch_scc1 L17_%=_x\n"
    "s_sleep 1\n"
    "s_mov_b64 exec, 0xfffff\n"
    "buffer_load_dword v82, v0, %[srdw], s78 offen sc1\n"
    "s_mov_b64 exec, -1\n"
    "s_waitcnt vmcnt(0)\n"
    "v_cmp_ne_u32 vcc, %[tag], v82\n"
    "s_nop 1\n"
    "s_and_b32 vcc_lo, vcc_lo, 0xfffff\n"
    "s_cbranch_scc1 L17_%=_l\n"
    "L17_%=_x:\n"
    "s_branch L18_%=\n"
    "L19_%=:\n"
    "L19_%=_l:\n"
    "s_sub_u32 s54, s54, 1\n"
    "s_cselect_b32 s54, 0, s54\n"
    "s_cbranch_scc1 L19_%=_x\n"
    "s_sleep 1\n"
    "s_mov_b64 exec, 0xf\n"
    "buffer_load_dword v78, v1, %[srdw], s84 offen sc1\n"
    "s_mov_b64 exec, -1\n"
    "s_waitcnt vmcnt(0)\n"
    "v_cmp_ne_u32 vcc, %[tag], v78\n"
    "s_nop 1\n"
    "s_and_b32 vcc_lo, vcc_lo, 0xf\n"
    "s_cbranch_scc1 L19_%=_l\n"
    "L19_%=_x:\n"
    "s_branch L20_%=\n"
    "L21_%=:\n"
    "L21_%=_l:\n"
    "s_sub_u32 s54, s54, 1\n"
    "s_cselect_b32 s54, 0, s54\n"
    "s_cbranch_scc1 L21_%=_x\n"
    "s_sleep 1\n"
    "s_mov_b64 exec, 0xf\n"
    "buffer_load_dword v78, v1, %[srdw], s87 offen sc1\n"
    "s_mov_b64 exec, -1\n"
    "s_waitcnt vmcnt(0)\n"
    "v_cmp_ne_u32 vcc, %[tag], v78\n"
    "s_nop 1\n"
    "s_and_b32 vcc_lo, vcc_lo, 0xf\n"
    "s_cbranch_scc1 L21_%=_l\n"
    "L21_%=_x:\n"
    "s_branch L22_%=\n"
    "L23_%=:\n"
    "L23_%=_l:\n"
    "s_sub_u32 s54, s54, 1\n"
    "s_cselect_b32 s54, 0, s54\n"
    "s_cbranch_scc1 L23_%=_x\n"
    "s_sleep 1\n"
    "v_lshlrev_b32 v79, 3, v0\n"
    "s_mov_b64 exec, 0xf\n"
    "buffer_load_dword v79, v79, %[srdw], s60 offen sc1\n"
    "s_mov_b64 exec, -1\n"
    "s_waitcnt vmcnt(0)\n"
    "v_cmp_ne_u32 vcc, %[tag], v79\n"
    "s_nop 1\n"
    "s_and_b32 vcc_lo, vcc_lo, 0xf\n"
    "s_cbranch_scc1 L23_%=_l\n"
    "L23_%=_x:\n"
    "s_branch L24_%=\n"
    "L25_%=:\n"
    "L25_%=_l:\n"
    "s_sub_u32 s54, s54, 1\n"
    "s_cselect_b32 s54, 0, s54\n"
    "s_cbranch_scc1 L25_%=_x\n"
    "s_sleep 1\n"
    "v_lshlrev_b32 v78, 3, v0\n"
    "s_mov_b64 exec, 0xf\n"
    "buffer_load_dword v78, v78, %[srdw], s61 offen sc1\n"
    "s_mov_b64 exec, -1\n"
    "s_waitcnt vmcnt(0)\n"
    "v_cmp_ne_u32 vcc, %[tag], v78\n"
    "s_nop 1\n"
    "s_and_b32 vcc_lo, vcc_lo, 0xf\n"
    "s_cbranch_scc1 L25_%=_l\n"
    "L25_%=_x:\n"
    "s_branch L26_%=\n"
    "L27_%=:\n"
    "L27_%=_l:\n"
    "s_sub_u32 s54, s54, 1\n"
    "s_cselect_b32 s54, 0, s54\n"
    "s_cbranch_scc1 L27_%=_x\n"
    "s_sleep 1\n"
    "buffer_load_dwordx4 v[76:79], v15, %[srdw], s80 offen sc1\n"
    "s_waitcnt vmcnt(0)\n"
    "v_cmp_ne_u32 vcc, %[tag], v77\n"
    "s_nop 1\n"
    "v_cmp_ne_u32 s[68:69], %[tag], v79\n"
    "s_nop 1\n"
    "s_or_b64 vcc, vcc, s[68:69]\n"
    "s_or_b32 s68, vcc_lo, vcc_hi\n"
    "s_and_b32 s68, s68, 0xff00ff\n"
    "s_cbranch_scc1 L27_%=_l\n"
    "L27_%=_x:\n"
    "s_branch L28_%=\n"
    "L29_%=:\n"
    "L29_%=_l:\n"
    "s_sub_u32 s54, s54, 1\n"
    "s_cselect_b32 s54, 0, s54\n"
    "s_cbranch_scc1 L29_%=_x\n"
    "s_sleep 1\n"
    "buffer_load_dwordx4 v[76:79], v15, %[srdw], s87 offen sc1\n"
    "s_waitcnt vmcnt(0)\n"
    "v_cmp_ne_u32 vcc, %[tag], v77\n"
    "s_nop 1\n"
    "v_cmp_ne_u32 s[68:69], %[tag], v79\n"
    "s_nop 1\n"
    "s_or_b64 vcc, vcc, s[68:69]\n"
    "s_or_b32 s68, vcc_lo, vcc_hi\n"
    "s_and_b32 s68, s68, 0xff00ff\n"
    "s_cbranch_scc1 L29_%=_l\n"
    "L29_%=_x:\n"
    "s_branch L30_%=\n"
    "L33_%=:\n"
    "L33_%=_l:\n"
    "s_sub_u32 s54, s54, 1\n"
    "s_cselect_b32 s54, 0, s54\n"
    "s_cbranch_scc1 L33_%=_x\n"
    "s_sleep 1\n"
    "buffer_load_dwordx4 v[104:107], v15, %[srdw], s91 offen sc1\n"
    "s_waitcnt vmcnt(0)\n"
    "v_cmp_ne_u32 vcc, %[tag], v105\n"
    "s_nop 1\n"
    "v_cmp_ne_u32 s[68:69], %[tag], v107\n"
    "s_nop 1\n"
    "s_or_b64 vcc, vcc, s[68:69]\n"
    "s_or_b32 s68, vcc_lo, vcc_hi\n"
    "s_and_b32 s68, s68, 0xff00ff\n"
    "s_cbranch_scc1 L33_%=_l\n"
    "L33_%=_x:\n"
    "s_branch L34_%=\n"
    "L37_%=:\n"
    "L37_%=_l:\n"
    "s_sub_u32 s54, s54, 1\n"
    "s_cselect_b32 s54, 0, s54\n"
    "s_cbranch_scc1 L37_%=_x\n"
    "s_sleep 1\n"
    "buffer_load_dwordx2 v[96:97], v98, %[srdw], %[cs] offen sc1\n"
    "s_waitcnt vmcnt(0)\n"
    "v_cmp_ne_u32 vcc, %[tag], v97\n"
    "s_nop 1\n"
    "s_cmp_lg_u64 vcc, 0\n"
    "s_cbranch_scc1 L37_%=_l\n"
    "L37_%=_x:\n"
    "s_branch L38_%=\n"
    "L39_%=:\n"
    "s_mov_b32 s98, m0\n"
    "s_mov_b32 s99, s68\n"
    "s_waitcnt lgkmcnt(0)\n"
    "L39_%=_l:\n"
    "s_sub_u32 s54, s54, 1\n"
    "s_cselect_b32 s54, 0, s54\n"
    "s_cbranch_scc1 L39_%=_x\n"
    "v_mov_b32 v94, 0\n"
    "s_add_u32 m0, %[ldsx], 43008\n"
    "s_add_u32 s68, s42, 16187392\n"
    "buffer_load_dwordx4 v3, %[srdw], s68 offen offset:0 sc1 lds\n"
    "buffer_load_dwordx4 v3, %[srdw], s68 offen offset:1024 sc1 lds\n"
    "buffer_load_dwordx4 v3, %[srdw], s68 offen offset:2048 sc1 lds\n"
    "buffer_load_dwordx4 v3, %[srdw], s68 offen offset:3072 sc1 lds\n"
    "s_add_u32 m0, %[ldsx], 47104\n"
    "s_add_u32 s68, s42, 16191488\n"
    "buffer_load_dwordx4 v3, %[srdw], s68 offen offset:0 sc1 lds\n"
    "s_waitcnt vmcnt(0)\n"
    "s_nop 7\n"
    "ds_read2st64_b64 v[120:123], v5 offset0:84 offset1:85\n"
    "s_waitcnt lgkmcnt(0)\n"
    "v_mfma_f32_16x16x32_bf16 v[52:55], v[20:23], v[120:123], 0\n"
    "v_max3_u32 v94, v120, v121, v94\n"
    "v_mfma_f32_16x16x32_bf16 v[56:59], v[120:123], v[120:123], 0\n"
    "v_max3_u32 v94, v122, v123, v94\n"
    "s_nop 7\n"
    "ds_read2st64_b64 v[120:123], v5 offset0:86 offset1:87\n"
    "s_waitcnt lgkmcnt(0)\n"
    "v_mfma_f32_16x16x32_bf16 v[52:55], v[20:23], v[120:123], v[52:55]\n"
    "v_max3_u32 v94, v120, v121, v94\n"
    "v_mfma_f32_16x16x32_bf16 v[56:59], v[120:123], v[120:123], v[56:59]\n"
    "v_max3_u32 v94, v122, v123, v94\n"
    "s_nop 7\n"
    "ds_read2st64_b64 v[120:123], v5 offset0:88 offset1:89\n"
    "s_waitcnt lgkmcnt(0)\n"
    "v_mfma_f32_16x16x32_bf16 v[52:55], v[20:23], v[120:123], v[52:55]\n"
    "v_max3_u32 v94, v120, v121, v94\n"
    "v_mfma_f32_16x16x32_bf16 v[56:59], v[120:123], v[120:123], v[56:59]\n"
    "v_max3_u32 v94, v122, v123, v94\n"
    "s_nop 7\n"
    "ds_read2st64_b64 v[120:123], v5 offset0:90 offset1:91\n"
    "s_waitcnt lgkmcnt(0)\n"
    "v_mfma_f32_16x16x32_bf16 v[52:55], v[20:23], v[120:123], v[52:55]\n"
    "v_max3_u32 v94, v120, v121, v94\n"
    "v_mfma_f32_16x16x32_bf16 v[56:59], v[120:123], v[120:123], v[56:59]\n"
    "v_max3_u32 v94, v122, v123, v94\n"
    "s_nop 7\n"
    "ds_read2st64_b64 v[120:123], v5 offset0:92 offset1:93\n"
    "s_waitcnt lgkmcnt(0)\n"
    "v_mfma_f32_16x16x32_bf16 v[52:55], v[20:23], v[120:123], v[52:55]\n"
    "v_max3_u32 v94, v120, v121, v94\n"
    "v_mfma_f32_16x16x32_bf16 v[56:59], v[120:123], v[120:123], v[56:59]\n"
    "v_max3_u32 v94, v122, v123, v94\n"
    "v_cmp_eq_u32 vcc, -1, v94\n"
    "s_nop 1\n"
    "s_cmp_lg_u64 vcc, 0\n"
    "s_cbranch_scc1 L39_%=_l\n"
    "L39_%=_x:\n"
    "s_mov_b32 m0, s98\n"
    "s_mov_b32 s68, s99\n"
    "s_nop 7\n"
    "s_branch L40_%=\n"
    "L41_%=:\n"
    "L41_%=_l:\n"
    "s_sub_u32 s54, s54, 1\n"
    "s_cselect_b32 s54, 0, s54\n"
    "s_cbranch_scc1 L41_%=_x\n"
    "s_sleep 1\n"
    "buffer_load_dwordx4 v[108:111], v15, %[srdw], s56 offen sc1\n"
    "s_waitcnt vmcnt(0)\n"
    "v_cmp_ne_u32 vcc, %[tag], v109\n"
    "s_nop 1\n"
    "v_cmp_ne_u32 s[68:69], %[tag], v111\n"
    "s_nop 1\n"
    "s_or_b64 vcc, vcc, s[68:69]\n"
    "s_or_b32 s68, vcc_lo, vcc_hi\n"
    "s_and_b32 s68, s68, 0xff00ff\n"
    "s_cbranch_scc1 L41_%=_l\n"
    "L41_%=_x:\n"
    "s_branch L42_%=\n"
    "L43_%=:\n"
    "s_mov_b32 s98, m0\n"
    "s_mov_b32 s99, s68\n"
    "s_waitcnt lgkmcnt(0)\n"
    "L43_%=_l:\n"
    "s_sub_u32 s54, s54, 1\n"
    "s_cselect_b32 s54, 0, s54\n"
    "s_cbranch_scc1 L43_%=_x\n"
    "v_mov_b32 v94, 0\n"
    "s_add_u32 m0, %[ldsx], 0\n"
    "s_add_u32 s68, %[x], 16515072\n"
    "buffer_load_dwordx4 v3, %[srdw], s68 offen offset:0 sc1 lds\n"
    "buffer_load_dwordx4 v3, %[srdw], s68 offen offset:1024 sc1 lds\n"
    "buffer_load_dwordx4 v3, %[srdw], s68 offen offset:2048 sc1 lds\n"
    "buffer_load_dwordx4 v3, %[srdw], s68 offen offset:3072 sc1 lds\n"
    "s_add_u32 m0, %[ldsx], 4096\n"
    "s_add_u32 s68, %[x], 16519168\n"
    "buffer_load_dwordx4 v3, %[srdw], s68 offen offset:0 sc1 lds\n"
    "s_waitcnt vmcnt(0)\n"
    "s_nop 7\n"
    "ds_read2st64_b64 v[120:123], v5 offset0:0 offset1:1\n"
    "s_waitcnt lgkmcnt(0)\n"
    "v_mfma_f32_16x16x32_bf16 v[52:55], v[20:23], v[120:123], 0\n"
    "v_max3_u32 v94, v120, v121, v94\n"
    "v_mfma_f32_16x16x32_bf16 v[56:59], v[120:123], v[120:123], 0\n"
    "v_max3_u32 v94, v122, v123, v94\n"
    "s_nop 7\n"
    "ds_read2st64_b64 v[120:123], v5 offset0:2 offset1:3\n"
    "s_waitcnt lgkmcnt(0)\n"
    "v_mfma_f32_16x16x32_bf16 v[52:55], v[20:23], v[120:123], v[52:55]\n"
    "v_max3_u32 v94, v120, v121, v94\n"
    "v_mfma_f32_16x16x32_bf16 v[56:59], v[120:123], v[120:123], v[56:59]\n"
    "v_max3_u32 v94, v122, v123, v94\n"
    "s_nop 7\n"
    "ds_read2st64_b64 v[120:123], v5 offset0:4 offset1:5\n"
    "s_waitcnt lgkmcnt(0)\n"
    "v_mfma_f32_16x16x32_bf16 v[52:55], v[20:23], v[120:123], v[52:55]\n"
    "v_max3_u32 v94, v120, v121, v94\n"
    "v_mfma_f32_16x16x32_bf16 v[56:59], v[120:123], v[120:123], v[56:59]\n"
    "v_max3_u32 v94, v122, v123, v94\n"
    "s_nop 7\n"
    "ds_read2st64_b64 v[120:123], v5 offset0:6 offset1:7\n"
    "s_waitcnt lgkmcnt(0)\n"
    "v_mfma_f32_16x16x32_bf16 v[52:55], v[20:23], v[120:123], v[52:55]\n"
    "v_max3_u32 v94, v120, v121, v94\n"
    "v_mfma_f32_16x16x32_bf16 v[56:59], v[120:123], v[120:123], v[56:59]\n"
    "v_max3_u32 v94, v122, v123, v94\n"
    "s_nop 7\n"
    "ds_read2st64_b64 v[120:123], v5 offset0:8 offset1:9\n"
    "s_waitcnt lgkmcnt(0)\n"
    "v_mfma_f32_16x16x32_bf16 v[52:55], v[20:23], v[120:123], v[52:55]\n"
    "v_max3_u32 v94, v120, v121, v94\n"
    "v_mfma_f32_16x16x32_bf16 v[56:59], v[120:123], v[120:123], v[56:59]\n"
    "v_max3_u32 v94, v122, v123, v94\n"
    "v_cmp_eq_u32 vcc, -1, v94\n"
    "s_nop 1\n"
    "s_cmp_lg_u64 vcc, 0\n"
    "s_cbranch_scc1 L43_%=_l\n"
    "L43_%=_x:\n"
    "s_mov_b32 m0, s98\n"
    "s_mov_b32 s68, s99\n"
    "s_nop 7\n"
    "s_branch L44_%=\n"
    "L45_%=:\n"
    "s_mov_b32 s98, m0\n"
    "s_mov_b32 s99, s68\n"
    "s_waitcnt lgkmcnt(0)\n"
    "L45_%=_l:\n"
    "s_sub_u32 s54, s54, 1\n"
    "s_cselect_b32 s54, 0, s54\n"
    "s_cbranch_scc1 L45_%=_x\n"
    "v_mov_b32 v94, 0\n"
    "s_add_u32 m0, %[ldsx], 21504\n"
    "s_add_u32 s68, s42, 16842752\n"
    "buffer_load_dwordx4 v3, %[srdw], s68 offen offset:0 sc1 lds\n"
    "buffer_load_dwordx4 v3, %[srdw], s68 offen offset:1024 sc1 lds\n"
    "buffer_load_dwordx4 v3, %[srdw], s68 offen offset:2048 sc1 lds\n"
    "buffer_load_dwordx4 v3, %[srdw], s68 offen offset:3072 sc1 lds\n"
    "s_add_u32 m0, %[ldsx], 25600\n"
    "s_add_u32 s68, s42, 16846848\n"
    "buffer_load_dwordx4 v3, %[srdw], s68 offen offset:0 sc1 lds\n"
    "s_waitcnt vmcnt(0)\n"
    "s_nop 7\n"
    "ds_read2st64_b64 v[120:123], v5 offset0:42 offset1:43\n"
    "s_waitcnt lgkmcnt(0)\n"
    "v_mfma_f32_16x16x32_bf16 v[52:55], v[20:23], v[120:123], 0\n"
    "v_max3_u32 v94, v120, v121, v94\n"
    "v_mfma_f32_16x16x32_bf16 v[56:59], v[120:123], v[120:123], 0\n"
    "v_max3_u32 v94, v122, v123, v94\n"
    "s_nop 7\n"
    "ds_read2st64_b64 v[120:123], v5 offset0:44 offset1:45\n"
    "s_waitcnt lgkmcnt(0)\n"
    "v_mfma_f32_16x16x32_bf16 v[52:55], v[20:23], v[120:123], v[52:55]\n"
    "v_max3_u32 v94, v120, v121, v94\n"
    "v_mfma_f32_16x16x32_bf16 v[56:59], v[120:123], v[120:123], v[56:59]\n"
    "v_max3_u32 v94, v122, v123, v94\n"
    "s_nop 7\n"
    "ds_read2st64_b64 v[120:123], v5 offset0:46 offset1:47\n"
    "s_waitcnt lgkmcnt(0)\n"
    "v_mfma_f32_16x16x32_bf16 v[52:55], v[20:23], v[120:123], v[52:55]\n"
    "v_max3_u32 v94, v120, v121, v94\n"
    "v_mfma_f32_16x16x32_bf16 v[56:59], v[120:123], v[120:123], v[56:59]\n"
    "v_max3_u32 v94, v122, v123, v94\n"
    "s_nop 7\n"
    "ds_read2st64_b64 v[120:123], v5 offset0:48 offset1:49\n"
    "s_waitcnt lgkmcnt(0)\n"
    "v_mfma_f32_16x16x32_bf16 v[52:55], v[20:23], v[120:123], v[52:55]\n"
    "v_max3_u32 v94, v120, v121, v94\n"
    "v_mfma_f32_16x16x32_bf16 v[56:59], v[120:123], v[120:123], v[56:59]\n"
    "v_max3_u32 v94, v122, v123, v94\n"
    "s_nop 7\n"
    "ds_read2st64_b64 v[120:123], v5 offset0:50 offset1:51\n"
    "s_waitcnt lgkmcnt(0)\n"
    "v_mfma_f32_16x16x32_bf16 v[52:55], v[20:23], v[120:123], v[52:55]\n"
    "v_max3_u32 v94, v120, v121, v94\n"
    "v_mfma_f32_16x16x32_bf16 v[56:59], v[120:123], v[120:123], v[56:59]\n"
    "v_max3_u32 v94, v122, v123, v94\n"
    "v_cmp_eq_u32 vcc, -1, v94\n"
    "s_nop 1\n"
    "s_cmp_lg_u64 vcc, 0\n"
    "s_cbranch_scc1 L45_%=_l\n"
    "L45_%=_x:\n"
    "s_mov_b32 m0, s98\n"
    "s_mov_b32 s68, s99\n"
    "s_nop 7\n"
    "s_branch L46_%=\n"
    "L47_%=:\n"
    "s_mov_b32 s98, m0\n"
    "s_mov_b32 s99, s68\n"
    "s_waitcnt lgkmcnt(0)\n"
    "L47_%=_l:\n"
    "s_sub_u32 s54, s54, 1\n"
    "s_cselect_b32 s54, 0, s54\n"
    "s_cbranch_scc1 L47_%=_x\n"
    "v_mov_b32 v94, 0\n"
    "s_add_u32 m0, %[ldsx], 43008\n"
    "s_add_u32 s68, %[x], 17170432\n"
    "buffer_load_dwordx4 v3, %[srdw], s68 offen offset:0 sc1 lds\n"
    "buffer_load_dwordx4 v3, %[srdw], s68 offen offset:1024 sc1 lds\n"
    "buffer_load_dwordx4 v3, %[srdw], s68 offen offset:2048 sc1 lds\n"
    "buffer_load_dwordx4 v3, %[srdw], s68 offen offset:3072 sc1 lds\n"
    "s_add_u32 m0, %[ldsx], 47104\n"
    "s_add_u32 s68, %[x], 17174528\n"
    "buffer_load_dwordx4 v3, %[srdw], s68 offen offset:0 sc1 lds\n"
    "s_waitcnt vmcnt(0)\n"
    "s_nop 7\n"
    "ds_read2st64_b64 v[120:123], v5 offset0:84 offset1:85\n"
    "s_waitcnt lgkmcnt(0)\n"
    "v_mfma_f32_16x16x32_bf16 v[52:55], v[20:23], v[120:123], 0\n"
    "v_max3_u32 v94, v120, v121, v94\n"
    "v_mfma_f32_16x16x32_bf16 v[56:59], v[120:123], v[120:123], 0\n"
    "v_max3_u32 v94, v122, v123, v94\n"
    "s_nop 7\n"
    "ds_read2st64_b64 v[120:123], v5 offset0:86 offset1:87\n"
    "s_waitcnt lgkmcnt(0)\n"
    "v_mfma_f32_16x16x32_bf16 v[52:55], v[20:23], v[120:123], v[52:55]\n"
    "v_max3_u32 v94, v120, v121, v94\n"
    "v_mfma_f32_16x16x32_bf16 v[56:59], v[120:123], v[120:123], v[56:59]\n"
    "v_max3_u32 v94, v122, v123, v94\n"
    "s_nop 7\n"
    "ds_read2st64_b64 v[120:123], v5 offset0:88 offset1:89\n"
    "s_waitcnt lgkmcnt(0)\n"
    "v_mfma_f32_16x16x32_bf16 v[52:55], v[20:23], v[120:123], v[52:55]\n"
    "v_max3_u32 v94, v120, v121, v94\n"
    "v_mfma_f32_16x16x32_bf16 v[56:59], v[120:123], v[120:123], v[56:59]\n"
    "v_max3_u32 v94, v122, v123, v94\n"
    "s_nop 7\n"
    "ds_read2st64_b64 v[120:123], v5 offset0:90 offset1:91\n"
    "s_waitcnt lgkmcnt(0)\n"
    "v_mfma_f32_16x16x32_bf16 v[52:55], v[20:23], v[120:123], v[52:55]\n"
    "v_max3_u32 v94, v120, v121, v94\n"
    "v_mfma_f32_16x16x32_bf16 v[56:59], v[120:123], v[120:123], v[56:59]\n"
    "v_max3_u32 v94, v122, v123, v94\n"
    "s_nop 7\n"
    "ds_read2st64_b64 v[120:123], v5 offset0:92 offset1:93\n"
    "s_waitcnt lgkmcnt(0)\n"
    "v_mfma_f32_16x16x32_bf16 v[52:55], v[20:23], v[120:123], v[52:55]\n"
    "v_max3_u32 v94, v120, v121, v94\n"
    "v_mfma_f32_16x16x32_bf16 v[56:59], v[120:123], v[120:123], v[56:59]\n"
    "v_max3_u32 v94, v122, v123, v94\n"
    "v_cmp_eq_u32 vcc, -1, v94\n"
    "s_nop 1\n"
    "s_cmp_lg_u64 vcc, 0\n"
    "s_cbranch_scc1 L47_%=_l\n"
    "L47_%=_x:\n"
    "s_mov_b32 m0, s98\n"
    "s_mov_b32 s68, s99\n"
    "s_nop 7\n"
    "s_branch L48_%=\n"
    "L49_%=:\n"
    "s_mov_b32 s98, m0\n"
    "s_mov_b32 s99, s68\n"
    "s_waitcnt lgkmcnt(0)\n"
    "L49_%=_l:\n"
    "s_sub_u32 s54, s54, 1\n"
    "s_cselect_b32 s54, 0, s54\n"
    "s_cbranch_scc1 L49_%=_x\n"
    "v_mov_b32 v94, 0\n"
    "s_add_u32 m0, %[ldsx], 0\n"
    "s_add_u32 s68, s42, 17498112\n"
    "buffer_load_dwordx4 v3, %[srdw], s68 offen offset:0 sc1 lds\n"
    "buffer_load_dwordx4 v3, %[srdw], s68 offen offset:1024 sc1 lds\n"
    "buffer_load_dwordx4 v3, %[srdw], s68 offen offset:2048 sc1 lds\n"
    "buffer_load_dwordx4 v3, %[srdw], s68 offen offset:3072 sc1 lds\n"
    "s_add_u32 m0, %[ldsx], 4096\n"
    "s_add_u32 s68, s42, 17502208\n"
    "buffer_load_dwordx4 v3, %[srdw], s68 offen offset:0 sc1 lds\n"
    "s_waitcnt vmcnt(0)\n"
    "s_nop 7\n"
    "ds_read2st64_b64 v[120:123], v5 offset0:0 offset1:1\n"
    "s_waitcnt lgkmcnt(0)\n"
    "v_mfma_f32_16x16x32_bf16 v[52:55], v[20:23], v[120:123], 0\n"
    "v_max3_u32 v94, v120, v121, v94\n"
    "v_mfma_f32_16x16x32_bf16 v[56:59], v[120:123], v[120:123], 0\n"
    "v_max3_u32 v94, v122, v123, v94\n"
    "s_nop 7\n"
    "ds_read2st64_b64 v[120:123], v5 offset0:2 offset1:3\n"
    "s_waitcnt lgkmcnt(0)\n"
    "v_mfma_f32_16x16x32_bf16 v[52:55], v[20:23], v[120:123], v[52:55]\n"
    "v_max3_u32 v94, v120, v121, v94\n"
    "v_mfma_f32_16x16x32_bf16 v[56:59], v[120:123], v[120:123], v[56:59]\n"
    "v_max3_u32 v94, v122, v123, v94\n"
    "s_nop 7\n"
    "ds_read2st64_b64 v[120:123], v5 offset0:4 offset1:5\n"
    "s_waitcnt lgkmcnt(0)\n"
    "v_mfma_f32_16x16x32_bf16 v[52:55], v[20:23], v[120:123], v[52:55]\n"
    "v_max3_u32 v94, v120, v121, v94\n"
    "v_mfma_f32_16x16x32_bf16 v[56:59], v[120:123], v[120:123], v[56:59]\n"
    "v_max3_u32 v94, v122, v123, v94\n"
    "s_nop 7\n"
    "ds_read2st64_b64 v[120:123], v5 offset0:6 offset1:7\n"
    "s_waitcnt lgkmcnt(0)\n"
    "v_mfma_f32_16x16x32_bf16 v[52:55], v[20:23], v[120:123], v[52:55]\n"
    "v_max3_u32 v94, v120, v121, v94\n"
    "v_mfma_f32_16x16x32_bf16 v[56:59], v[120:123], v[120:123], v[56:59]\n"
    "v_max3_u32 v94, v122, v123, v94\n"
    "s_nop 7\n"
    "ds_read2st64_b64 v[120:123], v5 offset0:8 offset1:9\n"
    "s_waitcnt lgkmcnt(0)\n"
    "v_mfma_f32_16x16x32_bf16 v[52:55], v[20:23], v[120:123], v[52:55]\n"
    "v_max3_u32 v94, v120, v121, v94\n"
    "v_mfma_f32_16x16x32_bf16 v[56:59], v[120:123], v[120:123], v[56:59]\n"
    "v_max3_u32 v94, v122, v123, v94\n"
    "v_cmp_eq_u32 vcc, -1, v94\n"
    "s_nop 1\n"
    "s_cmp_lg_u64 vcc, 0\n"
    "s_cbranch_scc1 L49_%=_l\n"
    "L49_%=_x:\n"
    "s_mov_b32 m0, s98\n"
    "s_mov_b32 s68, s99\n"
    "s_nop 7\n"
    "s_branch L50_%=\n"
    "L51_%=:\n"
    "s_mov_b32 s98, m0\n"
    "s_mov_b32 s99, s68\n"
    "s_waitcnt lgkmcnt(0)\n"
    "L51_%=_l:\n"
    "s_sub_u32 s54, s54, 1\n"
    "s_cselect_b32 s54, 0, s54\n"
    "s_cbranch_scc1 L51_%=_x\n"
    "v_mov_b32 v94, 0\n"
    "s_add_u32 m0, %[ldsx], 21504\n"
    "s_add_u32 s68, %[x], 17825792\n"
    "buffer_load_dwordx4 v3, %[srdw], s68 offen offset:0 sc1 lds\n"
    "buffer_load_dwordx4 v3, %[srdw], s68 offen offset:1024 sc1 lds\n"
    "buffer_load_dwordx4 v3, %[srdw], s68 offen offset:2048 sc1 lds\n"
    "buffer_load_dwordx4 v3, %[srdw], s68 offen offset:3072 sc1 lds\n"
    "s_add_u32 m0, %[ldsx], 25600\n"
    "s_add_u32 s68, %[x], 17829888\n"
    "buffer_load_dwordx4 v3, %[srdw], s68 offen offset:0 sc1 lds\n"
    "s_waitcnt vmcnt(0)\n"
    "s_nop 7\n"
    "ds_read2st64_b64 v[120:123], v5 offset0:42 offset1:43\n"
    "s_waitcnt lgkmcnt(0)\n"
    "v_mfma_f32_16x16x32_bf16 v[52:55], v[20:23], v[120:123], 0\n"
    "v_max3_u32 v94, v120, v121, v94\n"
    "v_mfma_f32_16x16x32_bf16 v[56:59], v[120:123], v[120:123], 0\n"
    "v_max3_u32 v94, v122, v123, v94\n"
    "s_nop 7\n"
    "ds_read2st64_b64 v[120:123], v5 offset0:44 offset1:45\n"
    "s_waitcnt lgkmcnt(0)\n"
    "v_mfma_f32_16x16x32_bf16 v[52:55], v[20:23], v[120:123], v[52:55]\n"
    "v_max3_u32 v94, v120, v121, v94\n"
    "v_mfma_f32_16x16x32_bf16 v[56:59], v[120:123], v[120:123], v[56:59]\n"
    "v_max3_u32 v94, v122, v123, v94\n"
    "s_nop 7\n"
    "ds_read2st64_b64 v[120:123], v5 offset0:46 offset1:47\n"
    "s_waitcnt lgkmcnt(0)\n"
    "v_mfma_f32_16x16x32_bf16 v[52:55], v[20:23], v[120:123], v[52:55]\n"
    "v_max3_u32 v94, v120, v121, v94\n"
    "v_mfma_f32_16x16x32_bf16 v[56:59], v[120:123], v[120:123], v[56:59]\n"
    "v_max3_u32 v94, v122, v123, v94\n"
    "s_nop 7\n"
    "ds_read2st64_b64 v[120:123], v5 offset0:48 offset1:49\n"
    "s_waitcnt lgkmcnt(0)\n"
    "v_mfma_f32_16x16x32_bf16 v[52:55], v[20:23], v[120:123], v[52:55]\n"
    "v_max3_u32 v94, v120, v121, v94\n"
    "v_mfma_f32_16x16x32_bf16 v[56:59], v[120:123], v[120:123], v[56:59]\n"
    "v_max3_u32 v94, v122, v123, v94\n"
    "s_nop 7\n"
    "ds_read2st64_b64 v[120:123], v5 offset0:50 offset1:51\n"
    "s_waitcnt lgkmcnt(0)\n"
    "v_mfma_f32_16x16x32_bf16 v[52:55], v[20:23], v[120:123], v[52:55]\n"
    "v_max3_u32 v94, v120, v121, v94\n"
    "v_mfma_f32_16x16x32_bf16 v[56:59], v[120:123], v[120:123], v[56:59]\n"
    "v_max3_u32 v94, v122, v123, v94\n"
    "v_cmp_eq_u32 vcc, -1, v94\n"
    "s_nop 1\n"
    "s_cmp_lg_u64 vcc, 0\n"
    "s_cbranch_scc1 L51_%=_l\n"
    "L51_%=_x:\n"
    "s_mov_b32 m0, s98\n"
    "s_mov_b32 s68, s99\n"
    "s_nop 7\n"
    "s_branch L52_%=\n"
    "L53_%=:\n"
    "L53_%=_l:\n"
    "s_sub_u32 s54, s54, 1\n"
    "s_cselect_b32 s54, 0, s54\n"
    "s_cbranch_scc1 L53_%=_x\n"
    "s_sleep 1\n"
    "s_mov_b64 exec, 0xf\n"
    "buffer_load_dword v78, v1, %[srdw], s81 offen sc1\n"
    "s_mov_b64 exec, -1\n"
    "s_waitcnt vmcnt(0)\n"
    "v_cmp_ne_u32 vcc, %[tag], v78\n"
    "s_nop 1\n"
    "s_and_b32 vcc_lo, vcc_lo, 0xf\n"
    "s_cbranch_scc1 L53_%=_l\n"
    "L53_%=_x:\n"
    "s_branch L54_%=\n"
    "L55_%=:\n"
    "s_mov_b32 s98, m0\n"
    "s_mov_b32 s99, s68\n"
    "s_waitcnt lgkmcnt(0)\n"
    "L55_%=_l:\n"
    "s_sub_u32 s54, s54, 1\n"
    "s_cselect_b32 s54, 0, s54\n"
    "s_cbranch_scc1 L55_%=_x\n"
    "v_mov_b32 v94, 0\n"
    "s_add_u32 m0, %[ldsx], 43008\n"
    "s_add_u32 s68, s42, 18153472\n"
    "buffer_load_dwordx4 v3, %[srdw], s68 offen offset:0 sc1 lds\n"
    "buffer_load_dwordx4 v3, %[srdw], s68 offen offset:1024 sc1 lds\n"
    "buffer_load_dwordx4 v3, %[srdw], s68 offen offset:2048 sc1 lds\n"
    "buffer_load_dwordx4 v3, %[srdw], s68 offen offset:3072 sc1 lds\n"
    "s_add_u32 m0, %[ldsx], 47104\n"
    "s_add_u32 s68, s42, 18157568\n"
    "buffer_load_dwordx4 v3, %[srdw], s68 offen offset:0 sc1 lds\n"
    "s_waitcnt vmcnt(0)\n"
    "s_nop 7\n"
    "ds_read2st64_b64 v[120:123], v5 offset0:84 offset1:85\n"
    "s_waitcnt lgkmcnt(0)\n"
    "v_mfma_f32_16x16x32_bf16 v[52:55], v[20:23], v[120:123], 0\n"
    "v_max3_u32 v94, v120, v121, v94\n"
    "v_mfma_f32_16x16x32_bf16 v[56:59], v[120:123], v[120:123], 0\n"
    "v_max3_u32 v94, v122, v123, v94\n"
    "s_nop 7\n"
    "ds_read2st64_b64 v[120:123], v5 offset0:86 offset1:87\n"
    "s_waitcnt lgkmcnt(0)\n"
    "v_mfma_f32_16x16x32_bf16 v[52:55], v[20:23], v[120:123], v[52:55]\n"
    "v_max3_u32 v94, v120, v121, v94\n"
    "v_mfma_f32_16x16x32_bf16 v[56:59], v[120:123], v[120:123], v[56:59]\n"
    "v_max3_u32 v94, v122, v123, v94\n"
    "s_nop 7\n"
    "ds_read2st64_b64 v[120:123], v5 offset0:88 offset1:89\n"
    "s_waitcnt lgkmcnt(0)\n"
    "v_mfma_f32_16x16x32_bf16 v[52:55], v[20:23], v[120:123], v[52:55]\n"
    "v_max3_u32 v94, v120, v121, v94\n"
    "v_mfma_f32_16x16x32_bf16 v[56:59], v[120:123], v[120:123], v[56:59]\n"
    "v_max3_u32 v94, v122, v123, v94\n"
    "s_nop 7\n"
    "ds_read2st64_b64 v[120:123], v5 offset0:90 offset1:91\n"
    "s_waitcnt lgkmcnt(0)\n"
    "v_mfma_f32_16x16x32_bf16 v[52:55], v[20:23], v[120:123], v[52:55]\n"
    "v_max3_u32 v94, v120, v121, v94\n"
    "v_mfma_f32_16x16x32_bf16 v[56:59], v[120:123], v[120:123], v[56:59]\n"
    "v_max3_u32 v94, v122, v123, v94\n"
    "s_nop 7\n"
    "ds_read2st64_b64 v[120:123], v5 offset0:92 offset1:93\n"
    "s_waitcnt lgkmcnt(0)\n"
    "v_mfma_f32_16x16x32_bf16 v[52:55], v[20:23], v[120:123], v[52:55]\n"
    "v_max3_u32 v94, v120, v121, v94\n"
    "v_mfma_f32_16x16x32_bf16 v[56:59], v[120:123], v[120:123], v[56:59]\n"
    "v_max3_u32 v94, v122, v123, v94\n"
    "v_cmp_eq_u32 vcc, -1, v94\n"
    "s_nop 1\n"
    "s_cmp_lg_u64 vcc, 0\n"
    "s_cbranch_scc1 L55_%=_l\n"
    "L55_%=_x:\n"
    "s_mov_b32 m0, s98\n"
    "s_mov_b32 s68, s99\n"
    "s_nop 7\n"
    "s_branch L56_%=\n"
    "L57_%=:\n"
    "L57_%=_l:\n"
    "s_sub_u32 s54, s54, 1\n"
    "s_cselect_b32 s54, 0, s54\n"
    "s_cbranch_scc1 L57_%=_x\n"
    "s_sleep 1\n"
    "s_mov_b64 exec, 0xf\n"
    "buffer_load_dword v3, v1, %[srdw], s84 offen sc1\n"
    "s_mov_b64 exec, -1\n"
    "s_waitcnt vmcnt(0)\n"
    "v_cmp_ne_u32 vcc, %[tag], v3\n"
    "s_nop 1\n"
    "s_and_b32 vcc_lo, vcc_lo, 0xf\n"
    "s_cbranch_scc1 L57_%=_l\n"
    "L57_%=_x:\n"
    "s_branch L58_%=\n"
    "L31_%=:\n"
    "s_waitcnt vmcnt(0) lgkmcnt(0)\n"
    "ds_write_b128 v12, v[80:83] offset:0\n"
    "ds_write_b128 v12, v[84:87] offset:64\n"
    "ds_write_b128 v12, v[88:91] offset:128\n"
    "ds_write_b128 v12, v[92:95] offset:192\n"
    "s_waitcnt lgkmcnt(0)\n"
    "L31_%=_l:\n"
    "s_add_u32 s98, s44, 14286848\n"
    "buffer_load_dwordx4 v[80:83], v0, %[srdw], s98 offen offset:0 sc1\n"
    "buffer_load_dwordx4 v[84:87], v0, %[srdw], s98 offen offset:1024 sc1\n"
    "buffer_load_dwordx4 v[88:91], v0, %[srdw], s98 offen offset:2048 sc1\n"
    "s_add_u32 s98, s45, 14286848\n"
    "buffer_load_dwordx4 v[92:95], v0, %[srdw], s98 offen offset:0 sc1\n"
    "buffer_load_dwordx4 v[96:99], v0, %[srdw], s98 offen offset:1024 sc1\n"
    "buffer_load_dwordx4 v[100:103], v0, %[srdw], s98 offen offset:2048 sc1\n"
    "s_add_u32 s98, s46, 14286848\n"
    "buffer_load_dwordx4 v[104:107], v0, %[srdw], s98 offen offset:0 sc1\n"
    "buffer_load_dwordx4 v[108:111], v0, %[srdw], s98 offen offset:1024 sc1\n"
    "buffer_load_dwordx4 v[112:115], v0, %[srdw], s98 offen offset:2048 sc1\n"
    "s_add_u32 s98, s47, 14286848\n"
    "buffer_load_dwordx4 v[116:119], v0, %[srdw], s98 offen offset:0 sc1\n"
    "buffer_load_dwordx4 v[120:123], v0, %[srdw], s98 offen offset:1024 sc1\n"
    "buffer_load_dwordx4 v[124:127], v0, %[srdw], s98 offen offset:2048 sc1\n"
    "s_mov_b64 s[96:97], 0\n"
    "s_waitcnt vmcnt(0)\n"
    "v_cmp_eq_u32 vcc, -1, v80\n"
    "s_nop 1\n"
    "s_or_b64 s[96:97], s[96:97], vcc\n"
    "v_cmp_eq_u32 vcc, -1, v81\n"
    "s_nop 1\n"
    "s_or_b64 s[96:97], s[96:97], vcc\n"
    "v_cmp_eq_u32 vcc, -1, v82\n"
    "s_nop 1\n"
    "s_or_b64 s[96:97], s[96:97], vcc\n"
    "v_cmp_eq_u32 vcc, -1, v83\n"
    "s_nop 1\n"
    "s_or_b64 s[96:97], s[96:97], vcc\n"
    "v_cmp_eq_u32 vcc, -1, v84\n"
    "s_nop 1\n"
    "s_or_b64 s[96:97], s[96:97], vcc\n"
    "v_cmp_eq_u32 vcc, -1, v85\n"
    "s_nop 1\n"
    "s_or_b64 s[96:97], s[96:97], vcc\n"
    "v_cmp_eq_u32 vcc, -1, v86\n"
    "s_nop 1\n"
    "s_or_b64 s[96:97], s[96:97], vcc\n"
    "v_cmp_eq_u32 vcc, -1, v87\n"
    "s_nop 1\n"
    "s_or_b64 s[96:97], s[96:97], vcc\n"
    "v_cmp_eq_u32 vcc, -1, v88\n"
    "s_nop 1\n"
    "s_or_b64 s[96:97], s[96:97], vcc\n"
    "v_cmp_eq_u32 vcc, -1, v89\n"
    "s_nop 1\n"
    "s_or_b64 s[96:97], s[96:97], vcc\n"
    "v_cmp_eq_u32 vcc, -1, v90\n"
    "s_nop 1\n"
    "s_or_b64 s[96:97], s[96:97], vcc\n"
    "v_cmp_eq_u32 vcc, -1, v91\n"
    "s_nop 1\n"
    "s_or_b64 s[96:97], s[96:97], vcc\n"
    "v_cmp_eq_u32 vcc, -1, v92\n"
    "s_nop 1\n"
    "s_or_b64 s[96:97], s[96:97], vcc\n"
    "v_cmp_eq_u32 vcc, -1, v93\n"
    "s_nop 1\n"
    "s_or_b64 s[96:97], s[96:97], vcc\n"
    "v_cmp_eq_u32 vcc, -1, v94\n"
    "s_nop 1\n"
    "s_or_b64 s[96:97], s[96:97], vcc\n"
    "v_cmp_eq_u32 vcc, -1, v95\n"
    "s_nop 1\n"
    "s_or_b64 s[96:97], s[96:97], vcc\n"
    "v_cmp_eq_u32 vcc, -1, v96\n"
    "s_nop 1\n"
    "s_or_b64 s[96:97], s[96:97], vcc\n"
    "v_cmp_eq_u32 vcc, -1, v97\n"
    "s_nop 1\n"
    "s_or_b64 s[96:97], s[96:97], vcc\n"
    "v_cmp_eq_u32 vcc, -1, v98\n"
    "s_nop 1\n"
    "s_or_b64 s[96:97], s[96:97], vcc\n"
    "v_cmp_eq_u32 vcc, -1, v99\n"
    "s_nop 1\n"
    "s_or_b64 s[96:97], s[96:97], vcc\n"
    "v_cmp_eq_u32 vcc, -1, v100\n"
    "s_nop 1\n"
    "s_or_b64 s[96:97], s[96:97], vcc\n"
    "v_cmp_eq_u32 vcc, -1, v101\n"
    "s_nop 1\n"
    "s_or_b64 s[96:97], s[96:97], vcc\n"
    "v_cmp_eq_u32 vcc, -1, v102\n"
    "s_nop 1\n"
    "s_or_b64 s[96:97], s[96:97], vcc\n"
    "v_cmp_eq_u32 vcc, -1, v103\n"
    "s_nop 1\n"
    "s_or_b64 s[96:97], s[96:97], vcc\n"
    "v_cmp_eq_u32 vcc, -1, v104\n"
    "s_nop 1\n"
    "s_or_b64 s[96:97], s[96:97], vcc\n"
    "v_cmp_eq_u32 vcc, -1, v105\n"
    "s_nop 1\n"
    "s_or_b64 s[96:97], s[96:97], vcc\n"
    "v_cmp_eq_u32 vcc, -1, v106\n"
    "s_nop 1\n"
    "s_or_b64 s[96:97], s[96:97], vcc\n"
    "v_cmp_eq_u32 vcc, -1, v107\n"
    "s_nop 1\n"
    "s_or_b64 s[96:97], s[96:97], vcc\n"
    "v_cmp_eq_u32 vcc, -1, v108\n"
    "s_nop 1\n"
    "s_or_b64 s[96:97], s[96:97], vcc\n"
    "v_cmp_eq_u32 vcc, -1, v109\n"
    "s_nop 1\n"
    "s_or_b64 s[96:97], s[96:97], vcc\n"
    "v_cmp_eq_u32 vcc, -1, v110\n"
    "s_nop 1\n"
    "s_or_b64 s[96:97], s[96:97], vcc\n"
    "v_cmp_eq_u32 vcc, -1, v111\n"
    "s_nop 1\n"
    "s_or_b64 s[96:97], s[96:97], vcc\n"
    "v_cmp_eq_u32 vcc, -1, v112\n"
    "s_nop 1\n"
    "s_or_b64 s[96:97], s[96:97], vcc\n"
    "v_cmp_eq_u32 vcc, -1, v113\n"
    "s_nop 1\n"
    "s_or_b64 s[96:97], s[96:97], vcc\n"
    "v_cmp_eq_u32 vcc, -1, v114\n"
    "s_nop 1\n"
    "s_or_b64 s[96:97], s[96:97], vcc\n"
    "v_cmp_eq_u32 vcc, -1, v115\n"
    "s_nop 1\n"
    "s_or_b64 s[96:97], s[96:97], vcc\n"
    "v_cmp_eq_u32 vcc, -1, v116\n"
    "s_nop 1\n"
    "s_or_b64 s[96:97], s[96:97], vcc\n"
    "v_cmp_eq_u32 vcc, -1, v117\n"
    "s_nop 1\n"
    "s_or_b64 s[96:97], s[96:97], vcc\n"
    "v_cmp_eq_u32 vcc, -1, v118\n"
    "s_nop 1\n"
    "s_or_b64 s[96:97], s[96:97], vcc\n"
    "v_cmp_eq_u32 vcc, -1, v119\n"
    "s_nop 1\n"
    "s_or_b64 s[96:97], s[96:97], vcc\n"
    "v_cmp_eq_u32 vcc, -1, v120\n"
    "s_nop 1\n"
    "s_or_b64 s[96:97], s[96:97], vcc\n"
    "v_cmp_eq_u32 vcc, -1, v121\n"
    "s_nop 1\n"
    "s_or_b64 s[96:97], s[96:97], vcc\n"
    "v_cmp_eq_u32 vcc, -1, v122\n"
    "s_nop 1\n"
    "s_or_b64 s[96:97], s[96:97], vcc\n"
    "v_cmp_eq_u32 vcc, -1, v123\n"
    "s_nop 1\n"
    "s_or_b64 s[96:97], s[96:97], vcc\n"
    "v_cmp_eq_u32 vcc, -1, v124\n"
    "s_nop 1\n"
    "s_or_b64 s[96:97], s[96:97], vcc\n"
    "v_cmp_eq_u32 vcc, -1, v125\n"
    "s_nop 1\n"
    "s_or_b64 s[96:97], s[96:97], vcc\n"
    "v_cmp_eq_u32 vcc, -1, v126\n"
    "s_nop 1\n"
    "s_or_b64 s[96:97], s[96:97], vcc\n"
    "v_cmp_eq_u32 vcc, -1, v127\n"
    "s_nop 1\n"
    "s_or_b64 s[96:97], s[96:97], vcc\n"
    "s_cmp_eq_u64 s[96:97], 0\n"
    "s_cbranch_scc1 L31_%=_x\n"
    "s_sub_u32 s54, s54, 1\n"
    "s_cbranch_scc0 L31_%=_l\n"
    "s_mov_b32 s54, 0\n"
    "L31_%=_x:\n"
    "v_mfma_f32_16x16x32_bf16 v[40:43], a[240:243], v[80:83], 0\n"
    "s_nop 7\n"
    "ds_read_b128 v[24:27], v11 offset:0\n"
    "s_waitcnt lgkmcnt(0)\n"
    "v_mfma_f32_16x16x32_bf16 v[44:47], v[24:27], v[80:83], 0\n"
    "v_mfma_f32_16x16x32_bf16 v[48:51], a[244:247], v[80:83], 0\n"
    "s_nop 7\n"
    "ds_read_b128 v[24:27], v11 offset:1024\n"
    "s_waitcnt lgkmcnt(0)\n"
    "v_mfma_f32_16x16x32_bf16 v[52:55], v[24:27], v[80:83], 0\n"
    "v_mfma_f32_16x16x32_bf16 v[56:59], a[248:251], v[80:83], 0\n"
    "v_mfma_f32_16x16x32_bf16 v[40:43], a[252:255], v[84:87], v[40:43]\n"
    "s_nop 7\n"
    "ds_read_b128 v[24:27], v11 offset:2048\n"
    "s_waitcnt lgkmcnt(0)\n"
    "v_mfma_f32_16x16x32_bf16 v[44:47], v[24:27], v[84:87], v[44:47]\n"
    "v_mfma_f32_16x16x32_bf16 v[48:51], v[128:131], v[84:87], v[48:51]\n"
    "s_nop 7\n"
    "ds_read_b128 v[24:27], v11 offset:3072\n"
    "s_waitcnt lgkmcnt(0)\n"
    "v_mfma_f32_16x16x32_bf16 v[52:55], v[24:27], v[84:87], v[52:55]\n"
    "v_mfma_f32_16x16x32_bf16 v[56:59], v[132:135], v[84:87], v[56:59]\n"
    "v_mfma_f32_16x16x32_bf16 v[40:43], v[136:139], v[88:91], v[40:43]\n"
    "s_nop 7\n"
    "ds_read_b128 v[24:27], v11 offset:4096\n"
    "s_waitcnt lgkmcnt(0)\n"
    "v_mfma_f32_16x16x32_bf16 v[44:47], v[24:27], v[88:91], v[44:47]\n"
    "v_mfma_f32_16x16x32_bf16 v[48:51], v[140:143], v[88:91], v[48:51]\n"
    "s_nop 7\n"
    "ds_read_b128 v[24:27], v11 offset:5120\n"
    "s_waitcnt lgkmcnt(0)\n"
    "v_mfma_f32_16x16x32_bf16 v[52:55], v[24:27], v[88:91], v[52:55]\n"
    "v_mfma_f32_16x16x32_bf16 v[56:59], v[144:147], v[88:91], v[56:59]\n"
    "v_mfma_f32_16x16x32_bf16 v[40:43], v[148:151], v[92:95], v[40:43]\n"
    "s_nop 7\n"
    "ds_read_b128 v[24:27], v11 offset:6144\n"
    "s_waitcnt lgkmcnt(0)\n"
    "v_mfma_f32_16x16x32_bf16 v[44:47], v[24:27], v[92:95], v[44:47]\n"
    "v_mfma_f32_16x16x32_bf16 v[48:51], v[152:155], v[92:95], v[48:51]\n"
    "s_nop 7\n"
    "ds_read_b128 v[24:27], v11 offset:7168\n"
    "s_waitcnt lgkmcnt(0)\n"
    "v_mfma_f32_16x16x32_bf16 v[52:55], v[24:27], v[92:95], v[52:55]\n"
    "v_mfma_f32_16x16x32_bf16 v[56:59], v[156:159], v[92:95], v[56:59]\n"
    "v_mfma_f32_16x16x32_bf16 v[40:43], v[160:163], v[96:99], v[40:43]\n"
    "s_nop 7\n"
    "ds_read_b128 v[24:27], v11 offset:8192\n"
    "s_waitcnt lgkmcnt(0)\n"
    "v_mfma_f32_16x16x32_bf16 v[44:47], v[24:27], v[96:99], v[44:47]\n"
    "v_mfma_f32_16x16x32_bf16 v[48:51], v[164:167], v[96:99], v[48:51]\n"
    "s_nop 7\n"
    "ds_read_b128 v[24:27], v11 offset:9216\n"
    "s_waitcnt lgkmcnt(0)\n"
    "v_mfma_f32_16x16x32_bf16 v[52:55], v[24:27], v[96:99], v[52:55]\n"
    "v_mfma_f32_16x16x32_bf16 v[56:59], v[168:171], v[96:99], v[56:59]\n"
    "v_mfma_f32_16x16x32_bf16 v[40:43], v[172:175], v[100:103], v[40:43]\n"
    "s_nop 7\n"
    "ds_read_b128 v[24:27], v11 offset:10240\n"
    "s_waitcnt lgkmcnt(0)\n"
    "v_mfma_f32_16x16x32_bf16 v[44:47], v[24:27], v[100:103], v[44:47]\n"
    "v_mfma_f32_16x16x32_bf16 v[48:51], v[176:179], v[100:103], v[48:51]\n"
    "s_nop 7\n"
    "ds_read_b128 v[24:27], v11 offset:11264\n"
    "s_waitcnt lgkmcnt(0)\n"
    "v_mfma_f32_16x16x32_bf16 v[52:55], v[24:27], v[100:103], v[52:55]\n"
    "v_mfma_f32_16x16x32_bf16 v[56:59], v[180:183], v[100:103], v[56:59]\n"
    "v_mfma_f32_16x16x32_bf16 v[40:43], v[184:187], v[104:107], v[40:43]\n"
    "s_nop 7\n"
    "ds_read_b128 v[24:27], v11 offset:12288\n"
    "s_waitcnt lgkmcnt(0)\n"
    "v_mfma_f32_16x16x32_bf16 v[44:47], v[24:27], v[104:107], v[44:47]\n"
    "v_mfma_f32_16x16x32_bf16 v[48:51], v[188:191], v[104:107], v[48:51]\n"
    "s_nop 7\n"
    "ds_read_b128 v[24:27], v11 offset:13312\n"
    "s_waitcnt lgkmcnt(0)\n"
    "v_mfma_f32_16x16x32_bf16 v[52:55], v[24:27], v[104:107], v[52:55]\n"
    "v_mfma_f32_16x16x32_bf16 v[56:59], v[192:195], v[104:107], v[56:59]\n"
    "v_mfma_f32_16x16x32_bf16 v[40:43], v[196:199], v[108:111], v[40:43]\n"
    "s_nop 7\n"
    "ds_read_b128 v[24:27], v11 offset:14336\n"
    "s_waitcnt lgkmcnt(0)\n"
    "v_mfma_f32_16x16x32_bf16 v[44:47], v[24:27], v[108:111], v[44:47]\n"
    "v_mfma_f32_16x16x32_bf16 v[48:51], v[200:203], v[108:111], v[48:51]\n"
    "s_nop 7\n"
    "ds_read_b128 v[24:27], v11 offset:15360\n"
    "s_waitcnt lgkmcnt(0)\n"
    "v_mfma_f32_16x16x32_bf16 v[52:55], v[24:27], v[108:111], v[52:55]\n"
    "v_mfma_f32_16x16x32_bf16 v[56:59], v[204:207], v[108:111], v[56:59]\n"
    "v_mfma_f32_16x16x32_bf16 v[40:43], v[208:211], v[112:115], v[40:43]\n"
    "v_mfma_f32_16x16x32_bf16 v[40:43], v[220:223], v[116:119], v[40:43]\n"
    "v_mfma_f32_16x16x32_bf16 v[40:43], v[232:235], v[120:123], v[40:43]\n"
    "v_mfma_f32_16x16x32_bf16 v[40:43], v[244:247], v[124:127], v[40:43]\n"
    "s_nop 7\n"
    "ds_read_b128 v[24:27], v11 offset:16384\n"
    "s_waitcnt lgkmcnt(0)\n"
    "v_mfma_f32_16x16x32_bf16 v[44:47], v[24:27], v[112:115], v[44:47]\n"
    "s_nop 7\n"
    "ds_read_b128 v[24:27], v11 offset:18432\n"
    "s_waitcnt lgkmcnt(0)\n"
    "v_mfma_f32_16x16x32_bf16 v[44:47], v[24:27], v[116:119], v[44:47]\n"
    "s_nop 7\n"
    "ds_read_b128 v[24:27], v11 offset:20480\n"
    "s_waitcnt lgkmcnt(0)\n"
    "v_mfma_f32_16x16x32_bf16 v[44:47], v[24:27], v[120:123], v[44:47]\n"
    "s_nop 7\n"
    "ds_read_b128 v[24:27], v11 offset:22528\n"
    "s_waitcnt lgkmcnt(0)\n"
    "v_mfma_f32_16x16x32_bf16 v[44:47], v[24:27], v[124:127], v[44:47]\n"
    "s_nop 7\n"
    "ds_read_b128 v[24:27], v11 offset:20480\n"
    "ds_read_b128 v[80:83], v12 offset:0\n"
    "ds_read_b128 v[84:87], v12 offset:64\n"
    "ds_read_b128 v[88:91], v12 offset:128\n"
    "ds_read_b128 v[92:95], v12 offset:192\n"
    "s_waitcnt vmcnt(0) lgkmcnt(0)\n"
    "s_nop 7\n"
    "s_branch L32_%=\n"
    "L63_%=:\n"
    "s_waitcnt vmcnt(0) lgkmcnt(0)\n"
    "s_waitcnt lgkmcnt(0)\n"
    "L63_%=_l:\n"
    "s_add_u32 s98, s44, 18481152\n"
    "buffer_load_dwordx4 a[48:51], v0, %[srdw], s98 offen offset:0 sc1\n"
    "buffer_load_dwordx4 a[52:55], v0, %[srdw], s98 offen offset:1024 sc1\n"
    "buffer_load_dwordx4 a[56:59], v0, %[srdw], s98 offen offset:2048 sc1\n"
    "s_add_u32 s98, s45, 18481152\n"
    "buffer_load_dwordx4 a[60:63], v0, %[srdw], s98 offen offset:0 sc1\n"
    "buffer_load_dwordx4 a[64:67], v0, %[srdw], s98 offen offset:1024 sc1\n"
    "buffer_load_dwordx4 a[68:71], v0, %[srdw], s98 offen offset:2048 sc1\n"
    "s_add_u32 s98, s46, 18481152\n"
    "buffer_load_dwordx4 a[72:75], v0, %[srdw], s98 offen offset:0 sc1\n"
    "buffer_load_dwordx4 a[76:79], v0, %[srdw], s98 offen offset:1024 sc1\n"
    "buffer_load_dwordx4 a[80:83], v0, %[srdw], s98 offen offset:2048 sc1\n"
    "s_add_u32 s98, s47, 18481152\n"
    "buffer_load_dwordx4 a[84:87], v0, %[srdw], s98 offen offset:0 sc1\n"
    "buffer_load_dwordx4 a[88:91], v0, %[srdw], s98 offen offset:1024 sc1\n"
    "buffer_load_dwordx4 a[92:95], v0, %[srdw], s98 offen offset:2048 sc1\n"
    "s_mov_b64 s[96:97], 0\n"
    "s_waitcnt vmcnt(0)\n"
    "v_accvgpr_read_b32 v24, a48\n"
    "s_nop 1\n"
    "v_cmp_eq_u32 vcc, -1, v24\n"
    "s_nop 1\n"
    "s_or_b64 s[96:97], s[96:97], vcc\n"
    "v_accvgpr_read_b32 v24, a49\n"
    "s_nop 1\n"
    "v_cmp_eq_u32 vcc, -1, v24\n"
    "s_nop 1\n"
    "s_or_b64 s[96:97], s[96:97], vcc\n"
    "v_accvgpr_read_b32 v24, a50\n"
    "s_nop 1\n"
    "v_cmp_eq_u32 vcc, -1, v24\n"
    "s_nop 1\n"
    "s_or_b64 s[96:97], s[96:97], vcc\n"
    "v_accvgpr_read_b32 v24, a51\n"
    "s_nop 1\n"
    "v_cmp_eq_u32 vcc, -1, v24\n"
    "s_nop 1\n"
    "s_or_b64 s[96:97], s[96:97], vcc\n"
    "v_accvgpr_read_b32 v24, a52\n"
    "s_nop 1\n"
    "v_cmp_eq_u32 vcc, -1, v24\n"
    "s_nop 1\n"
    "s_or_b64 s[96:97], s[96:97], vcc\n"
    "v_accvgpr_read_b32 v24, a53\n"
    "s_nop 1\n"
    "v_cmp_eq_u32 vcc, -1, v24\n"
    "s_nop 1\n"
    "s_or_b64 s[96:97], s[96:97], vcc\n"
    "v_accvgpr_read_b32 v24, a54\n"
    "s_nop 1\n"
    "v_cmp_eq_u32 vcc, -1, v24\n"
    "s_nop 1\n"
    "s_or_b64 s[96:97], s[96:97], vcc\n"
    "v_accvgpr_read_b32 v24, a55\n"
    "s_nop 1\n"
    "v_cmp_eq_u32 vcc, -1, v24\n"
    "s_nop 1\n"
    "s_or_b64 s[96:97], s[96:97], vcc\n"
    "v_accvgpr_read_b32 v24, a56\n"
    "s_nop 1\n"
    "v_cmp_eq_u32 vcc, -1, v24\n"
    "s_nop 1\n"
    "s_or_b64 s[96:97], s[96:97], vcc\n"
    "v_accvgpr_read_b32 v24, a57\n"
    "s_nop 1\n"
    "v_cmp_eq_u32 vcc, -1, v24\n"
    "s_nop 1\n"
    "s_or_b64 s[96:97], s[96:97], vcc\n"
    "v_accvgpr_read_b32 v24, a58\n"
    "s_nop 1\n"
    "v_cmp_eq_u32 vcc, -1, v24\n"
    "s_nop 1\n"
    "s_or_b64 s[96:97], s[96:97], vcc\n"
    "v_accvgpr_read_b32 v24, a59\n"
    "s_nop 1\n"
    "v_cmp_eq_u32 vcc, -1, v24\n"
    "s_nop 1\n"
    "s_or_b64 s[96:97], s[96:97], vcc\n"
    "v_accvgpr_read_b32 v24, a60\n"
    "s_nop 1\n"
    "v_cmp_eq_u32 vcc, -1, v24\n"
    "s_nop 1\n"
    "s_or_b64 s[96:97], s[96:97], vcc\n"
    "v_accvgpr_read_b32 v24, a61\n"
    "s_nop 1\n"
    "v_cmp_eq_u32 vcc, -1, v24\n"
    "s_nop 1\n"
    "s_or_b64 s[96:97], s[96:97], vcc\n"
    "v_accvgpr_read_b32 v24, a62\n"
    "s_nop 1\n"
    "v_cmp_eq_u32 vcc, -1, v24\n"
    "s_nop 1\n"
    "s_or_b64 s[96:97], s[96:97], vcc\n"
    "v_accvgpr_read_b32 v24, a63\n"
    "s_nop 1\n"
    "v_cmp_eq_u32 vcc, -1, v24\n"
    "s_nop 1\n"
    "s_or_b64 s[96:97], s[96:97], vcc\n"
    "v_accvgpr_read_b32 v24, a64\n"
    "s_nop 1\n"
    "v_cmp_eq_u32 vcc, -1, v24\n"
    "s_nop 1\n"
    "s_or_b64 s[96:97], s[96:97], vcc\n"
    "v_accvgpr_read_b32 v24, a65\n"
    "s_nop 1\n"
    "v_cmp_eq_u32 vcc, -1, v24\n"
    "s_nop 1\n"
    "s_or_b64 s[96:97], s[96:97], vcc\n"
    "v_accvgpr_read_b32 v24, a66\n"
    "s_nop 1\n"
    "v_cmp_eq_u32 vcc, -1, v24\n"
    "s_nop 1\n"
    "s_or_b64 s[96:97], s[96:97], vcc\n"
    "v_accvgpr_read_b32 v24, a67\n"
    "s_nop 1\n"
    "v_cmp_eq_u32 vcc, -1, v24\n"
    "s_nop 1\n"
    "s_or_b64 s[96:97], s[96:97], vcc\n"
    "v_accvgpr_read_b32 v24, a68\n"
    "s_nop 1\n"
    "v_cmp_eq_u32 vcc, -1, v24\n"
    "s_nop 1\n"
    "s_or_b64 s[96:97], s[96:97], vcc\n"
    "v_accvgpr_read_b32 v24, a69\n"
    "s_nop 1\n"
    "v_cmp_eq_u32 vcc, -1, v24\n"
    "s_nop 1\n"
    "s_or_b64 s[96:97], s[96:97], vcc\n"
    "v_accvgpr_read_b32 v24, a70\n"
    "s_nop 1\n"
    "v_cmp_eq_u32 vcc, -1, v24\n"
    "s_nop 1\n"
    "s_or_b64 s[96:97], s[96:97], vcc\n"
    "v_accvgpr_read_b32 v24, a71\n"
    "s_nop 1\n"
    "v_cmp_eq_u32 vcc, -1, v24\n"
    "s_nop 1\n"
    "s_or_b64 s[96:97], s[96:97], vcc\n"
    "v_accvgpr_read_b32 v24, a72\n"
    "s_nop 1\n"
    "v_cmp_eq_u32 vcc, -1, v24\n"
    "s_nop 1\n"
    "s_or_b64 s[96:97], s[96:97], vcc\n"
    "v_accvgpr_read_b32 v24, a73\n"
    "s_nop 1\n"
    "v_cmp_eq_u32 vcc, -1, v24\n"
    "s_nop 1\n"
    "s_or_b64 s[96:97], s[96:97], vcc\n"
    "v_accvgpr_read_b32 v24, a74\n"
    "s_nop 1\n"
    "v_cmp_eq_u32 vcc, -1, v24\n"
    "s_nop 1\n"
    "s_or_b64 s[96:97], s[96:97], vcc\n"
    "v_accvgpr_read_b32 v24, a75\n"
    "s_nop 1\n"
    "v_cmp_eq_u32 vcc, -1, v24\n"
    "s_nop 1\n"
    "s_or_b64 s[96:97], s[96:97], vcc\n"
    "v_accvgpr_read_b32 v24, a76\n"
    "s_nop 1\n"
    "v_cmp_eq_u32 vcc, -1, v24\n"
    "s_nop 1\n"
    "s_or_b64 s[96:97], s[96:97], vcc\n"
    "v_accvgpr_read_b32 v24, a77\n"
    "s_nop 1\n"
    "v_cmp_eq_u32 vcc, -1, v24\n"
    "s_nop 1\n"
    "s_or_b64 s[96:97], s[96:97], vcc\n"
    "v_accvgpr_read_b32 v24, a78\n"
    "s_nop 1\n"
    "v_cmp_eq_u32 vcc, -1, v24\n"
    "s_nop 1\n"
    "s_or_b64 s[96:97], s[96:97], vcc\n"
    "v_accvgpr_read_b32 v24, a79\n"
    "s_nop 1\n"
    "v_cmp_eq_u32 vcc, -1, v24\n"
    "s_nop 1\n"
    "s_or_b64 s[96:97], s[96:97], vcc\n"
    "v_accvgpr_read_b32 v24, a80\n"
    "s_nop 1\n"
    "v_cmp_eq_u32 vcc, -1, v24\n"
    "s_nop 1\n"
    "s_or_b64 s[96:97], s[96:97], vcc\n"
    "v_accvgpr_read_b32 v24, a81\n"
    "s_nop 1\n"
    "v_cmp_eq_u32 vcc, -1, v24\n"
    "s_nop 1\n"
    "s_or_b64 s[96:97], s[96:97], vcc\n"
    "v_accvgpr_read_b32 v24, a82\n"
    "s_nop 1\n"
    "v_cmp_eq_u32 vcc, -1, v24\n"
    "s_nop 1\n"
    "s_or_b64 s[96:97], s[96:97], vcc\n"
    "v_accvgpr_read_b32 v24, a83\n"
    "s_nop 1\n"
    "v_cmp_eq_u32 vcc, -1, v24\n"
    "s_nop 1\n"
    "s_or_b64 s[96:97], s[96:97], vcc\n"
    "v_accvgpr_read_b32 v24, a84\n"
    "s_nop 1\n"
    "v_cmp_eq_u32 vcc, -1, v24\n"
    "s_nop 1\n"
    "s_or_b64 s[96:97], s[96:97], vcc\n"
    "v_accvgpr_read_b32 v24, a85\n"
    "s_nop 1\n"
    "v_cmp_eq_u32 vcc, -1, v24\n"
    "s_nop 1\n"
    "s_or_b64 s[96:97], s[96:97], vcc\n"
    "v_accvgpr_read_b32 v24, a86\n"
    "s_nop 1\n"
    "v_cmp_eq_u32 vcc, -1, v24\n"
    "s_nop 1\n"
    "s_or_b64 s[96:97], s[96:97], vcc\n"
    "v_accvgpr_read_b32 v24, a87\n"
    "s_nop 1\n"
    "v_cmp_eq_u32 vcc, -1, v24\n"
    "s_nop 1\n"
    "s_or_b64 s[96:97], s[96:97], vcc\n"
    "v_accvgpr_read_b32 v24, a88\n"
    "s_nop 1\n"
    "v_cmp_eq_u32 vcc, -1, v24\n"
    "s_nop 1\n"
    "s_or_b64 s[96:97], s[96:97], vcc\n"
    "v_accvgpr_read_b32 v24, a89\n"
    "s_nop 1\n"
    "v_cmp_eq_u32 vcc, -1, v24\n"
    "s_nop 1\n"
    "s_or_b64 s[96:97], s[96:97], vcc\n"
    "v_accvgpr_read_b32 v24, a90\n"
    "s_nop 1\n"
    "v_cmp_eq_u32 vcc, -1, v24\n"
    "s_nop 1\n"
    "s_or_b64 s[96:97], s[96:97], vcc\n"
    "v_accvgpr_read_b32 v24, a91\n"
    "s_nop 1\n"
    "v_cmp_eq_u32 vcc, -1, v24\n"
    "s_nop 1\n"
    "s_or_b64 s[96:97], s[96:97], vcc\n"
    "v_accvgpr_read_b32 v24, a92\n"
    "s_nop 1\n"
    "v_cmp_eq_u32 vcc, -1, v24\n"
    "s_nop 1\n"
    "s_or_b64 s[96:97], s[96:97], vcc\n"
    "v_accvgpr_read_b32 v24, a93\n"
    "s_nop 1\n"
    "v_cmp_eq_u32 vcc, -1, v24\n"
    "s_nop 1\n"
    "s_or_b64 s[96:97], s[96:97], vcc\n"
    "v_accvgpr_read_b32 v24, a94\n"
    "s_nop 1\n"
    "v_cmp_eq_u32 vcc, -1, v24\n"
    "s_nop 1\n"
    "s_or_b64 s[96:97], s[96:97], vcc\n"
    "v_accvgpr_read_b32 v24, a95\n"
    "s_nop 1\n"
    "v_cmp_eq_u32 vcc, -1, v24\n"
    "s_nop 1\n"
    "s_or_b64 s[96:97], s[96:97], vcc\n"
    "s_cmp_eq_u64 s[96:97], 0\n"
    "s_cbranch_scc1 L63_%=_x\n"
    "s_sub_u32 s54, s54, 1\n"
    "s_cbranch_scc0 L63_%=_l\n"
    "s_mov_b32 s54, 0\n"
    "L63_%=_x:\n"
    "v_mfma_f32_16x16x32_bf16 v[40:43], a[240:243], a[48:51], 0\n"
    "s_nop 7\n"
    "ds_read_b128 v[24:27], v11 offset:0\n"
    "s_waitcnt lgkmcnt(0)\n"
    "v_mfma_f32_16x16x32_bf16 v[44:47], v[24:27], a[48:51], 0\n"
    "v_mfma_f32_16x16x32_bf16 v[48:51], a[244:247], a[48:51], 0\n"
    "s_nop 7\n"
    "ds_read_b128 v[24:27], v11 offset:1024\n"
    "s_waitcnt lgkmcnt(0)\n"
    "v_mfma_f32_16x16x32_bf16 v[52:55], v[24:27], a[48:51], 0\n"
    "v_mfma_f32_16x16x32_bf16 v[56:59], a[248:251], a[48:51], 0\n"
    "v_mfma_f32_16x16x32_bf16 v[40:43], a[252:255], a[52:55], v[40:43]\n"
    "s_nop 7\n"
    "ds_read_b128 v[24:27], v11 offset:2048\n"
    "s_waitcnt lgkmcnt(0)\n"
    "v_mfma_f32_16x16x32_bf16 v[44:47], v[24:27], a[52:55], v[44:47]\n"
    "v_mfma_f32_16x16x32_bf16 v[48:51], v[128:131], a[52:55], v[48:51]\n"
    "s_nop 7\n"
    "ds_read_b128 v[24:27], v11 offset:3072\n"
    "s_waitcnt lgkmcnt(0)\n"
    "v_mfma_f32_16x16x32_bf16 v[52:55], v[24:27], a[52:55], v[52:55]\n"
    "v_mfma_f32_16x16x32_bf16 v[56:59], v[132:135], a[52:55], v[56:59]\n"
    "v_mfma_f32_16x16x32_bf16 v[40:43], v[136:139], a[56:59], v[40:43]\n"
    "s_nop 7\n"
    "ds_read_b128 v[24:27], v11 offset:4096\n"
    "s_waitcnt lgkmcnt(0)\n"
    "v_mfma_f32_16x16x32_bf16 v[44:47], v[24:27], a[56:59], v[44:47]\n"
    "v_mfma_f32_16x16x32_bf16 v[48:51], v[140:143], a[56:59], v[48:51]\n"
    "s_nop 7\n"
    "ds_read_b128 v[24:27], v11 offset:5120\n"
    "s_waitcnt lgkmcnt(0)\n"
    "v_mfma_f32_16x16x32_bf16 v[52:55], v[24:27], a[56:59], v[52:55]\n"
    "v_mfma_f32_16x16x32_bf16 v[56:59], v[144:147], a[56:59], v[56:59]\n"
    "v_mfma_f32_16x16x32_bf16 v[40:43], v[148:151], a[60:63], v[40:43]\n"
    "s_nop 7\n"
    "ds_read_b128 v[24:27], v11 offset:6144\n"
    "s_waitcnt lgkmcnt(0)\n"
    "v_mfma_f32_16x16x32_bf16 v[44:47], v[24:27], a[60:63], v[44:47]\n"
    "v_mfma_f32_16x16x32_bf16 v[48:51], v[152:155], a[60:63], v[48:51]\n"
    "s_nop 7\n"
    "ds_read_b128 v[24:27], v11 offset:7168\n"
    "s_waitcnt lgkmcnt(0)\n"
    "v_mfma_f32_16x16x32_bf16 v[52:55], v[24:27], a[60:63], v[52:55]\n"
    "v_mfma_f32_16x16x32_bf16 v[56:59], v[156:159], a[60:63], v[56:59]\n"
    "v_mfma_f32_16x16x32_bf16 v[40:43], v[160:163], a[64:67], v[40:43]\n"
    "s_nop 7\n"
    "ds_read_b128 v[24:27], v11 offset:8192\n"
    "s_waitcnt lgkmcnt(0)\n"
    "v_mfma_f32_16x16x32_bf16 v[44:47], v[24:27], a[64:67], v[44:47]\n"
    "v_mfma_f32_16x16x32_bf16 v[48:51], v[164:167], a[64:67], v[48:51]\n"
    "s_nop 7\n"
    "ds_read_b128 v[24:27], v11 offset:9216\n"
    "s_waitcnt lgkmcnt(0)\n"
    "v_mfma_f32_16x16x32_bf16 v[52:55], v[24:27], a[64:67], v[52:55]\n"
    "v_mfma_f32_16x16x32_bf16 v[56:59], v[168:171], a[64:67], v[56:59]\n"
    "v_mfma_f32_16x16x32_bf16 v[40:43], v[172:175], a[68:71], v[40:43]\n"
    "s_nop 7\n"
    "ds_read_b128 v[24:27], v11 offset:10240\n"
    "s_waitcnt lgkmcnt(0)\n"
    "v_mfma_f32_16x16x32_bf16 v[44:47], v[24:27], a[68:71], v[44:47]\n"
    "v_mfma_f32_16x16x32_bf16 v[48:51], v[176:179], a[68:71], v[48:51]\n"
    "s_nop 7\n"
    "ds_read_b128 v[24:27], v11 offset:11264\n"
    "s_waitcnt lgkmcnt(0)\n"
    "v_mfma_f32_16x16x32_bf16 v[52:55], v[24:27], a[68:71], v[52:55]\n"
    "v_mfma_f32_16x16x32_bf16 v[56:59], v[180:183], a[68:71], v[56:59]\n"
    "v_mfma_f32_16x16x32_bf16 v[40:43], v[184:187], a[72:75], v[40:43]\n"
    "s_nop 7\n"
    "ds_read_b128 v[24:27], v11 offset:12288\n"
    "s_waitcnt lgkmcnt(0)\n"
    "v_mfma_f32_16x16x32_bf16 v[44:47], v[24:27], a[72:75], v[44:47]\n"
    "v_mfma_f32_16x16x32_bf16 v[48:51], v[188:191], a[72:75], v[48:51]\n"
    "s_nop 7\n"
    "ds_read_b128 v[24:27], v11 offset:13312\n"
    "s_waitcnt lgkmcnt(0)\n"
    "v_mfma_f32_16x16x32_bf16 v[52:55], v[24:27], a[72:75], v[52:55]\n"
    "v_mfma_f32_16x16x32_bf16 v[56:59], v[192:195], a[72:75], v[56:59]\n"
    "v_mfma_f32_16x16x32_bf16 v[40:43], v[196:199], a[76:79], v[40:43]\n"
    "s_nop 7\n"
    "ds_read_b128 v[24:27], v11 offset:14336\n"
    "s_waitcnt lgkmcnt(0)\n"
    "v_mfma_f32_16x16x32_bf16 v[44:47], v[24:27], a[76:79], v[44:47]\n"
    "v_mfma_f32_16x16x32_bf16 v[48:51], v[200:203], a[76:79], v[48:51]\n"
    "s_nop 7\n"
    "ds_read_b128 v[24:27], v11 offset:15360\n"
    "s_waitcnt lgkmcnt(0)\n"
    "v_mfma_f32_16x16x32_bf16 v[52:55], v[24:27], a[76:79], v[52:55]\n"
    "v_mfma_f32_16x16x32_bf16 v[56:59], v[204:207], a[76:79], v[56:59]\n"
    "v_mfma_f32_16x16x32_bf16 v[40:43], v[208:211], a[80:83], v[40:43]\n"
    "v_mfma_f32_16x16x32_bf16 v[40:43], v[220:223], a[84:87], v[40:43]\n"
    "v_mfma_f32_16x16x32_bf16 v[40:43], v[232:235], a[88:91], v[40:43]\n"
    "v_mfma_f32_16x16x32_bf16 v[40:43], v[244:247], a[92:95], v[40:43]\n"
    "s_nop 7\n"
    "ds_read_b128 v[24:27], v11 offset:16384\n"
    "s_waitcnt lgkmcnt(0)\n"
    "v_mfma_f32_16x16x32_bf16 v[44:47], v[24:27], a[80:83], v[44:47]\n"
    "s_nop 7\n"
    "ds_read_b128 v[24:27], v11 offset:18432\n"
    "s_waitcnt lgkmcnt(0)\n"
    "v_mfma_f32_16x16x32_bf16 v[44:47], v[24:27], a[84:87], v[44:47]\n"
    "s_nop 7\n"
    "ds_read_b128 v[24:27], v11 offset:20480\n"
    "s_waitcnt lgkmcnt(0)\n"
    "v_mfma_f32_16x16x32_bf16 v[44:47], v[24:27], a[88:91], v[44:47]\n"
    "s_nop 7\n"
    "ds_read_b128 v[24:27], v11 offset:22528\n"
    "s_waitcnt lgkmcnt(0)\n"
    "v_mfma_f32_16x16x32_bf16 v[44:47], v[24:27], a[92:95], v[44:47]\n"
    "s_nop 7\n"
    "ds_read_b128 v[24:27], v11 offset:17408\n"
    "s_waitcnt vmcnt(0) lgkmcnt(0)\n"
    "s_nop 7\n"
    "s_branch L64_%=\n"
    "L65_%=:\n"
    "s_waitcnt vmcnt(0) lgkmcnt(0)\n"
    "s_waitcnt lgkmcnt(0)\n"
    "L65_%=_l:\n"
    "s_add_u32 s98, s44, 20054016\n"
    "buffer_load_dwordx4 v[80:83], v0, %[srdw], s98 offen offset:0 sc1\n"
    "buffer_load_dwordx4 v[84:87], v0, %[srdw], s98 offen offset:1024 sc1\n"
    "buffer_load_dwordx4 v[88:91], v0, %[srdw], s98 offen offset:2048 sc1\n"
    "s_add_u32 s98, s45, 20054016\n"
    "buffer_load_dwordx4 v[92:95], v0, %[srdw], s98 offen offset:0 sc1\n"
    "buffer_load_dwordx4 v[96:99], v0, %[srdw], s98 offen offset:1024 sc1\n"
    "buffer_load_dwordx4 v[100:103], v0, %[srdw], s98 offen offset:2048 sc1\n"
    "s_add_u32 s98, s46, 20054016\n"
    "buffer_load_dwordx4 v[104:107], v0, %[srdw], s98 offen offset:0 sc1\n"
    "buffer_load_dwordx4 v[108:111], v0, %[srdw], s98 offen offset:1024 sc1\n"
    "buffer_load_dwordx4 v[112:115], v0, %[srdw], s98 offen offset:2048 sc1\n"
    "s_add_u32 s98, s47, 20054016\n"
    "buffer_load_dwordx4 v[116:119], v0, %[srdw], s98 offen offset:0 sc1\n"
    "buffer_load_dwordx4 v[120:123], v0, %[srdw], s98 offen offset:1024 sc1\n"
    "buffer_load_dwordx4 v[124:127], v0, %[srdw], s98 offen offset:2048 sc1\n"
    "s_mov_b64 s[96:97], 0\n"
    "s_waitcnt vmcnt(0)\n"
    "v_cmp_eq_u32 vcc, -1, v80\n"
    "s_nop 1\n"
    "s_or_b64 s[96:97], s[96:97], vcc\n"
    "v_cmp_eq_u32 vcc, -1, v81\n"
    "s_nop 1\n"
    "s_or_b64 s[96:97], s[96:97], vcc\n"
    "v_cmp_eq_u32 vcc, -1, v82\n"
    "s_nop 1\n"
    "s_or_b64 s[96:97], s[96:97], vcc\n"
    "v_cmp_eq_u32 vcc, -1, v83\n"
    "s_nop 1\n"
    "s_or_b64 s[96:97], s[96:97], vcc\n"
    "v_cmp_eq_u32 vcc, -1, v84\n"
    "s_nop 1\n"
    "s_or_b64 s[96:97], s[96:97], vcc\n"
    "v_cmp_eq_u32 vcc, -1, v85\n"
    "s_nop 1\n"
    "s_or_b64 s[96:97], s[96:97], vcc\n"
    "v_cmp_eq_u32 vcc, -1, v86\n"
    "s_nop 1\n"
    "s_or_b64 s[96:97], s[96:97], vcc\n"
    "v_cmp_eq_u32 vcc, -1, v87\n"
    "s_nop 1\n"
    "s_or_b64 s[96:97], s[96:97], vcc\n"
    "v_cmp_eq_u32 vcc, -1, v88\n"
    "s_nop 1\n"
    "s_or_b64 s[96:97], s[96:97], vcc\n"
    "v_cmp_eq_u32 vcc, -1, v89\n"
    "s_nop 1\n"
    "s_or_b64 s[96:97], s[96:97], vcc\n"
    "v_cmp_eq_u32 vcc, -1, v90\n"
    "s_nop 1\n"
    "s_or_b64 s[96:97], s[96:97], vcc\n"
    "v_cmp_eq_u32 vcc, -1, v91\n"
    "s_nop 1\n"
    "s_or_b64 s[96:97], s[96:97], vcc\n"
    "v_cmp_eq_u32 vcc, -1, v92\n"
    "s_nop 1\n"
    "s_or_b64 s[96:97], s[96:97], vcc\n"
    "v_cmp_eq_u32 vcc, -1, v93\n"
    "s_nop 1\n"
    "s_or_b64 s[96:97], s[96:97], vcc\n"
    "v_cmp_eq_u32 vcc, -1, v94\n"
    "s_nop 1\n"
    "s_or_b64 s[96:97], s[96:97], vcc\n"
    "v_cmp_eq_u32 vcc, -1, v95\n"
    "s_nop 1\n"
    "s_or_b64 s[96:97], s[96:97], vcc\n"
    "v_cmp_eq_u32 vcc, -1, v96\n"
    "s_nop 1\n"
    "s_or_b64 s[96:97], s[96:97], vcc\n"
    "v_cmp_eq_u32 vcc, -1, v97\n"
    "s_nop 1\n"
    "s_or_b64 s[96:97], s[96:97], vcc\n"
    "v_cmp_eq_u32 vcc, -1, v98\n"
    "s_nop 1\n"
    "s_or_b64 s[96:97], s[96:97], vcc\n"
    "v_cmp_eq_u32 vcc, -1, v99\n"
    "s_nop 1\n"
    "s_or_b64 s[96:97], s[96:97], vcc\n"
    "v_cmp_eq_u32 vcc, -1, v100\n"
    "s_nop 1\n"
    "s_or_b64 s[96:97], s[96:97], vcc\n"
    "v_cmp_eq_u32 vcc, -1, v101\n"
    "s_nop 1\n"
    "s_or_b64 s[96:97], s[96:97], vcc\n"
    "v_cmp_eq_u32 vcc, -1, v102\n"
    "s_nop 1\n"
    "s_or_b64 s[96:97], s[96:97], vcc\n"
    "v_cmp_eq_u32 vcc, -1, v103\n"
    "s_nop 1\n"
    "s_or_b64 s[96:97], s[96:97], vcc\n"
    "v_cmp_eq_u32 vcc, -1, v104\n"
    "s_nop 1\n"
    "s_or_b64 s[96:97], s[96:97], vcc\n"
    "v_cmp_eq_u32 vcc, -1, v105\n"
    "s_nop 1\n"
    "s_or_b64 s[96:97], s[96:97], vcc\n"
    "v_cmp_eq_u32 vcc, -1, v106\n"
    "s_nop 1\n"
    "s_or_b64 s[96:97], s[96:97], vcc\n"
    "v_cmp_eq_u32 vcc, -1, v107\n"
    "s_nop 1\n"
    "s_or_b64 s[96:97], s[96:97], vcc\n"
    "v_cmp_eq_u32 vcc, -1, v108\n"
    "s_nop 1\n"
    "s_or_b64 s[96:97], s[96:97], vcc\n"
    "v_cmp_eq_u32 vcc, -1, v109\n"
    "s_nop 1\n"
    "s_or_b64 s[96:97], s[96:97], vcc\n"
    "v_cmp_eq_u32 vcc, -1, v110\n"
    "s_nop 1\n"
    "s_or_b64 s[96:97], s[96:97], vcc\n"
    "v_cmp_eq_u32 vcc, -1, v111\n"
    "s_nop 1\n"
    "s_or_b64 s[96:97], s[96:97], vcc\n"
    "v_cmp_eq_u32 vcc, -1, v112\n"
    "s_nop 1\n"
    "s_or_b64 s[96:97], s[96:97], vcc\n"
    "v_cmp_eq_u32 vcc, -1, v113\n"
    "s_nop 1\n"
    "s_or_b64 s[96:97], s[96:97], vcc\n"
    "v_cmp_eq_u32 vcc, -1, v114\n"
    "s_nop 1\n"
    "s_or_b64 s[96:97], s[96:97], vcc\n"
    "v_cmp_eq_u32 vcc, -1, v115\n"
    "s_nop 1\n"
    "s_or_b64 s[96:97], s[96:97], vcc\n"
    "v_cmp_eq_u32 vcc, -1, v116\n"
    "s_nop 1\n"
    "s_or_b64 s[96:97], s[96:97], vcc\n"
    "v_cmp_eq_u32 vcc, -1, v117\n"
    "s_nop 1\n"
    "s_or_b64 s[96:97], s[96:97], vcc\n"
    "v_cmp_eq_u32 vcc, -1, v118\n"
    "s_nop 1\n"
    "s_or_b64 s[96:97], s[96:97], vcc\n"
    "v_cmp_eq_u32 vcc, -1, v119\n"
    "s_nop 1\n"
    "s_or_b64 s[96:97], s[96:97], vcc\n"
    "v_cmp_eq_u32 vcc, -1, v120\n"
    "s_nop 1\n"
    "s_or_b64 s[96:97], s[96:97], vcc\n"
    "v_cmp_eq_u32 vcc, -1, v121\n"
    "s_nop 1\n"
    "s_or_b64 s[96:97], s[96:97], vcc\n"
    "v_cmp_eq_u32 vcc, -1, v122\n"
    "s_nop 1\n"
    "s_or_b64 s[96:97], s[96:97], vcc\n"
    "v_cmp_eq_u32 vcc, -1, v123\n"
    "s_nop 1\n"
    "s_or_b64 s[96:97], s[96:97], vcc\n"
    "v_cmp_eq_u32 vcc, -1, v124\n"
    "s_nop 1\n"
    "s_or_b64 s[96:97], s[96:97], vcc\n"
    "v_cmp_eq_u32 vcc, -1, v125\n"
    "s_nop 1\n"
    "s_or_b64 s[96:97], s[96:97], vcc\n"
    "v_cmp_eq_u32 vcc, -1, v126\n"
    "s_nop 1\n"
    "s_or_b64 s[96:97], s[96:97], vcc\n"
    "v_cmp_eq_u32 vcc, -1, v127\n"
    "s_nop 1\n"
    "s_or_b64 s[96:97], s[96:97], vcc\n"
    "s_cmp_eq_u64 s[96:97], 0\n"
    "s_cbranch_scc1 L65_%=_x\n"
    "s_sub_u32 s54, s54, 1\n"
    "s_cbranch_scc0 L65_%=_l\n"
    "s_mov_b32 s54, 0\n"
    "L65_%=_x:\n"
    "v_mfma_f32_16x16x32_bf16 v[40:43], a[240:243], v[80:83], 0\n"
    "s_nop 7\n"
    "ds_read_b128 v[24:27], v11 offset:0\n"
    "s_waitcnt lgkmcnt(0)\n"
    "v_mfma_f32_16x16x32_bf16 v[44:47], v[24:27], v[80:83], 0\n"
    "v_mfma_f32_16x16x32_bf16 v[48:51], a[244:247], v[80:83], 0\n"
    "s_nop 7\n"
    "ds_read_b128 v[24:27], v11 offset:1024\n"
    "s_waitcnt lgkmcnt(0)\n"
    "v_mfma_f32_16x16x32_bf16 v[52:55], v[24:27], v[80:83], 0\n"
    "v_mfma_f32_16x16x32_bf16 v[56:59], a[248:251], v[80:83], 0\n"
    "v_mfma_f32_16x16x32_bf16 v[40:43], a[252:255], v[84:87], v[40:43]\n"
    "s_nop 7\n"
    "ds_read_b128 v[24:27], v11 offset:2048\n"
    "s_waitcnt lgkmcnt(0)\n"
    "v_mfma_f32_16x16x32_bf16 v[44:47], v[24:27], v[84:87], v[44:47]\n"
    "v_mfma_f32_16x16x32_bf16 v[48:51], v[128:131], v[84:87], v[48:51]\n"
    "s_nop 7\n"
    "ds_read_b128 v[24:27], v11 offset:3072\n"
    "s_waitcnt lgkmcnt(0)\n"
    "v_mfma_f32_16x16x32_bf16 v[52:55], v[24:27], v[84:87], v[52:55]\n"
    "v_mfma_f32_16x16x32_bf16 v[56:59], v[132:135], v[84:87], v[56:59]\n"
    "v_mfma_f32_16x16x32_bf16 v[40:43], v[136:139], v[88:91], v[40:43]\n"
    "s_nop 7\n"
    "ds_read_b128 v[24:27], v11 offset:4096\n"
    "s_waitcnt lgkmcnt(0)\n"
    "v_mfma_f32_16x16x32_bf16 v[44:47], v[24:27], v[88:91], v[44:47]\n"
    "v_mfma_f32_16x16x32_bf16 v[48:51], v[140:143], v[88:91], v[48:51]\n"
    "s_nop 7\n"
    "ds_read_b128 v[24:27], v11 offset:5120\n"
    "s_waitcnt lgkmcnt(0)\n"
    "v_mfma_f32_16x16x32_bf16 v[52:55], v[24:27], v[88:91], v[52:55]\n"
    "v_mfma_f32_16x16x32_bf16 v[56:59], v[144:147], v[88:91], v[56:59]\n"
    "v_mfma_f32_16x16x32_bf16 v[40:43], v[148:151], v[92:95], v[40:43]\n"
    "s_nop 7\n"
    "ds_read_b128 v[24:27], v11 offset:6144\n"
    "s_waitcnt lgkmcnt(0)\n"
    "v_mfma_f32_16x16x32_bf16 v[44:47], v[24:27], v[92:95], v[44:47]\n"
    "v_mfma_f32_16x16x32_bf16 v[48:51], v[152:155], v[92:95], v[48:51]\n"
    "s_nop 7\n"
    "ds_read_b128 v[24:27], v11 offset:7168\n"
    "s_waitcnt lgkmcnt(0)\n"
    "v_mfma_f32_16x16x32_bf16 v[52:55], v[24:27], v[92:95], v[52:55]\n"
    "v_mfma_f32_16x16x32_bf16 v[56:59], v[156:159], v[92:95], v[56:59]\n"
    "v_mfma_f32_16x16x32_bf16 v[40:43], v[160:163], v[96:99], v[40:43]\n"
    "s_nop 7\n"
    "ds_read_b128 v[24:27], v11 offset:8192\n"
    "s_waitcnt lgkmcnt(0)\n"
    "v_mfma_f32_16x16x32_bf16 v[44:47], v[24:27], v[96:99], v[44:47]\n"
    "v_mfma_f32_16x16x32_bf16 v[48:51], v[164:167], v[96:99], v[48:51]\n"
    "s_nop 7\n"
    "ds_read_b128 v[24:27], v11 offset:9216\n"
    "s_waitcnt lgkmcnt(0)\n"
    "v_mfma_f32_16x16x32_bf16 v[52:55], v[24:27], v[96:99], v[52:55]\n"
    "v_mfma_f32_16x16x32_bf16 v[56:59], v[168:171], v[96:99], v[56:59]\n"
    "v_mfma_f32_16x16x32_bf16 v[40:43], v[172:175], v[100:103], v[40:43]\n"
    "s_nop 7\n"
    "ds_read_b128 v[24:27], v11 offset:10240\n"
    "s_waitcnt lgkmcnt(0)\n"
    "v_mfma_f32_16x16x32_bf16 v[44:47], v[24:27], v[100:103], v[44:47]\n"
    "v_mfma_f32_16x16x32_bf16 v[48:51], v[176:179], v[100:103], v[48:51]\n"
    "s_nop 7\n"
    "ds_read_b128 v[24:27], v11 offset:11264\n"
    "s_waitcnt lgkmcnt(0)\n"
    "v_mfma_f32_16x16x32_bf16 v[52:55], v[24:27], v[100:103], v[52:55]\n"
    "v_mfma_f32_16x16x32_bf16 v[56:59], v[180:183], v[100:103], v[56:59]\n"
    "v_mfma_f32_16x16x32_bf16 v[40:43], v[184:187], v[104:107], v[40:43]\n"
    "s_nop 7\n"
    "ds_read_b128 v[24:27], v11 offset:12288\n"
    "s_waitcnt lgkmcnt(0)\n"
    "v_mfma_f32_16x16x32_bf16 v[44:47], v[24:27], v[104:107], v[44:47]\n"
    "v_mfma_f32_16x16x32_bf16 v[48:51], v[188:191], v[104:107], v[48:51]\n"
    "s_nop 7\n"
    "ds_read_b128 v[24:27], v11 offset:13312\n"
    "s_waitcnt lgkmcnt(0)\n"
    "v_mfma_f32_16x16x32_bf16 v[52:55], v[24:27], v[104:107], v[52:55]\n"
    "v_mfma_f32_16x16x32_bf16 v[56:59], v[192:195], v[104:107], v[56:59]\n"
    "v_mfma_f32_16x16x32_bf16 v[40:43], v[196:199], v[108:111], v[40:43]\n"
    "s_nop 7\n"
    "ds_read_b128 v[24:27], v11 offset:14336\n"
    "s_waitcnt lgkmcnt(0)\n"
    "v_mfma_f32_16x16x32_bf16 v[44:47], v[24:27], v[108:111], v[44:47]\n"
    "v_mfma_f32_16x16x32_bf16 v[48:51], v[200:203], v[108:111], v[48:51]\n"
    "s_nop 7\n"
    "ds_read_b128 v[24:27], v11 offset:15360\n"
    "s_waitcnt lgkmcnt(0)\n"
    "v_mfma_f32_16x16x32_bf16 v[52:55], v[24:27], v[108:111], v[52:55]\n"
    "v_mfma_f32_16x16x32_bf16 v[56:59], v[204:207], v[108:111], v[56:59]\n"
    "v_mfma_f32_16x16x32_bf16 v[40:43], v[208:211], v[112:115], v[40:43]\n"
    "v_mfma_f32_16x16x32_bf16 v[40:43], v[220:223], v[116:119], v[40:43]\n"
    "v_mfma_f32_16x16x32_bf16 v[40:43], v[232:235], v[120:123], v[40:43]\n"
    "v_mfma_f32_16x16x32_bf16 v[40:43], v[244:247], v[124:127], v[40:43]\n"
    "s_nop 7\n"
    "ds_read_b128 v[24:27], v11 offset:16384\n"
    "s_waitcnt lgkmcnt(0)\n"
    "v_mfma_f32_16x16x32_bf16 v[44:47], v[24:27], v[112:115], v[44:47]\n"
    "s_nop 7\n"
    "ds_read_b128 v[24:27], v11 offset:18432\n"
    "s_waitcnt lgkmcnt(0)\n"
    "v_mfma_f32_16x16x32_bf16 v[44:47], v[24:27], v[116:119], v[44:47]\n"
    "s_nop 7\n"
    "ds_read_b128 v[24:27], v11 offset:20480\n"
    "s_waitcnt lgkmcnt(0)\n"
    "v_mfma_f32_16x16x32_bf16 v[44:47], v[24:27], v[120:123], v[44:47]\n"
    "s_nop 7\n"
    "ds_read_b128 v[24:27], v11 offset:22528\n"
    "s_waitcnt lgkmcnt(0)\n"
    "v_mfma_f32_16x16x32_bf16 v[44:47], v[24:27], v[124:127], v[44:47]\n"
    "s_nop 7\n"
    "ds_read_b128 v[24:27], v11 offset:17408\n"
    "s_waitcnt vmcnt(0) lgkmcnt(0)\n"
    "s_nop 7\n"
    "s_branch L66_%=\n"
    "L67_%=:\n"
    "L67_%=_l:\n"
    "s_sub_u32 s54, s54, 1\n"
    "s_cselect_b32 s54, 0, s54\n"
    "s_cbranch_scc1 L67_%=_x\n"
    "s_sleep 1\n"
    "s_mov_b64 exec, 0x1\n"
    "buffer_load_dword v246, v1, %[srdw], s62 offen sc1\n"
    "s_mov_b64 exec, -1\n"
    "s_waitcnt vmcnt(0)\n"
    "v_cmp_ne_u32 vcc, %[tag], v246\n"
    "s_nop 1\n"
    "s_and_b32 vcc_lo, vcc_lo, 0x1\n"
    "s_cbranch_scc1 L67_%=_l\n"
    "L67_%=_x:\n"
    "s_branch L68_%=\n"
    "L69_%=:\n"
    "L69_%=_l:\n"
    "s_sub_u32 s54, s54, 1\n"
    "s_cselect_b32 s54, 0, s54\n"
    "s_cbranch_scc1 L69_%=_x\n"
    "s_sleep 1\n"
    "s_mov_b32 exec_hi, 0xff\n"
    "buffer_load_dword v246, v1, %[srdw], s66 offen sc1\n"
    "s_mov_b32 exec_hi, -1\n"
    "s_waitcnt vmcnt(0)\n"
    "v_cmp_ne_u32 vcc, %[tag], v246\n"
    "s_nop 1\n"
    "s_and_b32 vcc_hi, vcc_hi, 0xff\n"
    "s_cmp_lg_u64 vcc, 0\n"
    "s_cbranch_scc1 L69_%=_l\n"
    "L69_%=_x:\n"
    "s_branch L70_%=\n"
    "L71_%=:\n"
    "L71_%=_l:\n"
    "s_sub_u32 s54, s54, 1\n"
    "s_cselect_b32 s54, 0, s54\n"
    "s_cbranch_scc1 L71_%=_x\n"
    "s_sleep 1\n"
    "s_mov_b64 exec, 0x1\n"
    "buffer_load_dword v246, v1, %[srdw], s62 offen sc1\n"
    "s_mov_b64 exec, -1\n"
    "s_waitcnt vmcnt(0)\n"
    "v_cmp_ne_u32 vcc, %[tag], v246\n"
    "s_nop 1\n"
    "s_and_b32 vcc_lo, vcc_lo, 0x1\n"
    "s_cbranch_scc1 L71_%=_l\n"
    "L71_%=_x:\n"
    "s_branch L72_%=\n"
    "L73_%=:\n"
    "L73_%=_l:\n"
    "s_sub_u32 s54, s54, 1\n"
    "s_cselect_b32 s54, 0, s54\n"
    "s_cbranch_scc1 L73_%=_x\n"
    "s_sleep 1\n"
    "s_mov_b32 exec_hi, 0xff\n"
    "buffer_load_dword v246, v1, %[srdw], s63 offen sc1\n"
    "s_mov_b32 exec_hi, -1\n"
    "s_waitcnt vmcnt(0)\n"
    "v_cmp_ne_u32 vcc, %[tag], v246\n"
    "s_nop 1\n"
    "s_and_b32 vcc_hi, vcc_hi, 0xff\n"
    "s_cmp_lg_u64 vcc, 0\n"
    "s_cbranch_scc1 L73_%=_l\n"
    "L73_%=_x:\n"
    "s_branch L74_%=\n"
    "kx_end:\n"
    : "+{v252}"(d0), "+{v253}"(d1), "+{v254}"(pf), "+{v[128:131]}"(qx[0]), "+{v[132:135]}"(qx[1]), "+{v[136:139]}"(qx[2]), "+{v[140:143]}"(qx[3]), "+{v[144:147]}"(qx[4]), "+{v0}"(V[0]), "+{v1}"(V[1]), "+{v2}"(V[2]), "+{v3}"(V[3]), "+{v4}"(V[4]), "+{v5}"(V[5]), "+{v6}"(V[6]), "+{v7}"(V[7]), "+{v8}"(V[8]), "+{v9}"(V[9]), "+{v10}"(V[10]), "+{v11}"(V[11]), "+{v12}"(V[12]), "+{v13}"(V[13]), "+{v14}"(V[14]), "+{v15}"(V[15]), "+{v16}"(V[16]), "+{v17}"(V[17]), "+{v18}"(V[18]), "+{v19}"(V[19]), "+{v76}"(V[20]), "+{v77}"(V[21]), "+{v247}"(V[22]), "+{v240}"(V[23]), "+{v241}"(V[24]), "+{v242}"(V[25]), "+{v243}"(V[26]), "+{v244}"(V[27]), "+{v245}"(V[28]), "+{v248}"(V[29]), "+{v249}"(V[30]), "+{v250}"(V[31]), "+{v251}"(V[32]), "+{v246}"(V[33]), "+{v78}"(V[34]), "+{v255}"(V[35])
    : [srdw] "{s[0:3]}"(srdw), [srdo] "{s[4:7]}"(srdo), [srdm] "{s[8:11]}"(srdm), "{s[44:47]}"(cq), "{s[42:43]}"(kb[2]), [tag] "{s12}"(tag), [t0] "{s13}"(t0), [rt0] "{s14}"(rt0),
      [p1] "{s15}"(Sx[0]), [q] "{s16}"(Sx[1]), [f3p] "{s17}"(Sx[2]), [yst] "{s18}"(Sx[3]), [fwp] "{s19}"(Sx[4]), [cs] "{s20}"(Sx[5]), [w1k] "{s21}"(Sx[6]), [wd] "{s22}"(Sx[7]), [fwd] "{s23}"(Sx[8]), [x] "{s24}"(Sx[9]), [ldsx] "{s25}"(Sx[10]), [hst] "{s26}"(Sx[11]), [f4p] "{s27}"(Sx[12]), [f6c] "{s28}"(Sx[13]), [hld] "{s29}"(Sx[14]), [f4c] "{s30}"(Sx[15]), [xv] "{s31}"(Sx[16]), [gr] "{s35}"(Sx[17]), [f6p] "{s36}"(Sx[18]), [ry] "{s37}"(Sx[19]), [ryy] "{s38}"(Sx[20]), [ldswd] "{s39}"(Sx[21]), [vo] "{s40}"(Sx[22]), [dbg] "{s41}"(Sx[23])
    : "v20", "v21", "v22", "v23", "v24", "v25", "v26", "v27", "v28", "v29", "v30", "v31", "v32", "v33", "v34", "v35", "v36", "v37", "v38", "v39", "v40", "v41", "v42", "v43", "v44", "v45", "v46", "v47", "v48", "v49", "v50", "v51", "v52", "v53", "v54", "v55", "v56", "v57", "v58", "v59", "v60", "v61", "v62", "v63", "v64", "v65", "v66", "v67", "v68", "v69", "v70", "v71", "v72", "v73", "v74", "v75", "v79", "v80", "v81", "v82", "v83", "v84", "v85", "v86", "v87", "v88", "v89", "v90", "v91", "v92", "v93", "v94", "v95", "v96", "v97", "v98", "v99", "v100", "v101", "v102", "v103", "v104", "v105", "v106", "v107", "v108", "v109", "v110", "v111", "v112", "v113", "v114", "v115", "v116", "v117", "v118", "v119", "v120", "v121", "v122", "v123", "v124", "v125", "v126", "v127", "v148", "v149", "v150", "v151", "v152", "v153", "v154", "v155", "v156", "v157", "v158", "v159", "v160", "v161", "v162", "v163", "v164", "v165", "v166", "v167", "v168", "v169", "v170", "v171", "v172", "v173", "v174", "v175", "v176", "v177", "v178", "v179", "v180", "v181", "v182", "v183", "v184", "v185", "v186", "v187", "v188", "v189", "v190", "v191", "v192", "v193", "v194", "v195", "v196", "v197", "v198", "v199", "v200", "v201", "v202", "v203", "v204", "v205", "v206", "v207", "v208", "v209", "v210", "v211", "v212", "v213", "v214", "v215", "v216", "v217", "v218", "v219", "v220", "v221", "v222", "v223", "v224", "v225", "v226", "v227", "v228", "v229", "v230", "v231", "v232", "v233", "v234", "v235", "v236", "v237", "v238", "v239", "a0", "a1", "a2", "a3", "a4", "a5", "a6", "a7", "a8", "a9", "a10", "a11", "a12", "a13", "a14", "a15", "a16", "a17", "a18", "a19", "a20", "a21", "a22", "a23", "a24", "a25", "a26", "a27", "a28", "a29", "a30", "a31", "a32", "a33", "a34", "a35", "a36", "a37", "a38", "a39", "a40", "a41", "a42", "a43", "a44", "a45", "a46", "a47", "a48", "a49", "a50", "a51", "a52", "a53", "a54", "a55", "a56", "a57", "a58", "a59", "a60", "a61", "a62", "a63", "a64", "a65", "a66", "a67", "a68", "a69", "a70", "a71", "a72", "a73", "a74", "a75", "a76", "a77", "a78", "a79", "a80", "a81", "a82", "a83", "a84", "a85", "a86", "a87", "a88", "a89", "a90", "a91", "a92", "a93", "a94", "a95", "a96", "a97", "a98", "a99", "a100", "a101", "a102", "a103", "a104", "a105", "a106", "a107", "a108", "a109", "a110", "a111", "a112", "a113", "a114", "a115", "a116", "a117", "a118", "a119", "a120", "a121", "a122", "a123", "a124", "a125", "a126", "a127", "a128", "a129", "a130", "a131", "a132", "a133", "a134", "a135", "a136", "a137", "a138", "a139", "a140", "a141", "a142", "a143", "a144", "a145", "a146", "a147", "a148", "a149", "a150", "a151", "a152", "a153", "a154", "a155", "a156", "a157", "a158", "a159", "a160", "a161", "a162", "a163", "a164", "a165", "a166", "a167", "a168", "a169", "a170", "a171", "a172", "a173", "a174", "a175", "a176", "a177", "a178", "a179", "a180", "a181", "a182", "a183", "a184", "a185", "a186", "a187", "a188", "a189", "a190", "a191", "a192", "a193", "a194", "a195", "a196", "a197", "a198", "a199", "a200", "a201", "a202", "a203", "a204", "a205", "a206", "a207", "a208", "a209", "a210", "a211", "a212", "a213", "a214", "a215", "a216", "a217", "a218", "a219", "a220", "a221", "a222", "a223", "a224", "a225", "a226", "a227", "a228", "a229", "a230", "a231", "a232", "a233", "a234", "a235", "a236", "a237", "a238", "a239", "a240", "a241", "a242", "a243", "a244", "a245", "a246", "a247", "a248", "a249", "a250", "a251", "a252", "a253", "a254", "a255", "s48", "s49", "s50", "s51", "s52", "s53", "s54", "s55", "s56", "s57", "s58", "s59", "s60", "s61", "s62", "s63", "s64", "s65", "s66", "s67", "s68", "s69", "s70", "s71", "s72", "s73", "s74", "s75", "s76", "s77", "s78", "s79", "s80", "s81", "s82", "s83", "s84", "s85", "s86", "s87", "s88", "s89", "s90", "s91", "s92", "s93", "s94", "s95", "s96", "s97", "s98", "s99", "vcc", "scc", "memory"
  );
}

extern "C" void kernel_launch(void* const* d_in, const int* in_sizes, int n_in, void* d_out, int out_size, void* d_ws, size_t ws_size, hipStream_t stream) {
  (void)hipFuncSetAttribute((const void*)k_main, hipFuncAttributeMaxDynamicSharedMemorySize, 163840);
  k_main<<<256, 256, 163840, stream>>>((const u16*)d_in[1], (const u16*)d_in[2], (const u16*)d_in[3], (const u16*)d_in[5], (char*)d_ws, (const u16*)d_in[4],
                                       (const u16*)d_in[0], (const u16*)d_in[7], (const u16*)d_in[8], (const u16*)d_in[9], (u16*)d_out);
}
```
